```python
import torch

def _softmax(x, axis=-1):
    return torch.softmax(x, dim=axis)

def _gelu(x):
    return torch.nn.functional.gelu(x, approximate='tanh')

def _silu(x):
    return torch.nn.functional.silu(x)

def _sigmoid(x):
    return torch.sigmoid(x)

def _layernorm(x, axis=-1, eps=1e-05):
    m = x.mean(dim=axis, keepdim=True)
    v = x.var(dim=axis, keepdim=True, unbiased=False)
    return (x - m) / torch.sqrt(v + eps)

def _scatter_add(tgt, idx, src, axis=0):
    return tgt.clone().index_add_(axis, idx, src)

def _segment_sum(src, seg, n):
    out = torch.zeros((n,) + src.shape[1:], dtype=src.dtype)
    return out.index_add_(0, seg, src)

def _pad(x, pw):
    flat = tuple((v for pair in reversed(pw) for v in pair))
    return torch.nn.functional.pad(x, flat)
kernel_name = 'adaLN_cond__gqa_block_cached__glu_gated_reglu__prompt'

def setup_inputs():

    _w = lambda a: (lambda t: t.to(torch.bfloat16) if t.is_floating_point() else t)(torch.as_tensor(a))

    def _A_setup():
        B, Sq, D = (1, 2048, 640)
        Hq, Hkv, Sk, Dh = (32, 32, 2048, 128)
        assert Hq % Hkv == 0, (Hq, Hkv)
        return {'x': _w((lambda *s: torch.randn(*s, dtype=torch.bfloat16) * 0.05)(B, Sq, D)), 'k_cache': _w((lambda *s: torch.randn(*s, dtype=torch.bfloat16) * 1.6)(B, Hkv, Sk, Dh)), 'v_cache': _w((lambda *s: torch.randn(*s, dtype=torch.bfloat16) * 0.8)(B, Hkv, Sk, Dh)), 'wq': _w((lambda *s: torch.randn(*s, dtype=torch.bfloat16) * 1.6)(D, Hq * Dh) * D ** (-0.5)), 'wo': _w((lambda *s: torch.randn(*s, dtype=torch.bfloat16) * 0.8)(Hq * Dh, D) * (Hq * Dh) ** (-0.5))}
    _A_in = _A_setup()

    def _B_setup():
        B, S, D, F = (1, 2048, 640, 1728)
        return {'x': _w((lambda *s: torch.randn(*s, dtype=torch.bfloat16) * 0.05)(B, S, D)), 'W_gate': _w((lambda *s: torch.randn(*s, dtype=torch.bfloat16) * 0.4)(D, F) * (2.0 / D) ** 0.5), 'W_up': _w((lambda *s: torch.randn(*s, dtype=torch.bfloat16) * 0.4)(D, F) * (2.0 / D) ** 0.5), 'W_down': _w((lambda *s: torch.randn(*s, dtype=torch.bfloat16) * 0.4)(F, D) * (1.0 / F) ** 0.5)}
    _B_in = _B_setup()
    return {'A_x': _A_in['x'], 'A_k_cache': _A_in['k_cache'], 'A_v_cache': _A_in['v_cache'], 'A_wq': _A_in['wq'], 'A_wo': _A_in['wo'], 'B_x': _B_in['x'], 'B_W_gate': _B_in['W_gate'], 'B_W_up': _B_in['W_up'], 'B_W_down': _B_in['W_down'], 'c': _w((lambda *s: torch.randn(*s, dtype=torch.bfloat16) * 0.2)(1, 640) * 8.0), 'w_mod': _w((lambda *s: torch.randn(*s, dtype=torch.bfloat16) * 0.2)(640, 6 * 640) * 640 ** (-0.5) * 8.0)}

def reference(A_x, A_k_cache, A_v_cache, A_wq, A_wo, B_x, B_W_gate, B_W_up, B_W_down, c, w_mod):

    def _A_ref(x, k_cache, v_cache, wq, wo):
        B, Sq, D = x.shape
        Hq, Dh = (32, 128)
        Hkv = k_cache.shape[1]
        rep = Hq // Hkv
        q = (lambda x, *d: x.permute(*d))((x @ wq).reshape(B, Sq, Hq, Dh), 0, 2, 1, 3)
        k = (lambda x, n, axis: torch.repeat_interleave(x, n, dim=axis))(k_cache, rep, axis=1)
        v = (lambda x, n, axis: torch.repeat_interleave(x, n, dim=axis))(v_cache, rep, axis=1)
        scores = torch.einsum('bhqd,bhkd->bhqk', q, k) * Dh ** (-0.5)
        p = _softmax(scores, axis=-1)
        o = torch.einsum('bhqk,bhkd->bhqd', p, v)
        return (lambda x, *d: x.permute(*d))(o, 0, 2, 1, 3).reshape(B, Sq, Hq * Dh) @ wo

    def _B_ref(x, W_gate, W_up, W_down):
        gate = torch.einsum('bsd,df->bsf', x, W_gate)
        up = torch.einsum('bsd,df->bsf', x, W_up)
        gate = gate * (lambda x, dt: x.to(dt))(gate > 0, gate.dtype)
        h = gate * up
        return torch.einsum('bsf,fd->bsd', h, W_down)
    x = A_x
    mod = (_silu(c) @ w_mod).reshape(c.shape[0], 6, 1, -1)
    sa, ca, ga, sb, cb, gb = (mod[:, 0].clone(), mod[:, 1].clone(), mod[:, 2].clone(), mod[:, 3].clone(), mod[:, 4].clone(), mod[:, 5].clone())
    h = _layernorm(x, axis=-1) * (1 + ca) + sa
    x = x + ga * _A_ref(h, A_k_cache, A_v_cache, A_wq, A_wo)
    h = _layernorm(x, axis=-1) * (1 + cb) + sb
    x = x + gb * _B_ref(h, B_W_gate, B_W_up, B_W_down)
    return x
```

```cpp
#include <hip/hip_runtime.h>
#include <hip/hip_bf16.h>
typedef unsigned short u16;
__device__ inline float bf2f(u16 x){ return __uint_as_float(((unsigned)x)<<16); }
__device__ inline u16 f2bf(float f){ __hip_bfloat16 b = __float2bfloat16(f); return *(u16*)&b; }
constexpr int S=2048, D=640, H=32, DH=128, F=1728;
__global__ void k_mod(const u16* c, const u16* w, float* mod){
  int n = blockIdx.x*blockDim.x+threadIdx.x; if(n>=6*D) return;
  float acc=0; for(int k=0;k<D;k++){ float x=bf2f(c[k]); float s=bf2f(f2bf(x/(1.f+__expf(-x)))); acc+=s*bf2f(w[k*6*D+n]); }
  mod[n]=bf2f(f2bf(acc));
}
__global__ void k_ln(const u16* x, const float* shift, const float* scale, u16* h){
  int r=blockIdx.x; __shared__ float red[2][64];
  float s=0; for(int k=threadIdx.x;k<D;k+=64) s+=bf2f(x[r*D+k]);
  for(int o=32;o;o>>=1) s+=__shfl_xor(s,o); float m=s/D;
  float v=0; for(int k=threadIdx.x;k<D;k+=64){float d=bf2f(x[r*D+k])-m; v+=d*d;}
  for(int o=32;o;o>>=1) v+=__shfl_xor(v,o); float rs=rsqrtf(v/D+1e-5f);
  for(int k=threadIdx.x;k<D;k+=64) h[r*D+k]=f2bf((bf2f(x[r*D+k])-m)*rs*(1.f+scale[k])+shift[k]);
}
__global__ void k_gemm(const u16* A, const u16* B, float* C, int M,int N,int K){
  __shared__ float As[16][17], Bs[16][17];
  int tx=threadIdx.x&15, ty=threadIdx.x>>4; int r=blockIdx.y*16+ty, c=blockIdx.x*16+tx; float acc=0;
  for(int k0=0;k0<K;k0+=16){ As[ty][tx]=bf2f(A[(size_t)r*K+k0+tx]); Bs[ty][tx]=bf2f(B[(size_t)(k0+ty)*N+c]); __syncthreads();
    for(int k=0;k<16;k++) acc+=As[ty][k]*Bs[k][tx]; __syncthreads(); }
  C[(size_t)r*N+c]=acc;
}
__global__ void k_attn(const float* q, const u16* K, const u16* V, u16* o){
  int r=blockIdx.x, h=blockIdx.y; __shared__ float qs[DH], p[S], red[4];
  int t=threadIdx.x; if(t<DH) qs[t]=bf2f(f2bf(q[(size_t)r*H*DH+h*DH+t])); __syncthreads();
  float mx=-1e30f; for(int j=t;j<S;j+=256){ float a=0; const u16* kp=K+((size_t)h*S+j)*DH; for(int d=0;d<DH;d++) a+=qs[d]*bf2f(kp[d]); a*=0.08838834764f; p[j]=a; mx=fmaxf(mx,a);}
  for(int o2=32;o2;o2>>=1) mx=fmaxf(mx,__shfl_xor(mx,o2)); if((t&63)==0) red[t>>6]=mx; __syncthreads(); mx=fmaxf(fmaxf(red[0],red[1]),fmaxf(red[2],red[3])); __syncthreads();
  float s=0; for(int j=t;j<S;j+=256){ float e=__expf(p[j]-mx); p[j]=e; s+=e;}
  for(int o2=32;o2;o2>>=1) s+=__shfl_xor(s,o2); if((t&63)==0) red[t>>6]=s; __syncthreads(); s=red[0]+red[1]+red[2]+red[3];
  if(t<DH){ float a=0; for(int j=0;j<S;j++) a+=p[j]*bf2f(V[((size_t)h*S+j)*DH+t]); o[(size_t)r*H*DH+h*DH+t]=f2bf(a/s);}
}
__global__ void k_res(const u16* x, const float* g, const float* y, u16* out){ int i=blockIdx.x*256+threadIdx.x; out[i]=f2bf(bf2f(x[i])+g[i%D]*y[i]); }
__global__ void k_glu(const float* g, const float* u, u16* h){ int i=blockIdx.x*256+threadIdx.x; h[i]=f2bf(fmaxf(g[i],0.f)*u[i]); }
extern "C" void kernel_launch(void* const* d_in, const int* in_sizes, int n_in, void* d_out, int out_size, void* d_ws, size_t ws_size, hipStream_t st){
  const u16 *x=(const u16*)d_in[0],*K=(const u16*)d_in[1],*V=(const u16*)d_in[2],*wq=(const u16*)d_in[3],*wo=(const u16*)d_in[4],*wg=(const u16*)d_in[6],*wu=(const u16*)d_in[7],*wd=(const u16*)d_in[8],*c=(const u16*)d_in[9],*wm=(const u16*)d_in[10];
  char* ws=(char*)d_ws; float* mod=(float*)ws; u16* h=(u16*)(ws+(1<<20)); float* q=(float*)(ws+(8<<20)); u16* o=(u16*)(ws+(48<<20)); float* y=(float*)(ws+(72<<20)); u16* x1=(u16*)(ws+(80<<20)); float* g=(float*)(ws+(88<<20)); float* u=(float*)(ws+(104<<20)); u16* hh=(u16*)(ws+(120<<20));
  k_mod<<<15,256,0,st>>>(c,wm,mod);
  k_ln<<<S,64,0,st>>>(x,mod,mod+D,h);
  k_gemm<<<dim3(H*DH/16,S/16),256,0,st>>>(h,wq,q,S,H*DH,D);
  k_attn<<<dim3(S,H),256,0,st>>>(q,K,V,o);
  k_gemm<<<dim3(D/16,S/16),256,0,st>>>(o,wo,y,S,D,H*DH);
  k_res<<<S*D/256,256,0,st>>>(x,mod+2*D,y,x1);
  k_ln<<<S,64,0,st>>>(x1,mod+3*D,mod+4*D,h);
  k_gemm<<<dim3(F/16,S/16),256,0,st>>>(h,wg,g,S,F,D);
  k_gemm<<<dim3(F/16,S/16),256,0,st>>>(h,wu,u,S,F,D);
  k_glu<<<S*F/256,256,0,st>>>(g,u,hh);
  k_gemm<<<dim3(D/16,S/16),256,0,st>>>(hh,wd,y,S,D,F);
  k_res<<<S*D/256,256,0,st>>>(x1,mod+5*D,y,(u16*)d_out);
}
```

```cpp
#include <hip/hip_runtime.h>

#define R10(p, n) p #n "0", p #n "1", p #n "2", p #n "3", p #n "4", p #n "5", p #n "6", p #n "7", p #n "8", p #n "9"
__global__ __launch_bounds__(256, 1) void k_main(const void* x, const void* wmod, const void* c, void* ws, const void* wq, const void* K, const void* V, void* out,
                                                 const void* wo, const void* wg, const void* wu, const void* wd) {
  __shared__ __attribute__((aligned(16))) char lds[163840];
  asm volatile(
    "L_top%=:\n\t"
    "s_load_dwordx16 s[60:75], s[0:1], 0\n\t"
    "s_load_dwordx8 s[84:91], s[0:1], 64\n\t"
    "v_lshrrev_b32 v1, 6, v0\n\t"
    "v_and_b32 v255, 63, v0\n\t"
    "s_and_b32 s16, s2, 7\n\t"
    "s_lshr_b32 s17, s2, 3\n\t"
    "v_readfirstlane_b32 s77, v1\n\t"
    "v_lshlrev_b32 v254, 4, v255\n\t"
    "s_mov_b32 s43, 65536\n\t"
    "s_cmp_lt_u32 s17, 16\n\t"
    "s_cbranch_scc1 L_fln%=\n\t"
    "L_fg%=:\n\t"
    "s_mul_i32 s6, s16, 5\n\t"
    "s_add_u32 s6, s6, s17\n\t"
    "s_sub_u32 s6, s6, 26\n\t"
    "s_mul_i32 s7, s16, 10\n\t"
    "s_add_u32 s7, s7, s17\n\t"
    "s_lshl_b32 s7, s7, 6\n\t"
    "s_add_u32 s7, s7, 1536\n\t"
    "s_lshr_b32 s4, s6, 1\n\t"
    "s_lshl_b32 s4, s4, 7\n\t"
    "s_and_b32 s6, s6, 1\n\t"
    "s_mul_i32 s5, s6, 320\n\t"
    "s_cmp_ge_u32 s17, 26\n\t"
    "s_cselect_b32 s4, s4, s7\n\t"
    "s_cselect_b32 s5, s5, 0\n\t"
    "s_cselect_b32 s3, 3, 2\n\t"
    "v_lshrrev_b32 v2, s3, v0\n\t"
    "v_lshlrev_b32 v3, s3, v2\n\t"
    "v_sub_u32 v3, v0, v3\n\t"
    "v_add_u32 v4, s5, v2\n\t"
    "v_mul_u32_u24 v5, 7680, v4\n\t"
    "v_lshl_add_u32 v5, v3, 4, v5\n\t"
    "v_lshlrev_b32 v6, 3, v0\n\t"
    "s_lshr_b32 s7, 1966080, s3\n\t"
    "s_cmp_lt_u32 s17, 31\n\t"
    "s_cselect_b32 s8, 4915200, 0\n\t"
    "s_waitcnt lgkmcnt(0)\n\t"
    "s_mov_b32 s28, s66\n\t"
    "s_and_b32 s29, s67, 0xffff\n\t"
    "s_mov_b32 s30, 67108864\n\t"
    "s_mov_b32 s31, 0x20000\n\t"
    "s_mov_b32 s32, s64\n\t"
    "s_and_b32 s33, s65, 0xffff\n\t"
    "s_mov_b32 s34, 1280\n\t"
    "s_mov_b32 s35, 0x20000\n\t"
    "s_mov_b32 s48, s62\n\t"
    "s_and_b32 s49, s63, 0xffff\n\t"
    "s_mov_b32 s50, s8\n\t"
    "s_mov_b32 s51, 0x20000\n\t"
    "buffer_load_dwordx2 v[62:63], v6, s[32:35], 0 offen\n\t"
    "buffer_load_dword v60, off, s[28:31], s43 sc1\n\t"
    "s_mov_b32 s9, s4\n\t"
    "s_cmp_ge_u32 s17, 26\n\t"
    "s_cbranch_scc1 L_nosl%=\n\t"
    "s_sleep 12\n\t"
    "L_nosl%=:\n\t"
    "buffer_load_dwordx4 v[8:11], v5, s[48:51], s9 offen\n\t"
    "s_add_u32 s9, s9, s7\n\t"
    "buffer_load_dwordx4 v[12:15], v5, s[48:51], s9 offen\n\t"
    "s_add_u32 s9, s9, s7\n\t"
    "buffer_load_dwordx4 v[16:19], v5, s[48:51], s9 offen\n\t"
    "s_add_u32 s9, s9, s7\n\t"
    "buffer_load_dwordx4 v[20:23], v5, s[48:51], s9 offen\n\t"
    "s_add_u32 s9, s9, s7\n\t"
    "buffer_load_dwordx4 v[24:27], v5, s[48:51], s9 offen\n\t"
    "s_add_u32 s9, s9, s7\n\t"
    "buffer_load_dwordx4 v[28:31], v5, s[48:51], s9 offen\n\t"
    "s_add_u32 s9, s9, s7\n\t"
    "buffer_load_dwordx4 v[32:35], v5, s[48:51], s9 offen\n\t"
    "s_add_u32 s9, s9, s7\n\t"
    "buffer_load_dwordx4 v[36:39], v5, s[48:51], s9 offen\n\t"
    "s_add_u32 s9, s9, s7\n\t"
    "buffer_load_dwordx4 v[40:43], v5, s[48:51], s9 offen\n\t"
    "s_add_u32 s9, s9, s7\n\t"
    "buffer_load_dwordx4 v[44:47], v5, s[48:51], s9 offen\n\t"
    "s_sub_u32 s9, s17, 26\n\t"
    "s_cmp_ge_u32 s9, 5\n\t"
    "s_cbranch_scc1 L_fc%=\n\t"
    "L_fz%=:\n\t"
    "s_mul_i32 s8, s16, 5\n\t"
    "s_add_u32 s8, s8, s9\n\t"
    "s_lshl2_add_u32 s8, s8, s77\n\t"
    "s_mul_i32 s8, s8, 768\n\t"
    "s_add_u32 s8, s8, 524288\n\t"
    "v_mov_b32 v100, 0\n\t"
    "v_mov_b32 v101, 0\n\t"
    "v_mov_b32 v102, 0\n\t"
    "v_mov_b32 v103, 0\n\t"
    "s_mul_i32 s9, s6, 10240\n\t"
    "s_add_u32 s9, s9, 106496\n\t"
    "s_lshl2_add_u32 s9, s4, s9\n\t"
    "v_lshlrev_b32 v105, 3, v255\n\t"
    "buffer_load_dword v104, v105, s[28:31], s9 offen sc1\n\t"
    "s_mov_b32 exec_hi, 0xffff\n\t"
    "buffer_store_dwordx4 v[100:103], v254, s[28:31], s8 offen sc1\n\t"
    "s_mov_b32 exec_hi, -1\n\t"
    "s_branch L_fc%=\n\t"
    "L_fln%=:\n\t"
    "v_and_b32 v2, 15, v0\n\t"
    "v_mul_u32_u24 v5, 1280, v2\n\t"
    "v_and_b32 v3, 48, v0\n\t"
    "v_add_u32 v5, v5, v3\n\t"
    "v_mul_u32_u24 v3, 320, v1\n\t"
    "v_add_u32 v5, v5, v3\n\t"
    "s_lshl4_add_u32 s4, s16, s17\n\t"
    "s_mul_i32 s5, s4, 20480\n\t"
    "s_waitcnt lgkmcnt(0)\n\t"
    "s_mov_b32 s52, s60\n\t"
    "s_and_b32 s53, s61, 0xffff\n\t"
    "s_mov_b32 s54, 2621440\n\t"
    "s_mov_b32 s55, 0x20000\n\t"
    "s_mov_b32 s28, s66\n\t"
    "s_and_b32 s29, s67, 0xffff\n\t"
    "s_mov_b32 s30, 67108864\n\t"
    "s_mov_b32 s31, 0x20000\n\t"
    "buffer_load_dwordx4 v[8:11], v5, s[52:55], s5 offen offset:0\n\t"
    "buffer_load_dwordx4 v[12:15], v5, s[52:55], s5 offen offset:64\n\t"
    "buffer_load_dwordx4 v[16:19], v5, s[52:55], s5 offen offset:128\n\t"
    "buffer_load_dwordx4 v[20:23], v5, s[52:55], s5 offen offset:192\n\t"
    "buffer_load_dwordx4 v[24:27], v5, s[52:55], s5 offen offset:256\n\t"
    "buffer_load_dword v60, off, s[28:31], s43 sc1\n\t"
    "L_fc%=:\n\t"
    "v_lshrrev_b32 v50, 4, v255\n\t"
    "v_and_b32 v51, 15, v255\n\t"
    "s_and_b32 s10, s17, 3\n\t"
    "s_lshl2_add_u32 s10, s16, s10\n\t"
    "s_lshl_b32 s37, s77, 12\n\t"
    "v_lshlrev_b32 v52, 2, v50\n\t"
    "v_xor_b32 v52, v52, v51\n\t"
    "v_lshlrev_b32 v52, 4, v52\n\t"
    "v_lshl_add_u32 v232, v50, 13, v52\n\t"
    "v_add_u32 v233, 31744, v232\n\t"
    "v_add_u32 v234, 63488, v232\n\t"
    "v_add_u32 v235, 95232, v232\n\t"
    "s_lshl_b32 s12, s10, 8\n\t"
    "s_add_u32 s44, s68, s12\n\t"
    "s_addc_u32 s45, s69, 0\n\t"
    "s_sub_u32 s12, 5242880, s12\n\t"
    "s_and_b32 s45, s45, 0xffff\n\t"
    "s_mov_b32 s46, s12\n\t"
    "s_mov_b32 s47, 0x20000\n\t"
    "s_lshl_b32 s42, s37, 5\n\t"
    "s_mov_b64 s[18:19], s[66:67]\n\t"
    "s_mov_b32 s52, s60\n\t"
    "s_and_b32 s53, s61, 0xffff\n\t"
    "s_mov_b32 s54, 2621440\n\t"
    "s_mov_b32 s55, 0x20000\n\t"
    "s_mov_b32 s56, s74\n\t"
    "s_and_b32 s57, s75, 0xffff\n\t"
    "s_mov_b32 s58, 2621440\n\t"
    "s_mov_b32 s59, 0x20000\n\t"
    "s_lshl_b32 s12, s10, 19\n\t"
    "s_add_u32 s32, s70, s12\n\t"
    "s_addc_u32 s33, s71, 0\n\t"
    "s_add_u32 s20, s32, 16384\n\t"
    "s_addc_u32 s21, s33, 0\n\t"
    "s_and_b32 s33, s33, 0xffff\n\t"
    "s_mov_b32 s34, 524288\n\t"
    "s_mov_b32 s35, 0x20000\n\t"
    "s_and_b32 s21, s21, 0xffff\n\t"
    "s_mov_b32 s22, 507904\n\t"
    "s_mov_b32 s23, 0x20000\n\t"
    "s_add_u32 s24, s72, s12\n\t"
    "s_addc_u32 s25, s73, 0\n\t"
    "s_and_b32 s25, s25, 0xffff\n\t"
    "s_mov_b32 s26, 524288\n\t"
    "s_mov_b32 s27, 0x20000\n\t"
    "s_and_b32 s11, s17, 28\n\t"
    "s_add_u32 s11, s11, s77\n\t"
    "s_mul_i32 s12, s11, 81920\n\t"
    "s_add_u32 s12, s12, 1048576\n\t"
    "s_add_u32 s48, s18, s12\n\t"
    "s_addc_u32 s49, s19, 0\n\t"
    "s_and_b32 s49, s49, 0xffff\n\t"
    "s_mov_b32 s50, 81920\n\t"
    "s_mov_b32 s51, 0x20000\n\t"
    "v_lshrrev_b32 v53, 2, v51\n\t"
    "v_and_b32 v54, 3, v255\n\t"
    "v_xor_b32 v55, v50, v51\n\t"
    "v_lshlrev_b32 v55, 4, v55\n\t"
    "v_lshl_add_u32 v250, v51, 8, v55\n\t"
    "v_lshl_add_u32 v236, v50, 8, v55\n\t"
    "v_xor_b32 v237, 64, v236\n\t"
    "v_xor_b32 v238, 128, v236\n\t"
    "v_xor_b32 v239, 192, v236\n\t"
    "v_lshlrev_b32 v55, 1, v50\n\t"
    "v_xor_b32 v55, v55, v51\n\t"
    "v_lshlrev_b32 v55, 4, v55\n\t"
    "v_lshl_add_u32 v240, v50, 8, v55\n\t"
    "v_xor_b32 v241, 128, v240\n\t"
    "v_lshrrev_b32 v55, 2, v255\n\t"
    "v_and_b32 v56, 7, v55\n\t"
    "v_lshlrev_b32 v56, 5, v56\n\t"
    "v_lshl_add_u32 v56, v55, 8, v56\n\t"
    "v_lshl_add_u32 v56, v54, 3, v56\n\t"
    "v_add_u32 v251, 65536, v56\n\t"
    "v_mul_u32_u24 v56, 320, v53\n\t"
    "v_lshl_add_u32 v56, v50, 11, v56\n\t"
    "v_lshl_add_u32 v56, v54, 4, v56\n\t"
    "v_mov_b32 v224, v56\n\t"
    "v_xor_b32 v225, 64, v224\n\t"
    "v_xor_b32 v226, 128, v224\n\t"
    "v_xor_b32 v227, 192, v224\n\t"
    "v_cmp_eq_u32 vcc, 0, v54\n\t"
    "v_mov_b32 v56, 0x3f803f80\n\t"
    "s_cmp_lt_u32 s17, 16\n\t"
    "v_cndmask_b32 v56, 0, v56, vcc\n\t"
    "v_accvgpr_write_b32 a192, v56\n\t"
    "v_accvgpr_write_b32 a193, v56\n\t"
    "v_accvgpr_write_b32 a194, v56\n\t"
    "v_accvgpr_write_b32 a195, v56\n\t"
    "s_cbranch_scc1 L_jln%=\n\t"
    "L_jg0%=:\n\t"
    "s_cmp_eq_u32 s17, 31\n\t"
    "s_cbranch_scc1 L_jidle%=\n\t"
    "L_jg%=:\n\t"
    "L_jgw%=:\n\t"
    "s_waitcnt vmcnt(11)\n\t"
    "v_lshlrev_b32 v64, 16, v62\n\t"
    "v_and_b32 v65, 0xffff0000, v62\n\t"
    "v_lshlrev_b32 v66, 16, v63\n\t"
    "v_and_b32 v67, 0xffff0000, v63\n\t"
    "v_mul_f32 v68, 0xbfb8aa3b, v64\n\t"
    "v_mul_f32 v69, 0xbfb8aa3b, v65\n\t"
    "v_mul_f32 v70, 0xbfb8aa3b, v66\n\t"
    "v_mul_f32 v71, 0xbfb8aa3b, v67\n\t"
    "v_exp_f32 v68, v68\n\t"
    "v_exp_f32 v69, v69\n\t"
    "v_exp_f32 v70, v70\n\t"
    "v_exp_f32 v71, v71\n\t"
    "v_add_f32 v68, 1.0, v68\n\t"
    "v_add_f32 v69, 1.0, v69\n\t"
    "v_add_f32 v70, 1.0, v70\n\t"
    "v_add_f32 v71, 1.0, v71\n\t"
    "v_rcp_f32 v68, v68\n\t"
    "v_rcp_f32 v69, v69\n\t"
    "v_rcp_f32 v70, v70\n\t"
    "v_rcp_f32 v71, v71\n\t"
    "v_mul_f32 v64, v64, v68\n\t"
    "v_mul_f32 v65, v65, v69\n\t"
    "v_mul_f32 v66, v66, v70\n\t"
    "v_mul_f32 v67, v67, v71\n\t"
    "v_cvt_pk_bf16_f32 v64, v64, v64\n\t"
    "v_and_b32 v64, 0xffff0000, v64\n\t"
    "v_cvt_pk_bf16_f32 v65, v65, v65\n\t"
    "v_and_b32 v65, 0xffff0000, v65\n\t"
    "v_cvt_pk_bf16_f32 v66, v66, v66\n\t"
    "v_and_b32 v66, 0xffff0000, v66\n\t"
    "v_cvt_pk_bf16_f32 v67, v67, v67\n\t"
    "v_and_b32 v67, 0xffff0000, v67\n\t"
    "s_sub_u32 s9, 8, s3\n\t"
    "v_lshlrev_b32 v6, 2, v0\n\t"
    "v_lshrrev_b32 v7, s9, v6\n\t"
    "v_lshlrev_b32 v3, s9, v7\n\t"
    "v_sub_u32 v6, v6, v3\n\t"
    "v_mad_u32_u24 v6, v6, 20, v7\n\t"
    "v_cmp_gt_u32 vcc, 160, v0\n\t"
    "v_mov_b32 v7, 1280\n\t"
    "s_lshr_b32 s9, s5, 3\n\t"
    "v_cndmask_b32 v6, v7, v6, vcc\n\t"
    "v_lshlrev_b32 v6, 2, v6\n\t"
    "v_add_u32 v6, 139264, v6\n\t"
    "ds_write_b32 v6, v64 offset:0\n\t"
    "ds_write_b32 v6, v65 offset:80\n\t"
    "ds_write_b32 v6, v66 offset:160\n\t"
    "ds_write_b32 v6, v67 offset:240\n\t"
    "s_add_u32 s9, s9, 139264\n\t"
    "v_mul_u32_u24 v4, 80, v2\n\t"
    "v_add_u32 v4, s9, v4\n\t"
    "v_mov_b32 v90, 0\n\t"
    "v_mov_b32 v91, 0\n\t"
    "v_mov_b32 v92, 0\n\t"
    "v_mov_b32 v93, 0\n\t"
    "v_mov_b32 v94, 0\n\t"
    "v_mov_b32 v95, 0\n\t"
    "v_mov_b32 v96, 0\n\t"
    "v_mov_b32 v97, 0\n\t"
    "v_and_b32 v6, 7, v255\n\t"
    "v_lshlrev_b32 v6, 5, v6\n\t"
    "v_lshl_add_u32 v6, v50, 3, v6\n\t"
    "v_lshl_add_u32 v6, v1, 8, v6\n\t"
    "v_add_u32 v6, 147456, v6\n\t"
    "v_lshlrev_b32 v7, 2, v255\n\t"
    "v_add_u32 v2, 147456, v7\n\t"
    "s_waitcnt lgkmcnt(0)\n\t"
    "s_barrier\n\t"
    "ds_read_b64 v[64:65], v4 offset:0\n\t"
    "ds_read_b64 v[66:67], v4 offset:8\n\t"
    "ds_read_b64 v[68:69], v4 offset:16\n\t"
    "ds_read_b64 v[70:71], v4 offset:24\n\t"
    "ds_read_b64 v[72:73], v4 offset:32\n\t"
    "s_waitcnt lgkmcnt(4)\n\t"
    "v_lshrrev_b32 v74, 16, v64\n\t"
    "v_lshrrev_b32 v75, 16, v65\n\t"
    "s_waitcnt lgkmcnt(3)\n\t"
    "v_lshrrev_b32 v76, 16, v66\n\t"
    "v_lshrrev_b32 v77, 16, v67\n\t"
    "s_waitcnt lgkmcnt(2)\n\t"
    "v_lshrrev_b32 v78, 16, v68\n\t"
    "v_lshrrev_b32 v79, 16, v69\n\t"
    "s_waitcnt lgkmcnt(1)\n\t"
    "v_lshrrev_b32 v80, 16, v70\n\t"
    "v_lshrrev_b32 v81, 16, v71\n\t"
    "s_waitcnt lgkmcnt(0)\n\t"
    "v_lshrrev_b32 v82, 16, v72\n\t"
    "v_lshrrev_b32 v83, 16, v73\n\t"
    "s_waitcnt vmcnt(9)\n\t"
    "v_dot2c_f32_bf16 v90, v8, v74\n\t"
    "v_dot2c_f32_bf16 v91, v8, v64\n\t"
    "v_dot2c_f32_bf16 v92, v9, v74\n\t"
    "v_dot2c_f32_bf16 v93, v9, v64\n\t"
    "v_dot2c_f32_bf16 v94, v10, v74\n\t"
    "v_dot2c_f32_bf16 v95, v10, v64\n\t"
    "v_dot2c_f32_bf16 v96, v11, v74\n\t"
    "v_dot2c_f32_bf16 v97, v11, v64\n\t"
    "s_waitcnt vmcnt(8)\n\t"
    "v_dot2c_f32_bf16 v90, v12, v75\n\t"
    "v_dot2c_f32_bf16 v91, v12, v65\n\t"
    "v_dot2c_f32_bf16 v92, v13, v75\n\t"
    "v_dot2c_f32_bf16 v93, v13, v65\n\t"
    "v_dot2c_f32_bf16 v94, v14, v75\n\t"
    "v_dot2c_f32_bf16 v95, v14, v65\n\t"
    "v_dot2c_f32_bf16 v96, v15, v75\n\t"
    "v_dot2c_f32_bf16 v97, v15, v65\n\t"
    "s_waitcnt vmcnt(7)\n\t"
    "v_dot2c_f32_bf16 v90, v16, v76\n\t"
    "v_dot2c_f32_bf16 v91, v16, v66\n\t"
    "v_dot2c_f32_bf16 v92, v17, v76\n\t"
    "v_dot2c_f32_bf16 v93, v17, v66\n\t"
    "v_dot2c_f32_bf16 v94, v18, v76\n\t"
    "v_dot2c_f32_bf16 v95, v18, v66\n\t"
    "v_dot2c_f32_bf16 v96, v19, v76\n\t"
    "v_dot2c_f32_bf16 v97, v19, v66\n\t"
    "s_waitcnt vmcnt(6)\n\t"
    "v_dot2c_f32_bf16 v90, v20, v77\n\t"
    "v_dot2c_f32_bf16 v91, v20, v67\n\t"
    "v_dot2c_f32_bf16 v92, v21, v77\n\t"
    "v_dot2c_f32_bf16 v93, v21, v67\n\t"
    "v_dot2c_f32_bf16 v94, v22, v77\n\t"
    "v_dot2c_f32_bf16 v95, v22, v67\n\t"
    "v_dot2c_f32_bf16 v96, v23, v77\n\t"
    "v_dot2c_f32_bf16 v97, v23, v67\n\t"
    "s_waitcnt vmcnt(5)\n\t"
    "v_dot2c_f32_bf16 v90, v24, v78\n\t"
    "v_dot2c_f32_bf16 v91, v24, v68\n\t"
    "v_dot2c_f32_bf16 v92, v25, v78\n\t"
    "v_dot2c_f32_bf16 v93, v25, v68\n\t"
    "v_dot2c_f32_bf16 v94, v26, v78\n\t"
    "v_dot2c_f32_bf16 v95, v26, v68\n\t"
    "v_dot2c_f32_bf16 v96, v27, v78\n\t"
    "v_dot2c_f32_bf16 v97, v27, v68\n\t"
    "s_waitcnt vmcnt(4)\n\t"
    "v_dot2c_f32_bf16 v90, v28, v79\n\t"
    "v_dot2c_f32_bf16 v91, v28, v69\n\t"
    "v_dot2c_f32_bf16 v92, v29, v79\n\t"
    "v_dot2c_f32_bf16 v93, v29, v69\n\t"
    "v_dot2c_f32_bf16 v94, v30, v79\n\t"
    "v_dot2c_f32_bf16 v95, v30, v69\n\t"
    "v_dot2c_f32_bf16 v96, v31, v79\n\t"
    "v_dot2c_f32_bf16 v97, v31, v69\n\t"
    "s_waitcnt vmcnt(3)\n\t"
    "v_dot2c_f32_bf16 v90, v32, v80\n\t"
    "v_dot2c_f32_bf16 v91, v32, v70\n\t"
    "v_dot2c_f32_bf16 v92, v33, v80\n\t"
    "v_dot2c_f32_bf16 v93, v33, v70\n\t"
    "v_dot2c_f32_bf16 v94, v34, v80\n\t"
    "v_dot2c_f32_bf16 v95, v34, v70\n\t"
    "v_dot2c_f32_bf16 v96, v35, v80\n\t"
    "v_dot2c_f32_bf16 v97, v35, v70\n\t"
    "s_waitcnt vmcnt(2)\n\t"
    "v_dot2c_f32_bf16 v90, v36, v81\n\t"
    "v_dot2c_f32_bf16 v91, v36, v71\n\t"
    "v_dot2c_f32_bf16 v92, v37, v81\n\t"
    "v_dot2c_f32_bf16 v93, v37, v71\n\t"
    "v_dot2c_f32_bf16 v94, v38, v81\n\t"
    "v_dot2c_f32_bf16 v95, v38, v71\n\t"
    "v_dot2c_f32_bf16 v96, v39, v81\n\t"
    "v_dot2c_f32_bf16 v97, v39, v71\n\t"
    "s_waitcnt vmcnt(1)\n\t"
    "v_dot2c_f32_bf16 v90, v40, v82\n\t"
    "v_dot2c_f32_bf16 v91, v40, v72\n\t"
    "v_dot2c_f32_bf16 v92, v41, v82\n\t"
    "v_dot2c_f32_bf16 v93, v41, v72\n\t"
    "v_dot2c_f32_bf16 v94, v42, v82\n\t"
    "v_dot2c_f32_bf16 v95, v42, v72\n\t"
    "v_dot2c_f32_bf16 v96, v43, v82\n\t"
    "v_dot2c_f32_bf16 v97, v43, v72\n\t"
    "s_waitcnt vmcnt(0)\n\t"
    "v_dot2c_f32_bf16 v90, v44, v83\n\t"
    "v_dot2c_f32_bf16 v91, v44, v73\n\t"
    "v_dot2c_f32_bf16 v92, v45, v83\n\t"
    "v_dot2c_f32_bf16 v93, v45, v73\n\t"
    "v_dot2c_f32_bf16 v94, v46, v83\n\t"
    "v_dot2c_f32_bf16 v95, v46, v73\n\t"
    "v_dot2c_f32_bf16 v96, v47, v83\n\t"
    "v_dot2c_f32_bf16 v97, v47, v73\n\t"
    "v_readfirstlane_b32 s97, v60\n\t"
    "s_nop 1\n\t"
    "v_permlane32_swap_b32 v90, v94\n\t"
    "v_permlane32_swap_b32 v91, v95\n\t"
    "v_permlane32_swap_b32 v92, v96\n\t"
    "v_permlane32_swap_b32 v93, v97\n\t"
    "v_add_f32 v90, v90, v94\n\t"
    "v_add_f32 v91, v91, v95\n\t"
    "v_add_f32 v92, v92, v96\n\t"
    "v_add_f32 v93, v93, v97\n\t"
    "s_mul_i32 s97, s97, 2654435761\n\t"
    "s_add_u32 s97, s97, 2135587861\n\t"
    "v_permlane16_swap_b32 v90, v92\n\t"
    "v_permlane16_swap_b32 v91, v93\n\t"
    "v_add_f32 v90, v90, v92\n\t"
    "v_add_f32 v91, v91, v93\n\t"
    "s_nop 1\n\t"
    "v_add_f32_dpp v90, v90, v90 row_ror:8 row_mask:0xf bank_mask:0xf\n\t"
    "v_add_f32_dpp v91, v91, v91 row_ror:8 row_mask:0xf bank_mask:0xf\n\t"
    "ds_write_b64 v6, v[90:91]\n\t"
    "s_waitcnt vmcnt(0) lgkmcnt(0)\n\t"
    "s_barrier\n\t"
    "s_cmp_lg_u32 s77, 0\n\t"
    "s_cbranch_scc1 L_jdone%=\n\t"
    "L_jg1%=:\n\t"
    "ds_read_b32 v64, v2 offset:0\n\t"
    "ds_read_b32 v65, v2 offset:256\n\t"
    "ds_read_b32 v66, v2 offset:512\n\t"
    "ds_read_b32 v67, v2 offset:768\n\t"
    "v_mov_b32 v69, s97\n\t"
    "v_mov_b32 v71, s97\n\t"
    "s_mul_i32 s9, s6, 10240\n\t"
    "s_add_u32 s9, s9, 106496\n\t"
    "s_lshl2_add_u32 s9, s4, s9\n\t"
    "v_lshlrev_b32 v72, 3, v255\n\t"
    "s_waitcnt lgkmcnt(2)\n\t"
    "v_add_f32 v64, v64, v65\n\t"
    "s_waitcnt lgkmcnt(0)\n\t"
    "v_add_f32 v66, v66, v67\n\t"
    "v_add_f32 v68, v64, v66\n\t"
    "s_cmp_lt_u32 s17, 26\n\t"
    "s_cbranch_scc1 L_jb%=\n\t"
    "L_ja%=:\n\t"
    "v_mov_b32_dpp v70, v68 quad_perm:[1,1,3,3] row_mask:0xf bank_mask:0xf\n\t"
    "s_mov_b32 exec_lo, 0x55555555\n\t"
    "s_mov_b32 exec_hi, 0x55555555\n\t"
    "buffer_store_dwordx4 v[68:71], v72, s[28:31], s9 offen sc1\n\t"
    "s_mov_b64 exec, -1\n\t"
    "s_branch L_jdone%=\n\t"
    "L_jb%=:\n\t"
    "v_mov_b32 v64, v68\n\t"
    "s_sub_u32 s9, s4, 5120\n\t"
    "s_cmp_lt_u32 s9, 1280\n\t"
    "s_cselect_b32 s9, 1.0, 0\n\t"
    "v_permlane32_swap_b32 v64, v68\n\t"
    "s_nop 0\n\t"
    "v_add_f32 v64, v64, v68\n\t"
    "v_cvt_pk_bf16_f32 v64, v64, v64\n\t"
    "v_and_b32 v64, 0xffff0000, v64\n\t"
    "v_add_f32 v64, s9, v64\n\t"
    "v_cvt_pk_bf16_f32 v64, v64, v64\n\t"
    "v_and_b32 v64, 0xffff0000, v64\n\t"
    "v_lshrrev_b32 v65, 1, v7\n\t"
    "s_lshl1_add_u32 s9, s4, 64512\n\t"
    "s_add_u32 s8, s4, 79360\n\t"
    "s_mov_b32 exec_hi, 0\n\t"
    "buffer_store_dword v64, v7, s[28:31], s9 offen sc1\n\t"
    "buffer_store_short_d16_hi v64, v65, s[28:31], s8 offen sc1\n\t"
    "s_mov_b32 exec_hi, -1\n\t"
    "s_branch L_jdone%=\n\t"
    "L_jidle%=:\n\t"
    "s_lshl_b32 s8, s16, 12\n\t"
    "s_add_u32 s9, s8, 69632\n\t"
    "s_lshl1_add_u32 s8, s8, 655360\n\t"
    "v_lshlrev_b32 v67, 5, v0\n\t"
    "buffer_load_dword v98, v67, s[28:31], s9 offen sc1\n\t"
    "buffer_load_dword v99, v67, s[28:31], s8 offen sc1\n\t"
    "v_lshlrev_b32 v67, 20, v0\n\t"
    "s_mov_b32 s7, 0x20000\n\t"
    "s_mov_b32 s4, s66\n\t"
    "s_and_b32 s5, s67, 0xffff\n\t"
    "s_mov_b32 s6, 60817408\n\t"
    "s_mov_b32 s8, 0\n\t"
    "buffer_load_dword v70, v67, s[4:7], s8 offen sc1\n\t"
    "s_mov_b32 s4, s74\n\t"
    "s_and_b32 s5, s75, 0xffff\n\t"
    "s_mov_b32 s6, 2621440\n\t"
    "s_mov_b32 s8, 0\n\t"
    "buffer_load_dword v71, v67, s[4:7], s8 offen sc1\n\t"
    "s_mov_b32 s4, s70\n\t"
    "s_and_b32 s5, s71, 0xffff\n\t"
    "s_mov_b32 s6, 16777216\n\t"
    "s_mov_b32 s8, 0\n\t"
    "buffer_load_dword v72, v67, s[4:7], s8 offen sc1\n\t"
    "s_mov_b32 s4, s72\n\t"
    "s_and_b32 s5, s73, 0xffff\n\t"
    "s_mov_b32 s6, 16777216\n\t"
    "s_mov_b32 s8, 0\n\t"
    "buffer_load_dword v73, v67, s[4:7], s8 offen sc1\n\t"
    "s_mov_b32 s4, s68\n\t"
    "s_and_b32 s5, s69, 0xffff\n\t"
    "s_mov_b32 s6, 5242880\n\t"
    "s_mov_b32 s8, 0\n\t"
    "buffer_load_dword v74, v67, s[4:7], s8 offen sc1\n\t"
    "s_mov_b32 s4, s84\n\t"
    "s_and_b32 s5, s85, 0xffff\n\t"
    "s_mov_b32 s6, 5242880\n\t"
    "s_mov_b32 s8, 0\n\t"
    "buffer_load_dword v75, v67, s[4:7], s8 offen sc1\n\t"
    "s_mov_b32 s4, s86\n\t"
    "s_and_b32 s5, s87, 0xffff\n\t"
    "s_mov_b32 s6, 2211840\n\t"
    "s_mov_b32 s8, 0\n\t"
    "buffer_load_dword v76, v67, s[4:7], s8 offen sc1\n\t"
    "s_mov_b32 s4, s88\n\t"
    "s_and_b32 s5, s89, 0xffff\n\t"
    "s_mov_b32 s6, 2211840\n\t"
    "s_mov_b32 s8, 0\n\t"
    "buffer_load_dword v77, v67, s[4:7], s8 offen sc1\n\t"
    "s_mov_b32 s4, s90\n\t"
    "s_and_b32 s5, s91, 0xffff\n\t"
    "s_mov_b32 s6, 2211840\n\t"
    "s_mov_b32 s8, 0\n\t"
    "buffer_load_dword v78, v67, s[4:7], s8 offen sc1\n\t"
    "s_getpc_b64 s[4:5]\n\t"
    "L_pc%=:\n\t"
    "s_and_b32 s5, s5, 0xffff\n\t"
    "s_mov_b32 s6, L_bottom%=-L_pc%=\n\t"
    "s_mov_b32 s7, 0x20000\n\t"
    "v_lshlrev_b32 v6, 7, v0\n\t"
    "s_mov_b32 s8, 32768\n\t"
    "buffer_load_dword v64, v6, s[4:7], 0 offen\n\t"
    "buffer_load_dword v65, v6, s[4:7], s8 offen\n\t"
    "s_mov_b32 s8, 65536\n\t"
    "buffer_load_dword v66, v6, s[4:7], s8 offen\n\t"
    "s_waitcnt vmcnt(24)\n\t"
    "v_readfirstlane_b32 s97, v60\n\t"
    "s_nop 0\n\t"
    "s_mul_i32 s97, s97, 2654435761\n\t"
    "s_add_u32 s97, s97, 2135587861\n\t"
    "L_jdone%=:\n\t"
    "s_lshl_b32 s9, s11, 9\n\t"
    "s_add_u32 s9, s9, 655360\n\t"
    "v_lshl_add_u32 v57, v51, 5, s9\n\t"
    "s_and_b32 s8, s17, 7\n\t"
    "s_lshr_b32 s3, s16, 1\n\t"
    "s_lshl3_add_u32 s3, s3, s8\n\t"
    "s_and_b32 s4, s16, 1\n\t"
    "s_lshr_b32 s5, s17, 3\n\t"
    "s_lshl2_add_u32 s4, s4, s5\n\t"
    "s_and_b32 s5, s16, 3\n\t"
    "s_mul_i32 s5, s5, 27\n\t"
    "s_add_u32 s5, s5, s17\n\t"
    "s_and_b32 s6, s5, 3\n\t"
    "s_lshr_b32 s5, s5, 2\n\t"
    "s_and_b32 s7, s16, 4\n\t"
    "s_add_u32 s6, s6, s7\n\t"
    "s_lshl2_add_u32 s7, s6, s77\n\t"
    "s_mul_i32 s9, s10, 16384\n\t"
    "s_add_u32 s9, s9, 16777216\n\t"
    "s_mul_i32 s39, s16, 8192\n\t"
    "s_add_u32 s39, s39, s9\n\t"
    "s_mul_i32 s9, s11, 592896\n\t"
    "s_add_u32 s39, s39, s9\n\t"
    "s_lshl2_add_u32 s9, s3, s77\n\t"
    "s_mul_i32 s15, s9, 1024\n\t"
    "s_add_u32 s15, s15, 131072\n\t"
    "s_mul_i32 s71, s9, 20480\n\t"
    "s_mul_i32 s72, s4, 160\n\t"
    "s_add_u32 s71, s71, s72\n\t"
    "s_lshl_b32 s72, s72, 1\n\t"
    "s_and_b32 s8, s9, 7\n\t"
    "s_mul_i32 s93, s8, 73728\n\t"
    "s_add_u32 s93, s93, 16777216\n\t"
    "s_mul_i32 s60, s3, 592896\n\t"
    "s_add_u32 s60, s60, s93\n\t"
    "s_xor_b32 s9, s3, 1\n\t"
    "s_mul_i32 s9, s9, 592896\n\t"
    "s_add_u32 s93, s93, s9\n\t"
    "s_lshl3_add_u32 s9, s3, s8\n\t"
    "s_andn2_b32 s9, s9, 8\n\t"
    "s_and_b32 s94, s16, 1\n\t"
    "s_lshl1_add_u32 s9, s9, s94\n\t"
    "s_mul_i32 s94, s9, 128\n\t"
    "s_add_u32 s94, s94, 524288\n\t"
    "s_lshl3_add_u32 s9, s4, s8\n\t"
    "s_mul_i32 s61, s9, 81920\n\t"
    "s_add_u32 s61, s61, 37748736\n\t"
    "s_lshl3_add_u32 s9, s3, s4\n\t"
    "s_lshl2_add_u32 s9, s9, s77\n\t"
    "s_mul_i32 s95, s9, 8192\n\t"
    "s_add_u32 s95, s95, 52428800\n\t"
    "s_mul_i32 s62, s7, 81920\n\t"
    "s_add_u32 s62, s62, 4194304\n\t"
    "s_mul_i32 s73, s7, 4096\n\t"
    "s_add_u32 s73, s73, 131072\n\t"
    "s_mul_i32 s9, s7, 27\n\t"
    "s_add_u32 s9, s9, s5\n\t"
    "s_mul_i32 s64, s9, 8192\n\t"
    "s_add_u32 s64, s64, 8388608\n\t"
    "s_lshr_b32 s9, s16, 2\n\t"
    "s_mul_i32 s9, s9, 5\n\t"
    "s_add_u32 s9, s9, s17\n\t"
    "s_sub_u32 s9, s9, 27\n\t"
    "s_and_b32 s98, s16, 3\n\t"
    "s_mul_i32 s98, s98, 7\n\t"
    "s_add_u32 s98, s98, s9\n\t"
    "s_cmp_lt_u32 s9, 7\n\t"
    "s_cselect_b32 s98, s98, 27\n\t"
    "s_cmp_lt_u32 s17, 27\n\t"
    "s_cselect_b32 s98, s5, s98\n\t"
    "s_mul_i32 s63, s98, 163840\n\t"
    "s_add_u32 s63, s63, s37\n\t"
    "s_add_u32 s63, s63, 47185920\n\t"
    "s_cmp_lt_u32 s98, 27\n\t"
    "s_cselect_b32 s63, s63, 67108864\n\t"
    "v_accvgpr_write_b32 a250, s98\n\t"
    "s_mul_i32 s9, s77, 57344\n\t"
    "s_add_u32 s9, s9, 8388608\n\t"
    "s_mul_i32 s65, s3, 221184\n\t"
    "s_add_u32 s65, s65, s9\n\t"
    "s_mul_i32 s9, s77, 71680\n\t"
    "s_add_u32 s9, s9, 44040192\n\t"
    "s_mul_i32 s66, s4, 276480\n\t"
    "s_add_u32 s66, s66, s9\n\t"
    "s_mul_i32 s9, s77, 2048\n\t"
    "s_add_u32 s9, s9, 4194304\n\t"
    "s_mul_i32 s14, s3, 81920\n\t"
    "s_add_u32 s14, s14, s9\n\t"
    "s_lshl3_add_u32 s9, s11, s16\n\t"
    "s_mul_i32 s67, s9, 256\n\t"
    "s_add_u32 s67, s67, 524288\n\t"
    "s_lshl2_add_u32 s9, s3, s77\n\t"
    "s_and_b32 s8, s16, 1\n\t"
    "s_lshl1_add_u32 s9, s9, s8\n\t"
    "s_mul_i32 s68, s9, 128\n\t"
    "s_add_u32 s68, s68, 589824\n\t"
    "s_mul_i32 s9, s5, 37\n\t"
    "s_lshr_b32 s9, s9, 8\n\t"
    "s_lshl2_add_u32 s9, s7, s9\n\t"
    "s_mul_i32 s69, s9, 256\n\t"
    "s_add_u32 s69, s69, 589824\n\t"
    "s_and_b32 s8, s16, 3\n\t"
    "s_lshl2_add_u32 s9, s7, s8\n\t"
    "s_mul_i32 s70, s9, 128\n\t"
    "s_add_u32 s70, s70, 622592\n\t"
    "s_mov_b32 s74, s17\n\t"
    "s_lshl2_add_u32 s9, s2, s77\n\t"
    "s_mul_i32 s13, s9, 256\n\t"
    "s_add_u32 s13, s13, 62914560\n\t"
    "s_lshr_b32 s10, s9, 3\n\t"
    "s_and_b32 s11, s9, 7\n\t"
    "s_mul_i32 s3, s11, 160\n\t"
    "s_mul_i32 s4, s10, 40960\n\t"
    "s_add_u32 s4, s4, s3\n\t"
    "s_add_u32 s0, s84, s4\n\t"
    "s_addc_u32 s1, s85, 0\n\t"
    "s_add_u32 s90, s90, s4\n\t"
    "s_addc_u32 s91, s91, 0\n\t"
    "s_mul_i32 s6, s11, 128\n\t"
    "s_add_u32 s6, s6, s10\n\t"
    "s_mul_i32 s6, s6, 5120\n\t"
    "s_add_u32 s6, s6, 37748736\n\t"
    "s_mul_i32 s8, s11, 54\n\t"
    "s_add_u32 s8, s8, s10\n\t"
    "s_mul_i32 s8, s8, 5120\n\t"
    "s_add_u32 s8, s8, 44204032\n\t"
    "s_add_u32 s2, s0, 3276800\n\t"
    "s_addc_u32 s3, s1, 0\n\t"
    "s_sub_u32 s4, s90, 1966080\n\t"
    "s_subb_u32 s5, s91, 0\n\t"
    "s_add_u32 s7, s6, 409600\n\t"
    "s_sub_u32 s10, s8, 409600\n\t"
    "s_cmp_lt_u32 s9, 384\n\t"
    "s_cselect_b64 s[2:3], s[2:3], s[4:5]\n\t"
    "s_cselect_b32 s7, s7, s10\n\t"
    "s_add_u32 s90, s90, 1310720\n\t"
    "s_addc_u32 s91, s91, 0\n\t"
    "s_bitcmp1_b32 s9, 0\n\t"
    "s_cselect_b64 s[86:87], s[88:89], s[86:87]\n\t"
    "s_cselect_b32 s11, 4096, 0\n\t"
    "s_add_u32 s11, s11, 47185920\n\t"
    "s_max_u32 s10, s9, 176\n\t"
    "s_sub_u32 s10, s10, 176\n\t"
    "s_lshr_b32 s10, s10, 1\n\t"
    "s_mul_hi_u32 s84, s10, 0x97b425f\n\t"
    "s_mul_i32 s85, s84, 27\n\t"
    "s_sub_u32 s85, s10, s85\n\t"
    "s_mul_i32 s88, s84, 110592\n\t"
    "s_lshl_b32 s89, s85, 7\n\t"
    "s_add_u32 s88, s88, s89\n\t"
    "s_add_u32 s4, s86, s88\n\t"
    "s_addc_u32 s5, s87, 0\n\t"
    "s_mul_i32 s85, s85, 20\n\t"
    "s_add_u32 s85, s85, s84\n\t"
    "s_mul_i32 s10, s85, 8192\n\t"
    "s_add_u32 s10, s10, s11\n\t"
    "s_cmp_lt_u32 s9, 176\n\t"
    "s_cselect_b64 s[4:5], s[90:91], s[4:5]\n\t"
    "s_cselect_b32 s8, s8, s10\n\t"
    "s_add_u32 s10, s9, 464\n\t"
    "s_lshr_b32 s10, s10, 1\n\t"
    "s_mul_hi_u32 s84, s10, 0x97b425f\n\t"
    "s_mul_i32 s85, s84, 27\n\t"
    "s_sub_u32 s85, s10, s85\n\t"
    "s_mul_i32 s88, s84, 110592\n\t"
    "s_lshl_b32 s89, s85, 7\n\t"
    "s_add_u32 s88, s88, s89\n\t"
    "s_add_u32 s90, s86, s88\n\t"
    "s_addc_u32 s91, s87, 0\n\t"
    "s_mul_i32 s85, s85, 20\n\t"
    "s_add_u32 s85, s85, s84\n\t"
    "s_mul_i32 s10, s85, 8192\n\t"
    "s_add_u32 s10, s10, s11\n\t"
    "v_accvgpr_write_b32 a252, s90\n\t"
    "v_accvgpr_write_b32 a253, s91\n\t"
    "v_add_u32 v228, 0x10000, v224\n\t"
    "v_add_u32 v229, 0x10000, v225\n\t"
    "v_add_u32 v230, 0x10000, v226\n\t"
    "v_add_u32 v231, 0x10000, v227\n\t"
    "s_and_b32 s84, s17, 7\n\t"
    "s_lshl2_add_u32 s84, s84, s77\n\t"
    "s_lshl_b32 s84, s84, 11\n\t"
    "v_lshlrev_b32 v249, 26, v50\n\t"
    "v_lshl_add_u32 v249, v51, 7, v249\n\t"
    "v_add_u32 v249, s84, v249\n\t"
    "s_waitcnt vmcnt(0)\n\t"
    "s_barrier\n\t"
    "s_sub_u32 s38, s17, 26\n\t"
    "s_cmp_lt_u32 s38, 4\n\t"
    "s_cselect_b32 s38, 3, 7\n\t"
    "s_cselect_b32 s40, 1, -1\n\t"
    "s_lshl_b32 m0, s38, 14\n\t"
    "s_add_u32 m0, m0, s37\n\t"
    "s_lshl_b32 s36, s38, 19\n\t"
    "s_add_u32 s36, s36, s42\n\t"
    "s_add_u32 s38, s38, s40\n\t"
    "buffer_load_dwordx4 v232, s[44:47], s36 offen offset:0  lds\n\t"
    "buffer_load_dwordx4 v233, s[44:47], s36 offen offset:1024  lds\n\t"
    "buffer_load_dwordx4 v234, s[44:47], s36 offen offset:2048  lds\n\t"
    "buffer_load_dwordx4 v235, s[44:47], s36 offen offset:3072  lds\n\t"
    "s_add_u32 m0, s37, 0\n\t"
    "s_add_u32 s36, s42, 0\n\t"
    "buffer_load_dwordx4 v232, s[44:47], s36 offen offset:0  lds\n\t"
    "buffer_load_dwordx4 v233, s[44:47], s36 offen offset:1024  lds\n\t"
    "buffer_load_dwordx4 v234, s[44:47], s36 offen offset:2048  lds\n\t"
    "buffer_load_dwordx4 v235, s[44:47], s36 offen offset:3072  lds\n\t"
    "s_add_u32 m0, s37, 16384\n\t"
    "s_add_u32 s36, s42, 524288\n\t"
    "buffer_load_dwordx4 v232, s[44:47], s36 offen offset:0  lds\n\t"
    "buffer_load_dwordx4 v233, s[44:47], s36 offen offset:1024  lds\n\t"
    "buffer_load_dwordx4 v234, s[44:47], s36 offen offset:2048  lds\n\t"
    "buffer_load_dwordx4 v235, s[44:47], s36 offen offset:3072  lds\n\t"
    "s_add_u32 m0, s37, 32768\n\t"
    "s_add_u32 s36, s42, 1048576\n\t"
    "buffer_load_dwordx4 v232, s[44:47], s36 offen offset:0  lds\n\t"
    "buffer_load_dwordx4 v233, s[44:47], s36 offen offset:1024  lds\n\t"
    "buffer_load_dwordx4 v234, s[44:47], s36 offen offset:2048  lds\n\t"
    "buffer_load_dwordx4 v235, s[44:47], s36 offen offset:3072  lds\n\t"
    "s_lshl_b32 m0, s38, 14\n\t"
    "s_add_u32 m0, m0, s37\n\t"
    "s_lshl_b32 s36, s38, 19\n\t"
    "s_add_u32 s36, s36, s42\n\t"
    "s_add_u32 s38, s38, s40\n\t"
    "buffer_load_dwordx4 v232, s[44:47], s36 offen offset:0  lds\n\t"
    "buffer_load_dwordx4 v233, s[44:47], s36 offen offset:1024  lds\n\t"
    "buffer_load_dwordx4 v234, s[44:47], s36 offen offset:2048  lds\n\t"
    "buffer_load_dwordx4 v235, s[44:47], s36 offen offset:3072  lds\n\t"
    "s_lshl_b32 m0, s38, 14\n\t"
    "s_add_u32 m0, m0, s37\n\t"
    "s_lshl_b32 s36, s38, 19\n\t"
    "s_add_u32 s36, s36, s42\n\t"
    "s_add_u32 s38, s38, s40\n\t"
    "buffer_load_dwordx4 v232, s[44:47], s36 offen offset:0  lds\n\t"
    "buffer_load_dwordx4 v233, s[44:47], s36 offen offset:1024  lds\n\t"
    "buffer_load_dwordx4 v234, s[44:47], s36 offen offset:2048  lds\n\t"
    "buffer_load_dwordx4 v235, s[44:47], s36 offen offset:3072  lds\n\t"
    "s_lshl_b32 m0, s38, 14\n\t"
    "s_add_u32 m0, m0, s37\n\t"
    "s_lshl_b32 s36, s38, 19\n\t"
    "s_add_u32 s36, s36, s42\n\t"
    "s_add_u32 s38, s38, s40\n\t"
    "buffer_load_dwordx4 v232, s[44:47], s36 offen offset:0  lds\n\t"
    "buffer_load_dwordx4 v233, s[44:47], s36 offen offset:1024  lds\n\t"
    "buffer_load_dwordx4 v234, s[44:47], s36 offen offset:2048  lds\n\t"
    "buffer_load_dwordx4 v235, s[44:47], s36 offen offset:3072  lds\n\t"
    "s_lshl_b32 m0, s38, 14\n\t"
    "s_add_u32 m0, m0, s37\n\t"
    "s_lshl_b32 s36, s38, 19\n\t"
    "s_add_u32 s36, s36, s42\n\t"
    "s_add_u32 s38, s38, s40\n\t"
    "buffer_load_dwordx4 v232, s[44:47], s36 offen offset:0  lds\n\t"
    "buffer_load_dwordx4 v233, s[44:47], s36 offen offset:1024  lds\n\t"
    "buffer_load_dwordx4 v234, s[44:47], s36 offen offset:2048  lds\n\t"
    "buffer_load_dwordx4 v235, s[44:47], s36 offen offset:3072  lds\n\t"
    "s_waitcnt vmcnt(16)\n\t"
    "s_barrier\n\t"
    "ds_read_b64_tr_b16 a[64:65], v224 offset:0\n\t"
    "ds_read_b64_tr_b16 a[66:67], v224 offset:1024\n\t"
    "ds_read_b64_tr_b16 a[96:97], v224 offset:8192\n\t"
    "ds_read_b64_tr_b16 a[98:99], v224 offset:9216\n\t"
    "ds_read_b64_tr_b16 a[68:69], v224 offset:8\n\t"
    "ds_read_b64_tr_b16 a[70:71], v224 offset:1032\n\t"
    "ds_read_b64_tr_b16 a[100:101], v224 offset:8200\n\t"
    "ds_read_b64_tr_b16 a[102:103], v224 offset:9224\n\t"
    "ds_read_b64_tr_b16 a[72:73], v225 offset:0\n\t"
    "ds_read_b64_tr_b16 a[74:75], v225 offset:1024\n\t"
    "ds_read_b64_tr_b16 a[104:105], v225 offset:8192\n\t"
    "ds_read_b64_tr_b16 a[106:107], v225 offset:9216\n\t"
    "ds_read_b64_tr_b16 a[76:77], v225 offset:8\n\t"
    "ds_read_b64_tr_b16 a[78:79], v225 offset:1032\n\t"
    "ds_read_b64_tr_b16 a[108:109], v225 offset:8200\n\t"
    "ds_read_b64_tr_b16 a[110:111], v225 offset:9224\n\t"
    "ds_read_b64_tr_b16 a[80:81], v226 offset:0\n\t"
    "ds_read_b64_tr_b16 a[82:83], v226 offset:1024\n\t"
    "ds_read_b64_tr_b16 a[112:113], v226 offset:8192\n\t"
    "ds_read_b64_tr_b16 a[114:115], v226 offset:9216\n\t"
    "ds_read_b64_tr_b16 a[84:85], v226 offset:8\n\t"
    "ds_read_b64_tr_b16 a[86:87], v226 offset:1032\n\t"
    "ds_read_b64_tr_b16 a[116:117], v226 offset:8200\n\t"
    "ds_read_b64_tr_b16 a[118:119], v226 offset:9224\n\t"
    "ds_read_b64_tr_b16 a[88:89], v227 offset:0\n\t"
    "ds_read_b64_tr_b16 a[90:91], v227 offset:1024\n\t"
    "ds_read_b64_tr_b16 a[120:121], v227 offset:8192\n\t"
    "ds_read_b64_tr_b16 a[122:123], v227 offset:9216\n\t"
    "ds_read_b64_tr_b16 a[92:93], v227 offset:8\n\t"
    "ds_read_b64_tr_b16 a[94:95], v227 offset:1032\n\t"
    "ds_read_b64_tr_b16 a[124:125], v227 offset:8200\n\t"
    "ds_read_b64_tr_b16 a[126:127], v227 offset:9224\n\t"
    "s_branch L_fend%=\n\t"
    "L_jln%=:\n\t"
    "s_add_u32 m0, s37, 0\n\t"
    "s_add_u32 s36, s42, 0\n\t"
    "buffer_load_dwordx4 v232, s[44:47], s36 offen offset:0  lds\n\t"
    "buffer_load_dwordx4 v233, s[44:47], s36 offen offset:1024  lds\n\t"
    "buffer_load_dwordx4 v234, s[44:47], s36 offen offset:2048  lds\n\t"
    "buffer_load_dwordx4 v235, s[44:47], s36 offen offset:3072  lds\n\t"
    "s_add_u32 m0, s37, 16384\n\t"
    "s_add_u32 s36, s42, 524288\n\t"
    "buffer_load_dwordx4 v232, s[44:47], s36 offen offset:0  lds\n\t"
    "buffer_load_dwordx4 v233, s[44:47], s36 offen offset:1024  lds\n\t"
    "buffer_load_dwordx4 v234, s[44:47], s36 offen offset:2048  lds\n\t"
    "buffer_load_dwordx4 v235, s[44:47], s36 offen offset:3072  lds\n\t"
    "s_add_u32 m0, s37, 32768\n\t"
    "s_add_u32 s36, s42, 1048576\n\t"
    "buffer_load_dwordx4 v232, s[44:47], s36 offen offset:0  lds\n\t"
    "buffer_load_dwordx4 v233, s[44:47], s36 offen offset:1024  lds\n\t"
    "buffer_load_dwordx4 v234, s[44:47], s36 offen offset:2048  lds\n\t"
    "buffer_load_dwordx4 v235, s[44:47], s36 offen offset:3072  lds\n\t"
    "s_lshr_b32 s5, s4, 2\n\t"
    "s_mul_i32 s5, s5, 73728\n\t"
    "s_add_u32 s5, s5, 1048576\n\t"
    "s_lshl_b32 s6, s4, 11\n\t"
    "s_add_u32 s5, s5, s6\n\t"
    "s_mul_i32 s6, s77, 5\n\t"
    "s_and_b32 s7, s6, 1\n\t"
    "s_mul_i32 s7, s7, 3072\n\t"
    "s_lshl_b32 s8, s6, 12\n\t"
    "s_sub_u32 s8, s8, s7\n\t"
    "s_add_u32 s12, s5, s8\n\t"
    "s_add_u32 s6, s6, 1\n\t"
    "s_and_b32 s7, s6, 1\n\t"
    "s_mul_i32 s7, s7, 3072\n\t"
    "s_lshl_b32 s8, s6, 12\n\t"
    "s_sub_u32 s8, s8, s7\n\t"
    "s_add_u32 s75, s5, s8\n\t"
    "s_add_u32 s6, s6, 1\n\t"
    "s_and_b32 s7, s6, 1\n\t"
    "s_mul_i32 s7, s7, 3072\n\t"
    "s_lshl_b32 s8, s6, 12\n\t"
    "s_sub_u32 s8, s8, s7\n\t"
    "s_add_u32 s76, s5, s8\n\t"
    "s_add_u32 s6, s6, 1\n\t"
    "s_and_b32 s7, s6, 1\n\t"
    "s_mul_i32 s7, s7, 3072\n\t"
    "s_lshl_b32 s8, s6, 12\n\t"
    "s_sub_u32 s8, s8, s7\n\t"
    "s_add_u32 s40, s5, s8\n\t"
    "s_add_u32 s6, s6, 1\n\t"
    "s_and_b32 s7, s6, 1\n\t"
    "s_mul_i32 s7, s7, 3072\n\t"
    "s_lshl_b32 s8, s6, 12\n\t"
    "s_sub_u32 s8, s8, s7\n\t"
    "s_add_u32 s41, s5, s8\n\t"
    "s_add_u32 s6, s6, 1\n\t"
    "s_lshl2_add_u32 s99, s4, s77\n\t"
    "s_mul_i32 s99, s99, 32\n\t"
    "s_add_u32 s99, s99, 655360\n\t"
    "s_lshl_b32 s9, s11, 9\n\t"
    "s_add_u32 s9, s9, 655360\n\t"
    "v_lshl_add_u32 v57, v51, 5, s9\n\t"
    "s_and_b32 s8, s17, 7\n\t"
    "s_lshr_b32 s3, s16, 1\n\t"
    "s_lshl3_add_u32 s3, s3, s8\n\t"
    "s_and_b32 s4, s16, 1\n\t"
    "s_lshr_b32 s5, s17, 3\n\t"
    "s_lshl2_add_u32 s4, s4, s5\n\t"
    "s_and_b32 s5, s16, 3\n\t"
    "s_mul_i32 s5, s5, 27\n\t"
    "s_add_u32 s5, s5, s17\n\t"
    "s_and_b32 s6, s5, 3\n\t"
    "s_lshr_b32 s5, s5, 2\n\t"
    "s_and_b32 s7, s16, 4\n\t"
    "s_add_u32 s6, s6, s7\n\t"
    "s_lshl2_add_u32 s7, s6, s77\n\t"
    "s_mul_i32 s9, s10, 16384\n\t"
    "s_add_u32 s9, s9, 16777216\n\t"
    "s_mul_i32 s39, s16, 8192\n\t"
    "s_add_u32 s39, s39, s9\n\t"
    "s_mul_i32 s9, s11, 592896\n\t"
    "s_add_u32 s39, s39, s9\n\t"
    "s_lshl2_add_u32 s9, s3, s77\n\t"
    "s_mul_i32 s15, s9, 1024\n\t"
    "s_add_u32 s15, s15, 131072\n\t"
    "s_mul_i32 s71, s9, 20480\n\t"
    "s_mul_i32 s72, s4, 160\n\t"
    "s_add_u32 s71, s71, s72\n\t"
    "s_lshl_b32 s72, s72, 1\n\t"
    "s_and_b32 s8, s9, 7\n\t"
    "s_mul_i32 s93, s8, 73728\n\t"
    "s_add_u32 s93, s93, 16777216\n\t"
    "s_mul_i32 s60, s3, 592896\n\t"
    "s_add_u32 s60, s60, s93\n\t"
    "s_xor_b32 s9, s3, 1\n\t"
    "s_mul_i32 s9, s9, 592896\n\t"
    "s_add_u32 s93, s93, s9\n\t"
    "s_lshl3_add_u32 s9, s3, s8\n\t"
    "s_andn2_b32 s9, s9, 8\n\t"
    "s_and_b32 s94, s16, 1\n\t"
    "s_lshl1_add_u32 s9, s9, s94\n\t"
    "s_mul_i32 s94, s9, 128\n\t"
    "s_add_u32 s94, s94, 524288\n\t"
    "s_lshl3_add_u32 s9, s4, s8\n\t"
    "s_mul_i32 s61, s9, 81920\n\t"
    "s_add_u32 s61, s61, 37748736\n\t"
    "s_lshl3_add_u32 s9, s3, s4\n\t"
    "s_lshl2_add_u32 s9, s9, s77\n\t"
    "s_mul_i32 s95, s9, 8192\n\t"
    "s_add_u32 s95, s95, 52428800\n\t"
    "s_mul_i32 s62, s7, 81920\n\t"
    "s_add_u32 s62, s62, 4194304\n\t"
    "s_mul_i32 s73, s7, 4096\n\t"
    "s_add_u32 s73, s73, 131072\n\t"
    "s_mul_i32 s9, s7, 27\n\t"
    "s_add_u32 s9, s9, s5\n\t"
    "s_mul_i32 s64, s9, 8192\n\t"
    "s_add_u32 s64, s64, 8388608\n\t"
    "s_lshr_b32 s9, s16, 2\n\t"
    "s_mul_i32 s9, s9, 5\n\t"
    "s_add_u32 s9, s9, s17\n\t"
    "s_sub_u32 s9, s9, 27\n\t"
    "s_and_b32 s98, s16, 3\n\t"
    "s_mul_i32 s98, s98, 7\n\t"
    "s_add_u32 s98, s98, s9\n\t"
    "s_cmp_lt_u32 s9, 7\n\t"
    "s_cselect_b32 s98, s98, 27\n\t"
    "s_cmp_lt_u32 s17, 27\n\t"
    "s_cselect_b32 s98, s5, s98\n\t"
    "s_mul_i32 s63, s98, 163840\n\t"
    "s_add_u32 s63, s63, s37\n\t"
    "s_add_u32 s63, s63, 47185920\n\t"
    "s_cmp_lt_u32 s98, 27\n\t"
    "s_cselect_b32 s63, s63, 67108864\n\t"
    "v_accvgpr_write_b32 a250, s98\n\t"
    "s_mul_i32 s9, s77, 57344\n\t"
    "s_add_u32 s9, s9, 8388608\n\t"
    "s_mul_i32 s65, s3, 221184\n\t"
    "s_add_u32 s65, s65, s9\n\t"
    "s_mul_i32 s9, s77, 71680\n\t"
    "s_add_u32 s9, s9, 44040192\n\t"
    "s_mul_i32 s66, s4, 276480\n\t"
    "s_add_u32 s66, s66, s9\n\t"
    "s_mul_i32 s9, s77, 2048\n\t"
    "s_add_u32 s9, s9, 4194304\n\t"
    "s_mul_i32 s14, s3, 81920\n\t"
    "s_add_u32 s14, s14, s9\n\t"
    "s_lshl3_add_u32 s9, s11, s16\n\t"
    "s_mul_i32 s67, s9, 256\n\t"
    "s_add_u32 s67, s67, 524288\n\t"
    "s_lshl2_add_u32 s9, s3, s77\n\t"
    "s_and_b32 s8, s16, 1\n\t"
    "s_lshl1_add_u32 s9, s9, s8\n\t"
    "s_mul_i32 s68, s9, 128\n\t"
    "s_add_u32 s68, s68, 589824\n\t"
    "s_mul_i32 s9, s5, 37\n\t"
    "s_lshr_b32 s9, s9, 8\n\t"
    "s_lshl2_add_u32 s9, s7, s9\n\t"
    "s_mul_i32 s69, s9, 256\n\t"
    "s_add_u32 s69, s69, 589824\n\t"
    "s_and_b32 s8, s16, 3\n\t"
    "s_lshl2_add_u32 s9, s7, s8\n\t"
    "s_mul_i32 s70, s9, 128\n\t"
    "s_add_u32 s70, s70, 622592\n\t"
    "s_mov_b32 s74, s17\n\t"
    "s_lshl2_add_u32 s9, s2, s77\n\t"
    "s_mul_i32 s13, s9, 256\n\t"
    "s_add_u32 s13, s13, 62914560\n\t"
    "s_lshr_b32 s10, s9, 3\n\t"
    "s_and_b32 s11, s9, 7\n\t"
    "s_mul_i32 s3, s11, 160\n\t"
    "s_mul_i32 s4, s10, 40960\n\t"
    "s_add_u32 s4, s4, s3\n\t"
    "s_add_u32 s0, s84, s4\n\t"
    "s_addc_u32 s1, s85, 0\n\t"
    "s_add_u32 s90, s90, s4\n\t"
    "s_addc_u32 s91, s91, 0\n\t"
    "s_mul_i32 s6, s11, 128\n\t"
    "s_add_u32 s6, s6, s10\n\t"
    "s_mul_i32 s6, s6, 5120\n\t"
    "s_add_u32 s6, s6, 37748736\n\t"
    "s_mul_i32 s8, s11, 54\n\t"
    "s_add_u32 s8, s8, s10\n\t"
    "s_mul_i32 s8, s8, 5120\n\t"
    "s_add_u32 s8, s8, 44204032\n\t"
    "s_add_u32 s2, s0, 3276800\n\t"
    "s_addc_u32 s3, s1, 0\n\t"
    "s_sub_u32 s4, s90, 1966080\n\t"
    "s_subb_u32 s5, s91, 0\n\t"
    "s_add_u32 s7, s6, 409600\n\t"
    "s_sub_u32 s10, s8, 409600\n\t"
    "s_cmp_lt_u32 s9, 384\n\t"
    "s_cselect_b64 s[2:3], s[2:3], s[4:5]\n\t"
    "s_cselect_b32 s7, s7, s10\n\t"
    "s_add_u32 s90, s90, 1310720\n\t"
    "s_addc_u32 s91, s91, 0\n\t"
    "s_bitcmp1_b32 s9, 0\n\t"
    "s_cselect_b64 s[86:87], s[88:89], s[86:87]\n\t"
    "s_cselect_b32 s11, 4096, 0\n\t"
    "s_add_u32 s11, s11, 47185920\n\t"
    "s_max_u32 s10, s9, 176\n\t"
    "s_sub_u32 s10, s10, 176\n\t"
    "s_lshr_b32 s10, s10, 1\n\t"
    "s_mul_hi_u32 s84, s10, 0x97b425f\n\t"
    "s_mul_i32 s85, s84, 27\n\t"
    "s_sub_u32 s85, s10, s85\n\t"
    "s_mul_i32 s88, s84, 110592\n\t"
    "s_lshl_b32 s89, s85, 7\n\t"
    "s_add_u32 s88, s88, s89\n\t"
    "s_add_u32 s4, s86, s88\n\t"
    "s_addc_u32 s5, s87, 0\n\t"
    "s_mul_i32 s85, s85, 20\n\t"
    "s_add_u32 s85, s85, s84\n\t"
    "s_mul_i32 s10, s85, 8192\n\t"
    "s_add_u32 s10, s10, s11\n\t"
    "s_cmp_lt_u32 s9, 176\n\t"
    "s_cselect_b64 s[4:5], s[90:91], s[4:5]\n\t"
    "s_cselect_b32 s8, s8, s10\n\t"
    "s_add_u32 s10, s9, 464\n\t"
    "s_lshr_b32 s10, s10, 1\n\t"
    "s_mul_hi_u32 s84, s10, 0x97b425f\n\t"
    "s_mul_i32 s85, s84, 27\n\t"
    "s_sub_u32 s85, s10, s85\n\t"
    "s_mul_i32 s88, s84, 110592\n\t"
    "s_lshl_b32 s89, s85, 7\n\t"
    "s_add_u32 s88, s88, s89\n\t"
    "s_add_u32 s90, s86, s88\n\t"
    "s_addc_u32 s91, s87, 0\n\t"
    "s_mul_i32 s85, s85, 20\n\t"
    "s_add_u32 s85, s85, s84\n\t"
    "s_mul_i32 s10, s85, 8192\n\t"
    "s_add_u32 s10, s10, s11\n\t"
    "v_accvgpr_write_b32 a252, s90\n\t"
    "v_accvgpr_write_b32 a253, s91\n\t"
    "v_add_u32 v228, 0x10000, v224\n\t"
    "v_add_u32 v229, 0x10000, v225\n\t"
    "v_add_u32 v230, 0x10000, v226\n\t"
    "v_add_u32 v231, 0x10000, v227\n\t"
    "s_and_b32 s84, s17, 7\n\t"
    "s_lshl2_add_u32 s84, s84, s77\n\t"
    "s_lshl_b32 s84, s84, 11\n\t"
    "v_lshlrev_b32 v249, 26, v50\n\t"
    "v_lshl_add_u32 v249, v51, 7, v249\n\t"
    "v_add_u32 v249, s84, v249\n\t"
    "v_mov_b32 v6, 0x3f803f80\n\t"
    "v_mov_b32 v4, 0\n\t"
    "v_mov_b32 v5, 0\n\t"
    "v_lshlrev_b32 v58, 4, v51\n\t"
    "v_add_u32 v58, 147456, v58\n\t"
    "v_lshl_add_u32 v59, v1, 2, v58\n\t"
    "L_lnw%=:\n\t"
    "s_waitcnt vmcnt(17)\n\t"
    "v_dot2c_f32_bf16 v4, v8, v6\n\t"
    "v_lshlrev_b32 v64, 16, v8\n\t"
    "v_and_b32 v65, 0xffff0000, v8\n\t"
    "v_dot2c_f32_bf16 v5, v9, v6\n\t"
    "v_lshlrev_b32 v66, 16, v9\n\t"
    "v_and_b32 v67, 0xffff0000, v9\n\t"
    "v_dot2c_f32_bf16 v4, v10, v6\n\t"
    "v_lshlrev_b32 v68, 16, v10\n\t"
    "v_and_b32 v69, 0xffff0000, v10\n\t"
    "v_dot2c_f32_bf16 v5, v11, v6\n\t"
    "v_lshlrev_b32 v70, 16, v11\n\t"
    "v_and_b32 v71, 0xffff0000, v11\n\t"
    "s_waitcnt vmcnt(16)\n\t"
    "v_dot2c_f32_bf16 v4, v12, v6\n\t"
    "v_lshlrev_b32 v72, 16, v12\n\t"
    "v_and_b32 v73, 0xffff0000, v12\n\t"
    "v_dot2c_f32_bf16 v5, v13, v6\n\t"
    "v_lshlrev_b32 v74, 16, v13\n\t"
    "v_and_b32 v75, 0xffff0000, v13\n\t"
    "v_dot2c_f32_bf16 v4, v14, v6\n\t"
    "v_lshlrev_b32 v76, 16, v14\n\t"
    "v_and_b32 v77, 0xffff0000, v14\n\t"
    "v_dot2c_f32_bf16 v5, v15, v6\n\t"
    "v_lshlrev_b32 v78, 16, v15\n\t"
    "v_and_b32 v79, 0xffff0000, v15\n\t"
    "s_waitcnt vmcnt(15)\n\t"
    "v_dot2c_f32_bf16 v4, v16, v6\n\t"
    "v_lshlrev_b32 v80, 16, v16\n\t"
    "v_and_b32 v81, 0xffff0000, v16\n\t"
    "v_dot2c_f32_bf16 v5, v17, v6\n\t"
    "v_lshlrev_b32 v82, 16, v17\n\t"
    "v_and_b32 v83, 0xffff0000, v17\n\t"
    "v_dot2c_f32_bf16 v4, v18, v6\n\t"
    "v_lshlrev_b32 v84, 16, v18\n\t"
    "v_and_b32 v85, 0xffff0000, v18\n\t"
    "v_dot2c_f32_bf16 v5, v19, v6\n\t"
    "v_lshlrev_b32 v86, 16, v19\n\t"
    "v_and_b32 v87, 0xffff0000, v19\n\t"
    "s_waitcnt vmcnt(14)\n\t"
    "v_dot2c_f32_bf16 v4, v20, v6\n\t"
    "v_lshlrev_b32 v88, 16, v20\n\t"
    "v_and_b32 v89, 0xffff0000, v20\n\t"
    "v_dot2c_f32_bf16 v5, v21, v6\n\t"
    "v_lshlrev_b32 v90, 16, v21\n\t"
    "v_and_b32 v91, 0xffff0000, v21\n\t"
    "v_dot2c_f32_bf16 v4, v22, v6\n\t"
    "v_lshlrev_b32 v92, 16, v22\n\t"
    "v_and_b32 v93, 0xffff0000, v22\n\t"
    "v_dot2c_f32_bf16 v5, v23, v6\n\t"
    "v_lshlrev_b32 v94, 16, v23\n\t"
    "v_and_b32 v95, 0xffff0000, v23\n\t"
    "s_waitcnt vmcnt(13)\n\t"
    "v_dot2c_f32_bf16 v4, v24, v6\n\t"
    "v_lshlrev_b32 v96, 16, v24\n\t"
    "v_and_b32 v97, 0xffff0000, v24\n\t"
    "v_dot2c_f32_bf16 v5, v25, v6\n\t"
    "v_lshlrev_b32 v98, 16, v25\n\t"
    "v_and_b32 v99, 0xffff0000, v25\n\t"
    "v_dot2c_f32_bf16 v4, v26, v6\n\t"
    "v_lshlrev_b32 v100, 16, v26\n\t"
    "v_and_b32 v101, 0xffff0000, v26\n\t"
    "v_dot2c_f32_bf16 v5, v27, v6\n\t"
    "v_lshlrev_b32 v102, 16, v27\n\t"
    "v_and_b32 v103, 0xffff0000, v27\n\t"
    "s_waitcnt vmcnt(12)\n\t"
    "v_readfirstlane_b32 s97, v60\n\t"
    "v_add_f32 v4, v4, v5\n\t"
    "v_mov_b32 v5, v4\n\t"
    "s_nop 1\n\t"
    "v_permlane16_swap_b32 v4, v5\n\t"
    "s_nop 0\n\t"
    "v_add_f32 v4, v4, v5\n\t"
    "v_mov_b32 v5, v4\n\t"
    "s_nop 1\n\t"
    "v_permlane32_swap_b32 v4, v5\n\t"
    "s_nop 0\n\t"
    "v_add_f32 v4, v4, v5\n\t"
    "ds_write_b32 v59, v4 offset:0\n\t"
    "s_waitcnt lgkmcnt(0)\n\t"
    "s_barrier\n\t"
    "ds_read_b128 v[4:7], v58 offset:0\n\t"
    "s_waitcnt lgkmcnt(0)\n\t"
    "v_pk_add_f32 v[4:5], v[4:5], v[6:7]\n\t"
    "v_add_f32 v4, v4, v5\n\t"
    "v_mul_f32 v4, 0xbacccccd, v4\n\t"
    "s_mul_i32 s97, s97, 2654435761\n\t"
    "s_add_u32 s97, s97, 2135587861\n\t"
    "v_pk_add_f32 v[64:65], v[64:65], v[4:5] op_sel_hi:[1,0]\n\t"
    "v_pk_add_f32 v[66:67], v[66:67], v[4:5] op_sel_hi:[1,0]\n\t"
    "v_pk_add_f32 v[68:69], v[68:69], v[4:5] op_sel_hi:[1,0]\n\t"
    "v_pk_add_f32 v[70:71], v[70:71], v[4:5] op_sel_hi:[1,0]\n\t"
    "v_pk_add_f32 v[72:73], v[72:73], v[4:5] op_sel_hi:[1,0]\n\t"
    "v_pk_add_f32 v[74:75], v[74:75], v[4:5] op_sel_hi:[1,0]\n\t"
    "v_pk_add_f32 v[76:77], v[76:77], v[4:5] op_sel_hi:[1,0]\n\t"
    "v_pk_add_f32 v[78:79], v[78:79], v[4:5] op_sel_hi:[1,0]\n\t"
    "v_pk_add_f32 v[80:81], v[80:81], v[4:5] op_sel_hi:[1,0]\n\t"
    "v_pk_add_f32 v[82:83], v[82:83], v[4:5] op_sel_hi:[1,0]\n\t"
    "v_pk_add_f32 v[84:85], v[84:85], v[4:5] op_sel_hi:[1,0]\n\t"
    "v_pk_add_f32 v[86:87], v[86:87], v[4:5] op_sel_hi:[1,0]\n\t"
    "v_pk_add_f32 v[88:89], v[88:89], v[4:5] op_sel_hi:[1,0]\n\t"
    "v_pk_add_f32 v[90:91], v[90:91], v[4:5] op_sel_hi:[1,0]\n\t"
    "v_pk_add_f32 v[92:93], v[92:93], v[4:5] op_sel_hi:[1,0]\n\t"
    "v_pk_add_f32 v[94:95], v[94:95], v[4:5] op_sel_hi:[1,0]\n\t"
    "v_pk_add_f32 v[96:97], v[96:97], v[4:5] op_sel_hi:[1,0]\n\t"
    "v_pk_add_f32 v[98:99], v[98:99], v[4:5] op_sel_hi:[1,0]\n\t"
    "v_pk_add_f32 v[100:101], v[100:101], v[4:5] op_sel_hi:[1,0]\n\t"
    "v_pk_add_f32 v[102:103], v[102:103], v[4:5] op_sel_hi:[1,0]\n\t"
    "v_pk_mul_f32 v[4:5], v[64:65], v[64:65]\n\t"
    "v_pk_mul_f32 v[2:3], v[66:67], v[66:67]\n\t"
    "v_pk_fma_f32 v[4:5], v[68:69], v[68:69], v[4:5]\n\t"
    "v_pk_fma_f32 v[2:3], v[70:71], v[70:71], v[2:3]\n\t"
    "v_pk_fma_f32 v[4:5], v[72:73], v[72:73], v[4:5]\n\t"
    "v_pk_fma_f32 v[2:3], v[74:75], v[74:75], v[2:3]\n\t"
    "v_pk_fma_f32 v[4:5], v[76:77], v[76:77], v[4:5]\n\t"
    "v_pk_fma_f32 v[2:3], v[78:79], v[78:79], v[2:3]\n\t"
    "v_pk_fma_f32 v[4:5], v[80:81], v[80:81], v[4:5]\n\t"
    "v_pk_fma_f32 v[2:3], v[82:83], v[82:83], v[2:3]\n\t"
    "v_pk_fma_f32 v[4:5], v[84:85], v[84:85], v[4:5]\n\t"
    "v_pk_fma_f32 v[2:3], v[86:87], v[86:87], v[2:3]\n\t"
    "v_pk_fma_f32 v[4:5], v[88:89], v[88:89], v[4:5]\n\t"
    "v_pk_fma_f32 v[2:3], v[90:91], v[90:91], v[2:3]\n\t"
    "v_pk_fma_f32 v[4:5], v[92:93], v[92:93], v[4:5]\n\t"
    "v_pk_fma_f32 v[2:3], v[94:95], v[94:95], v[2:3]\n\t"
    "v_pk_fma_f32 v[4:5], v[96:97], v[96:97], v[4:5]\n\t"
    "v_pk_fma_f32 v[2:3], v[98:99], v[98:99], v[2:3]\n\t"
    "v_pk_fma_f32 v[4:5], v[100:101], v[100:101], v[4:5]\n\t"
    "v_pk_fma_f32 v[2:3], v[102:103], v[102:103], v[2:3]\n\t"
    "v_pk_add_f32 v[4:5], v[4:5], v[2:3]\n\t"
    "v_add_f32 v4, v4, v5\n\t"
    "v_mov_b32 v5, v4\n\t"
    "s_nop 1\n\t"
    "v_permlane16_swap_b32 v4, v5\n\t"
    "s_nop 0\n\t"
    "v_add_f32 v4, v4, v5\n\t"
    "v_mov_b32 v5, v4\n\t"
    "s_nop 1\n\t"
    "v_permlane32_swap_b32 v4, v5\n\t"
    "s_nop 0\n\t"
    "v_add_f32 v4, v4, v5\n\t"
    "ds_write_b32 v59, v4 offset:256\n\t"
    "s_waitcnt lgkmcnt(0)\n\t"
    "s_barrier\n\t"
    "ds_read_b128 v[4:7], v58 offset:256\n\t"
    "s_waitcnt lgkmcnt(0)\n\t"
    "v_pk_add_f32 v[4:5], v[4:5], v[6:7]\n\t"
    "v_add_f32 v4, v4, v5\n\t"
    "v_mov_b32 v5, 0x3727c5ac\n\t"
    "v_fmac_f32 v5, 0x3acccccd, v4\n\t"
    "v_rsq_f32 v4, v5\n\t"
    "s_nop 0\n\t"
    "v_pk_mul_f32 v[64:65], v[64:65], v[4:5] op_sel_hi:[1,0]\n\t"
    "v_pk_mul_f32 v[66:67], v[66:67], v[4:5] op_sel_hi:[1,0]\n\t"
    "v_pk_mul_f32 v[68:69], v[68:69], v[4:5] op_sel_hi:[1,0]\n\t"
    "v_pk_mul_f32 v[70:71], v[70:71], v[4:5] op_sel_hi:[1,0]\n\t"
    "v_pk_mul_f32 v[72:73], v[72:73], v[4:5] op_sel_hi:[1,0]\n\t"
    "v_pk_mul_f32 v[74:75], v[74:75], v[4:5] op_sel_hi:[1,0]\n\t"
    "v_pk_mul_f32 v[76:77], v[76:77], v[4:5] op_sel_hi:[1,0]\n\t"
    "v_pk_mul_f32 v[78:79], v[78:79], v[4:5] op_sel_hi:[1,0]\n\t"
    "v_pk_mul_f32 v[80:81], v[80:81], v[4:5] op_sel_hi:[1,0]\n\t"
    "v_pk_mul_f32 v[82:83], v[82:83], v[4:5] op_sel_hi:[1,0]\n\t"
    "v_pk_mul_f32 v[84:85], v[84:85], v[4:5] op_sel_hi:[1,0]\n\t"
    "v_pk_mul_f32 v[86:87], v[86:87], v[4:5] op_sel_hi:[1,0]\n\t"
    "v_pk_mul_f32 v[88:89], v[88:89], v[4:5] op_sel_hi:[1,0]\n\t"
    "v_pk_mul_f32 v[90:91], v[90:91], v[4:5] op_sel_hi:[1,0]\n\t"
    "v_pk_mul_f32 v[92:93], v[92:93], v[4:5] op_sel_hi:[1,0]\n\t"
    "v_pk_mul_f32 v[94:95], v[94:95], v[4:5] op_sel_hi:[1,0]\n\t"
    "v_pk_mul_f32 v[96:97], v[96:97], v[4:5] op_sel_hi:[1,0]\n\t"
    "v_pk_mul_f32 v[98:99], v[98:99], v[4:5] op_sel_hi:[1,0]\n\t"
    "v_pk_mul_f32 v[100:101], v[100:101], v[4:5] op_sel_hi:[1,0]\n\t"
    "v_pk_mul_f32 v[102:103], v[102:103], v[4:5] op_sel_hi:[1,0]\n\t"
    "v_cmp_le_u32 vcc, 16, v255\n\t"
    "v_mov_b32 v3, 3840\n\t"
    "v_mov_b32 v104, s97\n\t"
    "v_mov_b32 v105, s97\n\t"
    "v_mov_b32 v106, s97\n\t"
    "v_mov_b32 v107, s97\n\t"
    "v_and_b32 v7, 0x1f0, v254\n\t"
    "v_add_u32 v63, v7, v3\n\t"
    "v_cndmask_b32 v3, 0, v3, vcc\n\t"
    "v_cndmask_b32 v6, 0, 1.0, vcc\n\t"
    "v_add_u32 v3, v3, v254\n\t"
    "s_mul_i32 s84, s77, 1280\n\t"
    "s_add_u32 s84, s84, 106496\n\t"
    "s_add_u32 s85, s84, 10240\n\t"
    "s_lshr_b32 s86, s37, 1\n\t"
    "s_add_u32 s86, s86, 131072\n\t"
    "v_lshl_add_u32 v4, v255, 3, s86\n\t"
    "v_lshl_add_u32 v5, v50, 5, s86\n\t"
    "v_lshrrev_b32 v7, 1, v7\n\t"
    "v_add_u32 v7, s86, v7\n\t"
    "s_mov_b32 s38, 0x20000\n\t"
    "L_gath%=:\n\t"
    "buffer_load_dwordx4 v[192:195], v254, s[28:31], s84 offen offset:0 sc1\n\t"
    "buffer_load_dwordx4 v[196:199], v3, s[28:31], s84 offen offset:1024 sc1\n\t"
    "buffer_load_dwordx4 v[200:203], v63, s[28:31], s84 offen offset:2048 sc1\n\t"
    "buffer_load_dwordx4 v[204:207], v254, s[28:31], s85 offen offset:0 sc1\n\t"
    "buffer_load_dwordx4 v[208:211], v3, s[28:31], s85 offen offset:1024 sc1\n\t"
    "buffer_load_dwordx4 v[212:215], v63, s[28:31], s85 offen offset:2048 sc1\n\t"
    "s_sub_u32 s38, s38, 1\n\t"
    "s_waitcnt vmcnt(0)\n\t"
    "v_xor_b32 v2, s97, v193\n\t"
    "v_bitop3_b32 v2, v195, s97, v2 bitop3:0xbe\n\t"
    "v_bitop3_b32 v2, v197, s97, v2 bitop3:0xbe\n\t"
    "v_bitop3_b32 v2, v199, s97, v2 bitop3:0xbe\n\t"
    "v_bitop3_b32 v2, v201, s97, v2 bitop3:0xbe\n\t"
    "v_bitop3_b32 v2, v203, s97, v2 bitop3:0xbe\n\t"
    "v_bitop3_b32 v2, v205, s97, v2 bitop3:0xbe\n\t"
    "v_bitop3_b32 v2, v207, s97, v2 bitop3:0xbe\n\t"
    "v_bitop3_b32 v2, v209, s97, v2 bitop3:0xbe\n\t"
    "v_bitop3_b32 v2, v211, s97, v2 bitop3:0xbe\n\t"
    "v_bitop3_b32 v2, v213, s97, v2 bitop3:0xbe\n\t"
    "v_bitop3_b32 v2, v215, s97, v2 bitop3:0xbe\n\t"
    "v_cmp_ne_u32 vcc, 0, v2\n\t"
    "v_add_f32 v216, v192, v204\n\t"
    "v_add_f32 v217, v194, v206\n\t"
    "v_add_f32 v218, v196, v208\n\t"
    "v_add_f32 v219, v198, v210\n\t"
    "v_add_f32 v220, v200, v212\n\t"
    "v_add_f32 v221, v202, v214\n\t"
    "s_cbranch_scc1 L_gathd%=\n\t"
    "s_cbranch_vccnz L_gath%=\n\t"
    "L_gathd%=:\n\t"
    "v_cvt_pk_bf16_f32 v217, v216, v217\n\t"
    "v_lshlrev_b32 v216, 16, v217\n\t"
    "v_and_b32 v217, 0xffff0000, v217\n\t"
    "v_mul_f32 v216, 0x3e0293ee, v216\n\t"
    "v_mul_f32 v217, 0x3e0293ee, v217\n\t"
    "ds_write_b64 v4, v[216:217] offset:0\n\t"
    "v_cvt_pk_bf16_f32 v219, v218, v219\n\t"
    "v_lshlrev_b32 v218, 16, v219\n\t"
    "v_and_b32 v219, 0xffff0000, v219\n\t"
    "v_add_f32 v218, v6, v218\n\t"
    "v_add_f32 v219, v6, v219\n\t"
    "v_cvt_pk_bf16_f32 v219, v218, v219\n\t"
    "v_lshlrev_b32 v218, 16, v219\n\t"
    "v_and_b32 v219, 0xffff0000, v219\n\t"
    "v_mul_f32 v218, 0x3e0293ee, v218\n\t"
    "v_mul_f32 v219, 0x3e0293ee, v219\n\t"
    "ds_write_b64 v4, v[218:219] offset:512\n\t"
    "v_cvt_pk_bf16_f32 v221, v220, v221\n\t"
    "v_lshlrev_b32 v220, 16, v221\n\t"
    "v_and_b32 v221, 0xffff0000, v221\n\t"
    "v_add_f32 v220, 1.0, v220\n\t"
    "v_add_f32 v221, 1.0, v221\n\t"
    "v_cvt_pk_bf16_f32 v221, v220, v221\n\t"
    "v_lshlrev_b32 v220, 16, v221\n\t"
    "v_and_b32 v221, 0xffff0000, v221\n\t"
    "v_mul_f32 v220, 0x3e0293ee, v220\n\t"
    "v_mul_f32 v221, 0x3e0293ee, v221\n\t"
    "ds_write_b64 v7, v[220:221] offset:1024\n\t"
    "ds_read_b128 v[108:111], v5 offset:0\n\t"
    "ds_read_b128 v[112:115], v5 offset:16\n\t"
    "ds_read_b128 v[116:119], v5 offset:640\n\t"
    "ds_read_b128 v[120:123], v5 offset:656\n\t"
    "ds_read_b128 v[124:127], v5 offset:128\n\t"
    "ds_read_b128 v[128:131], v5 offset:144\n\t"
    "ds_read_b128 v[132:135], v5 offset:768\n\t"
    "ds_read_b128 v[136:139], v5 offset:784\n\t"
    "ds_read_b128 v[140:143], v5 offset:256\n\t"
    "ds_read_b128 v[144:147], v5 offset:272\n\t"
    "ds_read_b128 v[148:151], v5 offset:896\n\t"
    "ds_read_b128 v[152:155], v5 offset:912\n\t"
    "ds_read_b128 v[156:159], v5 offset:384\n\t"
    "ds_read_b128 v[160:163], v5 offset:400\n\t"
    "ds_read_b128 v[164:167], v5 offset:1024\n\t"
    "ds_read_b128 v[168:171], v5 offset:1040\n\t"
    "ds_read_b128 v[172:175], v5 offset:512\n\t"
    "ds_read_b128 v[176:179], v5 offset:528\n\t"
    "ds_read_b128 v[180:183], v5 offset:1152\n\t"
    "ds_read_b128 v[184:187], v5 offset:1168\n\t"
    "s_waitcnt lgkmcnt(15)\n\t"
    "v_pk_fma_f32 v[64:65], v[64:65], v[116:117], v[108:109]\n\t"
    "v_pk_fma_f32 v[66:67], v[66:67], v[118:119], v[110:111]\n\t"
    "v_pk_fma_f32 v[68:69], v[68:69], v[120:121], v[112:113]\n\t"
    "v_pk_fma_f32 v[70:71], v[70:71], v[122:123], v[114:115]\n\t"
    "v_cvt_pk_bf16_f32 v8, v64, v65\n\t"
    "v_cvt_pk_bf16_f32 v9, v66, v67\n\t"
    "v_cvt_pk_bf16_f32 v10, v68, v69\n\t"
    "v_cvt_pk_bf16_f32 v11, v70, v71\n\t"
    "buffer_store_dwordx4 v[8:11], v254, s[28:31], s12 offen sc1\n\t"
    "s_waitcnt lgkmcnt(13)\n\t"
    "v_pk_fma_f32 v[72:73], v[72:73], v[132:133], v[124:125]\n\t"
    "v_pk_fma_f32 v[74:75], v[74:75], v[134:135], v[126:127]\n\t"
    "s_waitcnt lgkmcnt(12)\n\t"
    "v_pk_fma_f32 v[76:77], v[76:77], v[136:137], v[128:129]\n\t"
    "v_pk_fma_f32 v[78:79], v[78:79], v[138:139], v[130:131]\n\t"
    "v_cvt_pk_bf16_f32 v12, v72, v73\n\t"
    "v_cvt_pk_bf16_f32 v13, v74, v75\n\t"
    "v_cvt_pk_bf16_f32 v14, v76, v77\n\t"
    "v_cvt_pk_bf16_f32 v15, v78, v79\n\t"
    "buffer_store_dwordx4 v[12:15], v254, s[28:31], s75 offen sc1\n\t"
    "s_waitcnt lgkmcnt(9)\n\t"
    "v_pk_fma_f32 v[80:81], v[80:81], v[148:149], v[140:141]\n\t"
    "v_pk_fma_f32 v[82:83], v[82:83], v[150:151], v[142:143]\n\t"
    "s_waitcnt lgkmcnt(8)\n\t"
    "v_pk_fma_f32 v[84:85], v[84:85], v[152:153], v[144:145]\n\t"
    "v_pk_fma_f32 v[86:87], v[86:87], v[154:155], v[146:147]\n\t"
    "v_cvt_pk_bf16_f32 v16, v80, v81\n\t"
    "v_cvt_pk_bf16_f32 v17, v82, v83\n\t"
    "v_cvt_pk_bf16_f32 v18, v84, v85\n\t"
    "v_cvt_pk_bf16_f32 v19, v86, v87\n\t"
    "buffer_store_dwordx4 v[16:19], v254, s[28:31], s76 offen sc1\n\t"
    "s_waitcnt lgkmcnt(5)\n\t"
    "v_pk_fma_f32 v[88:89], v[88:89], v[164:165], v[156:157]\n\t"
    "v_pk_fma_f32 v[90:91], v[90:91], v[166:167], v[158:159]\n\t"
    "s_waitcnt lgkmcnt(4)\n\t"
    "v_pk_fma_f32 v[92:93], v[92:93], v[168:169], v[160:161]\n\t"
    "v_pk_fma_f32 v[94:95], v[94:95], v[170:171], v[162:163]\n\t"
    "v_cvt_pk_bf16_f32 v20, v88, v89\n\t"
    "v_cvt_pk_bf16_f32 v21, v90, v91\n\t"
    "v_cvt_pk_bf16_f32 v22, v92, v93\n\t"
    "v_cvt_pk_bf16_f32 v23, v94, v95\n\t"
    "buffer_store_dwordx4 v[20:23], v254, s[28:31], s40 offen sc1\n\t"
    "s_waitcnt lgkmcnt(1)\n\t"
    "v_pk_fma_f32 v[96:97], v[96:97], v[180:181], v[172:173]\n\t"
    "v_pk_fma_f32 v[98:99], v[98:99], v[182:183], v[174:175]\n\t"
    "s_waitcnt lgkmcnt(0)\n\t"
    "v_pk_fma_f32 v[100:101], v[100:101], v[184:185], v[176:177]\n\t"
    "v_pk_fma_f32 v[102:103], v[102:103], v[186:187], v[178:179]\n\t"
    "v_cvt_pk_bf16_f32 v24, v96, v97\n\t"
    "v_cvt_pk_bf16_f32 v25, v98, v99\n\t"
    "v_cvt_pk_bf16_f32 v26, v100, v101\n\t"
    "v_cvt_pk_bf16_f32 v27, v102, v103\n\t"
    "buffer_store_dwordx4 v[24:27], v254, s[28:31], s41 offen sc1\n\t"
    "s_barrier\n\t"
    "ds_read_b64_tr_b16 a[64:65], v224 offset:0\n\t"
    "ds_read_b64_tr_b16 a[66:67], v224 offset:1024\n\t"
    "ds_read_b64_tr_b16 a[96:97], v224 offset:8192\n\t"
    "ds_read_b64_tr_b16 a[98:99], v224 offset:9216\n\t"
    "ds_read_b64_tr_b16 a[68:69], v224 offset:8\n\t"
    "ds_read_b64_tr_b16 a[70:71], v224 offset:1032\n\t"
    "ds_read_b64_tr_b16 a[100:101], v224 offset:8200\n\t"
    "ds_read_b64_tr_b16 a[102:103], v224 offset:9224\n\t"
    "ds_read_b64_tr_b16 a[72:73], v225 offset:0\n\t"
    "ds_read_b64_tr_b16 a[74:75], v225 offset:1024\n\t"
    "ds_read_b64_tr_b16 a[104:105], v225 offset:8192\n\t"
    "ds_read_b64_tr_b16 a[106:107], v225 offset:9216\n\t"
    "ds_read_b64_tr_b16 a[76:77], v225 offset:8\n\t"
    "ds_read_b64_tr_b16 a[78:79], v225 offset:1032\n\t"
    "ds_read_b64_tr_b16 a[108:109], v225 offset:8200\n\t"
    "ds_read_b64_tr_b16 a[110:111], v225 offset:9224\n\t"
    "ds_read_b64_tr_b16 a[80:81], v226 offset:0\n\t"
    "ds_read_b64_tr_b16 a[82:83], v226 offset:1024\n\t"
    "ds_read_b64_tr_b16 a[112:113], v226 offset:8192\n\t"
    "ds_read_b64_tr_b16 a[114:115], v226 offset:9216\n\t"
    "ds_read_b64_tr_b16 a[84:85], v226 offset:8\n\t"
    "ds_read_b64_tr_b16 a[86:87], v226 offset:1032\n\t"
    "ds_read_b64_tr_b16 a[116:117], v226 offset:8200\n\t"
    "ds_read_b64_tr_b16 a[118:119], v226 offset:9224\n\t"
    "ds_read_b64_tr_b16 a[88:89], v227 offset:0\n\t"
    "ds_read_b64_tr_b16 a[90:91], v227 offset:1024\n\t"
    "ds_read_b64_tr_b16 a[120:121], v227 offset:8192\n\t"
    "ds_read_b64_tr_b16 a[122:123], v227 offset:9216\n\t"
    "ds_read_b64_tr_b16 a[92:93], v227 offset:8\n\t"
    "ds_read_b64_tr_b16 a[94:95], v227 offset:1032\n\t"
    "ds_read_b64_tr_b16 a[124:125], v227 offset:8200\n\t"
    "ds_read_b64_tr_b16 a[126:127], v227 offset:9224\n\t"
    "s_waitcnt vmcnt(0)\n\t"
    "s_mov_b64 exec, 3\n\t"
    "buffer_store_dwordx4 v[104:107], v254, s[28:31], s99 offen sc1\n\t"
    "s_mov_b64 exec, -1\n\t"
    "s_add_u32 m0, s37, 49152\n\t"
    "s_add_u32 s36, s42, 1572864\n\t"
    "buffer_load_dwordx4 v232, s[44:47], s36 offen offset:0  lds\n\t"
    "buffer_load_dwordx4 v233, s[44:47], s36 offen offset:1024  lds\n\t"
    "buffer_load_dwordx4 v234, s[44:47], s36 offen offset:2048  lds\n\t"
    "buffer_load_dwordx4 v235, s[44:47], s36 offen offset:3072  lds\n\t"
    "s_add_u32 m0, s37, 65536\n\t"
    "s_add_u32 s36, s42, 2097152\n\t"
    "buffer_load_dwordx4 v232, s[44:47], s36 offen offset:0  lds\n\t"
    "buffer_load_dwordx4 v233, s[44:47], s36 offen offset:1024  lds\n\t"
    "buffer_load_dwordx4 v234, s[44:47], s36 offen offset:2048  lds\n\t"
    "buffer_load_dwordx4 v235, s[44:47], s36 offen offset:3072  lds\n\t"
    "s_add_u32 m0, s37, 81920\n\t"
    "s_add_u32 s36, s42, 2621440\n\t"
    "buffer_load_dwordx4 v232, s[44:47], s36 offen offset:0  lds\n\t"
    "buffer_load_dwordx4 v233, s[44:47], s36 offen offset:1024  lds\n\t"
    "buffer_load_dwordx4 v234, s[44:47], s36 offen offset:2048  lds\n\t"
    "buffer_load_dwordx4 v235, s[44:47], s36 offen offset:3072  lds\n\t"
    "s_add_u32 m0, s37, 98304\n\t"
    "s_add_u32 s36, s42, 3145728\n\t"
    "buffer_load_dwordx4 v232, s[44:47], s36 offen offset:0  lds\n\t"
    "buffer_load_dwordx4 v233, s[44:47], s36 offen offset:1024  lds\n\t"
    "buffer_load_dwordx4 v234, s[44:47], s36 offen offset:2048  lds\n\t"
    "buffer_load_dwordx4 v235, s[44:47], s36 offen offset:3072  lds\n\t"
    "s_add_u32 m0, s37, 114688\n\t"
    "s_add_u32 s36, s42, 3670016\n\t"
    "buffer_load_dwordx4 v232, s[44:47], s36 offen offset:0  lds\n\t"
    "buffer_load_dwordx4 v233, s[44:47], s36 offen offset:1024  lds\n\t"
    "buffer_load_dwordx4 v234, s[44:47], s36 offen offset:2048  lds\n\t"
    "buffer_load_dwordx4 v235, s[44:47], s36 offen offset:3072  lds\n\t"
    "L_fend%=:\n\t"
    "s_mov_b32 s96, s97\n\t"
    "s_mov_b32 s38, 0x20000\n\t"
    "L_fpoll%=:\n\t"
    "buffer_load_dword v248, v57, s[28:31], 0 offen sc1\n\t"
    "s_waitcnt vmcnt(0)\n\t"
    "v_cmp_ne_u32 vcc, s97, v248\n\t"
    "s_nop 4\n\t"
    "s_cbranch_vccz L_fpolld%=\n\t"
    "s_sub_u32 s38, s38, 1\n\t"
    "s_cbranch_scc0 L_fpoll%=\n\t"
    "L_fpolld%=:\n\t"
    "s_movk_i32 s75, 0\n\t"
    "buffer_load_dwordx4 v[128:131], v254, s[48:51], s75 offen offset:0 sc1\n\t"
    "buffer_load_dwordx4 v[132:135], v254, s[48:51], s75 offen offset:1024 sc1\n\t"
    "buffer_load_dwordx4 v[136:139], v254, s[48:51], s75 offen offset:2048 sc1\n\t"
    "buffer_load_dwordx4 v[140:143], v254, s[48:51], s75 offen offset:3072 sc1\n\t"
    "s_movk_i32 s75, 4096\n\t"
    "buffer_load_dwordx4 v[144:147], v254, s[48:51], s75 offen offset:0 sc1\n\t"
    "buffer_load_dwordx4 v[148:151], v254, s[48:51], s75 offen offset:1024 sc1\n\t"
    "buffer_load_dwordx4 v[152:155], v254, s[48:51], s75 offen offset:2048 sc1\n\t"
    "buffer_load_dwordx4 v[156:159], v254, s[48:51], s75 offen offset:3072 sc1\n\t"
    "s_movk_i32 s75, 8192\n\t"
    "buffer_load_dwordx4 v[160:163], v254, s[48:51], s75 offen offset:0 sc1\n\t"
    "buffer_load_dwordx4 v[164:167], v254, s[48:51], s75 offen offset:1024 sc1\n\t"
    "buffer_load_dwordx4 v[168:171], v254, s[48:51], s75 offen offset:2048 sc1\n\t"
    "buffer_load_dwordx4 v[172:175], v254, s[48:51], s75 offen offset:3072 sc1\n\t"
    "s_movk_i32 s75, 12288\n\t"
    "buffer_load_dwordx4 v[176:179], v254, s[48:51], s75 offen offset:0 sc1\n\t"
    "buffer_load_dwordx4 v[180:183], v254, s[48:51], s75 offen offset:1024 sc1\n\t"
    "buffer_load_dwordx4 v[184:187], v254, s[48:51], s75 offen offset:2048 sc1\n\t"
    "buffer_load_dwordx4 v[188:191], v254, s[48:51], s75 offen offset:3072 sc1\n\t"
    "s_waitcnt vmcnt(14) lgkmcnt(15)\n\t"
    "v_mfma_f32_16x16x32_bf16 v[0:3], a[64:67], v[128:131], 0\n\t"
    "v_mfma_f32_16x16x32_bf16 v[0:3], a[96:99], v[132:135], v[0:3]\n\t"
    "v_mfma_f32_16x16x32_bf16 v[4:7], a[68:71], v[128:131], 0\n\t"
    "ds_read_b64_tr_b16 a[128:129], v224 offset:16384\n\t"
    "v_mfma_f32_16x16x32_bf16 v[4:7], a[100:103], v[132:135], v[4:7]\n\t"
    "ds_read_b64_tr_b16 a[130:131], v224 offset:17408\n\t"
    "v_mfma_f32_16x16x32_bf16 v[8:11], a[72:75], v[128:131], 0\n\t"
    "ds_read_b64_tr_b16 a[160:161], v224 offset:24576\n\t"
    "v_mfma_f32_16x16x32_bf16 v[8:11], a[104:107], v[132:135], v[8:11]\n\t"
    "s_movk_i32 s75, 16384\n\t"
    "buffer_load_dwordx4 v[192:195], v254, s[48:51], s75 offen offset:0 sc1\n\t"
    "v_mfma_f32_16x16x32_bf16 v[12:15], a[76:79], v[128:131], 0\n\t"
    "v_mfma_f32_16x16x32_bf16 v[12:15], a[108:111], v[132:135], v[12:15]\n\t"
    "s_waitcnt lgkmcnt(11)\n\t"
    "ds_read_b64_tr_b16 a[162:163], v224 offset:25600\n\t"
    "v_mfma_f32_16x16x32_bf16 v[16:19], a[80:83], v[128:131], 0\n\t"
    "ds_read_b64_tr_b16 a[132:133], v224 offset:16392\n\t"
    "v_mfma_f32_16x16x32_bf16 v[16:19], a[112:115], v[132:135], v[16:19]\n\t"
    "v_mfma_f32_16x16x32_bf16 v[20:23], a[84:87], v[128:131], 0\n\t"
    "ds_read_b64_tr_b16 a[134:135], v224 offset:17416\n\t"
    "v_mfma_f32_16x16x32_bf16 v[20:23], a[116:119], v[132:135], v[20:23]\n\t"
    "s_waitcnt lgkmcnt(6)\n\t"
    "buffer_load_dwordx4 v[196:199], v254, s[48:51], s75 offen offset:1024 sc1\n\t"
    "v_mfma_f32_16x16x32_bf16 v[24:27], a[88:91], v[128:131], 0\n\t"
    "ds_read_b64_tr_b16 a[164:165], v224 offset:24584\n\t"
    "v_mfma_f32_16x16x32_bf16 v[24:27], a[120:123], v[132:135], v[24:27]\n\t"
    "v_mfma_f32_16x16x32_bf16 v[28:31], a[92:95], v[128:131], 0\n\t"
    "ds_read_b64_tr_b16 a[166:167], v224 offset:25608\n\t"
    "v_mfma_f32_16x16x32_bf16 v[28:31], a[124:127], v[132:135], v[28:31]\n\t"
    "s_waitcnt vmcnt(14)\n\t"
    "ds_read_b64_tr_b16 a[136:137], v225 offset:16384\n\t"
    "v_mfma_f32_16x16x32_bf16 v[32:35], a[64:67], v[136:139], 0\n\t"
    "v_mfma_f32_16x16x32_bf16 v[32:35], a[96:99], v[140:143], v[32:35]\n\t"
    "buffer_load_dwordx4 v[200:203], v254, s[48:51], s75 offen offset:2048 sc1\n\t"
    "v_mfma_f32_16x16x32_bf16 v[36:39], a[68:71], v[136:139], 0\n\t"
    "ds_read_b64_tr_b16 a[138:139], v225 offset:17408\n\t"
    "v_mfma_f32_16x16x32_bf16 v[36:39], a[100:103], v[140:143], v[36:39]\n\t"
    "ds_read_b64_tr_b16 a[168:169], v225 offset:24576\n\t"
    "v_mfma_f32_16x16x32_bf16 v[40:43], a[72:75], v[136:139], 0\n\t"
    "v_mfma_f32_16x16x32_bf16 v[40:43], a[104:107], v[140:143], v[40:43]\n\t"
    "ds_read_b64_tr_b16 a[170:171], v225 offset:25600\n\t"
    "v_mfma_f32_16x16x32_bf16 v[44:47], a[76:79], v[136:139], 0\n\t"
    "ds_read_b64_tr_b16 a[140:141], v225 offset:16392\n\t"
    "v_mfma_f32_16x16x32_bf16 v[44:47], a[108:111], v[140:143], v[44:47]\n\t"
    "buffer_load_dwordx4 v[204:207], v254, s[48:51], s75 offen offset:3072 sc1\n\t"
    "v_mfma_f32_16x16x32_bf16 v[48:51], a[80:83], v[136:139], 0\n\t"
    "v_mfma_f32_16x16x32_bf16 v[48:51], a[112:115], v[140:143], v[48:51]\n\t"
    "ds_read_b64_tr_b16 a[142:143], v225 offset:17416\n\t"
    "v_mfma_f32_16x16x32_bf16 v[52:55], a[84:87], v[136:139], 0\n\t"
    "ds_read_b64_tr_b16 a[172:173], v225 offset:24584\n\t"
    "v_mfma_f32_16x16x32_bf16 v[52:55], a[116:119], v[140:143], v[52:55]\n\t"
    "v_mfma_f32_16x16x32_bf16 v[56:59], a[88:91], v[136:139], 0\n\t"
    "ds_read_b64_tr_b16 a[174:175], v225 offset:25608\n\t"
    "v_mfma_f32_16x16x32_bf16 v[56:59], a[120:123], v[140:143], v[56:59]\n\t"
    "s_movk_i32 s75, 20480\n\t"
    "buffer_load_dwordx4 v[208:211], v254, s[48:51], s75 offen offset:0 sc1\n\t"
    "v_mfma_f32_16x16x32_bf16 v[60:63], a[92:95], v[136:139], 0\n\t"
    "ds_read_b64_tr_b16 a[144:145], v226 offset:16384\n\t"
    "v_mfma_f32_16x16x32_bf16 v[60:63], a[124:127], v[140:143], v[60:63]\n\t"
    "s_waitcnt vmcnt(15)\n\t"
    "ds_read_b64_tr_b16 a[146:147], v226 offset:17408\n\t"
    "v_mfma_f32_16x16x32_bf16 v[64:67], a[64:67], v[144:147], 0\n\t"
    "v_mfma_f32_16x16x32_bf16 v[64:67], a[96:99], v[148:151], v[64:67]\n\t"
    "ds_read_b64_tr_b16 a[176:177], v226 offset:24576\n\t"
    "v_mfma_f32_16x16x32_bf16 v[68:71], a[68:71], v[144:147], 0\n\t"
    "ds_read_b64_tr_b16 a[178:179], v226 offset:25600\n\t"
    "v_mfma_f32_16x16x32_bf16 v[68:71], a[100:103], v[148:151], v[68:71]\n\t"
    "buffer_load_dwordx4 v[212:215], v254, s[48:51], s75 offen offset:1024 sc1\n\t"
    "v_mfma_f32_16x16x32_bf16 v[72:75], a[72:75], v[144:147], 0\n\t"
    "v_mfma_f32_16x16x32_bf16 v[72:75], a[104:107], v[148:151], v[72:75]\n\t"
    "ds_read_b64_tr_b16 a[148:149], v226 offset:16392\n\t"
    "v_mfma_f32_16x16x32_bf16 v[76:79], a[76:79], v[144:147], 0\n\t"
    "ds_read_b64_tr_b16 a[150:151], v226 offset:17416\n\t"
    "v_mfma_f32_16x16x32_bf16 v[76:79], a[108:111], v[148:151], v[76:79]\n\t"
    "v_mfma_f32_16x16x32_bf16 v[80:83], a[80:83], v[144:147], 0\n\t"
    "ds_read_b64_tr_b16 a[180:181], v226 offset:24584\n\t"
    "v_mfma_f32_16x16x32_bf16 v[80:83], a[112:115], v[148:151], v[80:83]\n\t"
    "buffer_load_dwordx4 v[216:219], v254, s[48:51], s75 offen offset:2048 sc1\n\t"
    "v_mfma_f32_16x16x32_bf16 v[84:87], a[84:87], v[144:147], 0\n\t"
    "ds_read_b64_tr_b16 a[182:183], v226 offset:25608\n\t"
    "v_mfma_f32_16x16x32_bf16 v[84:87], a[116:119], v[148:151], v[84:87]\n\t"
    "v_mfma_f32_16x16x32_bf16 v[88:91], a[88:91], v[144:147], 0\n\t"
    "ds_read_b64_tr_b16 a[152:153], v227 offset:16384\n\t"
    "v_mfma_f32_16x16x32_bf16 v[88:91], a[120:123], v[148:151], v[88:91]\n\t"
    "ds_read_b64_tr_b16 a[154:155], v227 offset:17408\n\t"
    "v_mfma_f32_16x16x32_bf16 v[92:95], a[92:95], v[144:147], 0\n\t"
    "v_mfma_f32_16x16x32_bf16 v[92:95], a[124:127], v[148:151], v[92:95]\n\t"
    "s_waitcnt vmcnt(15)\n\t"
    "buffer_load_dwordx4 v[220:223], v254, s[48:51], s75 offen offset:3072 sc1\n\t"
    "v_mfma_f32_16x16x32_bf16 v[96:99], a[64:67], v[152:155], 0\n\t"
    "ds_read_b64_tr_b16 a[184:185], v227 offset:24576\n\t"
    "v_mfma_f32_16x16x32_bf16 v[96:99], a[96:99], v[156:159], v[96:99]\n\t"
    "ds_read_b64_tr_b16 a[186:187], v227 offset:25600\n\t"
    "v_mfma_f32_16x16x32_bf16 v[100:103], a[68:71], v[152:155], 0\n\t"
    "v_mfma_f32_16x16x32_bf16 v[100:103], a[100:103], v[156:159], v[100:103]\n\t"
    "ds_read_b64_tr_b16 a[156:157], v227 offset:16392\n\t"
    "v_mfma_f32_16x16x32_bf16 v[104:107], a[72:75], v[152:155], 0\n\t"
    "ds_read_b64_tr_b16 a[158:159], v227 offset:17416\n\t"
    "v_mfma_f32_16x16x32_bf16 v[104:107], a[104:107], v[156:159], v[104:107]\n\t"
    "s_movk_i32 s75, 24576\n\t"
    "buffer_load_dwordx4 a[196:199], v254, s[48:51], s75 offen offset:0 sc1\n\t"
    "v_mfma_f32_16x16x32_bf16 v[108:111], a[76:79], v[152:155], 0\n\t"
    "v_mfma_f32_16x16x32_bf16 v[108:111], a[108:111], v[156:159], v[108:111]\n\t"
    "ds_read_b64_tr_b16 a[188:189], v227 offset:24584\n\t"
    "v_mfma_f32_16x16x32_bf16 v[112:115], a[80:83], v[152:155], 0\n\t"
    "ds_read_b64_tr_b16 a[190:191], v227 offset:25608\n\t"
    "v_mfma_f32_16x16x32_bf16 v[112:115], a[112:115], v[156:159], v[112:115]\n\t"
    "v_mfma_f32_16x16x32_bf16 v[116:119], a[84:87], v[152:155], 0\n\t"
    "v_mfma_f32_16x16x32_bf16 v[116:119], a[116:119], v[156:159], v[116:119]\n\t"
    "buffer_load_dwordx4 a[200:203], v254, s[48:51], s75 offen offset:1024 sc1\n\t"
    "v_mfma_f32_16x16x32_bf16 v[120:123], a[88:91], v[152:155], 0\n\t"
    "v_mfma_f32_16x16x32_bf16 v[120:123], a[120:123], v[156:159], v[120:123]\n\t"
    "v_mfma_f32_16x16x32_bf16 v[124:127], a[92:95], v[152:155], 0\n\t"
    "v_mfma_f32_16x16x32_bf16 v[124:127], a[124:127], v[156:159], v[124:127]\n\t"
    "s_waitcnt vmcnt(16) lgkmcnt(15)\n\t"
    "v_mfma_f32_16x16x32_bf16 v[0:3], a[128:131], v[160:163], v[0:3]\n\t"
    "v_mfma_f32_16x16x32_bf16 v[0:3], a[160:163], v[164:167], v[0:3]\n\t"
    "buffer_load_dwordx4 a[204:207], v254, s[48:51], s75 offen offset:2048 sc1\n\t"
    "v_mfma_f32_16x16x32_bf16 v[4:7], a[132:135], v[160:163], v[4:7]\n\t"
    "ds_read_b64_tr_b16 a[64:65], v224 offset:32768\n\t"
    "v_mfma_f32_16x16x32_bf16 v[4:7], a[164:167], v[164:167], v[4:7]\n\t"
    "ds_read_b64_tr_b16 a[66:67], v224 offset:33792\n\t"
    "v_mfma_f32_16x16x32_bf16 v[8:11], a[136:139], v[160:163], v[8:11]\n\t"
    "ds_read_b64_tr_b16 a[96:97], v224 offset:40960\n\t"
    "v_mfma_f32_16x16x32_bf16 v[8:11], a[168:171], v[164:167], v[8:11]\n\t"
    "v_mfma_f32_16x16x32_bf16 v[12:15], a[140:143], v[160:163], v[12:15]\n\t"
    "ds_read_b64_tr_b16 a[98:99], v224 offset:41984\n\t"
    "v_mfma_f32_16x16x32_bf16 v[12:15], a[172:175], v[164:167], v[12:15]\n\t"
    "s_waitcnt lgkmcnt(12)\n\t"
    "buffer_load_dwordx4 a[208:211], v254, s[48:51], s75 offen offset:3072 sc1\n\t"
    "v_mfma_f32_16x16x32_bf16 v[16:19], a[144:147], v[160:163], v[16:19]\n\t"
    "ds_read_b64_tr_b16 a[68:69], v224 offset:32776\n\t"
    "v_mfma_f32_16x16x32_bf16 v[16:19], a[176:179], v[164:167], v[16:19]\n\t"
    "v_mfma_f32_16x16x32_bf16 v[20:23], a[148:151], v[160:163], v[20:23]\n\t"
    "ds_read_b64_tr_b16 a[70:71], v224 offset:33800\n\t"
    "v_mfma_f32_16x16x32_bf16 v[20:23], a[180:183], v[164:167], v[20:23]\n\t"
    "s_waitcnt lgkmcnt(6)\n\t"
    "ds_read_b64_tr_b16 a[100:101], v224 offset:40968\n\t"
    "v_mfma_f32_16x16x32_bf16 v[24:27], a[152:155], v[160:163], v[24:27]\n\t"
    "v_mfma_f32_16x16x32_bf16 v[24:27], a[184:187], v[164:167], v[24:27]\n\t"
    "s_movk_i32 s75, 28672\n\t"
    "buffer_load_dwordx4 a[212:215], v254, s[48:51], s75 offen offset:0 sc1\n\t"
    "v_mfma_f32_16x16x32_bf16 v[28:31], a[156:159], v[160:163], v[28:31]\n\t"
    "ds_read_b64_tr_b16 a[102:103], v224 offset:41992\n\t"
    "v_mfma_f32_16x16x32_bf16 v[28:31], a[188:191], v[164:167], v[28:31]\n\t"
    "s_waitcnt vmcnt(17)\n\t"
    "ds_read_b64_tr_b16 a[72:73], v225 offset:32768\n\t"
    "v_mfma_f32_16x16x32_bf16 v[32:35], a[128:131], v[168:171], v[32:35]\n\t"
    "v_mfma_f32_16x16x32_bf16 v[32:35], a[160:163], v[172:175], v[32:35]\n\t"
    "ds_read_b64_tr_b16 a[74:75], v225 offset:33792\n\t"
    "v_mfma_f32_16x16x32_bf16 v[36:39], a[132:135], v[168:171], v[36:39]\n\t"
    "ds_read_b64_tr_b16 a[104:105], v225 offset:40960\n\t"
    "v_mfma_f32_16x16x32_bf16 v[36:39], a[164:167], v[172:175], v[36:39]\n\t"
    "buffer_load_dwordx4 a[216:219], v254, s[48:51], s75 offen offset:1024 sc1\n\t"
    "v_mfma_f32_16x16x32_bf16 v[40:43], a[136:139], v[168:171], v[40:43]\n\t"
    "v_mfma_f32_16x16x32_bf16 v[40:43], a[168:171], v[172:175], v[40:43]\n\t"
    "ds_read_b64_tr_b16 a[106:107], v225 offset:41984\n\t"
    "v_mfma_f32_16x16x32_bf16 v[44:47], a[140:143], v[168:171], v[44:47]\n\t"
    "ds_read_b64_tr_b16 a[76:77], v225 offset:32776\n\t"
    "v_mfma_f32_16x16x32_bf16 v[44:47], a[172:175], v[172:175], v[44:47]\n\t"
    "v_mfma_f32_16x16x32_bf16 v[48:51], a[144:147], v[168:171], v[48:51]\n\t"
    "ds_read_b64_tr_b16 a[78:79], v225 offset:33800\n\t"
    "v_mfma_f32_16x16x32_bf16 v[48:51], a[176:179], v[172:175], v[48:51]\n\t"
    "buffer_load_dwordx4 a[220:223], v254, s[48:51], s75 offen offset:2048 sc1\n\t"
    "v_mfma_f32_16x16x32_bf16 v[52:55], a[148:151], v[168:171], v[52:55]\n\t"
    "ds_read_b64_tr_b16 a[108:109], v225 offset:40968\n\t"
    "v_mfma_f32_16x16x32_bf16 v[52:55], a[180:183], v[172:175], v[52:55]\n\t"
    "v_mfma_f32_16x16x32_bf16 v[56:59], a[152:155], v[168:171], v[56:59]\n\t"
    "ds_read_b64_tr_b16 a[110:111], v225 offset:41992\n\t"
    "v_mfma_f32_16x16x32_bf16 v[56:59], a[184:187], v[172:175], v[56:59]\n\t"
    "ds_read_b64_tr_b16 a[80:81], v226 offset:32768\n\t"
    "v_mfma_f32_16x16x32_bf16 v[60:63], a[156:159], v[168:171], v[60:63]\n\t"
    "ds_read_b64_tr_b16 a[82:83], v226 offset:33792\n\t"
    "v_mfma_f32_16x16x32_bf16 v[60:63], a[188:191], v[172:175], v[60:63]\n\t"
    "s_waitcnt vmcnt(17)\n\t"
    "buffer_load_dwordx4 a[224:227], v254, s[48:51], s75 offen offset:3072 sc1\n\t"
    "v_mfma_f32_16x16x32_bf16 v[64:67], a[128:131], v[176:179], v[64:67]\n\t"
    "v_mfma_f32_16x16x32_bf16 v[64:67], a[160:163], v[180:183], v[64:67]\n\t"
    "ds_read_b64_tr_b16 a[112:113], v226 offset:40960\n\t"
    "v_mfma_f32_16x16x32_bf16 v[68:71], a[132:135], v[176:179], v[68:71]\n\t"
    "ds_read_b64_tr_b16 a[114:115], v226 offset:41984\n\t"
    "v_mfma_f32_16x16x32_bf16 v[68:71], a[164:167], v[180:183], v[68:71]\n\t"
    "v_mfma_f32_16x16x32_bf16 v[72:75], a[136:139], v[176:179], v[72:75]\n\t"
    "ds_read_b64_tr_b16 a[84:85], v226 offset:32776\n\t"
    "v_mfma_f32_16x16x32_bf16 v[72:75], a[168:171], v[180:183], v[72:75]\n\t"
    "s_mov_b32 s75, 32768\n\t"
    "buffer_load_dwordx4 a[0:3], v254, s[48:51], s75 offen offset:0 sc1\n\t"
    "v_mfma_f32_16x16x32_bf16 v[76:79], a[140:143], v[176:179], v[76:79]\n\t"
    "ds_read_b64_tr_b16 a[86:87], v226 offset:33800\n\t"
    "v_mfma_f32_16x16x32_bf16 v[76:79], a[172:175], v[180:183], v[76:79]\n\t"
    "v_mfma_f32_16x16x32_bf16 v[80:83], a[144:147], v[176:179], v[80:83]\n\t"
    "ds_read_b64_tr_b16 a[116:117], v226 offset:40968\n\t"
    "v_mfma_f32_16x16x32_bf16 v[80:83], a[176:179], v[180:183], v[80:83]\n\t"
    "ds_read_b64_tr_b16 a[118:119], v226 offset:41992\n\t"
    "v_mfma_f32_16x16x32_bf16 v[84:87], a[148:151], v[176:179], v[84:87]\n\t"
    "v_mfma_f32_16x16x32_bf16 v[84:87], a[180:183], v[180:183], v[84:87]\n\t"
    "buffer_load_dwordx4 a[4:7], v254, s[48:51], s75 offen offset:1024 sc1\n\t"
    "v_mfma_f32_16x16x32_bf16 v[88:91], a[152:155], v[176:179], v[88:91]\n\t"
    "ds_read_b64_tr_b16 a[88:89], v227 offset:32768\n\t"
    "v_mfma_f32_16x16x32_bf16 v[88:91], a[184:187], v[180:183], v[88:91]\n\t"
    "ds_read_b64_tr_b16 a[90:91], v227 offset:33792\n\t"
    "v_mfma_f32_16x16x32_bf16 v[92:95], a[156:159], v[176:179], v[92:95]\n\t"
    "v_mfma_f32_16x16x32_bf16 v[92:95], a[188:191], v[180:183], v[92:95]\n\t"
    "s_waitcnt vmcnt(18)\n\t"
    "ds_read_b64_tr_b16 a[120:121], v227 offset:40960\n\t"
    "v_mfma_f32_16x16x32_bf16 v[96:99], a[128:131], v[184:187], v[96:99]\n\t"
    "ds_read_b64_tr_b16 a[122:123], v227 offset:41984\n\t"
    "v_mfma_f32_16x16x32_bf16 v[96:99], a[160:163], v[188:191], v[96:99]\n\t"
    "buffer_load_dwordx4 a[8:11], v254, s[48:51], s75 offen offset:2048 sc1\n\t"
    "v_mfma_f32_16x16x32_bf16 v[100:103], a[132:135], v[184:187], v[100:103]\n\t"
    "v_mfma_f32_16x16x32_bf16 v[100:103], a[164:167], v[188:191], v[100:103]\n\t"
    "ds_read_b64_tr_b16 a[92:93], v227 offset:32776\n\t"
    "v_mfma_f32_16x16x32_bf16 v[104:107], a[136:139], v[184:187], v[104:107]\n\t"
    "ds_read_b64_tr_b16 a[94:95], v227 offset:33800\n\t"
    "v_mfma_f32_16x16x32_bf16 v[104:107], a[168:171], v[188:191], v[104:107]\n\t"
    "v_mfma_f32_16x16x32_bf16 v[108:111], a[140:143], v[184:187], v[108:111]\n\t"
    "ds_read_b64_tr_b16 a[124:125], v227 offset:40968\n\t"
    "v_mfma_f32_16x16x32_bf16 v[108:111], a[172:175], v[188:191], v[108:111]\n\t"
    "buffer_load_dwordx4 a[12:15], v254, s[48:51], s75 offen offset:3072 sc1\n\t"
    "v_mfma_f32_16x16x32_bf16 v[112:115], a[144:147], v[184:187], v[112:115]\n\t"
    "ds_read_b64_tr_b16 a[126:127], v227 offset:41992\n\t"
    "v_mfma_f32_16x16x32_bf16 v[112:115], a[176:179], v[188:191], v[112:115]\n\t"
    "v_mfma_f32_16x16x32_bf16 v[116:119], a[148:151], v[184:187], v[116:119]\n\t"
    "v_mfma_f32_16x16x32_bf16 v[116:119], a[180:183], v[188:191], v[116:119]\n\t"
    "v_mfma_f32_16x16x32_bf16 v[120:123], a[152:155], v[184:187], v[120:123]\n\t"
    "v_mfma_f32_16x16x32_bf16 v[120:123], a[184:187], v[188:191], v[120:123]\n\t"
    "s_mov_b32 s75, 36864\n\t"
    "buffer_load_dwordx4 a[16:19], v254, s[48:51], s75 offen offset:0 sc1\n\t"
    "v_mfma_f32_16x16x32_bf16 v[124:127], a[156:159], v[184:187], v[124:127]\n\t"
    "v_mfma_f32_16x16x32_bf16 v[124:127], a[188:191], v[188:191], v[124:127]\n\t"
    "s_waitcnt vmcnt(20) lgkmcnt(15)\n\t"
    "v_mfma_f32_16x16x32_bf16 v[0:3], a[64:67], v[192:195], v[0:3]\n\t"
    "s_waitcnt vmcnt(19) lgkmcnt(0)\n\t"
    "v_mfma_f32_16x16x32_bf16 v[0:3], a[96:99], v[196:199], v[0:3]\n\t"
    "s_barrier\n\t"
    "v_mfma_f32_16x16x32_bf16 v[4:7], a[68:71], v[192:195], v[4:7]\n\t"
    "ds_read_b64_tr_b16 a[128:129], v224 offset:49152\n\t"
    "v_mfma_f32_16x16x32_bf16 v[4:7], a[100:103], v[196:199], v[4:7]\n\t"
    "s_add_u32 m0, s37, 0\n\t"
    "ds_read_b64_tr_b16 a[130:131], v224 offset:50176\n\t"
    "v_mfma_f32_16x16x32_bf16 v[8:11], a[72:75], v[192:195], v[8:11]\n\t"
    "s_add_u32 s36, s42, 4194304\n\t"
    "ds_read_b64_tr_b16 a[160:161], v224 offset:57344\n\t"
    "v_mfma_f32_16x16x32_bf16 v[8:11], a[104:107], v[196:199], v[8:11]\n\t"
    "buffer_load_dwordx4 a[20:23], v254, s[48:51], s75 offen offset:1024 sc1\n\t"
    "v_mfma_f32_16x16x32_bf16 v[12:15], a[76:79], v[192:195], v[12:15]\n\t"
    "buffer_load_dwordx4 v232, s[44:47], s36 offen offset:0  lds\n\t"
    "v_mfma_f32_16x16x32_bf16 v[12:15], a[108:111], v[196:199], v[12:15]\n\t"
    "v_mfma_f32_16x16x32_bf16 v[16:19], a[80:83], v[192:195], v[16:19]\n\t"
    "ds_read_b64_tr_b16 a[162:163], v224 offset:58368\n\t"
    "v_mfma_f32_16x16x32_bf16 v[16:19], a[112:115], v[196:199], v[16:19]\n\t"
    "ds_read_b64_tr_b16 a[132:133], v224 offset:49160\n\t"
    "v_mfma_f32_16x16x32_bf16 v[20:23], a[84:87], v[192:195], v[20:23]\n\t"
    "ds_read_b64_tr_b16 a[134:135], v224 offset:50184\n\t"
    "v_mfma_f32_16x16x32_bf16 v[20:23], a[116:119], v[196:199], v[20:23]\n\t"
    "buffer_load_dwordx4 a[24:27], v254, s[48:51], s75 offen offset:2048 sc1\n\t"
    "v_mfma_f32_16x16x32_bf16 v[24:27], a[88:91], v[192:195], v[24:27]\n\t"
    "v_mfma_f32_16x16x32_bf16 v[24:27], a[120:123], v[196:199], v[24:27]\n\t"
    "ds_read_b64_tr_b16 a[164:165], v224 offset:57352\n\t"
    "v_mfma_f32_16x16x32_bf16 v[28:31], a[92:95], v[192:195], v[28:31]\n\t"
    "ds_read_b64_tr_b16 a[166:167], v224 offset:58376\n\t"
    "v_mfma_f32_16x16x32_bf16 v[28:31], a[124:127], v[196:199], v[28:31]\n\t"
    "s_waitcnt vmcnt(20)\n\t"
    "ds_read_b64_tr_b16 a[136:137], v225 offset:49152\n\t"
    "v_mfma_f32_16x16x32_bf16 v[32:35], a[64:67], v[200:203], v[32:35]\n\t"
    "v_mfma_f32_16x16x32_bf16 v[32:35], a[96:99], v[204:207], v[32:35]\n\t"
    "buffer_load_dwordx4 a[28:31], v254, s[48:51], s75 offen offset:3072 sc1\n\t"
    "v_mfma_f32_16x16x32_bf16 v[36:39], a[68:71], v[200:203], v[36:39]\n\t"
    "ds_read_b64_tr_b16 a[138:139], v225 offset:50176\n\t"
    "v_mfma_f32_16x16x32_bf16 v[36:39], a[100:103], v[204:207], v[36:39]\n\t"
    "ds_read_b64_tr_b16 a[168:169], v225 offset:57344\n\t"
    "v_mfma_f32_16x16x32_bf16 v[40:43], a[72:75], v[200:203], v[40:43]\n\t"
    "ds_read_b64_tr_b16 a[170:171], v225 offset:58368\n\t"
    "v_mfma_f32_16x16x32_bf16 v[40:43], a[104:107], v[204:207], v[40:43]\n\t"
    "v_mfma_f32_16x16x32_bf16 v[44:47], a[76:79], v[200:203], v[44:47]\n\t"
    "buffer_load_dwordx4 v233, s[44:47], s36 offen offset:1024  lds\n\t"
    "v_mfma_f32_16x16x32_bf16 v[44:47], a[108:111], v[204:207], v[44:47]\n\t"
    "s_mov_b32 s75, 40960\n\t"
    "buffer_load_dwordx4 a[32:35], v254, s[48:51], s75 offen offset:0 sc1\n\t"
    "v_mfma_f32_16x16x32_bf16 v[48:51], a[80:83], v[200:203], v[48:51]\n\t"
    "ds_read_b64_tr_b16 a[140:141], v225 offset:49160\n\t"
    "v_mfma_f32_16x16x32_bf16 v[48:51], a[112:115], v[204:207], v[48:51]\n\t"
    "ds_read_b64_tr_b16 a[142:143], v225 offset:50184\n\t"
    "v_mfma_f32_16x16x32_bf16 v[52:55], a[84:87], v[200:203], v[52:55]\n\t"
    "ds_read_b64_tr_b16 a[172:173], v225 offset:57352\n\t"
    "v_mfma_f32_16x16x32_bf16 v[52:55], a[116:119], v[204:207], v[52:55]\n\t"
    "v_mfma_f32_16x16x32_bf16 v[56:59], a[88:91], v[200:203], v[56:59]\n\t"
    "ds_read_b64_tr_b16 a[174:175], v225 offset:58376\n\t"
    "v_mfma_f32_16x16x32_bf16 v[56:59], a[120:123], v[204:207], v[56:59]\n\t"
    "buffer_load_dwordx4 a[36:39], v254, s[48:51], s75 offen offset:1024 sc1\n\t"
    "v_mfma_f32_16x16x32_bf16 v[60:63], a[92:95], v[200:203], v[60:63]\n\t"
    "ds_read_b64_tr_b16 a[144:145], v226 offset:49152\n\t"
    "v_mfma_f32_16x16x32_bf16 v[60:63], a[124:127], v[204:207], v[60:63]\n\t"
    "s_waitcnt vmcnt(22)\n\t"
    "ds_read_b64_tr_b16 a[146:147], v226 offset:50176\n\t"
    "v_mfma_f32_16x16x32_bf16 v[64:67], a[64:67], v[208:211], v[64:67]\n\t"
    "v_mfma_f32_16x16x32_bf16 v[64:67], a[96:99], v[212:215], v[64:67]\n\t"
    "ds_read_b64_tr_b16 a[176:177], v226 offset:57344\n\t"
    "v_mfma_f32_16x16x32_bf16 v[68:71], a[68:71], v[208:211], v[68:71]\n\t"
    "ds_read_b64_tr_b16 a[178:179], v226 offset:58368\n\t"
    "v_mfma_f32_16x16x32_bf16 v[68:71], a[100:103], v[212:215], v[68:71]\n\t"
    "buffer_load_dwordx4 a[40:43], v254, s[48:51], s75 offen offset:2048 sc1\n\t"
    "v_mfma_f32_16x16x32_bf16 v[72:75], a[72:75], v[208:211], v[72:75]\n\t"
    "ds_read_b64_tr_b16 a[148:149], v226 offset:49160\n\t"
    "v_mfma_f32_16x16x32_bf16 v[72:75], a[104:107], v[212:215], v[72:75]\n\t"
    "v_mfma_f32_16x16x32_bf16 v[76:79], a[76:79], v[208:211], v[76:79]\n\t"
    "buffer_load_dwordx4 v234, s[44:47], s36 offen offset:2048  lds\n\t"
    "v_mfma_f32_16x16x32_bf16 v[76:79], a[108:111], v[212:215], v[76:79]\n\t"
    "ds_read_b64_tr_b16 a[150:151], v226 offset:50184\n\t"
    "v_mfma_f32_16x16x32_bf16 v[80:83], a[80:83], v[208:211], v[80:83]\n\t"
    "ds_read_b64_tr_b16 a[180:181], v226 offset:57352\n\t"
    "v_mfma_f32_16x16x32_bf16 v[80:83], a[112:115], v[212:215], v[80:83]\n\t"
    "buffer_load_dwordx4 a[44:47], v254, s[48:51], s75 offen offset:3072 sc1\n\t"
    "v_mfma_f32_16x16x32_bf16 v[84:87], a[84:87], v[208:211], v[84:87]\n\t"
    "ds_read_b64_tr_b16 a[182:183], v226 offset:58376\n\t"
    "v_mfma_f32_16x16x32_bf16 v[84:87], a[116:119], v[212:215], v[84:87]\n\t"
    "v_mfma_f32_16x16x32_bf16 v[88:91], a[88:91], v[208:211], v[88:91]\n\t"
    "ds_read_b64_tr_b16 a[152:153], v227 offset:49152\n\t"
    "v_mfma_f32_16x16x32_bf16 v[88:91], a[120:123], v[212:215], v[88:91]\n\t"
    "ds_read_b64_tr_b16 a[154:155], v227 offset:50176\n\t"
    "v_mfma_f32_16x16x32_bf16 v[92:95], a[92:95], v[208:211], v[92:95]\n\t"
    "ds_read_b64_tr_b16 a[184:185], v227 offset:57344\n\t"
    "v_mfma_f32_16x16x32_bf16 v[92:95], a[124:127], v[212:215], v[92:95]\n\t"
    "s_waitcnt vmcnt(23)\n\t"
    "s_mov_b32 s75, 45056\n\t"
    "buffer_load_dwordx4 a[48:51], v254, s[48:51], s75 offen offset:0 sc1\n\t"
    "v_mfma_f32_16x16x32_bf16 v[96:99], a[64:67], v[216:219], v[96:99]\n\t"
    "v_mfma_f32_16x16x32_bf16 v[96:99], a[96:99], v[220:223], v[96:99]\n\t"
    "ds_read_b64_tr_b16 a[186:187], v227 offset:58368\n\t"
    "v_mfma_f32_16x16x32_bf16 v[100:103], a[68:71], v[216:219], v[100:103]\n\t"
    "ds_read_b64_tr_b16 a[156:157], v227 offset:49160\n\t"
    "v_mfma_f32_16x16x32_bf16 v[100:103], a[100:103], v[220:223], v[100:103]\n\t"
    "ds_read_b64_tr_b16 a[158:159], v227 offset:50184\n\t"
    "v_mfma_f32_16x16x32_bf16 v[104:107], a[72:75], v[216:219], v[104:107]\n\t"
    "v_mfma_f32_16x16x32_bf16 v[104:107], a[104:107], v[220:223], v[104:107]\n\t"
    "buffer_load_dwordx4 a[52:55], v254, s[48:51], s75 offen offset:1024 sc1\n\t"
    "v_mfma_f32_16x16x32_bf16 v[108:111], a[76:79], v[216:219], v[108:111]\n\t"
    "buffer_load_dwordx4 v235, s[44:47], s36 offen offset:3072  lds\n\t"
    "v_mfma_f32_16x16x32_bf16 v[108:111], a[108:111], v[220:223], v[108:111]\n\t"
    "ds_read_b64_tr_b16 a[188:189], v227 offset:57352\n\t"
    "v_mfma_f32_16x16x32_bf16 v[112:115], a[80:83], v[216:219], v[112:115]\n\t"
    "ds_read_b64_tr_b16 a[190:191], v227 offset:58376\n\t"
    "v_mfma_f32_16x16x32_bf16 v[112:115], a[112:115], v[220:223], v[112:115]\n\t"
    "v_mfma_f32_16x16x32_bf16 v[116:119], a[84:87], v[216:219], v[116:119]\n\t"
    "v_mfma_f32_16x16x32_bf16 v[116:119], a[116:119], v[220:223], v[116:119]\n\t"
    "buffer_load_dwordx4 a[56:59], v254, s[48:51], s75 offen offset:2048 sc1\n\t"
    "v_mfma_f32_16x16x32_bf16 v[120:123], a[88:91], v[216:219], v[120:123]\n\t"
    "v_mfma_f32_16x16x32_bf16 v[120:123], a[120:123], v[220:223], v[120:123]\n\t"
    "v_mfma_f32_16x16x32_bf16 v[124:127], a[92:95], v[216:219], v[124:127]\n\t"
    "v_mfma_f32_16x16x32_bf16 v[124:127], a[124:127], v[220:223], v[124:127]\n\t"
    "s_waitcnt vmcnt(25) lgkmcnt(15)\n\t"
    "v_mfma_f32_16x16x32_bf16 v[0:3], a[128:131], a[196:199], v[0:3]\n\t"
    "v_mfma_f32_16x16x32_bf16 v[0:3], a[160:163], a[200:203], v[0:3]\n\t"
    "buffer_load_dwordx4 a[60:63], v254, s[48:51], s75 offen offset:3072 sc1\n\t"
    "v_mfma_f32_16x16x32_bf16 v[4:7], a[132:135], a[196:199], v[4:7]\n\t"
    "ds_read_b64_tr_b16 a[64:65], v228 offset:0\n\t"
    "v_mfma_f32_16x16x32_bf16 v[4:7], a[164:167], a[200:203], v[4:7]\n\t"
    "s_add_u32 m0, s37, 16384\n\t"
    "ds_read_b64_tr_b16 a[66:67], v228 offset:1024\n\t"
    "v_mfma_f32_16x16x32_bf16 v[8:11], a[136:139], a[196:199], v[8:11]\n\t"
    "s_add_u32 s36, s42, 4718592\n\t"
    "ds_read_b64_tr_b16 a[96:97], v228 offset:8192\n\t"
    "v_mfma_f32_16x16x32_bf16 v[8:11], a[168:171], a[200:203], v[8:11]\n\t"
    "v_mfma_f32_16x16x32_bf16 v[12:15], a[140:143], a[196:199], v[12:15]\n\t"
    "buffer_load_dwordx4 v232, s[44:47], s36 offen offset:0  lds\n\t"
    "v_mfma_f32_16x16x32_bf16 v[12:15], a[172:175], a[200:203], v[12:15]\n\t"
    "s_waitcnt lgkmcnt(11)\n\t"
    "s_mov_b32 s75, 49152\n\t"
    "buffer_load_dwordx4 v[128:131], v254, s[48:51], s75 offen offset:0 sc1\n\t"
    "v_mfma_f32_16x16x32_bf16 v[16:19], a[144:147], a[196:199], v[16:19]\n\t"
    "ds_read_b64_tr_b16 a[98:99], v228 offset:9216\n\t"
    "v_mfma_f32_16x16x32_bf16 v[16:19], a[176:179], a[200:203], v[16:19]\n\t"
    "ds_read_b64_tr_b16 a[68:69], v228 offset:8\n\t"
    "v_mfma_f32_16x16x32_bf16 v[20:23], a[148:151], a[196:199], v[20:23]\n\t"
    "ds_read_b64_tr_b16 a[70:71], v228 offset:1032\n\t"
    "v_mfma_f32_16x16x32_bf16 v[20:23], a[180:183], a[200:203], v[20:23]\n\t"
    "s_waitcnt lgkmcnt(6)\n\t"
    "v_mfma_f32_16x16x32_bf16 v[24:27], a[152:155], a[196:199], v[24:27]\n\t"
    "ds_read_b64_tr_b16 a[100:101], v228 offset:8200\n\t"
    "v_mfma_f32_16x16x32_bf16 v[24:27], a[184:187], a[200:203], v[24:27]\n\t"
    "buffer_load_dwordx4 v[132:135], v254, s[48:51], s75 offen offset:1024 sc1\n\t"
    "v_mfma_f32_16x16x32_bf16 v[28:31], a[156:159], a[196:199], v[28:31]\n\t"
    "ds_read_b64_tr_b16 a[102:103], v228 offset:9224\n\t"
    "v_mfma_f32_16x16x32_bf16 v[28:31], a[188:191], a[200:203], v[28:31]\n\t"
    "s_waitcnt vmcnt(27)\n\t"
    "ds_read_b64_tr_b16 a[72:73], v229 offset:0\n\t"
    "v_mfma_f32_16x16x32_bf16 v[32:35], a[128:131], a[204:207], v[32:35]\n\t"
    "v_mfma_f32_16x16x32_bf16 v[32:35], a[160:163], a[208:211], v[32:35]\n\t"
    "ds_read_b64_tr_b16 a[74:75], v229 offset:1024\n\t"
    "v_mfma_f32_16x16x32_bf16 v[36:39], a[132:135], a[204:207], v[36:39]\n\t"
    "ds_read_b64_tr_b16 a[104:105], v229 offset:8192\n\t"
    "v_mfma_f32_16x16x32_bf16 v[36:39], a[164:167], a[208:211], v[36:39]\n\t"
    "buffer_load_dwordx4 v[136:139], v254, s[48:51], s75 offen offset:2048 sc1\n\t"
    "v_mfma_f32_16x16x32_bf16 v[40:43], a[136:139], a[204:207], v[40:43]\n\t"
    "ds_read_b64_tr_b16 a[106:107], v229 offset:9216\n\t"
    "v_mfma_f32_16x16x32_bf16 v[40:43], a[168:171], a[208:211], v[40:43]\n\t"
    "v_mfma_f32_16x16x32_bf16 v[44:47], a[140:143], a[204:207], v[44:47]\n\t"
    "buffer_load_dwordx4 v233, s[44:47], s36 offen offset:1024  lds\n\t"
    "v_mfma_f32_16x16x32_bf16 v[44:47], a[172:175], a[208:211], v[44:47]\n\t"
    "ds_read_b64_tr_b16 a[76:77], v229 offset:8\n\t"
    "v_mfma_f32_16x16x32_bf16 v[48:51], a[144:147], a[204:207], v[48:51]\n\t"
    "ds_read_b64_tr_b16 a[78:79], v229 offset:1032\n\t"
    "v_mfma_f32_16x16x32_bf16 v[48:51], a[176:179], a[208:211], v[48:51]\n\t"
    "buffer_load_dwordx4 v[140:143], v254, s[48:51], s75 offen offset:3072 sc1\n\t"
    "v_mfma_f32_16x16x32_bf16 v[52:55], a[148:151], a[204:207], v[52:55]\n\t"
    "ds_read_b64_tr_b16 a[108:109], v229 offset:8200\n\t"
    "v_mfma_f32_16x16x32_bf16 v[52:55], a[180:183], a[208:211], v[52:55]\n\t"
    "v_mfma_f32_16x16x32_bf16 v[56:59], a[152:155], a[204:207], v[56:59]\n\t"
    "ds_read_b64_tr_b16 a[110:111], v229 offset:9224\n\t"
    "v_mfma_f32_16x16x32_bf16 v[56:59], a[184:187], a[208:211], v[56:59]\n\t"
    "ds_read_b64_tr_b16 a[80:81], v230 offset:0\n\t"
    "v_mfma_f32_16x16x32_bf16 v[60:63], a[156:159], a[204:207], v[60:63]\n\t"
    "ds_read_b64_tr_b16 a[82:83], v230 offset:1024\n\t"
    "v_mfma_f32_16x16x32_bf16 v[60:63], a[188:191], a[208:211], v[60:63]\n\t"
    "s_waitcnt vmcnt(28)\n\t"
    "s_mov_b32 s75, 53248\n\t"
    "buffer_load_dwordx4 v[144:147], v254, s[48:51], s75 offen offset:0 sc1\n\t"
    "v_mfma_f32_16x16x32_bf16 v[64:67], a[128:131], a[212:215], v[64:67]\n\t"
    "v_mfma_f32_16x16x32_bf16 v[64:67], a[160:163], a[216:219], v[64:67]\n\t"
    "ds_read_b64_tr_b16 a[112:113], v230 offset:8192\n\t"
    "v_mfma_f32_16x16x32_bf16 v[68:71], a[132:135], a[212:215], v[68:71]\n\t"
    "ds_read_b64_tr_b16 a[114:115], v230 offset:9216\n\t"
    "v_mfma_f32_16x16x32_bf16 v[68:71], a[164:167], a[216:219], v[68:71]\n\t"
    "ds_read_b64_tr_b16 a[84:85], v230 offset:8\n\t"
    "v_mfma_f32_16x16x32_bf16 v[72:75], a[136:139], a[212:215], v[72:75]\n\t"
    "v_mfma_f32_16x16x32_bf16 v[72:75], a[168:171], a[216:219], v[72:75]\n\t"
    "buffer_load_dwordx4 v[148:151], v254, s[48:51], s75 offen offset:1024 sc1\n\t"
    "v_mfma_f32_16x16x32_bf16 v[76:79], a[140:143], a[212:215], v[76:79]\n\t"
    "buffer_load_dwordx4 v234, s[44:47], s36 offen offset:2048  lds\n\t"
    "v_mfma_f32_16x16x32_bf16 v[76:79], a[172:175], a[216:219], v[76:79]\n\t"
    "ds_read_b64_tr_b16 a[86:87], v230 offset:1032\n\t"
    "v_mfma_f32_16x16x32_bf16 v[80:83], a[144:147], a[212:215], v[80:83]\n\t"
    "ds_read_b64_tr_b16 a[116:117], v230 offset:8200\n\t"
    "v_mfma_f32_16x16x32_bf16 v[80:83], a[176:179], a[216:219], v[80:83]\n\t"
    "ds_read_b64_tr_b16 a[118:119], v230 offset:9224\n\t"
    "v_mfma_f32_16x16x32_bf16 v[84:87], a[148:151], a[212:215], v[84:87]\n\t"
    "v_mfma_f32_16x16x32_bf16 v[84:87], a[180:183], a[216:219], v[84:87]\n\t"
    "buffer_load_dwordx4 v[152:155], v254, s[48:51], s75 offen offset:2048 sc1\n\t"
    "v_mfma_f32_16x16x32_bf16 v[88:91], a[152:155], a[212:215], v[88:91]\n\t"
    "ds_read_b64_tr_b16 a[88:89], v231 offset:0\n\t"
    "v_mfma_f32_16x16x32_bf16 v[88:91], a[184:187], a[216:219], v[88:91]\n\t"
    "ds_read_b64_tr_b16 a[90:91], v231 offset:1024\n\t"
    "v_mfma_f32_16x16x32_bf16 v[92:95], a[156:159], a[212:215], v[92:95]\n\t"
    "ds_read_b64_tr_b16 a[120:121], v231 offset:8192\n\t"
    "v_mfma_f32_16x16x32_bf16 v[92:95], a[188:191], a[216:219], v[92:95]\n\t"
    "s_waitcnt vmcnt(30)\n\t"
    "v_mfma_f32_16x16x32_bf16 v[96:99], a[128:131], a[220:223], v[96:99]\n\t"
    "ds_read_b64_tr_b16 a[122:123], v231 offset:9216\n\t"
    "v_mfma_f32_16x16x32_bf16 v[96:99], a[160:163], a[224:227], v[96:99]\n\t"
    "buffer_load_dwordx4 v[156:159], v254, s[48:51], s75 offen offset:3072 sc1\n\t"
    "v_mfma_f32_16x16x32_bf16 v[100:103], a[132:135], a[220:223], v[100:103]\n\t"
    "ds_read_b64_tr_b16 a[92:93], v231 offset:8\n\t"
    "v_mfma_f32_16x16x32_bf16 v[100:103], a[164:167], a[224:227], v[100:103]\n\t"
    "ds_read_b64_tr_b16 a[94:95], v231 offset:1032\n\t"
    "v_mfma_f32_16x16x32_bf16 v[104:107], a[136:139], a[220:223], v[104:107]\n\t"
    "v_mfma_f32_16x16x32_bf16 v[104:107], a[168:171], a[224:227], v[104:107]\n\t"
    "ds_read_b64_tr_b16 a[124:125], v231 offset:8200\n\t"
    "v_mfma_f32_16x16x32_bf16 v[108:111], a[140:143], a[220:223], v[108:111]\n\t"
    "buffer_load_dwordx4 v235, s[44:47], s36 offen offset:3072  lds\n\t"
    "v_mfma_f32_16x16x32_bf16 v[108:111], a[172:175], a[224:227], v[108:111]\n\t"
    "s_mov_b32 s75, 57344\n\t"
    "buffer_load_dwordx4 v[160:163], v254, s[48:51], s75 offen offset:0 sc1\n\t"
    "v_mfma_f32_16x16x32_bf16 v[112:115], a[144:147], a[220:223], v[112:115]\n\t"
    "ds_read_b64_tr_b16 a[126:127], v231 offset:9224\n\t"
    "v_mfma_f32_16x16x32_bf16 v[112:115], a[176:179], a[224:227], v[112:115]\n\t"
    "v_mfma_f32_16x16x32_bf16 v[116:119], a[148:151], a[220:223], v[116:119]\n\t"
    "v_mfma_f32_16x16x32_bf16 v[116:119], a[180:183], a[224:227], v[116:119]\n\t"
    "v_mfma_f32_16x16x32_bf16 v[120:123], a[152:155], a[220:223], v[120:123]\n\t"
    "v_mfma_f32_16x16x32_bf16 v[120:123], a[184:187], a[224:227], v[120:123]\n\t"
    "buffer_load_dwordx4 v[164:167], v254, s[48:51], s75 offen offset:1024 sc1\n\t"
    "v_mfma_f32_16x16x32_bf16 v[124:127], a[156:159], a[220:223], v[124:127]\n\t"
    "v_mfma_f32_16x16x32_bf16 v[124:127], a[188:191], a[224:227], v[124:127]\n\t"
    "s_waitcnt vmcnt(32) lgkmcnt(15)\n\t"
    "v_mfma_f32_16x16x32_bf16 v[0:3], a[64:67], a[0:3], v[0:3]\n\t"
    "v_mfma_f32_16x16x32_bf16 v[0:3], a[96:99], a[4:7], v[0:3]\n\t"
    "v_mfma_f32_16x16x32_bf16 v[4:7], a[68:71], a[0:3], v[4:7]\n\t"
    "ds_read_b64_tr_b16 a[128:129], v228 offset:16384\n\t"
    "v_mfma_f32_16x16x32_bf16 v[4:7], a[100:103], a[4:7], v[4:7]\n\t"
    "buffer_load_dwordx4 v[168:171], v254, s[48:51], s75 offen offset:2048 sc1\n\t"
    "v_mfma_f32_16x16x32_bf16 v[8:11], a[72:75], a[0:3], v[8:11]\n\t"
    "ds_read_b64_tr_b16 a[130:131], v228 offset:17408\n\t"
    "v_mfma_f32_16x16x32_bf16 v[8:11], a[104:107], a[4:7], v[8:11]\n\t"
    "ds_read_b64_tr_b16 a[160:161], v228 offset:24576\n\t"
    "v_mfma_f32_16x16x32_bf16 v[12:15], a[76:79], a[0:3], v[12:15]\n\t"
    "v_mfma_f32_16x16x32_bf16 v[12:15], a[108:111], a[4:7], v[12:15]\n\t"
    "s_waitcnt lgkmcnt(11)\n\t"
    "ds_read_b64_tr_b16 a[162:163], v228 offset:25600\n\t"
    "v_mfma_f32_16x16x32_bf16 v[16:19], a[80:83], a[0:3], v[16:19]\n\t"
    "ds_read_b64_tr_b16 a[132:133], v228 offset:16392\n\t"
    "v_mfma_f32_16x16x32_bf16 v[16:19], a[112:115], a[4:7], v[16:19]\n\t"
    "buffer_load_dwordx4 v[172:175], v254, s[48:51], s75 offen offset:3072 sc1\n\t"
    "v_mfma_f32_16x16x32_bf16 v[20:23], a[84:87], a[0:3], v[20:23]\n\t"
    "v_mfma_f32_16x16x32_bf16 v[20:23], a[116:119], a[4:7], v[20:23]\n\t"
    "s_waitcnt lgkmcnt(5)\n\t"
    "ds_read_b64_tr_b16 a[134:135], v228 offset:17416\n\t"
    "v_mfma_f32_16x16x32_bf16 v[24:27], a[88:91], a[0:3], v[24:27]\n\t"
    "ds_read_b64_tr_b16 a[164:165], v228 offset:24584\n\t"
    "v_mfma_f32_16x16x32_bf16 v[24:27], a[120:123], a[4:7], v[24:27]\n\t"
    "v_mfma_f32_16x16x32_bf16 v[28:31], a[92:95], a[0:3], v[28:31]\n\t"
    "ds_read_b64_tr_b16 a[166:167], v228 offset:25608\n\t"
    "v_mfma_f32_16x16x32_bf16 v[28:31], a[124:127], a[4:7], v[28:31]\n\t"
    "s_waitcnt vmcnt(32)\n\t"
    "s_mov_b32 s75, 61440\n\t"
    "buffer_load_dwordx4 v[176:179], v254, s[48:51], s75 offen offset:0 sc1\n\t"
    "v_mfma_f32_16x16x32_bf16 v[32:35], a[64:67], a[8:11], v[32:35]\n\t"
    "ds_read_b64_tr_b16 a[136:137], v229 offset:16384\n\t"
    "v_mfma_f32_16x16x32_bf16 v[32:35], a[96:99], a[12:15], v[32:35]\n\t"
    "v_mfma_f32_16x16x32_bf16 v[36:39], a[68:71], a[8:11], v[36:39]\n\t"
    "ds_read_b64_tr_b16 a[138:139], v229 offset:17408\n\t"
    "v_mfma_f32_16x16x32_bf16 v[36:39], a[100:103], a[12:15], v[36:39]\n\t"
    "ds_read_b64_tr_b16 a[168:169], v229 offset:24576\n\t"
    "v_mfma_f32_16x16x32_bf16 v[40:43], a[72:75], a[8:11], v[40:43]\n\t"
    "v_mfma_f32_16x16x32_bf16 v[40:43], a[104:107], a[12:15], v[40:43]\n\t"
    "buffer_load_dwordx4 v[180:183], v254, s[48:51], s75 offen offset:1024 sc1\n\t"
    "v_mfma_f32_16x16x32_bf16 v[44:47], a[76:79], a[8:11], v[44:47]\n\t"
    "ds_read_b64_tr_b16 a[170:171], v229 offset:25600\n\t"
    "v_mfma_f32_16x16x32_bf16 v[44:47], a[108:111], a[12:15], v[44:47]\n\t"
    "ds_read_b64_tr_b16 a[140:141], v229 offset:16392\n\t"
    "v_mfma_f32_16x16x32_bf16 v[48:51], a[80:83], a[8:11], v[48:51]\n\t"
    "v_mfma_f32_16x16x32_bf16 v[48:51], a[112:115], a[12:15], v[48:51]\n\t"
    "ds_read_b64_tr_b16 a[142:143], v229 offset:17416\n\t"
    "v_mfma_f32_16x16x32_bf16 v[52:55], a[84:87], a[8:11], v[52:55]\n\t"
    "ds_read_b64_tr_b16 a[172:173], v229 offset:24584\n\t"
    "v_mfma_f32_16x16x32_bf16 v[52:55], a[116:119], a[12:15], v[52:55]\n\t"
    "buffer_load_dwordx4 v[184:187], v254, s[48:51], s75 offen offset:2048 sc1\n\t"
    "v_mfma_f32_16x16x32_bf16 v[56:59], a[88:91], a[8:11], v[56:59]\n\t"
    "v_mfma_f32_16x16x32_bf16 v[56:59], a[120:123], a[12:15], v[56:59]\n\t"
    "ds_read_b64_tr_b16 a[174:175], v229 offset:25608\n\t"
    "v_mfma_f32_16x16x32_bf16 v[60:63], a[92:95], a[8:11], v[60:63]\n\t"
    "ds_read_b64_tr_b16 a[144:145], v230 offset:16384\n\t"
    "v_mfma_f32_16x16x32_bf16 v[60:63], a[124:127], a[12:15], v[60:63]\n\t"
    "s_waitcnt vmcnt(33)\n\t"
    "ds_read_b64_tr_b16 a[146:147], v230 offset:17408\n\t"
    "v_mfma_f32_16x16x32_bf16 v[64:67], a[64:67], a[16:19], v[64:67]\n\t"
    "v_mfma_f32_16x16x32_bf16 v[64:67], a[96:99], a[20:23], v[64:67]\n\t"
    "buffer_load_dwordx4 v[188:191], v254, s[48:51], s75 offen offset:3072 sc1\n\t"
    "v_mfma_f32_16x16x32_bf16 v[68:71], a[68:71], a[16:19], v[68:71]\n\t"
    "ds_read_b64_tr_b16 a[176:177], v230 offset:24576\n\t"
    "v_mfma_f32_16x16x32_bf16 v[68:71], a[100:103], a[20:23], v[68:71]\n\t"
    "ds_read_b64_tr_b16 a[178:179], v230 offset:25600\n\t"
    "v_mfma_f32_16x16x32_bf16 v[72:75], a[72:75], a[16:19], v[72:75]\n\t"
    "v_mfma_f32_16x16x32_bf16 v[72:75], a[104:107], a[20:23], v[72:75]\n\t"
    "ds_read_b64_tr_b16 a[148:149], v230 offset:16392\n\t"
    "v_mfma_f32_16x16x32_bf16 v[76:79], a[76:79], a[16:19], v[76:79]\n\t"
    "ds_read_b64_tr_b16 a[150:151], v230 offset:17416\n\t"
    "v_mfma_f32_16x16x32_bf16 v[76:79], a[108:111], a[20:23], v[76:79]\n\t"
    "s_mov_b32 s75, 65536\n\t"
    "buffer_load_dwordx4 v[192:195], v254, s[48:51], s75 offen offset:0 sc1\n\t"
    "v_mfma_f32_16x16x32_bf16 v[80:83], a[80:83], a[16:19], v[80:83]\n\t"
    "v_mfma_f32_16x16x32_bf16 v[80:83], a[112:115], a[20:23], v[80:83]\n\t"
    "ds_read_b64_tr_b16 a[180:181], v230 offset:24584\n\t"
    "v_mfma_f32_16x16x32_bf16 v[84:87], a[84:87], a[16:19], v[84:87]\n\t"
    "ds_read_b64_tr_b16 a[182:183], v230 offset:25608\n\t"
    "v_mfma_f32_16x16x32_bf16 v[84:87], a[116:119], a[20:23], v[84:87]\n\t"
    "v_mfma_f32_16x16x32_bf16 v[88:91], a[88:91], a[16:19], v[88:91]\n\t"
    "s_mov_b32 s44, s0\n\t"
    "ds_read_b64_tr_b16 a[152:153], v231 offset:16384\n\t"
    "v_mfma_f32_16x16x32_bf16 v[88:91], a[120:123], a[20:23], v[88:91]\n\t"
    "s_and_b32 s45, s1, 0xffff\n\t"
    "ds_read_b64_tr_b16 a[154:155], v231 offset:17408\n\t"
    "v_mfma_f32_16x16x32_bf16 v[92:95], a[92:95], a[16:19], v[92:95]\n\t"
    "s_cmp_lt_u32 s9, 640\n\t"
    "s_cselect_b32 s46, 39840, 0\n\t"
    "v_mfma_f32_16x16x32_bf16 v[92:95], a[124:127], a[20:23], v[92:95]\n\t"
    "s_waitcnt vmcnt(32)\n\t"
    "s_mov_b32 s42, 0\n\t"
    "ds_read_b64_tr_b16 a[184:185], v231 offset:24576\n\t"
    "v_mfma_f32_16x16x32_bf16 v[96:99], a[64:67], a[24:27], v[96:99]\n\t"
    "s_mov_b32 s11, s6\n\t"
    "ds_read_b64_tr_b16 a[186:187], v231 offset:25600\n\t"
    "v_mfma_f32_16x16x32_bf16 v[96:99], a[96:99], a[28:31], v[96:99]\n\t"
    "s_add_u32 s12, s6, 4096\n\t"
    "v_mfma_f32_16x16x32_bf16 v[100:103], a[68:71], a[24:27], v[100:103]\n\t"
    "buffer_load_dwordx4 v[196:199], v254, s[48:51], s75 offen offset:1024 sc1\n\t"
    "v_mfma_f32_16x16x32_bf16 v[100:103], a[100:103], a[28:31], v[100:103]\n\t"
    "ds_read_b64_tr_b16 a[156:157], v231 offset:16392\n\t"
    "v_mfma_f32_16x16x32_bf16 v[104:107], a[72:75], a[24:27], v[104:107]\n\t"
    "ds_read_b64_tr_b16 a[158:159], v231 offset:17416\n\t"
    "v_mfma_f32_16x16x32_bf16 v[104:107], a[104:107], a[28:31], v[104:107]\n\t"
    "v_mfma_f32_16x16x32_bf16 v[108:111], a[76:79], a[24:27], v[108:111]\n\t"
    "ds_read_b64_tr_b16 a[188:189], v231 offset:24584\n\t"
    "v_mfma_f32_16x16x32_bf16 v[108:111], a[108:111], a[28:31], v[108:111]\n\t"
    "ds_read_b64_tr_b16 a[190:191], v231 offset:25608\n\t"
    "v_mfma_f32_16x16x32_bf16 v[112:115], a[80:83], a[24:27], v[112:115]\n\t"
    "s_mov_b32 s75, 73728\n\t"
    "buffer_load_dwordx4 a[196:199], v254, s[48:51], s75 offen offset:0 sc1\n\t"
    "v_mfma_f32_16x16x32_bf16 v[112:115], a[112:115], a[28:31], v[112:115]\n\t"
    "v_mfma_f32_16x16x32_bf16 v[116:119], a[84:87], a[24:27], v[116:119]\n\t"
    "v_mfma_f32_16x16x32_bf16 v[116:119], a[116:119], a[28:31], v[116:119]\n\t"
    "v_mfma_f32_16x16x32_bf16 v[120:123], a[88:91], a[24:27], v[120:123]\n\t"
    "v_mfma_f32_16x16x32_bf16 v[120:123], a[120:123], a[28:31], v[120:123]\n\t"
    "v_mfma_f32_16x16x32_bf16 v[124:127], a[92:95], a[24:27], v[124:127]\n\t"
    "buffer_load_dwordx4 a[200:203], v254, s[48:51], s75 offen offset:1024 sc1\n\t"
    "v_mfma_f32_16x16x32_bf16 v[124:127], a[124:127], a[28:31], v[124:127]\n\t"
    "s_waitcnt vmcnt(32) lgkmcnt(15)\n\t"
    "v_mfma_f32_16x16x32_bf16 v[0:3], a[128:131], a[32:35], v[0:3]\n\t"
    "v_mfma_f32_16x16x32_bf16 v[0:3], a[160:163], a[36:39], v[0:3]\n\t"
    "v_mfma_f32_16x16x32_bf16 v[4:7], a[132:135], a[32:35], v[4:7]\n\t"
    "ds_read_b64_tr_b16 a[64:65], v228 offset:32768\n\t"
    "v_mfma_f32_16x16x32_bf16 v[4:7], a[164:167], a[36:39], v[4:7]\n\t"
    "ds_read_b64_tr_b16 a[66:67], v228 offset:33792\n\t"
    "v_mfma_f32_16x16x32_bf16 v[8:11], a[136:139], a[32:35], v[8:11]\n\t"
    "s_mov_b32 s75, 65536\n\t"
    "buffer_load_dwordx4 v[200:203], v254, s[48:51], s75 offen offset:2048 sc1\n\t"
    "v_mfma_f32_16x16x32_bf16 v[8:11], a[168:171], a[36:39], v[8:11]\n\t"
    "ds_read_b64_tr_b16 a[96:97], v228 offset:40960\n\t"
    "v_mfma_f32_16x16x32_bf16 v[12:15], a[140:143], a[32:35], v[12:15]\n\t"
    "v_mfma_f32_16x16x32_bf16 v[12:15], a[172:175], a[36:39], v[12:15]\n\t"
    "s_waitcnt lgkmcnt(11)\n\t"
    "ds_read_b64_tr_b16 a[98:99], v228 offset:41984\n\t"
    "v_mfma_f32_16x16x32_bf16 v[16:19], a[144:147], a[32:35], v[16:19]\n\t"
    "ds_read_b64_tr_b16 a[68:69], v228 offset:32776\n\t"
    "v_mfma_f32_16x16x32_bf16 v[16:19], a[176:179], a[36:39], v[16:19]\n\t"
    "v_mfma_f32_16x16x32_bf16 v[20:23], a[148:151], a[32:35], v[20:23]\n\t"
    "buffer_load_dwordx4 v[204:207], v254, s[48:51], s75 offen offset:3072 sc1\n\t"
    "v_mfma_f32_16x16x32_bf16 v[20:23], a[180:183], a[36:39], v[20:23]\n\t"
    "s_waitcnt lgkmcnt(5)\n\t"
    "ds_read_b64_tr_b16 a[70:71], v228 offset:33800\n\t"
    "v_mfma_f32_16x16x32_bf16 v[24:27], a[152:155], a[32:35], v[24:27]\n\t"
    "ds_read_b64_tr_b16 a[100:101], v228 offset:40968\n\t"
    "v_mfma_f32_16x16x32_bf16 v[24:27], a[184:187], a[36:39], v[24:27]\n\t"
    "v_mfma_f32_16x16x32_bf16 v[28:31], a[156:159], a[32:35], v[28:31]\n\t"
    "ds_read_b64_tr_b16 a[102:103], v228 offset:41992\n\t"
    "v_mfma_f32_16x16x32_bf16 v[28:31], a[188:191], a[36:39], v[28:31]\n\t"
    "s_waitcnt vmcnt(31)\n\t"
    "ds_read_b64_tr_b16 a[72:73], v229 offset:32768\n\t"
    "v_mfma_f32_16x16x32_bf16 v[32:35], a[128:131], a[40:43], v[32:35]\n\t"
    "s_mov_b32 s75, 73728\n\t"
    "buffer_load_dwordx4 a[204:207], v254, s[48:51], s75 offen offset:2048 sc1\n\t"
    "v_mfma_f32_16x16x32_bf16 v[32:35], a[160:163], a[44:47], v[32:35]\n\t"
    "v_mfma_f32_16x16x32_bf16 v[36:39], a[132:135], a[40:43], v[36:39]\n\t"
    "ds_read_b64_tr_b16 a[74:75], v229 offset:33792\n\t"
    "v_mfma_f32_16x16x32_bf16 v[36:39], a[164:167], a[44:47], v[36:39]\n\t"
    "ds_read_b64_tr_b16 a[104:105], v229 offset:40960\n\t"
    "v_mfma_f32_16x16x32_bf16 v[40:43], a[136:139], a[40:43], v[40:43]\n\t"
    "v_mfma_f32_16x16x32_bf16 v[40:43], a[168:171], a[44:47], v[40:43]\n\t"
    "ds_read_b64_tr_b16 a[106:107], v229 offset:41984\n\t"
    "v_mfma_f32_16x16x32_bf16 v[44:47], a[140:143], a[40:43], v[44:47]\n\t"
    "buffer_load_dwordx4 a[208:211], v254, s[48:51], s75 offen offset:3072 sc1\n\t"
    "v_mfma_f32_16x16x32_bf16 v[44:47], a[172:175], a[44:47], v[44:47]\n\t"
    "ds_read_b64_tr_b16 a[76:77], v229 offset:32776\n\t"
    "v_mfma_f32_16x16x32_bf16 v[48:51], a[144:147], a[40:43], v[48:51]\n\t"
    "v_mfma_f32_16x16x32_bf16 v[48:51], a[176:179], a[44:47], v[48:51]\n\t"
    "ds_read_b64_tr_b16 a[78:79], v229 offset:33800\n\t"
    "v_mfma_f32_16x16x32_bf16 v[52:55], a[148:151], a[40:43], v[52:55]\n\t"
    "ds_read_b64_tr_b16 a[108:109], v229 offset:40968\n\t"
    "v_mfma_f32_16x16x32_bf16 v[52:55], a[180:183], a[44:47], v[52:55]\n\t"
    "v_mfma_f32_16x16x32_bf16 v[56:59], a[152:155], a[40:43], v[56:59]\n\t"
    "s_mov_b32 s75, 69632\n\t"
    "buffer_load_dwordx4 v[208:211], v254, s[48:51], s75 offen offset:0 sc1\n\t"
    "v_mfma_f32_16x16x32_bf16 v[56:59], a[184:187], a[44:47], v[56:59]\n\t"
    "ds_read_b64_tr_b16 a[110:111], v229 offset:41992\n\t"
    "v_mfma_f32_16x16x32_bf16 v[60:63], a[156:159], a[40:43], v[60:63]\n\t"
    "ds_read_b64_tr_b16 a[80:81], v230 offset:32768\n\t"
    "v_mfma_f32_16x16x32_bf16 v[60:63], a[188:191], a[44:47], v[60:63]\n\t"
    "s_waitcnt vmcnt(32)\n\t"
    "ds_read_b64_tr_b16 a[82:83], v230 offset:33792\n\t"
    "v_mfma_f32_16x16x32_bf16 v[64:67], a[128:131], a[48:51], v[64:67]\n\t"
    "v_mfma_f32_16x16x32_bf16 v[64:67], a[160:163], a[52:55], v[64:67]\n\t"
    "ds_read_b64_tr_b16 a[112:113], v230 offset:40960\n\t"
    "v_mfma_f32_16x16x32_bf16 v[68:71], a[132:135], a[48:51], v[68:71]\n\t"
    "buffer_load_dwordx4 v[212:215], v254, s[48:51], s75 offen offset:1024 sc1\n\t"
    "v_mfma_f32_16x16x32_bf16 v[68:71], a[164:167], a[52:55], v[68:71]\n\t"
    "ds_read_b64_tr_b16 a[114:115], v230 offset:41984\n\t"
    "v_mfma_f32_16x16x32_bf16 v[72:75], a[136:139], a[48:51], v[72:75]\n\t"
    "v_mfma_f32_16x16x32_bf16 v[72:75], a[168:171], a[52:55], v[72:75]\n\t"
    "ds_read_b64_tr_b16 a[84:85], v230 offset:32776\n\t"
    "v_mfma_f32_16x16x32_bf16 v[76:79], a[140:143], a[48:51], v[76:79]\n\t"
    "ds_read_b64_tr_b16 a[86:87], v230 offset:33800\n\t"
    "v_mfma_f32_16x16x32_bf16 v[76:79], a[172:175], a[52:55], v[76:79]\n\t"
    "v_mfma_f32_16x16x32_bf16 v[80:83], a[144:147], a[48:51], v[80:83]\n\t"
    "s_mov_b32 s75, 77824\n\t"
    "buffer_load_dwordx4 a[212:215], v254, s[48:51], s75 offen offset:0 sc1\n\t"
    "v_mfma_f32_16x16x32_bf16 v[80:83], a[176:179], a[52:55], v[80:83]\n\t"
    "ds_read_b64_tr_b16 a[116:117], v230 offset:40968\n\t"
    "v_mfma_f32_16x16x32_bf16 v[84:87], a[148:151], a[48:51], v[84:87]\n\t"
    "ds_read_b64_tr_b16 a[118:119], v230 offset:41992\n\t"
    "v_mfma_f32_16x16x32_bf16 v[84:87], a[180:183], a[52:55], v[84:87]\n\t"
    "v_mfma_f32_16x16x32_bf16 v[88:91], a[152:155], a[48:51], v[88:91]\n\t"
    "ds_read_b64_tr_b16 a[88:89], v231 offset:32768\n\t"
    "v_mfma_f32_16x16x32_bf16 v[88:91], a[184:187], a[52:55], v[88:91]\n\t"
    "ds_read_b64_tr_b16 a[90:91], v231 offset:33792\n\t"
    "v_mfma_f32_16x16x32_bf16 v[92:95], a[156:159], a[48:51], v[92:95]\n\t"
    "buffer_load_dwordx4 a[216:219], v254, s[48:51], s75 offen offset:1024 sc1\n\t"
    "v_mfma_f32_16x16x32_bf16 v[92:95], a[188:191], a[52:55], v[92:95]\n\t"
    "s_waitcnt vmcnt(32)\n\t"
    "v_mfma_f32_16x16x32_bf16 v[96:99], a[128:131], a[56:59], v[96:99]\n\t"
    "ds_read_b64_tr_b16 a[120:121], v231 offset:40960\n\t"
    "v_mfma_f32_16x16x32_bf16 v[96:99], a[160:163], a[60:63], v[96:99]\n\t"
    "ds_read_b64_tr_b16 a[122:123], v231 offset:41984\n\t"
    "v_mfma_f32_16x16x32_bf16 v[100:103], a[132:135], a[56:59], v[100:103]\n\t"
    "v_mfma_f32_16x16x32_bf16 v[100:103], a[164:167], a[60:63], v[100:103]\n\t"
    "ds_read_b64_tr_b16 a[92:93], v231 offset:32776\n\t"
    "v_mfma_f32_16x16x32_bf16 v[104:107], a[136:139], a[56:59], v[104:107]\n\t"
    "s_mov_b32 s75, 69632\n\t"
    "buffer_load_dwordx4 v[216:219], v254, s[48:51], s75 offen offset:2048 sc1\n\t"
    "v_mfma_f32_16x16x32_bf16 v[104:107], a[168:171], a[60:63], v[104:107]\n\t"
    "ds_read_b64_tr_b16 a[94:95], v231 offset:33800\n\t"
    "v_mfma_f32_16x16x32_bf16 v[108:111], a[140:143], a[56:59], v[108:111]\n\t"
    "v_mfma_f32_16x16x32_bf16 v[108:111], a[172:175], a[60:63], v[108:111]\n\t"
    "ds_read_b64_tr_b16 a[124:125], v231 offset:40968\n\t"
    "v_mfma_f32_16x16x32_bf16 v[112:115], a[144:147], a[56:59], v[112:115]\n\t"
    "ds_read_b64_tr_b16 a[126:127], v231 offset:41992\n\t"
    "v_mfma_f32_16x16x32_bf16 v[112:115], a[176:179], a[60:63], v[112:115]\n\t"
    "v_mfma_f32_16x16x32_bf16 v[116:119], a[148:151], a[56:59], v[116:119]\n\t"
    "buffer_load_dwordx4 v[220:223], v254, s[48:51], s75 offen offset:3072 sc1\n\t"
    "v_mfma_f32_16x16x32_bf16 v[116:119], a[180:183], a[60:63], v[116:119]\n\t"
    "v_mfma_f32_16x16x32_bf16 v[120:123], a[152:155], a[56:59], v[120:123]\n\t"
    "v_mfma_f32_16x16x32_bf16 v[120:123], a[184:187], a[60:63], v[120:123]\n\t"
    "v_mfma_f32_16x16x32_bf16 v[124:127], a[156:159], a[56:59], v[124:127]\n\t"
    "v_mfma_f32_16x16x32_bf16 v[124:127], a[188:191], a[60:63], v[124:127]\n\t"
    "s_waitcnt vmcnt(32) lgkmcnt(15)\n\t"
    "v_mfma_f32_16x16x32_bf16 v[0:3], a[64:67], v[128:131], v[0:3]\n\t"
    "s_waitcnt vmcnt(22) lgkmcnt(0)\n\t"
    "v_mfma_f32_16x16x32_bf16 v[0:3], a[96:99], v[132:135], v[0:3]\n\t"
    "s_barrier\n\t"
    "v_mfma_f32_16x16x32_bf16 v[4:7], a[68:71], v[128:131], v[4:7]\n\t"
    "s_mov_b32 s75, 77824\n\t"
    "buffer_load_dwordx4 a[220:223], v254, s[48:51], s75 offen offset:2048 sc1\n\t"
    "v_mfma_f32_16x16x32_bf16 v[4:7], a[100:103], v[132:135], v[4:7]\n\t"
    "s_add_u32 m0, s37, 32768\n\t"
    "ds_read_b64_tr_b16 a[128:129], v228 offset:49152\n\t"
    "v_mfma_f32_16x16x32_bf16 v[8:11], a[72:75], v[128:131], v[8:11]\n\t"
    "s_add_u32 s36, s37, 0\n\t"
    "ds_read_b64_tr_b16 a[130:131], v228 offset:50176\n\t"
    "v_mfma_f32_16x16x32_bf16 v[8:11], a[104:107], v[132:135], v[8:11]\n\t"
    "ds_read_b64_tr_b16 a[160:161], v228 offset:57344\n\t"
    "v_mfma_f32_16x16x32_bf16 v[12:15], a[76:79], v[128:131], v[12:15]\n\t"
    "buffer_load_dwordx4 v236, s[32:35], s36 offen offset:0  lds\n\t"
    "v_mfma_f32_16x16x32_bf16 v[12:15], a[108:111], v[132:135], v[12:15]\n\t"
    "v_mfma_f32_16x16x32_bf16 v[16:19], a[80:83], v[128:131], v[16:19]\n\t"
    "buffer_load_dwordx4 a[224:227], v254, s[48:51], s75 offen offset:3072 sc1\n\t"
    "v_mfma_f32_16x16x32_bf16 v[16:19], a[112:115], v[132:135], v[16:19]\n\t"
    "ds_read_b64_tr_b16 a[162:163], v228 offset:58368\n\t"
    "v_mfma_f32_16x16x32_bf16 v[20:23], a[84:87], v[128:131], v[20:23]\n\t"
    "ds_read_b64_tr_b16 a[132:133], v228 offset:49160\n\t"
    "v_mfma_f32_16x16x32_bf16 v[20:23], a[116:119], v[132:135], v[20:23]\n\t"
    "ds_read_b64_tr_b16 a[134:135], v228 offset:50184\n\t"
    "v_mfma_f32_16x16x32_bf16 v[24:27], a[88:91], v[128:131], v[24:27]\n\t"
    "buffer_load_dwordx4 v237, s[32:35], s36 offen offset:1024  lds\n\t"
    "v_mfma_f32_16x16x32_bf16 v[24:27], a[120:123], v[132:135], v[24:27]\n\t"
    "v_mfma_f32_16x16x32_bf16 v[28:31], a[92:95], v[128:131], v[28:31]\n\t"
    "ds_read_b64_tr_b16 a[164:165], v228 offset:57352\n\t"
    "v_mfma_f32_16x16x32_bf16 v[28:31], a[124:127], v[132:135], v[28:31]\n\t"
    "ds_read_b64_tr_b16 a[166:167], v228 offset:58376\n\t"
    "v_mfma_f32_16x16x32_bf16 v[32:35], a[64:67], v[136:139], v[32:35]\n\t"
    "v_mfma_f32_16x16x32_bf16 v[32:35], a[96:99], v[140:143], v[32:35]\n\t"
    "ds_read_b64_tr_b16 a[136:137], v229 offset:49152\n\t"
    "v_mfma_f32_16x16x32_bf16 v[36:39], a[68:71], v[136:139], v[36:39]\n\t"
    "buffer_load_dwordx4 v238, s[32:35], s36 offen offset:2048  lds\n\t"
    "v_mfma_f32_16x16x32_bf16 v[36:39], a[100:103], v[140:143], v[36:39]\n\t"
    "ds_read_b64_tr_b16 a[138:139], v229 offset:50176\n\t"
    "v_mfma_f32_16x16x32_bf16 v[40:43], a[72:75], v[136:139], v[40:43]\n\t"
    "ds_read_b64_tr_b16 a[168:169], v229 offset:57344\n\t"
    "v_mfma_f32_16x16x32_bf16 v[40:43], a[104:107], v[140:143], v[40:43]\n\t"
    "v_mfma_f32_16x16x32_bf16 v[44:47], a[76:79], v[136:139], v[44:47]\n\t"
    "ds_read_b64_tr_b16 a[170:171], v229 offset:58368\n\t"
    "v_mfma_f32_16x16x32_bf16 v[44:47], a[108:111], v[140:143], v[44:47]\n\t"
    "ds_read_b64_tr_b16 a[140:141], v229 offset:49160\n\t"
    "v_mfma_f32_16x16x32_bf16 v[48:51], a[80:83], v[136:139], v[48:51]\n\t"
    "buffer_load_dwordx4 v239, s[32:35], s36 offen offset:3072  lds\n\t"
    "v_mfma_f32_16x16x32_bf16 v[48:51], a[112:115], v[140:143], v[48:51]\n\t"
    "ds_read_b64_tr_b16 a[142:143], v229 offset:50184\n\t"
    "v_mfma_f32_16x16x32_bf16 v[52:55], a[84:87], v[136:139], v[52:55]\n\t"
    "v_mfma_f32_16x16x32_bf16 v[52:55], a[116:119], v[140:143], v[52:55]\n\t"
    "ds_read_b64_tr_b16 a[172:173], v229 offset:57352\n\t"
    "v_mfma_f32_16x16x32_bf16 v[56:59], a[88:91], v[136:139], v[56:59]\n\t"
    "ds_read_b64_tr_b16 a[174:175], v229 offset:58376\n\t"
    "v_mfma_f32_16x16x32_bf16 v[56:59], a[120:123], v[140:143], v[56:59]\n\t"
    "s_add_u32 m0, s37, 65536\n\t"
    "v_mfma_f32_16x16x32_bf16 v[60:63], a[92:95], v[136:139], v[60:63]\n\t"
    "s_add_u32 s36, s37, 0\n\t"
    "ds_read_b64_tr_b16 a[144:145], v230 offset:49152\n\t"
    "v_mfma_f32_16x16x32_bf16 v[60:63], a[124:127], v[140:143], v[60:63]\n\t"
    "ds_read_b64_tr_b16 a[146:147], v230 offset:50176\n\t"
    "v_mfma_f32_16x16x32_bf16 v[64:67], a[64:67], v[144:147], v[64:67]\n\t"
    "buffer_load_dwordx4 v240, s[24:27], s36 offen offset:0  lds\n\t"
    "v_mfma_f32_16x16x32_bf16 v[64:67], a[96:99], v[148:151], v[64:67]\n\t"
    "ds_read_b64_tr_b16 a[176:177], v230 offset:57344\n\t"
    "v_mfma_f32_16x16x32_bf16 v[68:71], a[68:71], v[144:147], v[68:71]\n\t"
    "v_mfma_f32_16x16x32_bf16 v[68:71], a[100:103], v[148:151], v[68:71]\n\t"
    "ds_read_b64_tr_b16 a[178:179], v230 offset:58368\n\t"
    "v_mfma_f32_16x16x32_bf16 v[72:75], a[72:75], v[144:147], v[72:75]\n\t"
    "ds_read_b64_tr_b16 a[148:149], v230 offset:49160\n\t"
    "v_mfma_f32_16x16x32_bf16 v[72:75], a[104:107], v[148:151], v[72:75]\n\t"
    "ds_read_b64_tr_b16 a[150:151], v230 offset:50184\n\t"
    "v_mfma_f32_16x16x32_bf16 v[76:79], a[76:79], v[144:147], v[76:79]\n\t"
    "buffer_load_dwordx4 v241, s[24:27], s36 offen offset:1024  lds\n\t"
    "v_mfma_f32_16x16x32_bf16 v[76:79], a[108:111], v[148:151], v[76:79]\n\t"
    "v_mfma_f32_16x16x32_bf16 v[80:83], a[80:83], v[144:147], v[80:83]\n\t"
    "ds_read_b64_tr_b16 a[180:181], v230 offset:57352\n\t"
    "v_lshrrev_b32 v244, 3, v255\n\t"
    "v_mfma_f32_16x16x32_bf16 v[80:83], a[112:115], v[148:151], v[80:83]\n\t"
    "ds_read_b64_tr_b16 a[182:183], v230 offset:58376\n\t"
    "v_mul_u32_u24 v244, 1280, v244\n\t"
    "v_mfma_f32_16x16x32_bf16 v[84:87], a[84:87], v[144:147], v[84:87]\n\t"
    "v_and_b32 v242, 0x70, v254\n\t"
    "v_add_u32 v244, v244, v242\n\t"
    "v_mfma_f32_16x16x32_bf16 v[84:87], a[116:119], v[148:151], v[84:87]\n\t"
    "ds_read_b64_tr_b16 a[152:153], v231 offset:49152\n\t"
    "v_lshrrev_b32 v245, 1, v255\n\t"
    "v_mfma_f32_16x16x32_bf16 v[88:91], a[88:91], v[144:147], v[88:91]\n\t"
    "buffer_load_dwordx4 v240, s[24:27], s36 offen offset:2048  lds\n\t"
    "s_getpc_b64 s[48:49]\n\t"
    "L_pc4%=:\n\t"
    "s_add_u32 s48, s48, L_c0%=-L_pc4%=\n\t"
    "s_addc_u32 s49, s49, 0\n\t"
    "v_mfma_f32_16x16x32_bf16 v[88:91], a[120:123], v[148:151], v[88:91]\n\t"
    "s_and_b32 s49, s49, 0xffff\n\t"
    "s_mov_b32 s50, L_drain%=-L_c0%=\n\t"
    "v_mfma_f32_16x16x32_bf16 v[92:95], a[92:95], v[144:147], v[92:95]\n\t"
    "buffer_load_dword a246, v249, s[48:51], 0 offen\n\t"
    "v_mfma_f32_16x16x32_bf16 v[92:95], a[124:127], v[148:151], v[92:95]\n\t"
    "ds_read_b64_tr_b16 a[154:155], v231 offset:50176\n\t"
    "v_mul_u32_u24 v245, 1280, v245\n\t"
    "v_mfma_f32_16x16x32_bf16 v[96:99], a[64:67], v[152:155], v[96:99]\n\t"
    "ds_read_b64_tr_b16 a[184:185], v231 offset:57344\n\t"
    "v_and_b32 v242, 16, v254\n\t"
    "v_mfma_f32_16x16x32_bf16 v[96:99], a[96:99], v[156:159], v[96:99]\n\t"
    "v_add_u32 v242, 128, v242\n\t"
    "v_add_u32 v245, v245, v242\n\t"
    "v_mfma_f32_16x16x32_bf16 v[100:103], a[68:71], v[152:155], v[100:103]\n\t"
    "buffer_load_dwordx4 v241, s[24:27], s36 offen offset:3072  lds\n\t"
    "v_mfma_f32_16x16x32_bf16 v[100:103], a[100:103], v[156:159], v[100:103]\n\t"
    "ds_read_b64_tr_b16 a[186:187], v231 offset:58368\n\t"
    "s_mov_b32 s75, 9216\n\t"
    "v_mfma_f32_16x16x32_bf16 v[104:107], a[72:75], v[152:155], v[104:107]\n\t"
    "ds_read_b64_tr_b16 a[156:157], v231 offset:49160\n\t"
    "v_mfma_f32_16x16x32_bf16 v[104:107], a[104:107], v[156:159], v[104:107]\n\t"
    "ds_read_b64_tr_b16 a[158:159], v231 offset:50184\n\t"
    "v_mfma_f32_16x16x32_bf16 v[108:111], a[76:79], v[152:155], v[108:111]\n\t"
    "v_mfma_f32_16x16x32_bf16 v[108:111], a[108:111], v[156:159], v[108:111]\n\t"
    "s_add_u32 m0, s37, 49152\n\t"
    "ds_read_b64_tr_b16 a[188:189], v231 offset:57352\n\t"
    "v_mfma_f32_16x16x32_bf16 v[112:115], a[80:83], v[152:155], v[112:115]\n\t"
    "s_add_u32 s36, s37, 16384\n\t"
    "ds_read_b64_tr_b16 a[190:191], v231 offset:58376\n\t"
    "v_mfma_f32_16x16x32_bf16 v[112:115], a[112:115], v[156:159], v[112:115]\n\t"
    "v_mfma_f32_16x16x32_bf16 v[116:119], a[84:87], v[152:155], v[116:119]\n\t"
    "buffer_load_dwordx4 v236, s[32:35], s36 offen offset:0  lds\n\t"
    "v_mfma_f32_16x16x32_bf16 v[116:119], a[116:119], v[156:159], v[116:119]\n\t"
    "v_mfma_f32_16x16x32_bf16 v[120:123], a[88:91], v[152:155], v[120:123]\n\t"
    "v_mfma_f32_16x16x32_bf16 v[120:123], a[120:123], v[156:159], v[120:123]\n\t"
    "v_mfma_f32_16x16x32_bf16 v[124:127], a[92:95], v[152:155], v[124:127]\n\t"
    "v_mfma_f32_16x16x32_bf16 v[124:127], a[124:127], v[156:159], v[124:127]\n\t"
    "s_waitcnt vmcnt(32) lgkmcnt(15)\n\t"
    "v_mfma_f32_16x16x32_bf16 v[0:3], a[128:131], v[160:163], v[0:3]\n\t"
    "buffer_load_dwordx4 v237, s[32:35], s36 offen offset:1024  lds\n\t"
    "v_mfma_f32_16x16x32_bf16 v[0:3], a[160:163], v[164:167], v[0:3]\n\t"
    "v_mfma_f32_16x16x32_bf16 v[4:7], a[132:135], v[160:163], v[4:7]\n\t"
    "ds_read_b64_tr_b16 a[64:65], v224 offset:0\n\t"
    "v_xor_b32 v228, 0, v250\n\t"
    "v_mfma_f32_16x16x32_bf16 v[4:7], a[164:167], v[164:167], v[4:7]\n\t"
    "ds_read_b64_tr_b16 a[66:67], v224 offset:1024\n\t"
    "v_xor_b32 v229, 64, v250\n\t"
    "v_mfma_f32_16x16x32_bf16 v[8:11], a[136:139], v[160:163], v[8:11]\n\t"
    "ds_read_b64_tr_b16 a[96:97], v224 offset:8192\n\t"
    "v_xor_b32 v230, 128, v250\n\t"
    "v_mfma_f32_16x16x32_bf16 v[8:11], a[168:171], v[164:167], v[8:11]\n\t"
    "ds_read_b64_tr_b16 a[98:99], v224 offset:9216\n\t"
    "v_xor_b32 v231, 192, v250\n\t"
    "v_mfma_f32_16x16x32_bf16 v[12:15], a[140:143], v[160:163], v[12:15]\n\t"
    "buffer_load_dwordx4 v238, s[32:35], s36 offen offset:2048  lds\n\t"
    "v_mfma_f32_16x16x32_bf16 v[12:15], a[172:175], v[164:167], v[12:15]\n\t"
    "s_waitcnt lgkmcnt(12)\n\t"
    "v_xor_b32 v232, 128, v251\n\t"
    "v_xor_b32 v233, 160, v251\n\t"
    "v_mfma_f32_16x16x32_bf16 v[16:19], a[144:147], v[160:163], v[16:19]\n\t"
    "ds_read_b64_tr_b16 a[68:69], v224 offset:8\n\t"
    "v_xor_b32 v234, 192, v251\n\t"
    "v_mfma_f32_16x16x32_bf16 v[16:19], a[176:179], v[164:167], v[16:19]\n\t"
    "ds_read_b64_tr_b16 a[70:71], v224 offset:1032\n\t"
    "v_xor_b32 v235, 224, v251\n\t"
    "v_mfma_f32_16x16x32_bf16 v[20:23], a[148:151], v[160:163], v[20:23]\n\t"
    "ds_read_b64_tr_b16 a[100:101], v224 offset:8200\n\t"
    "v_lshrrev_b32 v243, 2, v255\n\t"
    "v_mfma_f32_16x16x32_bf16 v[20:23], a[180:183], v[164:167], v[20:23]\n\t"
    "s_waitcnt lgkmcnt(7)\n\t"
    "ds_read_b64_tr_b16 a[102:103], v224 offset:9224\n\t"
    "v_and_b32 v242, 3, v255\n\t"
    "v_mfma_f32_16x16x32_bf16 v[24:27], a[152:155], v[160:163], v[24:27]\n\t"
    "buffer_load_dwordx4 v239, s[32:35], s36 offen offset:3072  lds\n\t"
    "v_mfma_f32_16x16x32_bf16 v[24:27], a[184:187], v[164:167], v[24:27]\n\t"
    "v_lshlrev_b32 v242, 3, v242\n\t"
    "s_lshl1_add_u32 s43, s37, 131072\n\t"
    "v_mfma_f32_16x16x32_bf16 v[28:31], a[156:159], v[160:163], v[28:31]\n\t"
    "ds_read_b64_tr_b16 a[72:73], v225 offset:0\n\t"
    "v_add_u32 v242, s43, v242\n\t"
    "v_mfma_f32_16x16x32_bf16 v[28:31], a[188:191], v[164:167], v[28:31]\n\t"
    "s_waitcnt vmcnt(33)\n\t"
    "ds_read_b64_tr_b16 a[74:75], v225 offset:1024\n\t"
    "v_lshl_add_u32 v252, v243, 7, v242\n\t"
    "v_mfma_f32_16x16x32_bf16 v[32:35], a[128:131], v[168:171], v[32:35]\n\t"
    "ds_read_b64_tr_b16 a[104:105], v225 offset:8192\n\t"
    "v_lshl_add_u32 v243, v243, 5, v242\n\t"
    "v_mfma_f32_16x16x32_bf16 v[32:35], a[160:163], v[172:175], v[32:35]\n\t"
    "s_add_u32 m0, s37, 81920\n\t"
    "ds_read_b64_tr_b16 a[106:107], v225 offset:9216\n\t"
    "v_add_u32 v253, 2048, v252\n\t"
    "v_mfma_f32_16x16x32_bf16 v[36:39], a[132:135], v[168:171], v[36:39]\n\t"
    "s_add_u32 s36, s37, 16384\n\t"
    "v_mfma_f32_16x16x32_bf16 v[36:39], a[164:167], v[172:175], v[36:39]\n\t"
    "ds_read_b64_tr_b16 a[76:77], v225 offset:8\n\t"
    "v_mfma_f32_16x16x32_bf16 v[40:43], a[136:139], v[168:171], v[40:43]\n\t"
    "buffer_load_dwordx4 v240, s[24:27], s36 offen offset:0  lds\n\t"
    "v_mfma_f32_16x16x32_bf16 v[40:43], a[168:171], v[172:175], v[40:43]\n\t"
    "ds_read_b64_tr_b16 a[78:79], v225 offset:1032\n\t"
    "v_mfma_f32_16x16x32_bf16 v[44:47], a[140:143], v[168:171], v[44:47]\n\t"
    "ds_read_b64_tr_b16 a[108:109], v225 offset:8200\n\t"
    "v_mfma_f32_16x16x32_bf16 v[44:47], a[172:175], v[172:175], v[44:47]\n\t"
    "ds_read_b64_tr_b16 a[110:111], v225 offset:9224\n\t"
    "v_mfma_f32_16x16x32_bf16 v[48:51], a[144:147], v[168:171], v[48:51]\n\t"
    "v_mfma_f32_16x16x32_bf16 v[48:51], a[176:179], v[172:175], v[48:51]\n\t"
    "ds_read_b64_tr_b16 a[80:81], v226 offset:0\n\t"
    "v_mfma_f32_16x16x32_bf16 v[52:55], a[148:151], v[168:171], v[52:55]\n\t"
    "buffer_load_dwordx4 v241, s[24:27], s36 offen offset:1024  lds\n\t"
    "v_mfma_f32_16x16x32_bf16 v[52:55], a[180:183], v[172:175], v[52:55]\n\t"
    "ds_read_b64_tr_b16 a[82:83], v226 offset:1024\n\t"
    "v_mfma_f32_16x16x32_bf16 v[56:59], a[152:155], v[168:171], v[56:59]\n\t"
    "ds_read_b64_tr_b16 a[112:113], v226 offset:8192\n\t"
    "v_mfma_f32_16x16x32_bf16 v[56:59], a[184:187], v[172:175], v[56:59]\n\t"
    "ds_read_b64_tr_b16 a[114:115], v226 offset:9216\n\t"
    "v_mfma_f32_16x16x32_bf16 v[60:63], a[156:159], v[168:171], v[60:63]\n\t"
    "v_mfma_f32_16x16x32_bf16 v[60:63], a[188:191], v[172:175], v[60:63]\n\t"
    "s_waitcnt vmcnt(33)\n\t"
    "ds_read_b64_tr_b16 a[84:85], v226 offset:8\n\t"
    "v_mfma_f32_16x16x32_bf16 v[64:67], a[128:131], v[176:179], v[64:67]\n\t"
    "buffer_load_dwordx4 v240, s[24:27], s36 offen offset:2048  lds\n\t"
    "v_mfma_f32_16x16x32_bf16 v[64:67], a[160:163], v[180:183], v[64:67]\n\t"
    "ds_read_b64_tr_b16 a[86:87], v226 offset:1032\n\t"
    "v_mfma_f32_16x16x32_bf16 v[68:71], a[132:135], v[176:179], v[68:71]\n\t"
    "ds_read_b64_tr_b16 a[116:117], v226 offset:8200\n\t"
    "v_mfma_f32_16x16x32_bf16 v[68:71], a[164:167], v[180:183], v[68:71]\n\t"
    "ds_read_b64_tr_b16 a[118:119], v226 offset:9224\n\t"
    "v_mfma_f32_16x16x32_bf16 v[72:75], a[136:139], v[176:179], v[72:75]\n\t"
    "v_mfma_f32_16x16x32_bf16 v[72:75], a[168:171], v[180:183], v[72:75]\n\t"
    "ds_read_b64_tr_b16 a[88:89], v227 offset:0\n\t"
    "v_mfma_f32_16x16x32_bf16 v[76:79], a[140:143], v[176:179], v[76:79]\n\t"
    "buffer_load_dwordx4 v241, s[24:27], s36 offen offset:3072  lds\n\t"
    "v_mfma_f32_16x16x32_bf16 v[76:79], a[172:175], v[180:183], v[76:79]\n\t"
    "ds_read_b64_tr_b16 a[90:91], v227 offset:1024\n\t"
    "v_mfma_f32_16x16x32_bf16 v[80:83], a[144:147], v[176:179], v[80:83]\n\t"
    "ds_read_b64_tr_b16 a[120:121], v227 offset:8192\n\t"
    "v_mfma_f32_16x16x32_bf16 v[80:83], a[176:179], v[180:183], v[80:83]\n\t"
    "ds_read_b64_tr_b16 a[122:123], v227 offset:9216\n\t"
    "v_mfma_f32_16x16x32_bf16 v[84:87], a[148:151], v[176:179], v[84:87]\n\t"
    "v_mfma_f32_16x16x32_bf16 v[84:87], a[180:183], v[180:183], v[84:87]\n\t"
    "ds_read_b64_tr_b16 a[92:93], v227 offset:8\n\t"
    "v_mfma_f32_16x16x32_bf16 v[88:91], a[152:155], v[176:179], v[88:91]\n\t"
    "ds_read_b64_tr_b16 a[94:95], v227 offset:1032\n\t"
    "v_mfma_f32_16x16x32_bf16 v[88:91], a[184:187], v[180:183], v[88:91]\n\t"
    "ds_read_b64_tr_b16 a[124:125], v227 offset:8200\n\t"
    "v_mfma_f32_16x16x32_bf16 v[92:95], a[156:159], v[176:179], v[92:95]\n\t"
    "ds_read_b64_tr_b16 a[126:127], v227 offset:9224\n\t"
    "v_mfma_f32_16x16x32_bf16 v[92:95], a[188:191], v[180:183], v[92:95]\n\t"
    "s_waitcnt vmcnt(33)\n\t"
    "v_mfma_f32_16x16x32_bf16 v[96:99], a[128:131], v[184:187], v[96:99]\n\t"
    "v_mfma_f32_16x16x32_bf16 v[96:99], a[160:163], v[188:191], v[96:99]\n\t"
    "ds_read_b64_tr_b16 a[128:129], v224 offset:16384\n\t"
    "v_mfma_f32_16x16x32_bf16 v[100:103], a[132:135], v[184:187], v[100:103]\n\t"
    "ds_read_b64_tr_b16 a[130:131], v224 offset:17408\n\t"
    "v_mfma_f32_16x16x32_bf16 v[100:103], a[164:167], v[188:191], v[100:103]\n\t"
    "ds_read_b64_tr_b16 a[160:161], v224 offset:24576\n\t"
    "v_mfma_f32_16x16x32_bf16 v[104:107], a[136:139], v[184:187], v[104:107]\n\t"
    "ds_read_b64_tr_b16 a[162:163], v224 offset:25600\n\t"
    "v_mfma_f32_16x16x32_bf16 v[104:107], a[168:171], v[188:191], v[104:107]\n\t"
    "ds_read_b64_tr_b16 a[132:133], v224 offset:16392\n\t"
    "v_mfma_f32_16x16x32_bf16 v[108:111], a[140:143], v[184:187], v[108:111]\n\t"
    "ds_read_b64_tr_b16 a[134:135], v224 offset:17416\n\t"
    "v_mfma_f32_16x16x32_bf16 v[108:111], a[172:175], v[188:191], v[108:111]\n\t"
    "ds_read_b64_tr_b16 a[164:165], v224 offset:24584\n\t"
    "v_mfma_f32_16x16x32_bf16 v[112:115], a[144:147], v[184:187], v[112:115]\n\t"
    "ds_read_b64_tr_b16 a[166:167], v224 offset:25608\n\t"
    "v_mfma_f32_16x16x32_bf16 v[112:115], a[176:179], v[188:191], v[112:115]\n\t"
    "ds_read_b64_tr_b16 a[136:137], v225 offset:16384\n\t"
    "v_mfma_f32_16x16x32_bf16 v[116:119], a[148:151], v[184:187], v[116:119]\n\t"
    "ds_read_b64_tr_b16 a[138:139], v225 offset:17408\n\t"
    "v_mfma_f32_16x16x32_bf16 v[116:119], a[180:183], v[188:191], v[116:119]\n\t"
    "ds_read_b64_tr_b16 a[168:169], v225 offset:24576\n\t"
    "v_mfma_f32_16x16x32_bf16 v[120:123], a[152:155], v[184:187], v[120:123]\n\t"
    "ds_read_b64_tr_b16 a[170:171], v225 offset:25600\n\t"
    "v_mfma_f32_16x16x32_bf16 v[120:123], a[184:187], v[188:191], v[120:123]\n\t"
    "ds_read_b64_tr_b16 a[140:141], v225 offset:16392\n\t"
    "v_mfma_f32_16x16x32_bf16 v[124:127], a[156:159], v[184:187], v[124:127]\n\t"
    "ds_read_b64_tr_b16 a[142:143], v225 offset:17416\n\t"
    "v_mfma_f32_16x16x32_bf16 v[124:127], a[188:191], v[188:191], v[124:127]\n\t"
    "s_waitcnt vmcnt(32) lgkmcnt(15)\n\t"
    "ds_read_b64_tr_b16 a[172:173], v225 offset:24584\n\t"
    "v_mfma_f32_16x16x32_bf16 v[0:3], a[64:67], v[192:195], v[0:3]\n\t"
    "s_waitcnt vmcnt(31) lgkmcnt(0)\n\t"
    "ds_read_b64_tr_b16 a[174:175], v225 offset:25608\n\t"
    "v_mfma_f32_16x16x32_bf16 v[0:3], a[96:99], v[196:199], v[0:3]\n\t"
    "s_barrier\n\t"
    "ds_read_b64_tr_b16 a[144:145], v226 offset:16384\n\t"
    "v_mfma_f32_16x16x32_bf16 v[4:7], a[68:71], v[192:195], v[4:7]\n\t"
    "ds_read_b64_tr_b16 a[146:147], v226 offset:17408\n\t"
    "v_mfma_f32_16x16x32_bf16 v[4:7], a[100:103], v[196:199], v[4:7]\n\t"
    "s_add_u32 m0, s37, 0\n\t"
    "ds_read_b64_tr_b16 a[176:177], v226 offset:24576\n\t"
    "v_mfma_f32_16x16x32_bf16 v[8:11], a[72:75], v[192:195], v[8:11]\n\t"
    "s_add_u32 s36, s37, 32768\n\t"
    "ds_read_b64_tr_b16 a[178:179], v226 offset:25600\n\t"
    "v_mfma_f32_16x16x32_bf16 v[8:11], a[104:107], v[196:199], v[8:11]\n\t"
    "ds_read_b64_tr_b16 a[148:149], v226 offset:16392\n\t"
    "v_mfma_f32_16x16x32_bf16 v[12:15], a[76:79], v[192:195], v[12:15]\n\t"
    "buffer_load_dwordx4 v236, s[32:35], s36 offen offset:0  lds\n\t"
    "v_mfma_f32_16x16x32_bf16 v[12:15], a[108:111], v[196:199], v[12:15]\n\t"
    "ds_read_b64_tr_b16 a[150:151], v226 offset:17416\n\t"
    "v_mfma_f32_16x16x32_bf16 v[16:19], a[80:83], v[192:195], v[16:19]\n\t"
    "ds_read_b64_tr_b16 a[180:181], v226 offset:24584\n\t"
    "v_mfma_f32_16x16x32_bf16 v[16:19], a[112:115], v[196:199], v[16:19]\n\t"
    "ds_read_b64_tr_b16 a[182:183], v226 offset:25608\n\t"
    "v_mfma_f32_16x16x32_bf16 v[20:23], a[84:87], v[192:195], v[20:23]\n\t"
    "buffer_load_dwordx4 v237, s[32:35], s36 offen offset:1024  lds\n\t"
    "v_mfma_f32_16x16x32_bf16 v[20:23], a[116:119], v[196:199], v[20:23]\n\t"
    "ds_read_b64_tr_b16 a[152:153], v227 offset:16384\n\t"
    "v_mfma_f32_16x16x32_bf16 v[24:27], a[88:91], v[192:195], v[24:27]\n\t"
    "ds_read_b64_tr_b16 a[154:155], v227 offset:17408\n\t"
    "v_mfma_f32_16x16x32_bf16 v[24:27], a[120:123], v[196:199], v[24:27]\n\t"
    "ds_read_b64_tr_b16 a[184:185], v227 offset:24576\n\t"
    "v_mfma_f32_16x16x32_bf16 v[28:31], a[92:95], v[192:195], v[28:31]\n\t"
    "buffer_load_dwordx4 v238, s[32:35], s36 offen offset:2048  lds\n\t"
    "v_mfma_f32_16x16x32_bf16 v[28:31], a[124:127], v[196:199], v[28:31]\n\t"
    "s_waitcnt vmcnt(32)\n\t"
    "ds_read_b64_tr_b16 a[186:187], v227 offset:25600\n\t"
    "v_mfma_f32_16x16x32_bf16 v[0:3], a[128:131], a[196:199], v[0:3]\n\t"
    "ds_read_b64_tr_b16 a[156:157], v227 offset:16392\n\t"
    "v_mfma_f32_16x16x32_bf16 v[0:3], a[160:163], a[200:203], v[0:3]\n\t"
    "ds_read_b64_tr_b16 a[158:159], v227 offset:17416\n\t"
    "v_mfma_f32_16x16x32_bf16 v[4:7], a[132:135], a[196:199], v[4:7]\n\t"
    "buffer_load_dwordx4 v239, s[32:35], s36 offen offset:3072  lds\n\t"
    "v_mfma_f32_16x16x32_bf16 v[4:7], a[164:167], a[200:203], v[4:7]\n\t"
    "ds_read_b64_tr_b16 a[188:189], v227 offset:24584\n\t"
    "v_mfma_f32_16x16x32_bf16 v[8:11], a[136:139], a[196:199], v[8:11]\n\t"
    "ds_read_b64_tr_b16 a[190:191], v227 offset:25608\n\t"
    "v_mfma_f32_16x16x32_bf16 v[8:11], a[168:171], a[200:203], v[8:11]\n\t"
    "s_add_u32 m0, s37, 98304\n\t"
    "v_xor_b32 v224, 0, v250\n\t"
    "v_xor_b32 v225, 64, v250\n\t"
    "v_mfma_f32_16x16x32_bf16 v[12:15], a[140:143], a[196:199], v[12:15]\n\t"
    "s_waitcnt lgkmcnt(10)\n\t"
    "s_add_u32 s36, s37, 32768\n\t"
    "v_xor_b32 v226, 128, v250\n\t"
    "v_xor_b32 v227, 192, v250\n\t"
    "v_mfma_f32_16x16x32_bf16 v[12:15], a[172:175], a[200:203], v[12:15]\n\t"
    "v_mfma_f32_16x16x32_bf16 v[16:19], a[144:147], a[196:199], v[16:19]\n\t"
    "buffer_load_dwordx4 v240, s[24:27], s36 offen offset:0  lds\n\t"
    "v_mfma_f32_16x16x32_bf16 v[16:19], a[176:179], a[200:203], v[16:19]\n\t"
    "v_cvt_pk_bf16_f32 v0, v0, v1\n\t"
    "v_cvt_pk_bf16_f32 v1, v2, v3\n\t"
    "v_mfma_f32_16x16x32_bf16 v[20:23], a[148:151], a[196:199], v[20:23]\n\t"
    "s_waitcnt lgkmcnt(2)\n\t"
    "v_cvt_pk_bf16_f32 v2, v4, v5\n\t"
    "v_cvt_pk_bf16_f32 v3, v6, v7\n\t"
    "v_mfma_f32_16x16x32_bf16 v[20:23], a[180:183], a[200:203], v[20:23]\n\t"
    "v_accvgpr_write_b32 a0, v0\n\t"
    "v_accvgpr_write_b32 a1, v1\n\t"
    "v_mfma_f32_16x16x32_bf16 v[24:27], a[152:155], a[196:199], v[24:27]\n\t"
    "v_accvgpr_write_b32 a2, v2\n\t"
    "v_accvgpr_write_b32 a3, v3\n\t"
    "v_mfma_f32_16x16x32_bf16 v[24:27], a[184:187], a[200:203], v[24:27]\n\t"
    "v_cvt_pk_bf16_f32 v8, v8, v9\n\t"
    "v_cvt_pk_bf16_f32 v9, v10, v11\n\t"
    "v_mfma_f32_16x16x32_bf16 v[28:31], a[156:159], a[196:199], v[28:31]\n\t"
    "s_waitcnt lgkmcnt(0)\n\t"
    "v_cvt_pk_bf16_f32 v10, v12, v13\n\t"
    "v_cvt_pk_bf16_f32 v11, v14, v15\n\t"
    "v_mfma_f32_16x16x32_bf16 v[28:31], a[188:191], a[200:203], v[28:31]\n\t"
    "s_waitcnt vmcnt(33)\n\t"
    "v_accvgpr_write_b32 a4, v8\n\t"
    "v_accvgpr_write_b32 a5, v9\n\t"
    "v_mfma_f32_16x16x32_bf16 v[32:35], a[64:67], v[200:203], v[32:35]\n\t"
    "s_waitcnt vmcnt(18) lgkmcnt(0)\n\t"
    "buffer_load_dwordx4 v241, s[24:27], s36 offen offset:1024  lds\n\t"
    "v_mfma_f32_16x16x32_bf16 v[32:35], a[96:99], v[204:207], v[32:35]\n\t"
    "s_barrier\n\t"
    "v_accvgpr_write_b32 a6, v10\n\t"
    "v_accvgpr_write_b32 a7, v11\n\t"
    "v_mfma_f32_16x16x32_bf16 v[32:35], a[128:131], a[204:207], v[32:35]\n\t"
    "ds_read_b128 a[48:51], v228 offset:32768\n\t"
    "v_cvt_pk_bf16_f32 v16, v16, v17\n\t"
    "v_mfma_f32_16x16x32_bf16 v[32:35], a[160:163], a[208:211], v[32:35]\n\t"
    "ds_read_b128 a[52:55], v229 offset:32768\n\t"
    "v_cvt_pk_bf16_f32 v17, v18, v19\n\t"
    "v_mfma_f32_16x16x32_bf16 v[36:39], a[68:71], v[200:203], v[36:39]\n\t"
    "ds_read_b128 a[56:59], v230 offset:32768\n\t"
    "v_cvt_pk_bf16_f32 v18, v20, v21\n\t"
    "v_mfma_f32_16x16x32_bf16 v[36:39], a[100:103], v[204:207], v[36:39]\n\t"
    "ds_read_b128 a[60:63], v231 offset:32768\n\t"
    "v_cvt_pk_bf16_f32 v19, v22, v23\n\t"
    "v_mfma_f32_16x16x32_bf16 v[36:39], a[132:135], a[204:207], v[36:39]\n\t"
    "ds_read_b128 a[228:231], v228 offset:36864\n\t"
    "v_accvgpr_write_b32 a8, v16\n\t"
    "v_mfma_f32_16x16x32_bf16 v[36:39], a[164:167], a[208:211], v[36:39]\n\t"
    "ds_read_b128 a[232:235], v229 offset:36864\n\t"
    "v_accvgpr_write_b32 a9, v17\n\t"
    "v_mfma_f32_16x16x32_bf16 v[40:43], a[72:75], v[200:203], v[40:43]\n\t"
    "buffer_load_dwordx4 v240, s[24:27], s36 offen offset:2048  lds\n\t"
    "v_mfma_f32_16x16x32_bf16 v[40:43], a[104:107], v[204:207], v[40:43]\n\t"
    "ds_read_b128 a[236:239], v230 offset:36864\n\t"
    "v_accvgpr_write_b32 a10, v18\n\t"
    "v_mfma_f32_16x16x32_bf16 v[40:43], a[136:139], a[204:207], v[40:43]\n\t"
    "ds_read_b128 a[240:243], v231 offset:36864\n\t"
    "v_accvgpr_write_b32 a11, v19\n\t"
    "v_mfma_f32_16x16x32_bf16 v[40:43], a[168:171], a[208:211], v[40:43]\n\t"
    "v_cvt_pk_bf16_f32 v24, v24, v25\n\t"
    "v_cvt_pk_bf16_f32 v25, v26, v27\n\t"
    "v_mfma_f32_16x16x32_bf16 v[44:47], a[76:79], v[200:203], v[44:47]\n\t"
    "v_cvt_pk_bf16_f32 v26, v28, v29\n\t"
    "v_cvt_pk_bf16_f32 v27, v30, v31\n\t"
    "v_mfma_f32_16x16x32_bf16 v[44:47], a[108:111], v[204:207], v[44:47]\n\t"
    "v_accvgpr_write_b32 a12, v24\n\t"
    "v_accvgpr_write_b32 a13, v25\n\t"
    "v_mfma_f32_16x16x32_bf16 v[44:47], a[140:143], a[204:207], v[44:47]\n\t"
    "v_accvgpr_write_b32 a14, v26\n\t"
    "v_accvgpr_write_b32 a15, v27\n\t"
    "v_mfma_f32_16x16x32_bf16 v[44:47], a[172:175], a[208:211], v[44:47]\n\t"
    "v_cvt_pk_bf16_f32 v32, v32, v33\n\t"
    "v_cvt_pk_bf16_f32 v33, v34, v35\n\t"
    "v_mfma_f32_16x16x32_bf16 v[48:51], a[80:83], v[200:203], v[48:51]\n\t"
    "buffer_load_dwordx4 v241, s[24:27], s36 offen offset:3072  lds\n\t"
    "v_mfma_f32_16x16x32_bf16 v[48:51], a[112:115], v[204:207], v[48:51]\n\t"
    "v_cvt_pk_bf16_f32 v34, v36, v37\n\t"
    "v_cvt_pk_bf16_f32 v35, v38, v39\n\t"
    "v_mfma_f32_16x16x32_bf16 v[48:51], a[144:147], a[204:207], v[48:51]\n\t"
    "s_lshl1_add_u32 m0, s37, 131072\n\t"
    "v_accvgpr_write_b32 a16, v32\n\t"
    "v_accvgpr_write_b32 a17, v33\n\t"
    "v_mfma_f32_16x16x32_bf16 v[48:51], a[176:179], a[208:211], v[48:51]\n\t"
    "s_waitcnt lgkmcnt(4)\n\t"
    "buffer_load_dwordx4 v244, s[44:47], s42 offen offset:0  lds\n\t"
    "v_mfma_f32_16x16x32_bf16 v[0:3], a[48:51], a[0:3], 0\n\t"
    "v_accvgpr_write_b32 a18, v34\n\t"
    "v_accvgpr_write_b32 a19, v35\n\t"
    "v_mfma_f32_16x16x32_bf16 v[0:3], a[52:55], a[4:7], v[0:3]\n\t"
    "s_add_u32 m0, s37, 16384\n\t"
    "v_cvt_pk_bf16_f32 v40, v40, v41\n\t"
    "v_cvt_pk_bf16_f32 v41, v42, v43\n\t"
    "v_mfma_f32_16x16x32_bf16 v[0:3], a[56:59], a[8:11], v[0:3]\n\t"
    "s_add_u32 s36, s37, 49152\n\t"
    "v_cvt_pk_bf16_f32 v42, v44, v45\n\t"
    "v_cvt_pk_bf16_f32 v43, v46, v47\n\t"
    "v_mfma_f32_16x16x32_bf16 v[0:3], a[60:63], a[12:15], v[0:3]\n\t"
    "s_waitcnt lgkmcnt(0)\n\t"
    "v_accvgpr_write_b32 a20, v40\n\t"
    "v_accvgpr_write_b32 a21, v41\n\t"
    "v_mfma_f32_16x16x32_bf16 v[4:7], a[228:231], a[0:3], 0\n\t"
    "buffer_load_dwordx4 v236, s[32:35], s36 offen offset:0  lds\n\t"
    "v_mfma_f32_16x16x32_bf16 v[4:7], a[232:235], a[4:7], v[4:7]\n\t"
    "v_accvgpr_write_b32 a22, v42\n\t"
    "v_accvgpr_write_b32 a23, v43\n\t"
    "v_mfma_f32_16x16x32_bf16 v[4:7], a[236:239], a[8:11], v[4:7]\n\t"
    "v_xor_b32 v228, 0, v251\n\t"
    "v_xor_b32 v229, 32, v251\n\t"
    "v_mfma_f32_16x16x32_bf16 v[4:7], a[240:243], a[12:15], v[4:7]\n\t"
    "v_xor_b32 v230, 64, v251\n\t"
    "v_xor_b32 v231, 96, v251\n\t"
    "v_mfma_f32_16x16x32_bf16 v[52:55], a[84:87], v[200:203], v[52:55]\n\t"
    "v_mfma_f32_16x16x32_bf16 v[52:55], a[116:119], v[204:207], v[52:55]\n\t"
    "v_mfma_f32_16x16x32_bf16 v[52:55], a[148:151], a[204:207], v[52:55]\n\t"
    "buffer_load_dwordx4 v237, s[32:35], s36 offen offset:1024  lds\n\t"
    "v_mfma_f32_16x16x32_bf16 v[52:55], a[180:183], a[208:211], v[52:55]\n\t"
    "v_mfma_f32_16x16x32_bf16 v[56:59], a[88:91], v[200:203], v[56:59]\n\t"
    "v_max3_f32 v192, v0, v1, v2\n\t"
    "v_max3_f32 v192, v3, v4, v192\n\t"
    "v_mfma_f32_16x16x32_bf16 v[56:59], a[120:123], v[204:207], v[56:59]\n\t"
    "v_max3_f32 v192, v5, v6, v192\n\t"
    "v_max_f32 v192, v7, v192\n\t"
    "v_mfma_f32_16x16x32_bf16 v[56:59], a[152:155], a[204:207], v[56:59]\n\t"
    "v_mov_b32 v193, v192\n\t"
    "s_nop 1\n\t"
    "v_mfma_f32_16x16x32_bf16 v[56:59], a[184:187], a[208:211], v[56:59]\n\t"
    "v_cvt_pk_bf16_f32 v48, v48, v49\n\t"
    "v_cvt_pk_bf16_f32 v49, v50, v51\n\t"
    "v_mfma_f32_16x16x32_bf16 v[60:63], a[92:95], v[200:203], v[60:63]\n\t"
    "buffer_load_dwordx4 v238, s[32:35], s36 offen offset:2048  lds\n\t"
    "v_mfma_f32_16x16x32_bf16 v[60:63], a[124:127], v[204:207], v[60:63]\n\t"
    "v_cvt_pk_bf16_f32 v50, v52, v53\n\t"
    "v_cvt_pk_bf16_f32 v51, v54, v55\n\t"
    "v_mfma_f32_16x16x32_bf16 v[60:63], a[156:159], a[204:207], v[60:63]\n\t"
    "v_accvgpr_write_b32 a24, v48\n\t"
    "v_accvgpr_write_b32 a25, v49\n\t"
    "v_mfma_f32_16x16x32_bf16 v[60:63], a[188:191], a[208:211], v[60:63]\n\t"
    "v_accvgpr_write_b32 a26, v50\n\t"
    "v_accvgpr_write_b32 a27, v51\n\t"
    "v_mfma_f32_16x16x32_bf16 v[64:67], a[64:67], v[208:211], v[64:67]\n\t"
    "v_permlane16_swap_b32 v192, v193\n\t"
    "v_max_f32 v192, v192, v193\n\t"
    "v_mfma_f32_16x16x32_bf16 v[64:67], a[96:99], v[212:215], v[64:67]\n\t"
    "v_mov_b32 v193, v192\n\t"
    "s_nop 1\n\t"
    "v_mfma_f32_16x16x32_bf16 v[64:67], a[128:131], a[212:215], v[64:67]\n\t"
    "buffer_load_dwordx4 v239, s[32:35], s36 offen offset:3072  lds\n\t"
    "v_mfma_f32_16x16x32_bf16 v[64:67], a[160:163], a[216:219], v[64:67]\n\t"
    "v_cvt_pk_bf16_f32 v56, v56, v57\n\t"
    "v_cvt_pk_bf16_f32 v57, v58, v59\n\t"
    "v_mfma_f32_16x16x32_bf16 v[68:71], a[68:71], v[208:211], v[68:71]\n\t"
    "v_cvt_pk_bf16_f32 v58, v60, v61\n\t"
    "v_cvt_pk_bf16_f32 v59, v62, v63\n\t"
    "v_mfma_f32_16x16x32_bf16 v[68:71], a[100:103], v[212:215], v[68:71]\n\t"
    "s_lshl1_add_u32 m0, s37, 131072\n\t"
    "v_accvgpr_write_b32 a28, v56\n\t"
    "v_accvgpr_write_b32 a29, v57\n\t"
    "v_mfma_f32_16x16x32_bf16 v[68:71], a[132:135], a[212:215], v[68:71]\n\t"
    "s_add_u32 s76, s42, s75\n\t"
    "buffer_load_dwordx4 v244, s[44:47], s76 offen offset:1024  lds\n\t"
    "v_mfma_f32_16x16x32_bf16 v[68:71], a[164:167], a[216:219], v[68:71]\n\t"
    "s_add_u32 m0, s37, 114688\n\t"
    "v_accvgpr_write_b32 a30, v58\n\t"
    "v_accvgpr_write_b32 a31, v59\n\t"
    "v_mfma_f32_16x16x32_bf16 v[72:75], a[72:75], v[208:211], v[72:75]\n\t"
    "s_add_u32 s36, s37, 49152\n\t"
    "v_permlane32_swap_b32 v192, v193\n\t"
    "v_max_f32 v192, v192, v193\n\t"
    "v_mfma_f32_16x16x32_bf16 v[72:75], a[104:107], v[212:215], v[72:75]\n\t"
    "v_sub_f32 v0, v0, v192\n\t"
    "v_sub_f32 v1, v1, v192\n\t"
    "v_mfma_f32_16x16x32_bf16 v[72:75], a[136:139], a[212:215], v[72:75]\n\t"
    "buffer_load_dwordx4 v240, s[24:27], s36 offen offset:0  lds\n\t"
    "v_mfma_f32_16x16x32_bf16 v[72:75], a[168:171], a[216:219], v[72:75]\n\t"
    "v_cvt_pk_bf16_f32 v64, v64, v65\n\t"
    "v_cvt_pk_bf16_f32 v65, v66, v67\n\t"
    "v_mfma_f32_16x16x32_bf16 v[32:35], a[48:51], a[16:19], 0\n\t"
    "v_cvt_pk_bf16_f32 v66, v68, v69\n\t"
    "v_cvt_pk_bf16_f32 v67, v70, v71\n\t"
    "v_mfma_f32_16x16x32_bf16 v[32:35], a[52:55], a[20:23], v[32:35]\n\t"
    "v_accvgpr_write_b32 a32, v64\n\t"
    "v_accvgpr_write_b32 a33, v65\n\t"
    "v_mfma_f32_16x16x32_bf16 v[32:35], a[56:59], a[24:27], v[32:35]\n\t"
    "v_accvgpr_write_b32 a34, v66\n\t"
    "v_accvgpr_write_b32 a35, v67\n\t"
    "v_mfma_f32_16x16x32_bf16 v[32:35], a[60:63], a[28:31], v[32:35]\n\t"
    "v_sub_f32 v2, v2, v192\n\t"
    "v_sub_f32 v3, v3, v192\n\t"
    "v_mfma_f32_16x16x32_bf16 v[36:39], a[228:231], a[16:19], 0\n\t"
    "buffer_load_dwordx4 v241, s[24:27], s36 offen offset:1024  lds\n\t"
    "v_mfma_f32_16x16x32_bf16 v[36:39], a[232:235], a[20:23], v[36:39]\n\t"
    "v_sub_f32 v4, v4, v192\n\t"
    "v_sub_f32 v5, v5, v192\n\t"
    "v_mfma_f32_16x16x32_bf16 v[36:39], a[236:239], a[24:27], v[36:39]\n\t"
    "v_sub_f32 v6, v6, v192\n\t"
    "v_sub_f32 v7, v7, v192\n\t"
    "v_mfma_f32_16x16x32_bf16 v[36:39], a[240:243], a[28:31], v[36:39]\n\t"
    "v_sub_f32 v195, 0, v192\n\t"
    "v_sub_f32 v194, 0, v192\n\t"
    "v_mfma_f32_16x16x32_bf16 v[76:79], a[76:79], v[208:211], v[76:79]\n\t"
    "v_sub_f32 v193, 0, v192\n\t"
    "v_sub_f32 v192, 0, v192\n\t"
    "v_mfma_f32_16x16x32_bf16 v[76:79], a[108:111], v[212:215], v[76:79]\n\t"
    "v_exp_f32 v0, v0\n\t"
    "v_mfma_f32_16x16x32_bf16 v[76:79], a[140:143], a[212:215], v[76:79]\n\t"
    "buffer_load_dwordx4 v240, s[24:27], s36 offen offset:2048  lds\n\t"
    "v_mfma_f32_16x16x32_bf16 v[76:79], a[172:175], a[216:219], v[76:79]\n\t"
    "v_exp_f32 v1, v1\n\t"
    "v_mfma_f32_16x16x32_bf16 v[80:83], a[80:83], v[208:211], v[80:83]\n\t"
    "v_exp_f32 v2, v2\n\t"
    "v_mfma_f32_16x16x32_bf16 v[80:83], a[112:115], v[212:215], v[80:83]\n\t"
    "v_exp_f32 v3, v3\n\t"
    "v_mfma_f32_16x16x32_bf16 v[80:83], a[144:147], a[212:215], v[80:83]\n\t"
    "v_exp_f32 v4, v4\n\t"
    "v_mfma_f32_16x16x32_bf16 v[80:83], a[176:179], a[216:219], v[80:83]\n\t"
    "v_cvt_pk_bf16_f32 v72, v72, v73\n\t"
    "v_cvt_pk_bf16_f32 v73, v74, v75\n\t"
    "v_mfma_f32_16x16x32_bf16 v[84:87], a[84:87], v[208:211], v[84:87]\n\t"
    "buffer_load_dwordx4 v241, s[24:27], s36 offen offset:3072  lds\n\t"
    "v_mfma_f32_16x16x32_bf16 v[84:87], a[116:119], v[212:215], v[84:87]\n\t"
    "v_cvt_pk_bf16_f32 v74, v76, v77\n\t"
    "v_cvt_pk_bf16_f32 v75, v78, v79\n\t"
    "v_mfma_f32_16x16x32_bf16 v[84:87], a[148:151], a[212:215], v[84:87]\n\t"
    "v_accvgpr_write_b32 a36, v72\n\t"
    "v_accvgpr_write_b32 a37, v73\n\t"
    "v_mfma_f32_16x16x32_bf16 v[84:87], a[180:183], a[216:219], v[84:87]\n\t"
    "s_lshl1_add_u32 m0, s37, 131072\n\t"
    "v_accvgpr_write_b32 a38, v74\n\t"
    "v_accvgpr_write_b32 a39, v75\n\t"
    "v_mfma_f32_16x16x32_bf16 v[88:91], a[88:91], v[208:211], v[88:91]\n\t"
    "s_add_u32 s76, s76, s75\n\t"
    "buffer_load_dwordx4 v244, s[44:47], s76 offen offset:2048  lds\n\t"
    "v_mfma_f32_16x16x32_bf16 v[88:91], a[120:123], v[212:215], v[88:91]\n\t"
    "v_exp_f32 v5, v5\n\t"
    "v_mfma_f32_16x16x32_bf16 v[88:91], a[152:155], a[212:215], v[88:91]\n\t"
    "v_exp_f32 v6, v6\n\t"
    "v_mfma_f32_16x16x32_bf16 v[88:91], a[184:187], a[216:219], v[88:91]\n\t"
    "v_cvt_pk_bf16_f32 v80, v80, v81\n\t"
    "v_cvt_pk_bf16_f32 v81, v82, v83\n\t"
    "v_mfma_f32_16x16x32_bf16 v[92:95], a[92:95], v[208:211], v[92:95]\n\t"
    "s_waitcnt vmcnt(27) lgkmcnt(0)\n\t"
    "v_cvt_pk_bf16_f32 v82, v84, v85\n\t"
    "v_cvt_pk_bf16_f32 v83, v86, v87\n\t"
    "v_mfma_f32_16x16x32_bf16 v[92:95], a[124:127], v[212:215], v[92:95]\n\t"
    "s_barrier\n\t"
    "v_accvgpr_write_b32 a40, v80\n\t"
    "v_accvgpr_write_b32 a41, v81\n\t"
    "v_mfma_f32_16x16x32_bf16 v[92:95], a[156:159], a[212:215], v[92:95]\n\t"
    "v_accvgpr_write_b32 a42, v82\n\t"
    "v_accvgpr_write_b32 a43, v83\n\t"
    "v_mfma_f32_16x16x32_bf16 v[92:95], a[188:191], a[216:219], v[92:95]\n\t"
    "v_exp_f32 v7, v7\n\t"
    "v_mfma_f32_16x16x32_bf16 v[96:99], a[64:67], v[216:219], v[96:99]\n\t"
    "v_cvt_pk_bf16_f32 v144, v0, v1\n\t"
    "v_cvt_pk_bf16_f32 v145, v2, v3\n\t"
    "v_mfma_f32_16x16x32_bf16 v[96:99], a[96:99], v[220:223], v[96:99]\n\t"
    "v_cvt_pk_bf16_f32 v146, v4, v5\n\t"
    "v_cvt_pk_bf16_f32 v147, v6, v7\n\t"
    "v_mfma_f32_16x16x32_bf16 v[96:99], a[128:131], a[220:223], v[96:99]\n\t"
    "v_max3_f32 v196, v32, v33, v34\n\t"
    "v_max3_f32 v196, v35, v36, v196\n\t"
    "v_mfma_f32_16x16x32_bf16 v[96:99], a[160:163], a[224:227], v[96:99]\n\t"
    "ds_read_b128 a[96:99], v224 offset:40960\n\t"
    "v_cvt_pk_bf16_f32 v88, v88, v89\n\t"
    "v_cvt_pk_bf16_f32 v89, v90, v91\n\t"
    "v_mfma_f32_16x16x32_bf16 v[100:103], a[68:71], v[216:219], v[100:103]\n\t"
    "ds_read_b64_tr_b16 a[128:129], v228 offset:0\n\t"
    "v_cvt_pk_bf16_f32 v90, v92, v93\n\t"
    "v_mfma_f32_16x16x32_bf16 v[100:103], a[100:103], v[220:223], v[100:103]\n\t"
    "ds_read_b64_tr_b16 a[130:131], v228 offset:4096\n\t"
    "v_cvt_pk_bf16_f32 v91, v94, v95\n\t"
    "v_mfma_f32_16x16x32_bf16 v[100:103], a[132:135], a[220:223], v[100:103]\n\t"
    "v_accvgpr_write_b32 a44, v88\n\t"
    "v_accvgpr_write_b32 a45, v89\n\t"
    "v_mfma_f32_16x16x32_bf16 v[100:103], a[164:167], a[224:227], v[100:103]\n\t"
    "ds_read_b128 a[100:103], v225 offset:40960\n\t"
    "v_accvgpr_write_b32 a46, v90\n\t"
    "v_mfma_f32_16x16x32_bf16 v[64:67], a[48:51], a[32:35], 0\n\t"
    "ds_read_b64_tr_b16 a[132:133], v229 offset:0\n\t"
    "v_accvgpr_write_b32 a47, v91\n\t"
    "v_mfma_f32_16x16x32_bf16 v[64:67], a[52:55], a[36:39], v[64:67]\n\t"
    "ds_read_b64_tr_b16 a[134:135], v229 offset:4096\n\t"
    "v_max3_f32 v196, v37, v38, v196\n\t"
    "v_mfma_f32_16x16x32_bf16 v[64:67], a[56:59], a[40:43], v[64:67]\n\t"
    "v_max_f32 v196, v39, v196\n\t"
    "v_mov_b32 v197, v196\n\t"
    "v_mfma_f32_16x16x32_bf16 v[64:67], a[60:63], a[44:47], v[64:67]\n\t"
    "v_cvt_pk_bf16_f32 v176, v96, v97\n\t"
    "v_cvt_pk_bf16_f32 v177, v98, v99\n\t"
    "v_mfma_f32_16x16x32_bf16 v[68:71], a[228:231], a[32:35], 0\n\t"
    "v_cvt_pk_bf16_f32 v178, v100, v101\n\t"
    "v_cvt_pk_bf16_f32 v179, v102, v103\n\t"
    "v_mfma_f32_16x16x32_bf16 v[68:71], a[232:235], a[36:39], v[68:71]\n\t"
    "s_nop 1\n\t"
    "v_permlane16_swap_b32 v196, v197\n\t"
    "v_mfma_f32_16x16x32_bf16 v[68:71], a[236:239], a[40:43], v[68:71]\n\t"
    "v_max_f32 v196, v196, v197\n\t"
    "v_mov_b32 v197, v196\n\t"
    "v_mfma_f32_16x16x32_bf16 v[68:71], a[240:243], a[44:47], v[68:71]\n\t"
    "s_nop 1\n\t"
    "v_permlane32_swap_b32 v196, v197\n\t"
    "v_mfma_f32_16x16x32_bf16 v[104:107], a[72:75], v[216:219], v[104:107]\n\t"
    "v_max_f32 v196, v196, v197\n\t"
    "v_sub_f32 v32, v32, v196\n\t"
    "v_mfma_f32_16x16x32_bf16 v[104:107], a[104:107], v[220:223], v[104:107]\n\t"
    "v_sub_f32 v33, v33, v196\n\t"
    "v_sub_f32 v34, v34, v196\n\t"
    "v_mfma_f32_16x16x32_bf16 v[104:107], a[136:139], a[220:223], v[104:107]\n\t"
    "v_sub_f32 v35, v35, v196\n\t"
    "v_sub_f32 v36, v36, v196\n\t"
    "v_mfma_f32_16x16x32_bf16 v[104:107], a[168:171], a[224:227], v[104:107]\n\t"
    "ds_read_b128 a[104:107], v226 offset:40960\n\t"
    "v_sub_f32 v37, v37, v196\n\t"
    "v_mfma_f32_16x16x32_bf16 v[108:111], a[76:79], v[216:219], v[108:111]\n\t"
    "ds_read_b64_tr_b16 a[136:137], v230 offset:0\n\t"
    "v_sub_f32 v38, v38, v196\n\t"
    "v_sub_f32 v39, v39, v196\n\t"
    "v_mfma_f32_16x16x32_bf16 v[108:111], a[108:111], v[220:223], v[108:111]\n\t"
    "ds_read_b64_tr_b16 a[138:139], v230 offset:4096\n\t"
    "v_sub_f32 v199, 0, v196\n\t"
    "v_sub_f32 v198, 0, v196\n\t"
    "v_mfma_f32_16x16x32_bf16 v[108:111], a[140:143], a[220:223], v[108:111]\n\t"
    "v_sub_f32 v197, 0, v196\n\t"
    "v_sub_f32 v196, 0, v196\n\t"
    "v_exp_f32 v32, v32\n\t"
    "v_mfma_f32_16x16x32_bf16 v[108:111], a[172:175], a[224:227], v[108:111]\n\t"
    "ds_read_b128 a[108:111], v227 offset:40960\n\t"
    "v_max3_f32 v200, v64, v65, v66\n\t"
    "v_mfma_f32_16x16x32_bf16 v[112:115], a[80:83], v[216:219], v[112:115]\n\t"
    "ds_read_b64_tr_b16 a[140:141], v231 offset:0\n\t"
    "v_max3_f32 v200, v67, v68, v200\n\t"
    "v_mfma_f32_16x16x32_bf16 v[112:115], a[112:115], v[220:223], v[112:115]\n\t"
    "s_lshl1_add_u32 m0, s37, 131072\n\t"
    "ds_read_b64_tr_b16 a[142:143], v231 offset:4096\n\t"
    "v_max3_f32 v200, v69, v70, v200\n\t"
    "v_mfma_f32_16x16x32_bf16 v[112:115], a[144:147], a[220:223], v[112:115]\n\t"
    "s_add_u32 s76, s76, s75\n\t"
    "buffer_load_dwordx4 v244, s[44:47], s76 offen offset:3072  lds\n\t"
    "v_mfma_f32_16x16x32_bf16 v[112:115], a[176:179], a[224:227], v[112:115]\n\t"
    "ds_read_b128 a[112:115], v224 offset:45056\n\t"
    "v_cvt_pk_bf16_f32 v180, v104, v105\n\t"
    "v_mfma_f32_16x16x32_bf16 v[116:119], a[84:87], v[216:219], v[116:119]\n\t"
    "ds_read_b64_tr_b16 a[144:145], v232 offset:0\n\t"
    "v_cvt_pk_bf16_f32 v181, v106, v107\n\t"
    "v_mfma_f32_16x16x32_bf16 v[116:119], a[116:119], v[220:223], v[116:119]\n\t"
    "ds_read_b64_tr_b16 a[146:147], v232 offset:4096\n\t"
    "v_cvt_pk_bf16_f32 v182, v108, v109\n\t"
    "v_mfma_f32_16x16x32_bf16 v[116:119], a[148:151], a[220:223], v[116:119]\n\t"
    "v_cvt_pk_bf16_f32 v183, v110, v111\n\t"
    "v_max_f32 v200, v71, v200\n\t"
    "v_mfma_f32_16x16x32_bf16 v[116:119], a[180:183], a[224:227], v[116:119]\n\t"
    "ds_read_b128 a[116:119], v225 offset:45056\n\t"
    "v_mov_b32 v201, v200\n\t"
    "v_mfma_f32_16x16x32_bf16 v[120:123], a[88:91], v[216:219], v[120:123]\n\t"
    "ds_read_b64_tr_b16 a[148:149], v233 offset:0\n\t"
    "s_nop 1\n\t"
    "v_mfma_f32_16x16x32_bf16 v[120:123], a[120:123], v[220:223], v[120:123]\n\t"
    "ds_read_b64_tr_b16 a[150:151], v233 offset:4096\n\t"
    "v_permlane16_swap_b32 v200, v201\n\t"
    "v_mfma_f32_16x16x32_bf16 v[120:123], a[152:155], a[220:223], v[120:123]\n\t"
    "v_exp_f32 v33, v33\n\t"
    "v_mfma_f32_16x16x32_bf16 v[120:123], a[184:187], a[224:227], v[120:123]\n\t"
    "ds_read_b128 a[120:123], v226 offset:45056\n\t"
    "v_cvt_pk_bf16_f32 v184, v112, v113\n\t"
    "v_mfma_f32_16x16x32_bf16 v[124:127], a[92:95], v[216:219], v[124:127]\n\t"
    "ds_read_b64_tr_b16 a[152:153], v234 offset:0\n\t"
    "v_cvt_pk_bf16_f32 v185, v114, v115\n\t"
    "v_mfma_f32_16x16x32_bf16 v[124:127], a[124:127], v[220:223], v[124:127]\n\t"
    "ds_read_b64_tr_b16 a[154:155], v234 offset:4096\n\t"
    "v_cvt_pk_bf16_f32 v186, v116, v117\n\t"
    "v_mfma_f32_16x16x32_bf16 v[124:127], a[156:159], a[220:223], v[124:127]\n\t"
    "v_cvt_pk_bf16_f32 v187, v118, v119\n\t"
    "v_max_f32 v200, v200, v201\n\t"
    "v_mfma_f32_16x16x32_bf16 v[124:127], a[188:191], a[224:227], v[124:127]\n\t"
    "ds_read_b128 a[124:127], v227 offset:45056\n\t"
    "v_mov_b32 v201, v200\n\t"
    "v_mfma_f32_16x16x32_bf16 v[96:99], a[48:51], v[176:179], 0\n\t"
    "ds_read_b64_tr_b16 a[156:157], v235 offset:0\n\t"
    "s_nop 1\n\t"
    "v_mfma_f32_16x16x32_bf16 v[96:99], a[52:55], v[180:183], v[96:99]\n\t"
    "ds_read_b64_tr_b16 a[158:159], v235 offset:4096\n\t"
    "v_permlane32_swap_b32 v200, v201\n\t"
    "v_mfma_f32_16x16x32_bf16 v[100:103], a[228:231], v[176:179], 0\n\t"
    "v_exp_f32 v34, v34\n\t"
    "v_mfma_f32_16x16x32_bf16 v[100:103], a[232:235], v[180:183], v[100:103]\n\t"
    "v_cvt_pk_bf16_f32 v188, v120, v121\n\t"
    "v_cvt_pk_bf16_f32 v189, v122, v123\n\t"
    "v_mfma_f32_16x16x32_bf16 v[96:99], a[56:59], v[184:187], v[96:99]\n\t"
    "v_cvt_pk_bf16_f32 v190, v124, v125\n\t"
    "v_cvt_pk_bf16_f32 v191, v126, v127\n\t"
    "v_mfma_f32_16x16x32_bf16 v[100:103], a[236:239], v[184:187], v[100:103]\n\t"
    "v_exp_f32 v35, v35\n\t"
    "v_mfma_f32_16x16x32_bf16 v[96:99], a[60:63], v[188:191], v[96:99]\n\t"
    "v_exp_f32 v36, v36\n\t"
    "v_mfma_f32_16x16x32_bf16 v[100:103], a[240:243], v[188:191], v[100:103]\n\t"
    "s_waitcnt lgkmcnt(14)\n\t"
    "v_exp_f32 v37, v37\n\t"
    "v_mfma_f32_16x16x32_bf16 v[128:131], a[96:99], a[0:3], v[192:195]\n\t"
    "s_mov_b32 s41, 0\n\t"
    "v_exp_f32 v38, v38\n\t"
    "v_mfma_f32_16x16x32_bf16 v[128:131], a[100:103], a[4:7], v[128:131]\n\t"
    "v_exp_f32 v39, v39\n\t"
    "v_mfma_f32_16x16x32_bf16 v[128:131], a[104:107], a[8:11], v[128:131]\n\t"
    "v_cvt_pk_bf16_f32 v148, v32, v33\n\t"
    "v_cvt_pk_bf16_f32 v149, v34, v35\n\t"
    "v_mfma_f32_16x16x32_bf16 v[128:131], a[108:111], a[12:15], v[128:131]\n\t"
    "s_waitcnt lgkmcnt(2)\n\t"
    "v_cvt_pk_bf16_f32 v150, v36, v37\n\t"
    "v_cvt_pk_bf16_f32 v151, v38, v39\n\t"
    "v_mfma_f32_16x16x32_bf16 v[132:135], a[124:127], a[12:15], v[192:195]\n\t"
    "v_max_f32 v200, v200, v201\n\t"
    "v_sub_f32 v64, v64, v200\n\t"
    "v_sub_f32 v65, v65, v200\n\t"
    "v_mfma_f32_16x16x32_bf16 v[132:135], a[120:123], a[8:11], v[132:135]\n\t"
    "v_sub_f32 v66, v66, v200\n\t"
    "v_sub_f32 v67, v67, v200\n\t"
    "v_sub_f32 v68, v68, v200\n\t"
    "v_mfma_f32_16x16x32_bf16 v[132:135], a[116:119], a[4:7], v[132:135]\n\t"
    "v_sub_f32 v69, v69, v200\n\t"
    "v_sub_f32 v70, v70, v200\n\t"
    "v_sub_f32 v71, v71, v200\n\t"
    "v_mfma_f32_16x16x32_bf16 v[132:135], a[112:115], a[0:3], v[132:135]\n\t"
    "v_sub_f32 v203, 0, v200\n\t"
    "v_sub_f32 v202, 0, v200\n\t"
    "v_sub_f32 v201, 0, v200\n\t"
    "v_mfma_f32_16x16x32_bf16 v[0:3], a[128:131], v[144:147], 0\n\t"
    "s_waitcnt vmcnt(24) lgkmcnt(0)\n\t"
    "v_sub_f32 v200, 0, v200\n\t"
    "v_max3_f32 v204, v96, v97, v98\n\t"
    "v_exp_f32 v64, v64\n\t"
    "v_mfma_f32_16x16x32_bf16 v[4:7], a[132:135], v[144:147], 0\n\t"
    "s_barrier\n\t"
    "v_exp_f32 v65, v65\n\t"
    "v_exp_f32 v66, v66\n\t"
    "v_mfma_f32_16x16x32_bf16 v[8:11], a[136:139], v[144:147], 0\n\t"
    "v_exp_f32 v67, v67\n\t"
    "v_exp_f32 v68, v68\n\t"
    "v_mfma_f32_16x16x32_bf16 v[12:15], a[140:143], v[144:147], 0\n\t"
    "s_lshl1_add_u32 m0, s37, 135168\n\t"
    "v_exp_f32 v69, v69\n\t"
    "v_exp_f32 v70, v70\n\t"
    "v_mfma_f32_16x16x32_bf16 v[16:19], a[144:147], v[144:147], 0\n\t"
    "buffer_load_dwordx4 v245, s[44:47], s42 offen offset:0  lds\n\t"
    "v_exp_f32 v71, v71\n\t"
    "v_mfma_f32_16x16x32_bf16 v[20:23], a[148:151], v[144:147], 0\n\t"
    "v_cvt_pk_bf16_f32 v152, v64, v65\n\t"
    "v_cvt_pk_bf16_f32 v153, v66, v67\n\t"
    "v_cvt_pk_bf16_f32 v154, v68, v69\n\t"
    "v_mfma_f32_16x16x32_bf16 v[24:27], a[152:155], v[144:147], 0\n\t"
    "v_cvt_pk_bf16_f32 v155, v70, v71\n\t"
    "v_max3_f32 v204, v99, v100, v204\n\t"
    "v_max3_f32 v204, v101, v102, v204\n\t"
    "v_mfma_f32_16x16x32_bf16 v[28:31], a[156:159], v[144:147], 0\n\t"
    "v_max_f32 v204, v103, v204\n\t"
    "v_mov_b32 v205, v204\n\t"
    "s_nop 1\n\t"
    "v_mfma_f32_16x16x32_bf16 v[208:211], a[192:195], v[144:147], 0\n\t"
    "v_permlane16_swap_b32 v204, v205\n\t"
    "v_max_f32 v204, v204, v205\n\t"
    "v_mov_b32 v205, v204\n\t"
    "v_mfma_f32_16x16x32_bf16 v[136:139], a[96:99], a[16:19], v[196:199]\n\t"
    "v_exp_f32 v128, v128\n\t"
    "s_nop 1\n\t"
    "v_mfma_f32_16x16x32_bf16 v[136:139], a[100:103], a[20:23], v[136:139]\n\t"
    "v_permlane32_swap_b32 v204, v205\n\t"
    "v_max_f32 v204, v204, v205\n\t"
    "v_sub_f32 v96, v96, v204\n\t"
    "v_mfma_f32_16x16x32_bf16 v[136:139], a[104:107], a[24:27], v[136:139]\n\t"
    "v_exp_f32 v129, v129\n\t"
    "v_sub_f32 v97, v97, v204\n\t"
    "v_mfma_f32_16x16x32_bf16 v[136:139], a[108:111], a[28:31], v[136:139]\n\t"
    "v_cvt_pk_bf16_f32 v160, v128, v129\n\t"
    "v_sub_f32 v98, v98, v204\n\t"
    "v_sub_f32 v99, v99, v204\n\t"
    "v_mfma_f32_16x16x32_bf16 v[140:143], a[124:127], a[28:31], v[196:199]\n\t"
    "v_exp_f32 v130, v130\n\t"
    "v_sub_f32 v100, v100, v204\n\t"
    "v_mfma_f32_16x16x32_bf16 v[140:143], a[120:123], a[24:27], v[140:143]\n\t"
    "v_sub_f32 v101, v101, v204\n\t"
    "v_sub_f32 v102, v102, v204\n\t"
    "v_sub_f32 v103, v103, v204\n\t"
    "v_mfma_f32_16x16x32_bf16 v[140:143], a[116:119], a[20:23], v[140:143]\n\t"
    "v_exp_f32 v131, v131\n\t"
    "v_sub_f32 v207, 0, v204\n\t"
    "v_mfma_f32_16x16x32_bf16 v[140:143], a[112:115], a[16:19], v[140:143]\n\t"
    "ds_read_b128 a[64:67], v224 offset:49152\n\t"
    "v_cvt_pk_bf16_f32 v161, v130, v131\n\t"
    "v_sub_f32 v206, 0, v204\n\t"
    "v_mfma_f32_16x16x32_bf16 v[32:35], a[128:131], v[148:151], 0\n\t"
    "v_exp_f32 v132, v132\n\t"
    "v_sub_f32 v205, 0, v204\n\t"
    "v_mfma_f32_16x16x32_bf16 v[36:39], a[132:135], v[148:151], 0\n\t"
    "ds_read_b128 a[68:71], v225 offset:49152\n\t"
    "v_sub_f32 v204, 0, v204\n\t"
    "v_exp_f32 v96, v96\n\t"
    "v_mfma_f32_16x16x32_bf16 v[40:43], a[136:139], v[148:151], 0\n\t"
    "v_exp_f32 v133, v133\n\t"
    "v_exp_f32 v97, v97\n\t"
    "v_mfma_f32_16x16x32_bf16 v[44:47], a[140:143], v[148:151], 0\n\t"
    "ds_read_b128 a[72:75], v226 offset:49152\n\t"
    "v_cvt_pk_bf16_f32 v162, v132, v133\n\t"
    "v_exp_f32 v98, v98\n\t"
    "v_mfma_f32_16x16x32_bf16 v[48:51], a[144:147], v[148:151], 0\n\t"
    "v_exp_f32 v134, v134\n\t"
    "v_exp_f32 v99, v99\n\t"
    "v_mfma_f32_16x16x32_bf16 v[52:55], a[148:151], v[148:151], 0\n\t"
    "ds_read_b128 a[76:79], v227 offset:49152\n\t"
    "v_mov_b32 v144, 0\n\t"
    "v_exp_f32 v100, v100\n\t"
    "v_mfma_f32_16x16x32_bf16 v[56:59], a[152:155], v[148:151], 0\n\t"
    "v_exp_f32 v135, v135\n\t"
    "v_exp_f32 v101, v101\n\t"
    "v_mfma_f32_16x16x32_bf16 v[60:63], a[156:159], v[148:151], 0\n\t"
    "ds_read_b128 a[80:83], v224 offset:53248\n\t"
    "v_cvt_pk_bf16_f32 v163, v134, v135\n\t"
    "v_exp_f32 v102, v102\n\t"
    "v_mfma_f32_16x16x32_bf16 v[212:215], a[192:195], v[148:151], 0\n\t"
    "ds_read_b128 a[84:87], v225 offset:53248\n\t"
    "v_mov_b32 v145, 0\n\t"
    "v_exp_f32 v103, v103\n\t"
    "v_mfma_f32_16x16x32_bf16 v[128:131], a[96:99], a[32:35], v[200:203]\n\t"
    "v_exp_f32 v136, v136\n\t"
    "v_cvt_pk_bf16_f32 v156, v96, v97\n\t"
    "v_mfma_f32_16x16x32_bf16 v[128:131], a[100:103], a[36:39], v[128:131]\n\t"
    "ds_read_b128 a[88:91], v226 offset:53248\n\t"
    "v_cvt_pk_bf16_f32 v157, v98, v99\n\t"
    "v_cvt_pk_bf16_f32 v158, v100, v101\n\t"
    "v_mfma_f32_16x16x32_bf16 v[128:131], a[104:107], a[40:43], v[128:131]\n\t"
    "v_exp_f32 v137, v137\n\t"
    "v_cvt_pk_bf16_f32 v159, v102, v103\n\t"
    "v_mfma_f32_16x16x32_bf16 v[128:131], a[108:111], a[44:47], v[128:131]\n\t"
    "ds_read_b128 a[92:95], v227 offset:53248\n\t"
    "v_cvt_pk_bf16_f32 v164, v136, v137\n\t"
    "v_mov_b32 v146, 0\n\t"
    "v_mfma_f32_16x16x32_bf16 v[132:135], a[124:127], a[44:47], v[200:203]\n\t"
    "v_exp_f32 v138, v138\n\t"
    "v_add_f32 v213, v213, v136\n\t"
    "v_mfma_f32_16x16x32_bf16 v[132:135], a[120:123], a[40:43], v[132:135]\n\t"
    "ds_read_b64_tr_b16 a[160:161], v228 offset:8192\n\t"
    "v_add_f32 v213, v213, v137\n\t"
    "v_add_f32 v213, v213, v138\n\t"
    "v_mfma_f32_16x16x32_bf16 v[132:135], a[116:119], a[36:39], v[132:135]\n\t"
    "v_exp_f32 v139, v139\n\t"
    "v_mov_b32 v147, 0\n\t"
    "v_mfma_f32_16x16x32_bf16 v[132:135], a[112:115], a[32:35], v[132:135]\n\t"
    "ds_read_b64_tr_b16 a[162:163], v228 offset:12288\n\t"
    "v_cvt_pk_bf16_f32 v165, v138, v139\n\t"
    "v_add_f32 v213, v213, v139\n\t"
    "v_mfma_f32_16x16x32_bf16 v[64:67], a[128:131], v[152:155], 0\n\t"
    "v_exp_f32 v140, v140\n\t"
    "v_mov_b32 v148, 0\n\t"
    "v_mfma_f32_16x16x32_bf16 v[68:71], a[132:135], v[152:155], 0\n\t"
    "ds_read_b64_tr_b16 a[164:165], v229 offset:8192\n\t"
    "v_add_f32 v213, v213, v140\n\t"
    "v_mov_b32 v149, 0\n\t"
    "v_mfma_f32_16x16x32_bf16 v[72:75], a[136:139], v[152:155], 0\n\t"
    "v_exp_f32 v141, v141\n\t"
    "v_mfma_f32_16x16x32_bf16 v[76:79], a[140:143], v[152:155], 0\n\t"
    "ds_read_b64_tr_b16 a[166:167], v229 offset:12288\n\t"
    "v_cvt_pk_bf16_f32 v166, v140, v141\n\t"
    "v_add_f32 v213, v213, v141\n\t"
    "v_mfma_f32_16x16x32_bf16 v[80:83], a[144:147], v[152:155], 0\n\t"
    "v_exp_f32 v142, v142\n\t"
    "v_mfma_f32_16x16x32_bf16 v[84:87], a[148:151], v[152:155], 0\n\t"
    "ds_read_b64_tr_b16 a[168:169], v230 offset:8192\n\t"
    "v_add_f32 v213, v213, v142\n\t"
    "v_mfma_f32_16x16x32_bf16 v[88:91], a[152:155], v[152:155], 0\n\t"
    "v_exp_f32 v143, v143\n\t"
    "v_mfma_f32_16x16x32_bf16 v[92:95], a[156:159], v[152:155], 0\n\t"
    "s_add_u32 m0, s37, 32768\n\t"
    "ds_read_b64_tr_b16 a[170:171], v230 offset:12288\n\t"
    "v_cvt_pk_bf16_f32 v167, v142, v143\n\t"
    "v_mfma_f32_16x16x32_bf16 v[216:219], a[192:195], v[152:155], 0\n\t"
    "s_add_u32 s36, s37, 49152\n\t"
    "ds_read_b64_tr_b16 a[172:173], v231 offset:8192\n\t"
    "v_add_f32 v213, v213, v143\n\t"
    "v_mfma_f32_16x16x32_bf16 v[136:139], a[96:99], v[176:179], v[204:207]\n\t"
    "v_exp_f32 v128, v128\n\t"
    "v_mfma_f32_16x16x32_bf16 v[136:139], a[100:103], v[180:183], v[136:139]\n\t"
    "buffer_load_dwordx4 v236, s[20:23], s36 offen offset:0  lds\n\t"
    "v_mfma_f32_16x16x32_bf16 v[136:139], a[104:107], v[184:187], v[136:139]\n\t"
    "v_exp_f32 v129, v129\n\t"
    "v_mfma_f32_16x16x32_bf16 v[136:139], a[108:111], v[188:191], v[136:139]\n\t"
    "ds_read_b64_tr_b16 a[174:175], v231 offset:12288\n\t"
    "v_cvt_pk_bf16_f32 v168, v128, v129\n\t"
    "v_mfma_f32_16x16x32_bf16 v[140:143], a[124:127], v[188:191], v[204:207]\n\t"
    "v_exp_f32 v130, v130\n\t"
    "v_mfma_f32_16x16x32_bf16 v[140:143], a[120:123], v[184:187], v[140:143]\n\t"
    "buffer_load_dwordx4 v237, s[20:23], s36 offen offset:1024  lds\n\t"
    "v_mfma_f32_16x16x32_bf16 v[140:143], a[116:119], v[180:183], v[140:143]\n\t"
    "v_exp_f32 v131, v131\n\t"
    "v_mfma_f32_16x16x32_bf16 v[140:143], a[112:115], v[176:179], v[140:143]\n\t"
    "v_cvt_pk_bf16_f32 v169, v130, v131\n\t"
    "v_mov_b32 v150, 0\n\t"
    "v_mfma_f32_16x16x32_bf16 v[96:99], a[128:131], v[156:159], 0\n\t"
    "v_exp_f32 v132, v132\n\t"
    "v_mfma_f32_16x16x32_bf16 v[100:103], a[132:135], v[156:159], 0\n\t"
    "buffer_load_dwordx4 v238, s[20:23], s36 offen offset:2048  lds\n\t"
    "v_mfma_f32_16x16x32_bf16 v[104:107], a[136:139], v[156:159], 0\n\t"
    "v_exp_f32 v133, v133\n\t"
    "v_mfma_f32_16x16x32_bf16 v[108:111], a[140:143], v[156:159], 0\n\t"
    "s_mov_b32 s44, s2\n\t"
    "v_cvt_pk_bf16_f32 v170, v132, v133\n\t"
    "v_mov_b32 v151, 0\n\t"
    "v_mfma_f32_16x16x32_bf16 v[112:115], a[144:147], v[156:159], 0\n\t"
    "s_and_b32 s45, s3, 0xffff\n\t"
    "v_exp_f32 v134, v134\n\t"
    "v_mfma_f32_16x16x32_bf16 v[116:119], a[148:151], v[156:159], 0\n\t"
    "buffer_load_dwordx4 v239, s[20:23], s36 offen offset:3072  lds\n\t"
    "v_mfma_f32_16x16x32_bf16 v[120:123], a[152:155], v[156:159], 0\n\t"
    "v_exp_f32 v135, v135\n\t"
    "v_mfma_f32_16x16x32_bf16 v[124:127], a[156:159], v[156:159], 0\n\t"
    "v_cvt_pk_bf16_f32 v171, v134, v135\n\t"
    "v_mfma_f32_16x16x32_bf16 v[220:223], a[192:195], v[156:159], 0\n\t"
    "L_c0%=:\n\t"
    "s_cmp_lt_u32 s9, 640\n\t"
    "s_cbranch_scc1 L_act%=\n\t"
    "L_idle%=:\n\t"
    "s_call_b64 s[98:99], L_loop%=\n\t"
    "L_i0%=:\n\t"
    "s_call_b64 s[98:99], L_loop%=\n\t"
    "L_i1%=:\n\t"
    "s_call_b64 s[98:99], L_loop%=\n\t"
    "L_i2%=:\n\t"
    "s_call_b64 s[98:99], L_loop%=\n\t"
    "L_i3%=:\n\t"
    "s_branch L_c4%=\n\t"
    "L_act%=:\n\t"
    "s_call_b64 s[98:99], L_sloop%=\n\t"
    "L_c1%=:\n\t"
    "s_mov_b32 s11, s7\n\t"
    "s_add_u32 s12, s7, 4096\n\t"
    "s_mov_b32 s44, s4\n\t"
    "s_and_b32 s45, s5, 0xffff\n\t"
    "v_accvgpr_read_b32 v246, a252\n\t"
    "v_accvgpr_read_b32 v247, a253\n\t"
    "s_cmp_lt_u32 s9, 176\n\t"
    "s_cbranch_scc1 L_r2a%=\n\t"
    "v_lshrrev_b32 v244, 3, v255\n\t"
    "v_mul_u32_u24 v244, 3456, v244\n\t"
    "v_and_b32 v242, 0x70, v254\n\t"
    "v_add_u32 v244, v244, v242\n\t"
    "v_mov_b32 v245, 67108864\n\t"
    "s_mov_b32 s75, 26624\n\t"
    "s_mov_b32 s46, 107264\n\t"
    "L_r2a%=:\n\t"
    "s_call_b64 s[98:99], L_sloop%=\n\t"
    "L_c2%=:\n\t"
    "v_readfirstlane_b32 s44, v246\n\t"
    "v_readfirstlane_b32 s45, v247\n\t"
    "s_mov_b32 s11, s8\n\t"
    "s_add_u32 s12, s8, 4096\n\t"
    "v_lshrrev_b32 v244, 3, v255\n\t"
    "v_mul_u32_u24 v244, 3456, v244\n\t"
    "v_and_b32 v242, 0x70, v254\n\t"
    "v_add_u32 v244, v244, v242\n\t"
    "v_mov_b32 v245, 67108864\n\t"
    "s_mov_b32 s75, 26624\n\t"
    "s_and_b32 s45, s45, 0xffff\n\t"
    "s_cmp_lt_u32 s9, 616\n\t"
    "s_cselect_b32 s46, 107264, 0\n\t"
    "s_cselect_b32 s10, s10, 67108864\n\t"
    "s_cmp_lt_u32 s9, 176\n\t"
    "s_cbranch_scc1 L_r2b%=\n\t"
    "v_and_b32 v242, 0x300, v254\n\t"
    "v_lshlrev_b32 v242, 1, v242\n\t"
    "v_add_u32 v252, v252, v242\n\t"
    "v_add_u32 v253, 512, v252\n\t"
    "s_mov_b32 s12, 67108864\n\t"
    "L_r2b%=:\n\t"
    "s_call_b64 s[98:99], L_sloop%=\n\t"
    "L_c3%=:\n\t"
    "s_mov_b32 s11, s10\n\t"
    "s_mov_b32 s12, 67108864\n\t"
    "s_mov_b32 s46, 0\n\t"
    "s_getpc_b64 s[48:49]\n\t"
    "L_pc3%=:\n\t"
    "s_add_u32 s48, s48, L_loop%=-L_pc3%=\n\t"
    "s_addc_u32 s49, s49, 0\n\t"
    "s_and_b32 s49, s49, 0xffff\n\t"
    "s_mov_b32 s50, L_drain%=-L_loop%=\n\t"
    "buffer_load_dword a246, v249, s[48:51], 0 offen\n\t"
    "s_cmp_ge_u32 s9, 176\n\t"
    "s_cbranch_scc1 L_r2c%=\n\t"
    "v_and_b32 v242, 0x300, v254\n\t"
    "v_lshlrev_b32 v242, 1, v242\n\t"
    "v_add_u32 v252, v252, v242\n\t"
    "v_add_u32 v253, 512, v252\n\t"
    "L_r2c%=:\n\t"
    "s_call_b64 s[98:99], L_sloop%=\n\t"
    "L_c4%=:\n\t"
    "s_call_b64 s[98:99], L_loop%=\n\t"
    "L_c5%=:\n\t"
    "s_cmp_lg_u32 s77, 0\n\t"
    "s_cbranch_scc1 L_sig0%=\n\t"
    "L_sigb0%=:\n\t"
    "v_lshlrev_b32 v246, 7, v255\n\t"
    "v_mov_b32 v247, 1\n\t"
    "s_add_u32 s16, s18, 638976\n\t"
    "s_addc_u32 s17, s19, 0\n\t"
    "s_mov_b64 exec, 0xff\n\t"
    "global_atomic_add v246, v247, s[16:17]\n\t"
    "s_mov_b64 exec, -1\n\t"
    "L_sig0%=:\n\t"
    "s_add_u32 s43, s95, 5120\n\t"
    "s_mov_b64 exec, 1\n\t"
    "buffer_load_dword a249, off, s[28:31], s43 sc1\n\t"
    "s_mov_b64 exec, -1\n\t"
    "s_call_b64 s[98:99], L_loop%=\n\t"
    "L_c6%=:\n\t"
    "s_and_b32 s40, s68, 0x380\n\t"
    "s_add_u32 s40, s40, 638976\n\t"
    "buffer_load_dword v242, off, s[28:31], s40 sc1\n\t"
    "s_getpc_b64 s[48:49]\n\t"
    "L_pc2%=:\n\t"
    "s_add_u32 s48, s48, L_drain%=-L_pc2%=\n\t"
    "s_addc_u32 s49, s49, 0\n\t"
    "s_and_b32 s49, s49, 0xffff\n\t"
    "s_mov_b32 s50, L_bottom%=-L_drain%=\n\t"
    "buffer_load_dword a246, v249, s[48:51], 0 offen\n\t"
    "s_call_b64 s[98:99], L_loop%=\n\t"
    "L_c7%=:\n\t"
    "v_readfirstlane_b32 s43, v242\n\t"
    "s_nop 0\n\t"
    "s_cmp_ge_u32 s43, 256\n\t"
    "s_cbranch_scc1 L_polld0%=\n\t"
    "L_pollb0%=:\n\t"
    "s_mov_b32 s97, 0x20000\n\t"
    "L_poll0%=:\n\t"
    "buffer_load_dword v242, off, s[28:31], s40 sc1\n\t"
    "s_waitcnt vmcnt(0)\n\t"
    "v_readfirstlane_b32 s43, v242\n\t"
    "s_nop 0\n\t"
    "s_cmp_ge_u32 s43, 256\n\t"
    "s_cbranch_scc1 L_polld0%=\n\t"
    "s_sleep 2\n\t"
    "s_sub_u32 s97, s97, 1\n\t"
    "s_cbranch_scc0 L_poll0%=\n\t"
    "L_polld0%=:\n\t"
    "v_accvgpr_read_b32 v242, a250\n\t"
    "s_cmp_lg_u32 s9, 0\n\t"
    "v_readfirstlane_b32 s10, v242\n\t"
    "s_cbranch_scc1 L_drain%=\n\t"
    "L_bump%=:\n\t"
    "v_mov_b32 v242, s96\n\t"
    "s_mov_b32 s43, 65536\n\t"
    "s_mov_b64 exec, 1\n\t"
    "buffer_store_dword v242, off, s[28:31], s43 sc1\n\t"
    "s_mov_b64 exec, -1\n\t"
    "s_branch L_drain%=\n\t"
    ".p2align 6\n\t"
    "L_sloop%=:\n\t"
    "s_waitcnt lgkmcnt(12)\n\t"
    "v_mfma_f32_16x16x32_bf16 v[128:131], a[64:67], a[32:35], v[200:203]\n\t"
    "v_exp_f32 v136, v136\n\t"
    "v_mfma_f32_16x16x32_bf16 v[128:131], a[68:71], a[36:39], v[128:131]\n\t"
    "s_add_u32 s36, s36, 16384\n\t"
    "ds_read_b64_tr_b16 a[176:177], v232 offset:8192\n\t"
    "v_add_f32 v221, v221, v136\n\t"
    "v_mfma_f32_16x16x32_bf16 v[128:131], a[72:75], a[40:43], v[128:131]\n\t"
    "v_exp_f32 v137, v137\n\t"
    "v_mfma_f32_16x16x32_bf16 v[128:131], a[76:79], a[44:47], v[128:131]\n\t"
    "s_waitcnt lgkmcnt(9)\n\t"
    "ds_read_b64_tr_b16 a[178:179], v232 offset:12288\n\t"
    "v_cvt_pk_bf16_f32 v172, v136, v137\n\t"
    "v_mfma_f32_16x16x32_bf16 v[132:135], a[92:95], a[44:47], v[200:203]\n\t"
    "v_exp_f32 v138, v138\n\t"
    "v_mfma_f32_16x16x32_bf16 v[132:135], a[88:91], a[40:43], v[132:135]\n\t"
    "v_add_f32 v221, v221, v137\n\t"
    "v_add_f32 v221, v221, v138\n\t"
    "v_mfma_f32_16x16x32_bf16 v[132:135], a[84:87], a[36:39], v[132:135]\n\t"
    "v_exp_f32 v139, v139\n\t"
    "v_mfma_f32_16x16x32_bf16 v[132:135], a[80:83], a[32:35], v[132:135]\n\t"
    "ds_read_b64_tr_b16 a[180:181], v233 offset:8192\n\t"
    "v_cvt_pk_bf16_f32 v173, v138, v139\n\t"
    "v_mfma_f32_16x16x32_bf16 v[208:211], a[192:195], v[144:147], v[208:211]\n\t"
    "v_exp_f32 v140, v140\n\t"
    "v_mfma_f32_16x16x32_bf16 v[0:3], a[128:131], v[144:147], v[0:3]\n\t"
    "s_waitcnt lgkmcnt(7)\n\t"
    "v_add_f32 v221, v221, v139\n\t"
    "v_add_f32 v221, v221, v140\n\t"
    "v_mfma_f32_16x16x32_bf16 v[0:3], a[160:163], v[160:163], v[0:3]\n\t"
    "v_exp_f32 v141, v141\n\t"
    "v_mfma_f32_16x16x32_bf16 v[4:7], a[132:135], v[144:147], v[4:7]\n\t"
    "ds_read_b64_tr_b16 a[182:183], v233 offset:12288\n\t"
    "v_cvt_pk_bf16_f32 v174, v140, v141\n\t"
    "v_mfma_f32_16x16x32_bf16 v[4:7], a[164:167], v[160:163], v[4:7]\n\t"
    "v_exp_f32 v142, v142\n\t"
    "v_mfma_f32_16x16x32_bf16 v[8:11], a[136:139], v[144:147], v[8:11]\n\t"
    "s_waitcnt lgkmcnt(4)\n\t"
    "v_add_f32 v221, v221, v141\n\t"
    "v_add_f32 v221, v221, v142\n\t"
    "v_mfma_f32_16x16x32_bf16 v[8:11], a[168:171], v[160:163], v[8:11]\n\t"
    "v_exp_f32 v143, v143\n\t"
    "v_mfma_f32_16x16x32_bf16 v[12:15], a[140:143], v[144:147], v[12:15]\n\t"
    "ds_read_b64_tr_b16 a[184:185], v234 offset:8192\n\t"
    "v_cvt_pk_bf16_f32 v175, v142, v143\n\t"
    "v_mfma_f32_16x16x32_bf16 v[12:15], a[172:175], v[160:163], v[12:15]\n\t"
    "ds_read_b64_tr_b16 a[186:187], v234 offset:12288\n\t"
    "v_add_f32 v221, v221, v143\n\t"
    "v_mfma_f32_16x16x32_bf16 v[136:139], a[64:67], v[176:179], v[204:207]\n\t"
    "v_exp_f32 v128, v128\n\t"
    "v_mfma_f32_16x16x32_bf16 v[136:139], a[68:71], v[180:183], v[136:139]\n\t"
    "ds_read_b64_tr_b16 a[188:189], v235 offset:8192\n\t"
    "ds_read_b64_tr_b16 a[190:191], v235 offset:12288\n\t"
    "v_mfma_f32_16x16x32_bf16 v[136:139], a[72:75], v[184:187], v[136:139]\n\t"
    "v_exp_f32 v129, v129\n\t"
    "v_mfma_f32_16x16x32_bf16 v[136:139], a[76:79], v[188:191], v[136:139]\n\t"
    "ds_read_b128 a[96:99], v224 offset:57344\n\t"
    "v_cvt_pk_bf16_f32 v152, v128, v129\n\t"
    "v_mfma_f32_16x16x32_bf16 v[140:143], a[92:95], v[188:191], v[204:207]\n\t"
    "v_exp_f32 v130, v130\n\t"
    "v_mfma_f32_16x16x32_bf16 v[140:143], a[88:91], v[184:187], v[140:143]\n\t"
    "ds_read_b128 a[100:103], v225 offset:57344\n\t"
    "ds_read_b128 a[104:107], v226 offset:57344\n\t"
    "v_mfma_f32_16x16x32_bf16 v[140:143], a[84:87], v[180:183], v[140:143]\n\t"
    "v_exp_f32 v131, v131\n\t"
    "v_mfma_f32_16x16x32_bf16 v[140:143], a[80:83], v[176:179], v[140:143]\n\t"
    "ds_read_b128 a[108:111], v227 offset:57344\n\t"
    "v_cvt_pk_bf16_f32 v153, v130, v131\n\t"
    "v_mfma_f32_16x16x32_bf16 v[32:35], a[128:131], v[148:151], v[32:35]\n\t"
    "v_exp_f32 v132, v132\n\t"
    "v_mfma_f32_16x16x32_bf16 v[32:35], a[160:163], v[164:167], v[32:35]\n\t"
    "ds_read_b128 a[112:115], v224 offset:61440\n\t"
    "ds_read_b128 a[116:119], v225 offset:61440\n\t"
    "v_mfma_f32_16x16x32_bf16 v[36:39], a[132:135], v[148:151], v[36:39]\n\t"
    "v_exp_f32 v133, v133\n\t"
    "v_mfma_f32_16x16x32_bf16 v[36:39], a[164:167], v[164:167], v[36:39]\n\t"
    "ds_read_b128 a[120:123], v226 offset:61440\n\t"
    "v_cvt_pk_bf16_f32 v154, v132, v133\n\t"
    "v_mfma_f32_16x16x32_bf16 v[40:43], a[136:139], v[148:151], v[40:43]\n\t"
    "v_exp_f32 v134, v134\n\t"
    "v_mfma_f32_16x16x32_bf16 v[40:43], a[168:171], v[164:167], v[40:43]\n\t"
    "ds_read_b128 a[124:127], v227 offset:61440\n\t"
    "v_mfma_f32_16x16x32_bf16 v[44:47], a[140:143], v[148:151], v[44:47]\n\t"
    "v_exp_f32 v135, v135\n\t"
    "v_mfma_f32_16x16x32_bf16 v[44:47], a[172:175], v[164:167], v[44:47]\n\t"
    "v_cvt_pk_bf16_f32 v155, v134, v135\n\t"
    "v_mfma_f32_16x16x32_bf16 v[128:131], a[64:67], a[0:3], v[192:195]\n\t"
    "v_exp_f32 v136, v136\n\t"
    "v_mfma_f32_16x16x32_bf16 v[128:131], a[68:71], a[4:7], v[128:131]\n\t"
    "v_add_f32 v221, v221, v136\n\t"
    "v_mfma_f32_16x16x32_bf16 v[128:131], a[72:75], a[8:11], v[128:131]\n\t"
    "s_waitcnt vmcnt(21) lgkmcnt(0)\n\t"
    "v_exp_f32 v137, v137\n\t"
    "v_mfma_f32_16x16x32_bf16 v[128:131], a[76:79], a[12:15], v[128:131]\n\t"
    "s_barrier\n\t"
    "v_cvt_pk_bf16_f32 v156, v136, v137\n\t"
    "v_mfma_f32_16x16x32_bf16 v[132:135], a[92:95], a[12:15], v[192:195]\n\t"
    "v_exp_f32 v138, v138\n\t"
    "v_mfma_f32_16x16x32_bf16 v[132:135], a[88:91], a[8:11], v[132:135]\n\t"
    "v_add_f32 v221, v221, v137\n\t"
    "v_add_f32 v221, v221, v138\n\t"
    "v_mfma_f32_16x16x32_bf16 v[132:135], a[84:87], a[4:7], v[132:135]\n\t"
    "v_exp_f32 v139, v139\n\t"
    "v_mfma_f32_16x16x32_bf16 v[132:135], a[80:83], a[0:3], v[132:135]\n\t"
    "ds_read_b64_tr_b16 a[128:129], v228 offset:16384\n\t"
    "v_cvt_pk_bf16_f32 v157, v138, v139\n\t"
    "v_mfma_f32_16x16x32_bf16 v[208:211], a[192:195], v[160:163], v[208:211]\n\t"
    "v_exp_f32 v140, v140\n\t"
    "v_mfma_f32_16x16x32_bf16 v[16:19], a[144:147], v[144:147], v[16:19]\n\t"
    "v_add_f32 v221, v221, v139\n\t"
    "v_add_f32 v221, v221, v140\n\t"
    "v_mfma_f32_16x16x32_bf16 v[16:19], a[176:179], v[160:163], v[16:19]\n\t"
    "v_exp_f32 v141, v141\n\t"
    "v_mfma_f32_16x16x32_bf16 v[20:23], a[148:151], v[144:147], v[20:23]\n\t"
    "ds_read_b64_tr_b16 a[130:131], v228 offset:20480\n\t"
    "v_cvt_pk_bf16_f32 v158, v140, v141\n\t"
    "v_mfma_f32_16x16x32_bf16 v[20:23], a[180:183], v[160:163], v[20:23]\n\t"
    "v_exp_f32 v142, v142\n\t"
    "v_mfma_f32_16x16x32_bf16 v[24:27], a[152:155], v[144:147], v[24:27]\n\t"
    "v_add_f32 v221, v221, v141\n\t"
    "v_add_f32 v221, v221, v142\n\t"
    "v_mfma_f32_16x16x32_bf16 v[24:27], a[184:187], v[160:163], v[24:27]\n\t"
    "v_exp_f32 v143, v143\n\t"
    "v_mfma_f32_16x16x32_bf16 v[28:31], a[156:159], v[144:147], v[28:31]\n\t"
    "ds_read_b64_tr_b16 a[132:133], v229 offset:16384\n\t"
    "v_cvt_pk_bf16_f32 v159, v142, v143\n\t"
    "v_mfma_f32_16x16x32_bf16 v[28:31], a[188:191], v[160:163], v[28:31]\n\t"
    "s_add_u32 m0, s37, 49152\n\t"
    "ds_read_b64_tr_b16 a[134:135], v229 offset:20480\n\t"
    "v_add_f32 v221, v221, v143\n\t"
    "v_mfma_f32_16x16x32_bf16 v[136:139], a[64:67], a[16:19], v[196:199]\n\t"
    "v_exp_f32 v128, v128\n\t"
    "v_mfma_f32_16x16x32_bf16 v[136:139], a[68:71], a[20:23], v[136:139]\n\t"
    "buffer_load_dwordx4 v236, s[20:23], s36 offen offset:0  lds\n\t"
    "v_mfma_f32_16x16x32_bf16 v[136:139], a[72:75], a[24:27], v[136:139]\n\t"
    "v_exp_f32 v129, v129\n\t"
    "v_mfma_f32_16x16x32_bf16 v[136:139], a[76:79], a[28:31], v[136:139]\n\t"
    "ds_read_b64_tr_b16 a[136:137], v230 offset:16384\n\t"
    "v_cvt_pk_bf16_f32 v144, v128, v129\n\t"
    "v_mfma_f32_16x16x32_bf16 v[140:143], a[92:95], a[28:31], v[196:199]\n\t"
    "v_exp_f32 v130, v130\n\t"
    "v_mfma_f32_16x16x32_bf16 v[140:143], a[88:91], a[24:27], v[140:143]\n\t"
    "buffer_load_dwordx4 v237, s[20:23], s36 offen offset:1024  lds\n\t"
    "v_mfma_f32_16x16x32_bf16 v[140:143], a[84:87], a[20:23], v[140:143]\n\t"
    "v_exp_f32 v131, v131\n\t"
    "v_mfma_f32_16x16x32_bf16 v[140:143], a[80:83], a[16:19], v[140:143]\n\t"
    "ds_read_b64_tr_b16 a[138:139], v230 offset:20480\n\t"
    "v_cvt_pk_bf16_f32 v145, v130, v131\n\t"
    "v_mfma_f32_16x16x32_bf16 v[48:51], a[144:147], v[148:151], v[48:51]\n\t"
    "v_exp_f32 v132, v132\n\t"
    "v_mfma_f32_16x16x32_bf16 v[48:51], a[176:179], v[164:167], v[48:51]\n\t"
    "buffer_load_dwordx4 v238, s[20:23], s36 offen offset:2048  lds\n\t"
    "v_mfma_f32_16x16x32_bf16 v[52:55], a[148:151], v[148:151], v[52:55]\n\t"
    "v_exp_f32 v133, v133\n\t"
    "v_mfma_f32_16x16x32_bf16 v[52:55], a[180:183], v[164:167], v[52:55]\n\t"
    "ds_read_b64_tr_b16 a[140:141], v231 offset:16384\n\t"
    "v_cvt_pk_bf16_f32 v146, v132, v133\n\t"
    "v_mfma_f32_16x16x32_bf16 v[56:59], a[152:155], v[148:151], v[56:59]\n\t"
    "v_exp_f32 v134, v134\n\t"
    "v_mfma_f32_16x16x32_bf16 v[56:59], a[184:187], v[164:167], v[56:59]\n\t"
    "buffer_load_dwordx4 v239, s[20:23], s36 offen offset:3072  lds\n\t"
    "v_mfma_f32_16x16x32_bf16 v[60:63], a[156:159], v[148:151], v[60:63]\n\t"
    "v_exp_f32 v135, v135\n\t"
    "v_mfma_f32_16x16x32_bf16 v[60:63], a[188:191], v[164:167], v[60:63]\n\t"
    "ds_read_b64_tr_b16 a[142:143], v231 offset:20480\n\t"
    "v_cvt_pk_bf16_f32 v147, v134, v135\n\t"
    "v_mfma_f32_16x16x32_bf16 v[128:131], a[96:99], a[0:3], v[192:195]\n\t"
    "v_exp_f32 v136, v136\n\t"
    "v_mfma_f32_16x16x32_bf16 v[128:131], a[100:103], a[4:7], v[128:131]\n\t"
    "ds_read_b64_tr_b16 a[144:145], v232 offset:16384\n\t"
    "v_add_f32 v213, v213, v136\n\t"
    "v_mfma_f32_16x16x32_bf16 v[128:131], a[104:107], a[8:11], v[128:131]\n\t"
    "v_exp_f32 v137, v137\n\t"
    "v_mfma_f32_16x16x32_bf16 v[128:131], a[108:111], a[12:15], v[128:131]\n\t"
    "ds_read_b64_tr_b16 a[146:147], v232 offset:20480\n\t"
    "v_cvt_pk_bf16_f32 v148, v136, v137\n\t"
    "v_mfma_f32_16x16x32_bf16 v[132:135], a[124:127], a[12:15], v[192:195]\n\t"
    "v_exp_f32 v138, v138\n\t"
    "v_mfma_f32_16x16x32_bf16 v[132:135], a[120:123], a[8:11], v[132:135]\n\t"
    "v_add_f32 v213, v213, v137\n\t"
    "v_add_f32 v213, v213, v138\n\t"
    "v_mfma_f32_16x16x32_bf16 v[132:135], a[116:119], a[4:7], v[132:135]\n\t"
    "v_exp_f32 v139, v139\n\t"
    "v_mfma_f32_16x16x32_bf16 v[132:135], a[112:115], a[0:3], v[132:135]\n\t"
    "ds_read_b64_tr_b16 a[148:149], v233 offset:16384\n\t"
    "v_cvt_pk_bf16_f32 v149, v138, v139\n\t"
    "v_mfma_f32_16x16x32_bf16 v[216:219], a[192:195], v[168:171], v[216:219]\n\t"
    "v_exp_f32 v140, v140\n\t"
    "v_mfma_f32_16x16x32_bf16 v[64:67], a[160:163], v[168:171], v[64:67]\n\t"
    "s_waitcnt lgkmcnt(7)\n\t"
    "v_add_f32 v213, v213, v139\n\t"
    "v_add_f32 v213, v213, v140\n\t"
    "v_mfma_f32_16x16x32_bf16 v[64:67], a[128:131], v[152:155], v[64:67]\n\t"
    "v_exp_f32 v141, v141\n\t"
    "v_mfma_f32_16x16x32_bf16 v[68:71], a[164:167], v[168:171], v[68:71]\n\t"
    "ds_read_b64_tr_b16 a[150:151], v233 offset:20480\n\t"
    "v_cvt_pk_bf16_f32 v150, v140, v141\n\t"
    "v_mfma_f32_16x16x32_bf16 v[68:71], a[132:135], v[152:155], v[68:71]\n\t"
    "v_exp_f32 v142, v142\n\t"
    "v_mfma_f32_16x16x32_bf16 v[72:75], a[168:171], v[168:171], v[72:75]\n\t"
    "s_waitcnt lgkmcnt(4)\n\t"
    "v_add_f32 v213, v213, v141\n\t"
    "v_add_f32 v213, v213, v142\n\t"
    "v_mfma_f32_16x16x32_bf16 v[72:75], a[136:139], v[152:155], v[72:75]\n\t"
    "v_exp_f32 v143, v143\n\t"
    "v_mfma_f32_16x16x32_bf16 v[76:79], a[172:175], v[168:171], v[76:79]\n\t"
    "ds_read_b64_tr_b16 a[152:153], v234 offset:16384\n\t"
    "v_cvt_pk_bf16_f32 v151, v142, v143\n\t"
    "v_mfma_f32_16x16x32_bf16 v[76:79], a[140:143], v[152:155], v[76:79]\n\t"
    "ds_read_b64_tr_b16 a[154:155], v234 offset:20480\n\t"
    "v_add_f32 v213, v213, v143\n\t"
    "v_mfma_f32_16x16x32_bf16 v[136:139], a[96:99], a[16:19], v[196:199]\n\t"
    "v_exp_f32 v128, v128\n\t"
    "v_mfma_f32_16x16x32_bf16 v[136:139], a[100:103], a[20:23], v[136:139]\n\t"
    "ds_read_b64_tr_b16 a[156:157], v235 offset:16384\n\t"
    "ds_read_b64_tr_b16 a[158:159], v235 offset:20480\n\t"
    "v_mfma_f32_16x16x32_bf16 v[136:139], a[104:107], a[24:27], v[136:139]\n\t"
    "v_exp_f32 v129, v129\n\t"
    "v_mfma_f32_16x16x32_bf16 v[136:139], a[108:111], a[28:31], v[136:139]\n\t"
    "ds_read_b128 a[64:67], v224 offset:0\n\t"
    "v_cvt_pk_bf16_f32 v160, v128, v129\n\t"
    "v_mfma_f32_16x16x32_bf16 v[140:143], a[124:127], a[28:31], v[196:199]\n\t"
    "v_exp_f32 v130, v130\n\t"
    "v_mfma_f32_16x16x32_bf16 v[140:143], a[120:123], a[24:27], v[140:143]\n\t"
    "ds_read_b128 a[68:71], v225 offset:0\n\t"
    "ds_read_b128 a[72:75], v226 offset:0\n\t"
    "v_mfma_f32_16x16x32_bf16 v[140:143], a[116:119], a[20:23], v[140:143]\n\t"
    "v_exp_f32 v131, v131\n\t"
    "v_mfma_f32_16x16x32_bf16 v[140:143], a[112:115], a[16:19], v[140:143]\n\t"
    "ds_read_b128 a[76:79], v227 offset:0\n\t"
    "v_cvt_pk_bf16_f32 v161, v130, v131\n\t"
    "v_mfma_f32_16x16x32_bf16 v[96:99], a[160:163], v[172:175], v[96:99]\n\t"
    "v_exp_f32 v132, v132\n\t"
    "v_mfma_f32_16x16x32_bf16 v[96:99], a[128:131], v[156:159], v[96:99]\n\t"
    "ds_read_b128 a[80:83], v224 offset:4096\n\t"
    "ds_read_b128 a[84:87], v225 offset:4096\n\t"
    "v_mfma_f32_16x16x32_bf16 v[100:103], a[164:167], v[172:175], v[100:103]\n\t"
    "v_exp_f32 v133, v133\n\t"
    "v_mfma_f32_16x16x32_bf16 v[100:103], a[132:135], v[156:159], v[100:103]\n\t"
    "ds_read_b128 a[88:91], v226 offset:4096\n\t"
    "v_cvt_pk_bf16_f32 v162, v132, v133\n\t"
    "v_mfma_f32_16x16x32_bf16 v[104:107], a[168:171], v[172:175], v[104:107]\n\t"
    "s_waitcnt vmcnt(9)\n\t"
    "v_exp_f32 v134, v134\n\t"
    "v_mfma_f32_16x16x32_bf16 v[104:107], a[136:139], v[156:159], v[104:107]\n\t"
    "ds_read_b128 a[92:95], v227 offset:4096\n\t"
    "ds_read_b64_tr_b16 a[224:225], v252 offset:0\n\t"
    "v_mfma_f32_16x16x32_bf16 v[108:111], a[172:175], v[172:175], v[108:111]\n\t"
    "v_exp_f32 v135, v135\n\t"
    "v_mfma_f32_16x16x32_bf16 v[108:111], a[140:143], v[156:159], v[108:111]\n\t"
    "ds_read_b64_tr_b16 a[226:227], v253 offset:0\n\t"
    "v_cvt_pk_bf16_f32 v163, v134, v135\n\t"
    "v_mfma_f32_16x16x32_bf16 v[128:131], a[96:99], a[32:35], v[200:203]\n\t"
    "v_exp_f32 v136, v136\n\t"
    "v_mfma_f32_16x16x32_bf16 v[128:131], a[100:103], a[36:39], v[128:131]\n\t"
    "ds_read_b64_tr_b16 a[160:161], v228 offset:24576\n\t"
    "v_add_f32 v213, v213, v136\n\t"
    "v_mfma_f32_16x16x32_bf16 v[128:131], a[104:107], a[40:43], v[128:131]\n\t"
    "v_exp_f32 v137, v137\n\t"
    "v_mfma_f32_16x16x32_bf16 v[128:131], a[108:111], a[44:47], v[128:131]\n\t"
    "ds_read_b64_tr_b16 a[162:163], v228 offset:28672\n\t"
    "v_cvt_pk_bf16_f32 v164, v136, v137\n\t"
    "v_mfma_f32_16x16x32_bf16 v[132:135], a[124:127], a[44:47], v[200:203]\n\t"
    "v_exp_f32 v138, v138\n\t"
    "v_mfma_f32_16x16x32_bf16 v[132:135], a[120:123], a[40:43], v[132:135]\n\t"
    "v_add_f32 v213, v213, v137\n\t"
    "v_add_f32 v213, v213, v138\n\t"
    "v_mfma_f32_16x16x32_bf16 v[132:135], a[116:119], a[36:39], v[132:135]\n\t"
    "v_exp_f32 v139, v139\n\t"
    "v_mfma_f32_16x16x32_bf16 v[132:135], a[112:115], a[32:35], v[132:135]\n\t"
    "ds_read_b64_tr_b16 a[164:165], v229 offset:24576\n\t"
    "v_cvt_pk_bf16_f32 v165, v138, v139\n\t"
    "v_mfma_f32_16x16x32_bf16 v[216:219], a[192:195], v[152:155], v[216:219]\n\t"
    "v_exp_f32 v140, v140\n\t"
    "v_mfma_f32_16x16x32_bf16 v[80:83], a[176:179], v[168:171], v[80:83]\n\t"
    "s_waitcnt lgkmcnt(15)\n\t"
    "ds_read_b64_tr_b16 a[228:229], v252 offset:32\n\t"
    "v_add_f32 v213, v213, v139\n\t"
    "v_add_f32 v213, v213, v140\n\t"
    "v_mfma_f32_16x16x32_bf16 v[80:83], a[144:147], v[152:155], v[80:83]\n\t"
    "v_exp_f32 v141, v141\n\t"
    "v_mfma_f32_16x16x32_bf16 v[84:87], a[180:183], v[168:171], v[84:87]\n\t"
    "ds_read_b64_tr_b16 a[166:167], v229 offset:28672\n\t"
    "v_cvt_pk_bf16_f32 v166, v140, v141\n\t"
    "v_mfma_f32_16x16x32_bf16 v[84:87], a[148:151], v[152:155], v[84:87]\n\t"
    "v_exp_f32 v142, v142\n\t"
    "v_mfma_f32_16x16x32_bf16 v[88:91], a[184:187], v[168:171], v[88:91]\n\t"
    "ds_read_b64_tr_b16 a[230:231], v253 offset:32\n\t"
    "v_add_f32 v213, v213, v141\n\t"
    "v_add_f32 v213, v213, v142\n\t"
    "v_mfma_f32_16x16x32_bf16 v[88:91], a[152:155], v[152:155], v[88:91]\n\t"
    "v_exp_f32 v143, v143\n\t"
    "v_mfma_f32_16x16x32_bf16 v[92:95], a[188:191], v[168:171], v[92:95]\n\t"
    "s_waitcnt lgkmcnt(15)\n\t"
    "ds_read_b64_tr_b16 a[168:169], v230 offset:24576\n\t"
    "v_cvt_pk_bf16_f32 v167, v142, v143\n\t"
    "v_mfma_f32_16x16x32_bf16 v[92:95], a[156:159], v[152:155], v[92:95]\n\t"
    "s_add_u32 m0, s37, 65536\n\t"
    "ds_read_b64_tr_b16 a[170:171], v230 offset:28672\n\t"
    "v_add_f32 v213, v213, v143\n\t"
    "v_mfma_f32_16x16x32_bf16 v[136:139], a[96:99], v[176:179], v[204:207]\n\t"
    "v_exp_f32 v128, v128\n\t"
    "v_mfma_f32_16x16x32_bf16 v[136:139], a[100:103], v[180:183], v[136:139]\n\t"
    "buffer_load_dwordx4 v240, s[24:27], s36 offen offset:0  lds\n\t"
    "v_mfma_f32_16x16x32_bf16 v[136:139], a[104:107], v[184:187], v[136:139]\n\t"
    "v_exp_f32 v129, v129\n\t"
    "v_mfma_f32_16x16x32_bf16 v[136:139], a[108:111], v[188:191], v[136:139]\n\t"
    "ds_read_b64_tr_b16 a[172:173], v231 offset:24576\n\t"
    "v_cvt_pk_bf16_f32 v168, v128, v129\n\t"
    "v_mfma_f32_16x16x32_bf16 v[140:143], a[124:127], v[188:191], v[204:207]\n\t"
    "v_exp_f32 v130, v130\n\t"
    "v_mfma_f32_16x16x32_bf16 v[140:143], a[120:123], v[184:187], v[140:143]\n\t"
    "buffer_load_dwordx4 v241, s[24:27], s36 offen offset:1024  lds\n\t"
    "v_mfma_f32_16x16x32_bf16 v[140:143], a[116:119], v[180:183], v[140:143]\n\t"
    "v_exp_f32 v131, v131\n\t"
    "v_mfma_f32_16x16x32_bf16 v[140:143], a[112:115], v[176:179], v[140:143]\n\t"
    "ds_read_b64_tr_b16 a[174:175], v231 offset:28672\n\t"
    "v_cvt_pk_bf16_f32 v169, v130, v131\n\t"
    "v_mfma_f32_16x16x32_bf16 v[112:115], a[176:179], v[172:175], v[112:115]\n\t"
    "v_exp_f32 v132, v132\n\t"
    "v_mfma_f32_16x16x32_bf16 v[112:115], a[144:147], v[156:159], v[112:115]\n\t"
    "buffer_load_dwordx4 v240, s[24:27], s36 offen offset:2048  lds\n\t"
    "v_mfma_f32_16x16x32_bf16 v[116:119], a[180:183], v[172:175], v[116:119]\n\t"
    "v_exp_f32 v133, v133\n\t"
    "v_mfma_f32_16x16x32_bf16 v[116:119], a[148:151], v[156:159], v[116:119]\n\t"
    "ds_read_b64_tr_b16 a[232:233], v252 offset:64\n\t"
    "v_cvt_pk_bf16_f32 v170, v132, v133\n\t"
    "v_mfma_f32_16x16x32_bf16 v[120:123], a[184:187], v[172:175], v[120:123]\n\t"
    "v_exp_f32 v134, v134\n\t"
    "v_mfma_f32_16x16x32_bf16 v[120:123], a[152:155], v[156:159], v[120:123]\n\t"
    "buffer_load_dwordx4 v241, s[24:27], s36 offen offset:3072  lds\n\t"
    "v_mfma_f32_16x16x32_bf16 v[124:127], a[188:191], v[172:175], v[124:127]\n\t"
    "v_exp_f32 v135, v135\n\t"
    "v_mfma_f32_16x16x32_bf16 v[124:127], a[156:159], v[156:159], v[124:127]\n\t"
    "ds_read_b64_tr_b16 a[234:235], v253 offset:64\n\t"
    "v_cvt_pk_bf16_f32 v171, v134, v135\n\t"
    "v_mfma_f32_16x16x32_bf16 v[128:131], a[64:67], a[32:35], v[200:203]\n\t"
    "s_waitcnt lgkmcnt(14)\n\t"
    "v_exp_f32 v136, v136\n\t"
    "v_mfma_f32_16x16x32_bf16 v[128:131], a[68:71], a[36:39], v[128:131]\n\t"
    "s_add_u32 s36, s36, 16384\n\t"
    "ds_read_b64_tr_b16 a[176:177], v232 offset:24576\n\t"
    "v_add_f32 v221, v221, v136\n\t"
    "v_mfma_f32_16x16x32_bf16 v[128:131], a[72:75], a[40:43], v[128:131]\n\t"
    "v_exp_f32 v137, v137\n\t"
    "v_mfma_f32_16x16x32_bf16 v[128:131], a[76:79], a[44:47], v[128:131]\n\t"
    "ds_read_b64_tr_b16 a[178:179], v232 offset:28672\n\t"
    "v_cvt_pk_bf16_f32 v172, v136, v137\n\t"
    "v_mfma_f32_16x16x32_bf16 v[132:135], a[92:95], a[44:47], v[200:203]\n\t"
    "v_exp_f32 v138, v138\n\t"
    "v_mfma_f32_16x16x32_bf16 v[132:135], a[88:91], a[40:43], v[132:135]\n\t"
    "v_add_f32 v221, v221, v137\n\t"
    "v_add_f32 v221, v221, v138\n\t"
    "v_mfma_f32_16x16x32_bf16 v[132:135], a[84:87], a[36:39], v[132:135]\n\t"
    "v_exp_f32 v139, v139\n\t"
    "v_mfma_f32_16x16x32_bf16 v[132:135], a[80:83], a[32:35], v[132:135]\n\t"
    "ds_read_b64_tr_b16 a[180:181], v233 offset:24576\n\t"
    "v_cvt_pk_bf16_f32 v173, v138, v139\n\t"
    "v_mfma_f32_16x16x32_bf16 v[208:211], a[192:195], v[144:147], v[208:211]\n\t"
    "v_exp_f32 v140, v140\n\t"
    "v_mfma_f32_16x16x32_bf16 v[0:3], a[128:131], v[144:147], v[0:3]\n\t"
    "s_waitcnt lgkmcnt(10)\n\t"
    "ds_read_b64_tr_b16 a[236:237], v252 offset:96\n\t"
    "v_add_f32 v221, v221, v139\n\t"
    "v_add_f32 v221, v221, v140\n\t"
    "v_mfma_f32_16x16x32_bf16 v[0:3], a[160:163], v[160:163], v[0:3]\n\t"
    "v_exp_f32 v141, v141\n\t"
    "v_mfma_f32_16x16x32_bf16 v[4:7], a[132:135], v[144:147], v[4:7]\n\t"
    "ds_read_b64_tr_b16 a[182:183], v233 offset:28672\n\t"
    "v_cvt_pk_bf16_f32 v174, v140, v141\n\t"
    "v_mfma_f32_16x16x32_bf16 v[4:7], a[164:167], v[160:163], v[4:7]\n\t"
    "v_exp_f32 v142, v142\n\t"
    "v_mfma_f32_16x16x32_bf16 v[8:11], a[136:139], v[144:147], v[8:11]\n\t"
    "s_waitcnt lgkmcnt(7)\n\t"
    "ds_read_b64_tr_b16 a[238:239], v253 offset:96\n\t"
    "v_add_f32 v221, v221, v141\n\t"
    "v_add_f32 v221, v221, v142\n\t"
    "v_mfma_f32_16x16x32_bf16 v[8:11], a[168:171], v[160:163], v[8:11]\n\t"
    "v_exp_f32 v143, v143\n\t"
    "v_mfma_f32_16x16x32_bf16 v[12:15], a[140:143], v[144:147], v[12:15]\n\t"
    "ds_read_b64_tr_b16 a[184:185], v234 offset:24576\n\t"
    "v_cvt_pk_bf16_f32 v175, v142, v143\n\t"
    "v_mfma_f32_16x16x32_bf16 v[12:15], a[172:175], v[160:163], v[12:15]\n\t"
    "ds_read_b64_tr_b16 a[186:187], v234 offset:28672\n\t"
    "v_add_f32 v221, v221, v143\n\t"
    "v_mfma_f32_16x16x32_bf16 v[136:139], a[64:67], v[176:179], v[204:207]\n\t"
    "v_exp_f32 v128, v128\n\t"
    "v_mfma_f32_16x16x32_bf16 v[136:139], a[68:71], v[180:183], v[136:139]\n\t"
    "ds_read_b64_tr_b16 a[188:189], v235 offset:24576\n\t"
    "ds_read_b64_tr_b16 a[190:191], v235 offset:28672\n\t"
    "v_mfma_f32_16x16x32_bf16 v[136:139], a[72:75], v[184:187], v[136:139]\n\t"
    "v_exp_f32 v129, v129\n\t"
    "v_mfma_f32_16x16x32_bf16 v[136:139], a[76:79], v[188:191], v[136:139]\n\t"
    "ds_read_b128 a[96:99], v224 offset:8192\n\t"
    "v_cvt_pk_bf16_f32 v152, v128, v129\n\t"
    "v_mfma_f32_16x16x32_bf16 v[140:143], a[92:95], v[188:191], v[204:207]\n\t"
    "v_exp_f32 v130, v130\n\t"
    "v_mfma_f32_16x16x32_bf16 v[140:143], a[88:91], v[184:187], v[140:143]\n\t"
    "ds_read_b128 a[100:103], v225 offset:8192\n\t"
    "ds_read_b128 a[104:107], v226 offset:8192\n\t"
    "v_mfma_f32_16x16x32_bf16 v[140:143], a[84:87], v[180:183], v[140:143]\n\t"
    "v_exp_f32 v131, v131\n\t"
    "v_mfma_f32_16x16x32_bf16 v[140:143], a[80:83], v[176:179], v[140:143]\n\t"
    "ds_read_b128 a[108:111], v227 offset:8192\n\t"
    "v_cvt_pk_bf16_f32 v153, v130, v131\n\t"
    "v_mfma_f32_16x16x32_bf16 v[32:35], a[128:131], v[148:151], v[32:35]\n\t"
    "v_exp_f32 v132, v132\n\t"
    "v_mfma_f32_16x16x32_bf16 v[32:35], a[160:163], v[164:167], v[32:35]\n\t"
    "ds_read_b128 a[112:115], v224 offset:12288\n\t"
    "ds_read_b128 a[116:119], v225 offset:12288\n\t"
    "v_mfma_f32_16x16x32_bf16 v[36:39], a[132:135], v[148:151], v[36:39]\n\t"
    "v_exp_f32 v133, v133\n\t"
    "v_mfma_f32_16x16x32_bf16 v[36:39], a[164:167], v[164:167], v[36:39]\n\t"
    "ds_read_b128 a[120:123], v226 offset:12288\n\t"
    "v_cvt_pk_bf16_f32 v154, v132, v133\n\t"
    "v_mfma_f32_16x16x32_bf16 v[40:43], a[136:139], v[148:151], v[40:43]\n\t"
    "v_exp_f32 v134, v134\n\t"
    "v_mfma_f32_16x16x32_bf16 v[40:43], a[168:171], v[164:167], v[40:43]\n\t"
    "ds_read_b128 a[124:127], v227 offset:12288\n\t"
    "v_mfma_f32_16x16x32_bf16 v[44:47], a[140:143], v[148:151], v[44:47]\n\t"
    "v_exp_f32 v135, v135\n\t"
    "v_mfma_f32_16x16x32_bf16 v[44:47], a[172:175], v[164:167], v[44:47]\n\t"
    "buffer_store_dwordx4 a[224:227], v254, s[28:31], s11 offen offset:0 sc1\n\t"
    "v_cvt_pk_bf16_f32 v155, v134, v135\n\t"
    "v_mfma_f32_16x16x32_bf16 v[128:131], a[64:67], a[0:3], v[192:195]\n\t"
    "v_exp_f32 v136, v136\n\t"
    "v_mfma_f32_16x16x32_bf16 v[128:131], a[68:71], a[4:7], v[128:131]\n\t"
    "v_add_f32 v221, v221, v136\n\t"
    "v_mfma_f32_16x16x32_bf16 v[128:131], a[72:75], a[8:11], v[128:131]\n\t"
    "s_waitcnt vmcnt(13) lgkmcnt(0)\n\t"
    "v_exp_f32 v137, v137\n\t"
    "v_mfma_f32_16x16x32_bf16 v[128:131], a[76:79], a[12:15], v[128:131]\n\t"
    "s_barrier\n\t"
    "v_cvt_pk_bf16_f32 v156, v136, v137\n\t"
    "v_mfma_f32_16x16x32_bf16 v[132:135], a[92:95], a[12:15], v[192:195]\n\t"
    "v_exp_f32 v138, v138\n\t"
    "v_mfma_f32_16x16x32_bf16 v[132:135], a[88:91], a[8:11], v[132:135]\n\t"
    "v_add_f32 v221, v221, v137\n\t"
    "v_add_f32 v221, v221, v138\n\t"
    "v_mfma_f32_16x16x32_bf16 v[132:135], a[84:87], a[4:7], v[132:135]\n\t"
    "v_exp_f32 v139, v139\n\t"
    "v_mfma_f32_16x16x32_bf16 v[132:135], a[80:83], a[0:3], v[132:135]\n\t"
    "ds_read_b64_tr_b16 a[128:129], v228 offset:32768\n\t"
    "v_cvt_pk_bf16_f32 v157, v138, v139\n\t"
    "v_mfma_f32_16x16x32_bf16 v[208:211], a[192:195], v[160:163], v[208:211]\n\t"
    "s_lshl1_add_u32 m0, s37, 131072\n\t"
    "v_exp_f32 v140, v140\n\t"
    "v_mfma_f32_16x16x32_bf16 v[16:19], a[144:147], v[144:147], v[16:19]\n\t"
    "buffer_load_dwordx4 v244, s[44:47], s42 offen offset:0  lds\n\t"
    "v_add_f32 v221, v221, v139\n\t"
    "v_add_f32 v221, v221, v140\n\t"
    "v_mfma_f32_16x16x32_bf16 v[16:19], a[176:179], v[160:163], v[16:19]\n\t"
    "v_exp_f32 v141, v141\n\t"
    "v_mfma_f32_16x16x32_bf16 v[20:23], a[148:151], v[144:147], v[20:23]\n\t"
    "ds_read_b64_tr_b16 a[130:131], v228 offset:36864\n\t"
    "v_cvt_pk_bf16_f32 v158, v140, v141\n\t"
    "v_mfma_f32_16x16x32_bf16 v[20:23], a[180:183], v[160:163], v[20:23]\n\t"
    "v_exp_f32 v142, v142\n\t"
    "v_mfma_f32_16x16x32_bf16 v[24:27], a[152:155], v[144:147], v[24:27]\n\t"
    "v_add_f32 v221, v221, v141\n\t"
    "v_add_f32 v221, v221, v142\n\t"
    "v_mfma_f32_16x16x32_bf16 v[24:27], a[184:187], v[160:163], v[24:27]\n\t"
    "v_exp_f32 v143, v143\n\t"
    "v_mfma_f32_16x16x32_bf16 v[28:31], a[156:159], v[144:147], v[28:31]\n\t"
    "ds_read_b64_tr_b16 a[132:133], v229 offset:32768\n\t"
    "v_cvt_pk_bf16_f32 v159, v142, v143\n\t"
    "v_mfma_f32_16x16x32_bf16 v[28:31], a[188:191], v[160:163], v[28:31]\n\t"
    "s_add_u32 m0, s37, 0\n\t"
    "ds_read_b64_tr_b16 a[134:135], v229 offset:36864\n\t"
    "v_add_f32 v221, v221, v143\n\t"
    "v_mfma_f32_16x16x32_bf16 v[136:139], a[64:67], a[16:19], v[196:199]\n\t"
    "v_exp_f32 v128, v128\n\t"
    "v_mfma_f32_16x16x32_bf16 v[136:139], a[68:71], a[20:23], v[136:139]\n\t"
    "buffer_load_dwordx4 v236, s[20:23], s36 offen offset:0  lds\n\t"
    "v_mfma_f32_16x16x32_bf16 v[136:139], a[72:75], a[24:27], v[136:139]\n\t"
    "v_exp_f32 v129, v129\n\t"
    "v_mfma_f32_16x16x32_bf16 v[136:139], a[76:79], a[28:31], v[136:139]\n\t"
    "ds_read_b64_tr_b16 a[136:137], v230 offset:32768\n\t"
    "v_cvt_pk_bf16_f32 v144, v128, v129\n\t"
    "v_mfma_f32_16x16x32_bf16 v[140:143], a[92:95], a[28:31], v[196:199]\n\t"
    "v_exp_f32 v130, v130\n\t"
    "v_mfma_f32_16x16x32_bf16 v[140:143], a[88:91], a[24:27], v[140:143]\n\t"
    "buffer_load_dwordx4 v237, s[20:23], s36 offen offset:1024  lds\n\t"
    "v_mfma_f32_16x16x32_bf16 v[140:143], a[84:87], a[20:23], v[140:143]\n\t"
    "v_exp_f32 v131, v131\n\t"
    "v_mfma_f32_16x16x32_bf16 v[140:143], a[80:83], a[16:19], v[140:143]\n\t"
    "ds_read_b64_tr_b16 a[138:139], v230 offset:36864\n\t"
    "v_cvt_pk_bf16_f32 v145, v130, v131\n\t"
    "v_mfma_f32_16x16x32_bf16 v[48:51], a[144:147], v[148:151], v[48:51]\n\t"
    "v_exp_f32 v132, v132\n\t"
    "v_mfma_f32_16x16x32_bf16 v[48:51], a[176:179], v[164:167], v[48:51]\n\t"
    "buffer_load_dwordx4 v238, s[20:23], s36 offen offset:2048  lds\n\t"
    "v_mfma_f32_16x16x32_bf16 v[52:55], a[148:151], v[148:151], v[52:55]\n\t"
    "v_exp_f32 v133, v133\n\t"
    "v_mfma_f32_16x16x32_bf16 v[52:55], a[180:183], v[164:167], v[52:55]\n\t"
    "ds_read_b64_tr_b16 a[140:141], v231 offset:32768\n\t"
    "v_cvt_pk_bf16_f32 v146, v132, v133\n\t"
    "v_mfma_f32_16x16x32_bf16 v[56:59], a[152:155], v[148:151], v[56:59]\n\t"
    "v_exp_f32 v134, v134\n\t"
    "v_mfma_f32_16x16x32_bf16 v[56:59], a[184:187], v[164:167], v[56:59]\n\t"
    "buffer_load_dwordx4 v239, s[20:23], s36 offen offset:3072  lds\n\t"
    "v_mfma_f32_16x16x32_bf16 v[60:63], a[156:159], v[148:151], v[60:63]\n\t"
    "v_exp_f32 v135, v135\n\t"
    "v_mfma_f32_16x16x32_bf16 v[60:63], a[188:191], v[164:167], v[60:63]\n\t"
    "ds_read_b64_tr_b16 a[142:143], v231 offset:36864\n\t"
    "v_cvt_pk_bf16_f32 v147, v134, v135\n\t"
    "v_mfma_f32_16x16x32_bf16 v[128:131], a[96:99], a[0:3], v[192:195]\n\t"
    "v_exp_f32 v136, v136\n\t"
    "v_mfma_f32_16x16x32_bf16 v[128:131], a[100:103], a[4:7], v[128:131]\n\t"
    "ds_read_b64_tr_b16 a[144:145], v232 offset:32768\n\t"
    "v_add_f32 v213, v213, v136\n\t"
    "v_mfma_f32_16x16x32_bf16 v[128:131], a[104:107], a[8:11], v[128:131]\n\t"
    "v_exp_f32 v137, v137\n\t"
    "v_mfma_f32_16x16x32_bf16 v[128:131], a[108:111], a[12:15], v[128:131]\n\t"
    "ds_read_b64_tr_b16 a[146:147], v232 offset:36864\n\t"
    "v_cvt_pk_bf16_f32 v148, v136, v137\n\t"
    "v_mfma_f32_16x16x32_bf16 v[132:135], a[124:127], a[12:15], v[192:195]\n\t"
    "v_exp_f32 v138, v138\n\t"
    "v_mfma_f32_16x16x32_bf16 v[132:135], a[120:123], a[8:11], v[132:135]\n\t"
    "v_add_f32 v213, v213, v137\n\t"
    "v_add_f32 v213, v213, v138\n\t"
    "v_mfma_f32_16x16x32_bf16 v[132:135], a[116:119], a[4:7], v[132:135]\n\t"
    "v_exp_f32 v139, v139\n\t"
    "v_mfma_f32_16x16x32_bf16 v[132:135], a[112:115], a[0:3], v[132:135]\n\t"
    "ds_read_b64_tr_b16 a[148:149], v233 offset:32768\n\t"
    "v_cvt_pk_bf16_f32 v149, v138, v139\n\t"
    "v_mfma_f32_16x16x32_bf16 v[216:219], a[192:195], v[168:171], v[216:219]\n\t"
    "v_exp_f32 v140, v140\n\t"
    "v_mfma_f32_16x16x32_bf16 v[64:67], a[160:163], v[168:171], v[64:67]\n\t"
    "s_waitcnt lgkmcnt(7)\n\t"
    "v_add_f32 v213, v213, v139\n\t"
    "v_add_f32 v213, v213, v140\n\t"
    "v_mfma_f32_16x16x32_bf16 v[64:67], a[128:131], v[152:155], v[64:67]\n\t"
    "v_exp_f32 v141, v141\n\t"
    "v_mfma_f32_16x16x32_bf16 v[68:71], a[164:167], v[168:171], v[68:71]\n\t"
    "ds_read_b64_tr_b16 a[150:151], v233 offset:36864\n\t"
    "v_cvt_pk_bf16_f32 v150, v140, v141\n\t"
    "v_mfma_f32_16x16x32_bf16 v[68:71], a[132:135], v[152:155], v[68:71]\n\t"
    "v_exp_f32 v142, v142\n\t"
    "v_mfma_f32_16x16x32_bf16 v[72:75], a[168:171], v[168:171], v[72:75]\n\t"
    "s_waitcnt lgkmcnt(4)\n\t"
    "v_add_f32 v213, v213, v141\n\t"
    "v_add_f32 v213, v213, v142\n\t"
    "v_mfma_f32_16x16x32_bf16 v[72:75], a[136:139], v[152:155], v[72:75]\n\t"
    "v_exp_f32 v143, v143\n\t"
    "v_mfma_f32_16x16x32_bf16 v[76:79], a[172:175], v[168:171], v[76:79]\n\t"
    "ds_read_b64_tr_b16 a[152:153], v234 offset:32768\n\t"
    "v_cvt_pk_bf16_f32 v151, v142, v143\n\t"
    "v_mfma_f32_16x16x32_bf16 v[76:79], a[140:143], v[152:155], v[76:79]\n\t"
    "ds_read_b64_tr_b16 a[154:155], v234 offset:36864\n\t"
    "v_add_f32 v213, v213, v143\n\t"
    "v_mfma_f32_16x16x32_bf16 v[136:139], a[96:99], a[16:19], v[196:199]\n\t"
    "v_exp_f32 v128, v128\n\t"
    "v_mfma_f32_16x16x32_bf16 v[136:139], a[100:103], a[20:23], v[136:139]\n\t"
    "ds_read_b64_tr_b16 a[156:157], v235 offset:32768\n\t"
    "ds_read_b64_tr_b16 a[158:159], v235 offset:36864\n\t"
    "v_mfma_f32_16x16x32_bf16 v[136:139], a[104:107], a[24:27], v[136:139]\n\t"
    "v_exp_f32 v129, v129\n\t"
    "v_mfma_f32_16x16x32_bf16 v[136:139], a[108:111], a[28:31], v[136:139]\n\t"
    "ds_read_b128 a[64:67], v224 offset:16384\n\t"
    "v_cvt_pk_bf16_f32 v160, v128, v129\n\t"
    "v_mfma_f32_16x16x32_bf16 v[140:143], a[124:127], a[28:31], v[196:199]\n\t"
    "v_exp_f32 v130, v130\n\t"
    "v_mfma_f32_16x16x32_bf16 v[140:143], a[120:123], a[24:27], v[140:143]\n\t"
    "ds_read_b128 a[68:71], v225 offset:16384\n\t"
    "ds_read_b128 a[72:75], v226 offset:16384\n\t"
    "v_mfma_f32_16x16x32_bf16 v[140:143], a[116:119], a[20:23], v[140:143]\n\t"
    "v_exp_f32 v131, v131\n\t"
    "v_mfma_f32_16x16x32_bf16 v[140:143], a[112:115], a[16:19], v[140:143]\n\t"
    "ds_read_b128 a[76:79], v227 offset:16384\n\t"
    "v_cvt_pk_bf16_f32 v161, v130, v131\n\t"
    "v_mfma_f32_16x16x32_bf16 v[96:99], a[160:163], v[172:175], v[96:99]\n\t"
    "v_exp_f32 v132, v132\n\t"
    "v_mfma_f32_16x16x32_bf16 v[96:99], a[128:131], v[156:159], v[96:99]\n\t"
    "ds_read_b128 a[80:83], v224 offset:20480\n\t"
    "ds_read_b128 a[84:87], v225 offset:20480\n\t"
    "v_mfma_f32_16x16x32_bf16 v[100:103], a[164:167], v[172:175], v[100:103]\n\t"
    "v_exp_f32 v133, v133\n\t"
    "v_mfma_f32_16x16x32_bf16 v[100:103], a[132:135], v[156:159], v[100:103]\n\t"
    "ds_read_b128 a[88:91], v226 offset:20480\n\t"
    "v_cvt_pk_bf16_f32 v162, v132, v133\n\t"
    "v_mfma_f32_16x16x32_bf16 v[104:107], a[168:171], v[172:175], v[104:107]\n\t"
    "v_exp_f32 v134, v134\n\t"
    "v_mfma_f32_16x16x32_bf16 v[104:107], a[136:139], v[156:159], v[104:107]\n\t"
    "ds_read_b128 a[92:95], v227 offset:20480\n\t"
    "v_mfma_f32_16x16x32_bf16 v[108:111], a[172:175], v[172:175], v[108:111]\n\t"
    "s_lshl1_add_u32 m0, s37, 131072\n\t"
    "v_exp_f32 v135, v135\n\t"
    "v_mfma_f32_16x16x32_bf16 v[108:111], a[140:143], v[156:159], v[108:111]\n\t"
    "s_add_u32 s76, s42, s75\n\t"
    "buffer_load_dwordx4 v244, s[44:47], s76 offen offset:1024  lds\n\t"
    "v_cvt_pk_bf16_f32 v163, v134, v135\n\t"
    "v_mfma_f32_16x16x32_bf16 v[128:131], a[96:99], a[32:35], v[200:203]\n\t"
    "v_exp_f32 v136, v136\n\t"
    "v_mfma_f32_16x16x32_bf16 v[128:131], a[100:103], a[36:39], v[128:131]\n\t"
    "ds_read_b64_tr_b16 a[160:161], v228 offset:40960\n\t"
    "v_add_f32 v213, v213, v136\n\t"
    "v_mfma_f32_16x16x32_bf16 v[128:131], a[104:107], a[40:43], v[128:131]\n\t"
    "v_exp_f32 v137, v137\n\t"
    "v_mfma_f32_16x16x32_bf16 v[128:131], a[108:111], a[44:47], v[128:131]\n\t"
    "ds_read_b64_tr_b16 a[162:163], v228 offset:45056\n\t"
    "v_cvt_pk_bf16_f32 v164, v136, v137\n\t"
    "v_mfma_f32_16x16x32_bf16 v[132:135], a[124:127], a[44:47], v[200:203]\n\t"
    "v_exp_f32 v138, v138\n\t"
    "v_mfma_f32_16x16x32_bf16 v[132:135], a[120:123], a[40:43], v[132:135]\n\t"
    "v_add_f32 v213, v213, v137\n\t"
    "v_add_f32 v213, v213, v138\n\t"
    "v_mfma_f32_16x16x32_bf16 v[132:135], a[116:119], a[36:39], v[132:135]\n\t"
    "v_exp_f32 v139, v139\n\t"
    "v_mfma_f32_16x16x32_bf16 v[132:135], a[112:115], a[32:35], v[132:135]\n\t"
    "ds_read_b64_tr_b16 a[164:165], v229 offset:40960\n\t"
    "v_cvt_pk_bf16_f32 v165, v138, v139\n\t"
    "v_mfma_f32_16x16x32_bf16 v[216:219], a[192:195], v[152:155], v[216:219]\n\t"
    "v_exp_f32 v140, v140\n\t"
    "v_mfma_f32_16x16x32_bf16 v[80:83], a[176:179], v[168:171], v[80:83]\n\t"
    "s_waitcnt lgkmcnt(15)\n\t"
    "v_add_f32 v213, v213, v139\n\t"
    "v_add_f32 v213, v213, v140\n\t"
    "v_mfma_f32_16x16x32_bf16 v[80:83], a[144:147], v[152:155], v[80:83]\n\t"
    "v_exp_f32 v141, v141\n\t"
    "v_mfma_f32_16x16x32_bf16 v[84:87], a[180:183], v[168:171], v[84:87]\n\t"
    "ds_read_b64_tr_b16 a[166:167], v229 offset:45056\n\t"
    "v_cvt_pk_bf16_f32 v166, v140, v141\n\t"
    "v_mfma_f32_16x16x32_bf16 v[84:87], a[148:151], v[152:155], v[84:87]\n\t"
    "v_exp_f32 v142, v142\n\t"
    "v_mfma_f32_16x16x32_bf16 v[88:91], a[184:187], v[168:171], v[88:91]\n\t"
    "s_waitcnt lgkmcnt(12)\n\t"
    "v_add_f32 v213, v213, v141\n\t"
    "v_add_f32 v213, v213, v142\n\t"
    "v_mfma_f32_16x16x32_bf16 v[88:91], a[152:155], v[152:155], v[88:91]\n\t"
    "v_exp_f32 v143, v143\n\t"
    "v_mfma_f32_16x16x32_bf16 v[92:95], a[188:191], v[168:171], v[92:95]\n\t"
    "ds_read_b64_tr_b16 a[168:169], v230 offset:40960\n\t"
    "v_cvt_pk_bf16_f32 v167, v142, v143\n\t"
    "v_mfma_f32_16x16x32_bf16 v[92:95], a[156:159], v[152:155], v[92:95]\n\t"
    "s_add_u32 m0, s37, 81920\n\t"
    "ds_read_b64_tr_b16 a[170:171], v230 offset:45056\n\t"
    "v_add_f32 v213, v213, v143\n\t"
    "v_mfma_f32_16x16x32_bf16 v[136:139], a[96:99], v[176:179], v[204:207]\n\t"
    "v_exp_f32 v128, v128\n\t"
    "v_mfma_f32_16x16x32_bf16 v[136:139], a[100:103], v[180:183], v[136:139]\n\t"
    "buffer_load_dwordx4 v240, s[24:27], s36 offen offset:0  lds\n\t"
    "v_mfma_f32_16x16x32_bf16 v[136:139], a[104:107], v[184:187], v[136:139]\n\t"
    "v_exp_f32 v129, v129\n\t"
    "v_mfma_f32_16x16x32_bf16 v[136:139], a[108:111], v[188:191], v[136:139]\n\t"
    "ds_read_b64_tr_b16 a[172:173], v231 offset:40960\n\t"
    "v_cvt_pk_bf16_f32 v168, v128, v129\n\t"
    "v_mfma_f32_16x16x32_bf16 v[140:143], a[124:127], v[188:191], v[204:207]\n\t"
    "v_exp_f32 v130, v130\n\t"
    "v_mfma_f32_16x16x32_bf16 v[140:143], a[120:123], v[184:187], v[140:143]\n\t"
    "buffer_load_dwordx4 v241, s[24:27], s36 offen offset:1024  lds\n\t"
    "v_mfma_f32_16x16x32_bf16 v[140:143], a[116:119], v[180:183], v[140:143]\n\t"
    "v_exp_f32 v131, v131\n\t"
    "v_mfma_f32_16x16x32_bf16 v[140:143], a[112:115], v[176:179], v[140:143]\n\t"
    "ds_read_b64_tr_b16 a[174:175], v231 offset:45056\n\t"
    "v_cvt_pk_bf16_f32 v169, v130, v131\n\t"
    "v_mfma_f32_16x16x32_bf16 v[112:115], a[176:179], v[172:175], v[112:115]\n\t"
    "v_exp_f32 v132, v132\n\t"
    "v_mfma_f32_16x16x32_bf16 v[112:115], a[144:147], v[156:159], v[112:115]\n\t"
    "buffer_load_dwordx4 v240, s[24:27], s36 offen offset:2048  lds\n\t"
    "v_mfma_f32_16x16x32_bf16 v[116:119], a[180:183], v[172:175], v[116:119]\n\t"
    "v_exp_f32 v133, v133\n\t"
    "v_mfma_f32_16x16x32_bf16 v[116:119], a[148:151], v[156:159], v[116:119]\n\t"
    "ds_read_b64_tr_b16 a[240:241], v243 offset:4096\n\t"
    "v_cvt_pk_bf16_f32 v170, v132, v133\n\t"
    "v_mfma_f32_16x16x32_bf16 v[120:123], a[184:187], v[172:175], v[120:123]\n\t"
    "v_exp_f32 v134, v134\n\t"
    "v_mfma_f32_16x16x32_bf16 v[120:123], a[152:155], v[156:159], v[120:123]\n\t"
    "buffer_load_dwordx4 v241, s[24:27], s36 offen offset:3072  lds\n\t"
    "v_mfma_f32_16x16x32_bf16 v[124:127], a[188:191], v[172:175], v[124:127]\n\t"
    "v_exp_f32 v135, v135\n\t"
    "v_mfma_f32_16x16x32_bf16 v[124:127], a[156:159], v[156:159], v[124:127]\n\t"
    "s_waitcnt lgkmcnt(13)\n\t"
    "ds_read_b64_tr_b16 a[242:243], v243 offset:4608\n\t"
    "v_cvt_pk_bf16_f32 v171, v134, v135\n\t"
    "v_mfma_f32_16x16x32_bf16 v[128:131], a[64:67], a[32:35], v[200:203]\n\t"
    "v_exp_f32 v136, v136\n\t"
    "v_mfma_f32_16x16x32_bf16 v[128:131], a[68:71], a[36:39], v[128:131]\n\t"
    "s_add_u32 s36, s36, 16384\n\t"
    "ds_read_b64_tr_b16 a[176:177], v232 offset:40960\n\t"
    "v_add_f32 v221, v221, v136\n\t"
    "v_mfma_f32_16x16x32_bf16 v[128:131], a[72:75], a[40:43], v[128:131]\n\t"
    "v_exp_f32 v137, v137\n\t"
    "v_mfma_f32_16x16x32_bf16 v[128:131], a[76:79], a[44:47], v[128:131]\n\t"
    "s_waitcnt lgkmcnt(11)\n\t"
    "ds_read_b64_tr_b16 a[178:179], v232 offset:45056\n\t"
    "v_cvt_pk_bf16_f32 v172, v136, v137\n\t"
    "v_mfma_f32_16x16x32_bf16 v[132:135], a[92:95], a[44:47], v[200:203]\n\t"
    "v_exp_f32 v138, v138\n\t"
    "v_mfma_f32_16x16x32_bf16 v[132:135], a[88:91], a[40:43], v[132:135]\n\t"
    "v_add_f32 v221, v221, v137\n\t"
    "v_add_f32 v221, v221, v138\n\t"
    "v_mfma_f32_16x16x32_bf16 v[132:135], a[84:87], a[36:39], v[132:135]\n\t"
    "v_exp_f32 v139, v139\n\t"
    "v_mfma_f32_16x16x32_bf16 v[132:135], a[80:83], a[32:35], v[132:135]\n\t"
    "ds_read_b64_tr_b16 a[180:181], v233 offset:40960\n\t"
    "v_cvt_pk_bf16_f32 v173, v138, v139\n\t"
    "v_mfma_f32_16x16x32_bf16 v[208:211], a[192:195], v[144:147], v[208:211]\n\t"
    "v_exp_f32 v140, v140\n\t"
    "v_mfma_f32_16x16x32_bf16 v[0:3], a[128:131], v[144:147], v[0:3]\n\t"
    "s_waitcnt lgkmcnt(9)\n\t"
    "v_add_f32 v221, v221, v139\n\t"
    "v_add_f32 v221, v221, v140\n\t"
    "v_mfma_f32_16x16x32_bf16 v[0:3], a[160:163], v[160:163], v[0:3]\n\t"
    "v_exp_f32 v141, v141\n\t"
    "v_mfma_f32_16x16x32_bf16 v[4:7], a[132:135], v[144:147], v[4:7]\n\t"
    "ds_read_b64_tr_b16 a[182:183], v233 offset:45056\n\t"
    "v_cvt_pk_bf16_f32 v174, v140, v141\n\t"
    "v_mfma_f32_16x16x32_bf16 v[4:7], a[164:167], v[160:163], v[4:7]\n\t"
    "v_exp_f32 v142, v142\n\t"
    "v_mfma_f32_16x16x32_bf16 v[8:11], a[136:139], v[144:147], v[8:11]\n\t"
    "s_waitcnt lgkmcnt(6)\n\t"
    "v_add_f32 v221, v221, v141\n\t"
    "v_add_f32 v221, v221, v142\n\t"
    "v_mfma_f32_16x16x32_bf16 v[8:11], a[168:171], v[160:163], v[8:11]\n\t"
    "v_exp_f32 v143, v143\n\t"
    "v_mfma_f32_16x16x32_bf16 v[12:15], a[140:143], v[144:147], v[12:15]\n\t"
    "ds_read_b64_tr_b16 a[184:185], v234 offset:40960\n\t"
    "v_cvt_pk_bf16_f32 v175, v142, v143\n\t"
    "v_mfma_f32_16x16x32_bf16 v[12:15], a[172:175], v[160:163], v[12:15]\n\t"
    "ds_read_b64_tr_b16 a[186:187], v234 offset:45056\n\t"
    "v_add_f32 v221, v221, v143\n\t"
    "v_mfma_f32_16x16x32_bf16 v[136:139], a[64:67], v[176:179], v[204:207]\n\t"
    "v_exp_f32 v128, v128\n\t"
    "v_mfma_f32_16x16x32_bf16 v[136:139], a[68:71], v[180:183], v[136:139]\n\t"
    "ds_read_b64_tr_b16 a[188:189], v235 offset:40960\n\t"
    "ds_read_b64_tr_b16 a[190:191], v235 offset:45056\n\t"
    "v_mfma_f32_16x16x32_bf16 v[136:139], a[72:75], v[184:187], v[136:139]\n\t"
    "v_exp_f32 v129, v129\n\t"
    "v_mfma_f32_16x16x32_bf16 v[136:139], a[76:79], v[188:191], v[136:139]\n\t"
    "ds_read_b128 a[96:99], v224 offset:24576\n\t"
    "v_cvt_pk_bf16_f32 v152, v128, v129\n\t"
    "v_mfma_f32_16x16x32_bf16 v[140:143], a[92:95], v[188:191], v[204:207]\n\t"
    "v_exp_f32 v130, v130\n\t"
    "v_mfma_f32_16x16x32_bf16 v[140:143], a[88:91], v[184:187], v[140:143]\n\t"
    "ds_read_b128 a[100:103], v225 offset:24576\n\t"
    "ds_read_b128 a[104:107], v226 offset:24576\n\t"
    "v_mfma_f32_16x16x32_bf16 v[140:143], a[84:87], v[180:183], v[140:143]\n\t"
    "v_exp_f32 v131, v131\n\t"
    "v_mfma_f32_16x16x32_bf16 v[140:143], a[80:83], v[176:179], v[140:143]\n\t"
    "ds_read_b128 a[108:111], v227 offset:24576\n\t"
    "v_cvt_pk_bf16_f32 v153, v130, v131\n\t"
    "v_mfma_f32_16x16x32_bf16 v[32:35], a[128:131], v[148:151], v[32:35]\n\t"
    "v_exp_f32 v132, v132\n\t"
    "v_mfma_f32_16x16x32_bf16 v[32:35], a[160:163], v[164:167], v[32:35]\n\t"
    "ds_read_b128 a[112:115], v224 offset:28672\n\t"
    "ds_read_b128 a[116:119], v225 offset:28672\n\t"
    "v_mfma_f32_16x16x32_bf16 v[36:39], a[132:135], v[148:151], v[36:39]\n\t"
    "v_exp_f32 v133, v133\n\t"
    "v_mfma_f32_16x16x32_bf16 v[36:39], a[164:167], v[164:167], v[36:39]\n\t"
    "ds_read_b128 a[120:123], v226 offset:28672\n\t"
    "v_cvt_pk_bf16_f32 v154, v132, v133\n\t"
    "v_mfma_f32_16x16x32_bf16 v[40:43], a[136:139], v[148:151], v[40:43]\n\t"
    "v_exp_f32 v134, v134\n\t"
    "v_mfma_f32_16x16x32_bf16 v[40:43], a[168:171], v[164:167], v[40:43]\n\t"
    "ds_read_b128 a[124:127], v227 offset:28672\n\t"
    "v_mfma_f32_16x16x32_bf16 v[44:47], a[140:143], v[148:151], v[44:47]\n\t"
    "v_exp_f32 v135, v135\n\t"
    "v_mfma_f32_16x16x32_bf16 v[44:47], a[172:175], v[164:167], v[44:47]\n\t"
    "buffer_store_dwordx4 a[228:231], v254, s[28:31], s11 offen offset:1024 sc1\n\t"
    "v_cvt_pk_bf16_f32 v155, v134, v135\n\t"
    "v_mfma_f32_16x16x32_bf16 v[128:131], a[64:67], a[0:3], v[192:195]\n\t"
    "v_exp_f32 v136, v136\n\t"
    "v_mfma_f32_16x16x32_bf16 v[128:131], a[68:71], a[4:7], v[128:131]\n\t"
    "v_add_f32 v221, v221, v136\n\t"
    "v_mfma_f32_16x16x32_bf16 v[128:131], a[72:75], a[8:11], v[128:131]\n\t"
    "s_waitcnt vmcnt(20) lgkmcnt(0)\n\t"
    "v_exp_f32 v137, v137\n\t"
    "v_mfma_f32_16x16x32_bf16 v[128:131], a[76:79], a[12:15], v[128:131]\n\t"
    "s_barrier\n\t"
    "v_cvt_pk_bf16_f32 v156, v136, v137\n\t"
    "v_mfma_f32_16x16x32_bf16 v[132:135], a[92:95], a[12:15], v[192:195]\n\t"
    "v_exp_f32 v138, v138\n\t"
    "v_mfma_f32_16x16x32_bf16 v[132:135], a[88:91], a[8:11], v[132:135]\n\t"
    "v_add_f32 v221, v221, v137\n\t"
    "v_add_f32 v221, v221, v138\n\t"
    "v_mfma_f32_16x16x32_bf16 v[132:135], a[84:87], a[4:7], v[132:135]\n\t"
    "v_exp_f32 v139, v139\n\t"
    "v_mfma_f32_16x16x32_bf16 v[132:135], a[80:83], a[0:3], v[132:135]\n\t"
    "ds_read_b64_tr_b16 a[128:129], v228 offset:49152\n\t"
    "v_cvt_pk_bf16_f32 v157, v138, v139\n\t"
    "v_mfma_f32_16x16x32_bf16 v[208:211], a[192:195], v[160:163], v[208:211]\n\t"
    "s_lshl1_add_u32 m0, s37, 135168\n\t"
    "v_exp_f32 v140, v140\n\t"
    "v_mfma_f32_16x16x32_bf16 v[16:19], a[144:147], v[144:147], v[16:19]\n\t"
    "buffer_load_dwordx4 v245, s[44:47], s42 offen offset:0  lds\n\t"
    "v_add_f32 v221, v221, v139\n\t"
    "v_add_f32 v221, v221, v140\n\t"
    "v_mfma_f32_16x16x32_bf16 v[16:19], a[176:179], v[160:163], v[16:19]\n\t"
    "v_exp_f32 v141, v141\n\t"
    "v_mfma_f32_16x16x32_bf16 v[20:23], a[148:151], v[144:147], v[20:23]\n\t"
    "ds_read_b64_tr_b16 a[130:131], v228 offset:53248\n\t"
    "v_cvt_pk_bf16_f32 v158, v140, v141\n\t"
    "v_mfma_f32_16x16x32_bf16 v[20:23], a[180:183], v[160:163], v[20:23]\n\t"
    "v_exp_f32 v142, v142\n\t"
    "v_mfma_f32_16x16x32_bf16 v[24:27], a[152:155], v[144:147], v[24:27]\n\t"
    "v_add_f32 v221, v221, v141\n\t"
    "v_add_f32 v221, v221, v142\n\t"
    "v_mfma_f32_16x16x32_bf16 v[24:27], a[184:187], v[160:163], v[24:27]\n\t"
    "v_exp_f32 v143, v143\n\t"
    "v_mfma_f32_16x16x32_bf16 v[28:31], a[156:159], v[144:147], v[28:31]\n\t"
    "ds_read_b64_tr_b16 a[132:133], v229 offset:49152\n\t"
    "v_cvt_pk_bf16_f32 v159, v142, v143\n\t"
    "v_mfma_f32_16x16x32_bf16 v[28:31], a[188:191], v[160:163], v[28:31]\n\t"
    "s_add_u32 m0, s37, 16384\n\t"
    "ds_read_b64_tr_b16 a[134:135], v229 offset:53248\n\t"
    "v_add_f32 v221, v221, v143\n\t"
    "v_mfma_f32_16x16x32_bf16 v[136:139], a[64:67], a[16:19], v[196:199]\n\t"
    "v_exp_f32 v128, v128\n\t"
    "v_mfma_f32_16x16x32_bf16 v[136:139], a[68:71], a[20:23], v[136:139]\n\t"
    "buffer_load_dwordx4 v236, s[20:23], s36 offen offset:0  lds\n\t"
    "v_mfma_f32_16x16x32_bf16 v[136:139], a[72:75], a[24:27], v[136:139]\n\t"
    "v_exp_f32 v129, v129\n\t"
    "v_mfma_f32_16x16x32_bf16 v[136:139], a[76:79], a[28:31], v[136:139]\n\t"
    "ds_read_b64_tr_b16 a[136:137], v230 offset:49152\n\t"
    "v_cvt_pk_bf16_f32 v144, v128, v129\n\t"
    "v_mfma_f32_16x16x32_bf16 v[140:143], a[92:95], a[28:31], v[196:199]\n\t"
    "v_exp_f32 v130, v130\n\t"
    "v_mfma_f32_16x16x32_bf16 v[140:143], a[88:91], a[24:27], v[140:143]\n\t"
    "buffer_load_dwordx4 v237, s[20:23], s36 offen offset:1024  lds\n\t"
    "v_mfma_f32_16x16x32_bf16 v[140:143], a[84:87], a[20:23], v[140:143]\n\t"
    "v_exp_f32 v131, v131\n\t"
    "v_mfma_f32_16x16x32_bf16 v[140:143], a[80:83], a[16:19], v[140:143]\n\t"
    "ds_read_b64_tr_b16 a[138:139], v230 offset:53248\n\t"
    "v_cvt_pk_bf16_f32 v145, v130, v131\n\t"
    "v_mfma_f32_16x16x32_bf16 v[48:51], a[144:147], v[148:151], v[48:51]\n\t"
    "v_exp_f32 v132, v132\n\t"
    "v_mfma_f32_16x16x32_bf16 v[48:51], a[176:179], v[164:167], v[48:51]\n\t"
    "buffer_load_dwordx4 v238, s[20:23], s36 offen offset:2048  lds\n\t"
    "v_mfma_f32_16x16x32_bf16 v[52:55], a[148:151], v[148:151], v[52:55]\n\t"
    "v_exp_f32 v133, v133\n\t"
    "v_mfma_f32_16x16x32_bf16 v[52:55], a[180:183], v[164:167], v[52:55]\n\t"
    "ds_read_b64_tr_b16 a[140:141], v231 offset:49152\n\t"
    "v_cvt_pk_bf16_f32 v146, v132, v133\n\t"
    "v_mfma_f32_16x16x32_bf16 v[56:59], a[152:155], v[148:151], v[56:59]\n\t"
    "v_exp_f32 v134, v134\n\t"
    "v_mfma_f32_16x16x32_bf16 v[56:59], a[184:187], v[164:167], v[56:59]\n\t"
    "buffer_load_dwordx4 v239, s[20:23], s36 offen offset:3072  lds\n\t"
    "v_mfma_f32_16x16x32_bf16 v[60:63], a[156:159], v[148:151], v[60:63]\n\t"
    "v_exp_f32 v135, v135\n\t"
    "v_mfma_f32_16x16x32_bf16 v[60:63], a[188:191], v[164:167], v[60:63]\n\t"
    "ds_read_b64_tr_b16 a[142:143], v231 offset:53248\n\t"
    "v_cvt_pk_bf16_f32 v147, v134, v135\n\t"
    "v_mfma_f32_16x16x32_bf16 v[128:131], a[96:99], a[0:3], v[192:195]\n\t"
    "v_exp_f32 v136, v136\n\t"
    "v_mfma_f32_16x16x32_bf16 v[128:131], a[100:103], a[4:7], v[128:131]\n\t"
    "ds_read_b64_tr_b16 a[144:145], v232 offset:49152\n\t"
    "v_add_f32 v213, v213, v136\n\t"
    "v_mfma_f32_16x16x32_bf16 v[128:131], a[104:107], a[8:11], v[128:131]\n\t"
    "v_exp_f32 v137, v137\n\t"
    "v_mfma_f32_16x16x32_bf16 v[128:131], a[108:111], a[12:15], v[128:131]\n\t"
    "ds_read_b64_tr_b16 a[146:147], v232 offset:53248\n\t"
    "v_cvt_pk_bf16_f32 v148, v136, v137\n\t"
    "v_mfma_f32_16x16x32_bf16 v[132:135], a[124:127], a[12:15], v[192:195]\n\t"
    "v_exp_f32 v138, v138\n\t"
    "v_mfma_f32_16x16x32_bf16 v[132:135], a[120:123], a[8:11], v[132:135]\n\t"
    "v_add_f32 v213, v213, v137\n\t"
    "v_add_f32 v213, v213, v138\n\t"
    "v_mfma_f32_16x16x32_bf16 v[132:135], a[116:119], a[4:7], v[132:135]\n\t"
    "v_exp_f32 v139, v139\n\t"
    "v_mfma_f32_16x16x32_bf16 v[132:135], a[112:115], a[0:3], v[132:135]\n\t"
    "ds_read_b64_tr_b16 a[148:149], v233 offset:49152\n\t"
    "v_cvt_pk_bf16_f32 v149, v138, v139\n\t"
    "v_mfma_f32_16x16x32_bf16 v[216:219], a[192:195], v[168:171], v[216:219]\n\t"
    "v_exp_f32 v140, v140\n\t"
    "v_mfma_f32_16x16x32_bf16 v[64:67], a[160:163], v[168:171], v[64:67]\n\t"
    "s_waitcnt lgkmcnt(7)\n\t"
    "v_add_f32 v213, v213, v139\n\t"
    "v_add_f32 v213, v213, v140\n\t"
    "v_mfma_f32_16x16x32_bf16 v[64:67], a[128:131], v[152:155], v[64:67]\n\t"
    "v_exp_f32 v141, v141\n\t"
    "v_mfma_f32_16x16x32_bf16 v[68:71], a[164:167], v[168:171], v[68:71]\n\t"
    "ds_read_b64_tr_b16 a[150:151], v233 offset:53248\n\t"
    "v_cvt_pk_bf16_f32 v150, v140, v141\n\t"
    "v_mfma_f32_16x16x32_bf16 v[68:71], a[132:135], v[152:155], v[68:71]\n\t"
    "v_exp_f32 v142, v142\n\t"
    "v_mfma_f32_16x16x32_bf16 v[72:75], a[168:171], v[168:171], v[72:75]\n\t"
    "s_waitcnt lgkmcnt(4)\n\t"
    "v_add_f32 v213, v213, v141\n\t"
    "v_add_f32 v213, v213, v142\n\t"
    "v_mfma_f32_16x16x32_bf16 v[72:75], a[136:139], v[152:155], v[72:75]\n\t"
    "v_exp_f32 v143, v143\n\t"
    "v_mfma_f32_16x16x32_bf16 v[76:79], a[172:175], v[168:171], v[76:79]\n\t"
    "ds_read_b64_tr_b16 a[152:153], v234 offset:49152\n\t"
    "v_cvt_pk_bf16_f32 v151, v142, v143\n\t"
    "v_mfma_f32_16x16x32_bf16 v[76:79], a[140:143], v[152:155], v[76:79]\n\t"
    "ds_read_b64_tr_b16 a[154:155], v234 offset:53248\n\t"
    "v_add_f32 v213, v213, v143\n\t"
    "v_mfma_f32_16x16x32_bf16 v[136:139], a[96:99], a[16:19], v[196:199]\n\t"
    "v_exp_f32 v128, v128\n\t"
    "v_mfma_f32_16x16x32_bf16 v[136:139], a[100:103], a[20:23], v[136:139]\n\t"
    "ds_read_b64_tr_b16 a[156:157], v235 offset:49152\n\t"
    "ds_read_b64_tr_b16 a[158:159], v235 offset:53248\n\t"
    "v_mfma_f32_16x16x32_bf16 v[136:139], a[104:107], a[24:27], v[136:139]\n\t"
    "v_exp_f32 v129, v129\n\t"
    "v_mfma_f32_16x16x32_bf16 v[136:139], a[108:111], a[28:31], v[136:139]\n\t"
    "ds_read_b128 a[64:67], v224 offset:32768\n\t"
    "v_cvt_pk_bf16_f32 v160, v128, v129\n\t"
    "v_mfma_f32_16x16x32_bf16 v[140:143], a[124:127], a[28:31], v[196:199]\n\t"
    "v_exp_f32 v130, v130\n\t"
    "v_mfma_f32_16x16x32_bf16 v[140:143], a[120:123], a[24:27], v[140:143]\n\t"
    "ds_read_b128 a[68:71], v225 offset:32768\n\t"
    "ds_read_b128 a[72:75], v226 offset:32768\n\t"
    "v_mfma_f32_16x16x32_bf16 v[140:143], a[116:119], a[20:23], v[140:143]\n\t"
    "v_exp_f32 v131, v131\n\t"
    "v_mfma_f32_16x16x32_bf16 v[140:143], a[112:115], a[16:19], v[140:143]\n\t"
    "ds_read_b128 a[76:79], v227 offset:32768\n\t"
    "v_cvt_pk_bf16_f32 v161, v130, v131\n\t"
    "v_mfma_f32_16x16x32_bf16 v[96:99], a[160:163], v[172:175], v[96:99]\n\t"
    "v_exp_f32 v132, v132\n\t"
    "v_mfma_f32_16x16x32_bf16 v[96:99], a[128:131], v[156:159], v[96:99]\n\t"
    "ds_read_b128 a[80:83], v224 offset:36864\n\t"
    "ds_read_b128 a[84:87], v225 offset:36864\n\t"
    "v_mfma_f32_16x16x32_bf16 v[100:103], a[164:167], v[172:175], v[100:103]\n\t"
    "v_exp_f32 v133, v133\n\t"
    "v_mfma_f32_16x16x32_bf16 v[100:103], a[132:135], v[156:159], v[100:103]\n\t"
    "ds_read_b128 a[88:91], v226 offset:36864\n\t"
    "v_cvt_pk_bf16_f32 v162, v132, v133\n\t"
    "v_mfma_f32_16x16x32_bf16 v[104:107], a[168:171], v[172:175], v[104:107]\n\t"
    "v_exp_f32 v134, v134\n\t"
    "v_mfma_f32_16x16x32_bf16 v[104:107], a[136:139], v[156:159], v[104:107]\n\t"
    "ds_read_b128 a[92:95], v227 offset:36864\n\t"
    "v_mfma_f32_16x16x32_bf16 v[108:111], a[172:175], v[172:175], v[108:111]\n\t"
    "s_lshl1_add_u32 m0, s37, 131072\n\t"
    "v_exp_f32 v135, v135\n\t"
    "v_mfma_f32_16x16x32_bf16 v[108:111], a[140:143], v[156:159], v[108:111]\n\t"
    "s_add_u32 s76, s76, s75\n\t"
    "buffer_load_dwordx4 v244, s[44:47], s76 offen offset:2048  lds\n\t"
    "v_cvt_pk_bf16_f32 v163, v134, v135\n\t"
    "v_mfma_f32_16x16x32_bf16 v[128:131], a[96:99], a[32:35], v[200:203]\n\t"
    "v_exp_f32 v136, v136\n\t"
    "v_mfma_f32_16x16x32_bf16 v[128:131], a[100:103], a[36:39], v[128:131]\n\t"
    "ds_read_b64_tr_b16 a[160:161], v228 offset:57344\n\t"
    "v_add_f32 v213, v213, v136\n\t"
    "v_mfma_f32_16x16x32_bf16 v[128:131], a[104:107], a[40:43], v[128:131]\n\t"
    "v_exp_f32 v137, v137\n\t"
    "v_mfma_f32_16x16x32_bf16 v[128:131], a[108:111], a[44:47], v[128:131]\n\t"
    "ds_read_b64_tr_b16 a[162:163], v228 offset:61440\n\t"
    "v_cvt_pk_bf16_f32 v164, v136, v137\n\t"
    "v_mfma_f32_16x16x32_bf16 v[132:135], a[124:127], a[44:47], v[200:203]\n\t"
    "v_exp_f32 v138, v138\n\t"
    "v_mfma_f32_16x16x32_bf16 v[132:135], a[120:123], a[40:43], v[132:135]\n\t"
    "v_add_f32 v213, v213, v137\n\t"
    "v_add_f32 v213, v213, v138\n\t"
    "v_mfma_f32_16x16x32_bf16 v[132:135], a[116:119], a[36:39], v[132:135]\n\t"
    "v_exp_f32 v139, v139\n\t"
    "v_mfma_f32_16x16x32_bf16 v[132:135], a[112:115], a[32:35], v[132:135]\n\t"
    "ds_read_b64_tr_b16 a[164:165], v229 offset:57344\n\t"
    "v_cvt_pk_bf16_f32 v165, v138, v139\n\t"
    "v_mfma_f32_16x16x32_bf16 v[216:219], a[192:195], v[152:155], v[216:219]\n\t"
    "v_exp_f32 v140, v140\n\t"
    "v_mfma_f32_16x16x32_bf16 v[80:83], a[176:179], v[168:171], v[80:83]\n\t"
    "s_waitcnt lgkmcnt(15)\n\t"
    "buffer_store_dwordx4 a[232:235], v254, s[28:31], s11 offen offset:2048 sc1\n\t"
    "v_add_f32 v213, v213, v139\n\t"
    "v_add_f32 v213, v213, v140\n\t"
    "v_mfma_f32_16x16x32_bf16 v[80:83], a[144:147], v[152:155], v[80:83]\n\t"
    "v_exp_f32 v141, v141\n\t"
    "v_mfma_f32_16x16x32_bf16 v[84:87], a[180:183], v[168:171], v[84:87]\n\t"
    "ds_read_b64_tr_b16 a[166:167], v229 offset:61440\n\t"
    "v_cvt_pk_bf16_f32 v166, v140, v141\n\t"
    "v_mfma_f32_16x16x32_bf16 v[84:87], a[148:151], v[152:155], v[84:87]\n\t"
    "v_exp_f32 v142, v142\n\t"
    "v_mfma_f32_16x16x32_bf16 v[88:91], a[184:187], v[168:171], v[88:91]\n\t"
    "s_waitcnt lgkmcnt(12)\n\t"
    "v_add_f32 v213, v213, v141\n\t"
    "v_add_f32 v213, v213, v142\n\t"
    "v_mfma_f32_16x16x32_bf16 v[88:91], a[152:155], v[152:155], v[88:91]\n\t"
    "v_exp_f32 v143, v143\n\t"
    "v_mfma_f32_16x16x32_bf16 v[92:95], a[188:191], v[168:171], v[92:95]\n\t"
    "ds_read_b64_tr_b16 a[168:169], v230 offset:57344\n\t"
    "v_cvt_pk_bf16_f32 v167, v142, v143\n\t"
    "v_mfma_f32_16x16x32_bf16 v[92:95], a[156:159], v[152:155], v[92:95]\n\t"
    "s_add_u32 m0, s37, 98304\n\t"
    "ds_read_b64_tr_b16 a[170:171], v230 offset:61440\n\t"
    "v_add_f32 v213, v213, v143\n\t"
    "v_mfma_f32_16x16x32_bf16 v[136:139], a[96:99], v[176:179], v[204:207]\n\t"
    "v_exp_f32 v128, v128\n\t"
    "v_mfma_f32_16x16x32_bf16 v[136:139], a[100:103], v[180:183], v[136:139]\n\t"
    "buffer_load_dwordx4 v240, s[24:27], s36 offen offset:0  lds\n\t"
    "v_mfma_f32_16x16x32_bf16 v[136:139], a[104:107], v[184:187], v[136:139]\n\t"
    "v_exp_f32 v129, v129\n\t"
    "v_mfma_f32_16x16x32_bf16 v[136:139], a[108:111], v[188:191], v[136:139]\n\t"
    "ds_read_b64_tr_b16 a[172:173], v231 offset:57344\n\t"
    "v_cvt_pk_bf16_f32 v168, v128, v129\n\t"
    "v_mfma_f32_16x16x32_bf16 v[140:143], a[124:127], v[188:191], v[204:207]\n\t"
    "v_exp_f32 v130, v130\n\t"
    "v_mfma_f32_16x16x32_bf16 v[140:143], a[120:123], v[184:187], v[140:143]\n\t"
    "buffer_load_dwordx4 v241, s[24:27], s36 offen offset:1024  lds\n\t"
    "v_mfma_f32_16x16x32_bf16 v[140:143], a[116:119], v[180:183], v[140:143]\n\t"
    "v_exp_f32 v131, v131\n\t"
    "v_mfma_f32_16x16x32_bf16 v[140:143], a[112:115], v[176:179], v[140:143]\n\t"
    "ds_read_b64_tr_b16 a[174:175], v231 offset:61440\n\t"
    "v_cvt_pk_bf16_f32 v169, v130, v131\n\t"
    "v_mfma_f32_16x16x32_bf16 v[112:115], a[176:179], v[172:175], v[112:115]\n\t"
    "v_exp_f32 v132, v132\n\t"
    "v_mfma_f32_16x16x32_bf16 v[112:115], a[144:147], v[156:159], v[112:115]\n\t"
    "buffer_load_dwordx4 v240, s[24:27], s36 offen offset:2048  lds\n\t"
    "v_mfma_f32_16x16x32_bf16 v[116:119], a[180:183], v[172:175], v[116:119]\n\t"
    "v_exp_f32 v133, v133\n\t"
    "v_mfma_f32_16x16x32_bf16 v[116:119], a[148:151], v[156:159], v[116:119]\n\t"
    "v_cvt_pk_bf16_f32 v170, v132, v133\n\t"
    "v_mfma_f32_16x16x32_bf16 v[120:123], a[184:187], v[172:175], v[120:123]\n\t"
    "v_exp_f32 v134, v134\n\t"
    "v_mfma_f32_16x16x32_bf16 v[120:123], a[152:155], v[156:159], v[120:123]\n\t"
    "buffer_load_dwordx4 v241, s[24:27], s36 offen offset:3072  lds\n\t"
    "v_mfma_f32_16x16x32_bf16 v[124:127], a[188:191], v[172:175], v[124:127]\n\t"
    "v_exp_f32 v135, v135\n\t"
    "v_mfma_f32_16x16x32_bf16 v[124:127], a[156:159], v[156:159], v[124:127]\n\t"
    "s_waitcnt lgkmcnt(12)\n\t"
    "v_cvt_pk_bf16_f32 v171, v134, v135\n\t"
    "v_mfma_f32_16x16x32_bf16 v[128:131], a[64:67], a[32:35], v[200:203]\n\t"
    "v_exp_f32 v136, v136\n\t"
    "v_mfma_f32_16x16x32_bf16 v[128:131], a[68:71], a[36:39], v[128:131]\n\t"
    "s_add_u32 s36, s36, 16384\n\t"
    "ds_read_b64_tr_b16 a[176:177], v232 offset:57344\n\t"
    "v_add_f32 v221, v221, v136\n\t"
    "v_mfma_f32_16x16x32_bf16 v[128:131], a[72:75], a[40:43], v[128:131]\n\t"
    "v_exp_f32 v137, v137\n\t"
    "v_mfma_f32_16x16x32_bf16 v[128:131], a[76:79], a[44:47], v[128:131]\n\t"
    "s_waitcnt lgkmcnt(9)\n\t"
    "ds_read_b64_tr_b16 a[178:179], v232 offset:61440\n\t"
    "v_cvt_pk_bf16_f32 v172, v136, v137\n\t"
    "v_mfma_f32_16x16x32_bf16 v[132:135], a[92:95], a[44:47], v[200:203]\n\t"
    "v_exp_f32 v138, v138\n\t"
    "v_mfma_f32_16x16x32_bf16 v[132:135], a[88:91], a[40:43], v[132:135]\n\t"
    "v_add_f32 v221, v221, v137\n\t"
    "v_add_f32 v221, v221, v138\n\t"
    "v_mfma_f32_16x16x32_bf16 v[132:135], a[84:87], a[36:39], v[132:135]\n\t"
    "v_exp_f32 v139, v139\n\t"
    "v_mfma_f32_16x16x32_bf16 v[132:135], a[80:83], a[32:35], v[132:135]\n\t"
    "ds_read_b64_tr_b16 a[180:181], v233 offset:57344\n\t"
    "v_cvt_pk_bf16_f32 v173, v138, v139\n\t"
    "v_mfma_f32_16x16x32_bf16 v[208:211], a[192:195], v[144:147], v[208:211]\n\t"
    "v_exp_f32 v140, v140\n\t"
    "v_mfma_f32_16x16x32_bf16 v[0:3], a[128:131], v[144:147], v[0:3]\n\t"
    "s_waitcnt lgkmcnt(7)\n\t"
    "v_add_f32 v221, v221, v139\n\t"
    "v_add_f32 v221, v221, v140\n\t"
    "v_mfma_f32_16x16x32_bf16 v[0:3], a[160:163], v[160:163], v[0:3]\n\t"
    "v_exp_f32 v141, v141\n\t"
    "v_mfma_f32_16x16x32_bf16 v[4:7], a[132:135], v[144:147], v[4:7]\n\t"
    "ds_read_b64_tr_b16 a[182:183], v233 offset:61440\n\t"
    "v_cvt_pk_bf16_f32 v174, v140, v141\n\t"
    "v_mfma_f32_16x16x32_bf16 v[4:7], a[164:167], v[160:163], v[4:7]\n\t"
    "v_exp_f32 v142, v142\n\t"
    "v_mfma_f32_16x16x32_bf16 v[8:11], a[136:139], v[144:147], v[8:11]\n\t"
    "s_waitcnt lgkmcnt(4)\n\t"
    "v_add_f32 v221, v221, v141\n\t"
    "v_add_f32 v221, v221, v142\n\t"
    "v_mfma_f32_16x16x32_bf16 v[8:11], a[168:171], v[160:163], v[8:11]\n\t"
    "v_exp_f32 v143, v143\n\t"
    "v_mfma_f32_16x16x32_bf16 v[12:15], a[140:143], v[144:147], v[12:15]\n\t"
    "ds_read_b64_tr_b16 a[184:185], v234 offset:57344\n\t"
    "v_cvt_pk_bf16_f32 v175, v142, v143\n\t"
    "v_mfma_f32_16x16x32_bf16 v[12:15], a[172:175], v[160:163], v[12:15]\n\t"
    "ds_read_b64_tr_b16 a[186:187], v234 offset:61440\n\t"
    "v_add_f32 v221, v221, v143\n\t"
    "v_mfma_f32_16x16x32_bf16 v[136:139], a[64:67], v[176:179], v[204:207]\n\t"
    "v_exp_f32 v128, v128\n\t"
    "v_mfma_f32_16x16x32_bf16 v[136:139], a[68:71], v[180:183], v[136:139]\n\t"
    "ds_read_b64_tr_b16 a[188:189], v235 offset:57344\n\t"
    "ds_read_b64_tr_b16 a[190:191], v235 offset:61440\n\t"
    "v_mfma_f32_16x16x32_bf16 v[136:139], a[72:75], v[184:187], v[136:139]\n\t"
    "v_exp_f32 v129, v129\n\t"
    "v_mfma_f32_16x16x32_bf16 v[136:139], a[76:79], v[188:191], v[136:139]\n\t"
    "ds_read_b128 a[96:99], v224 offset:40960\n\t"
    "v_cvt_pk_bf16_f32 v152, v128, v129\n\t"
    "v_mfma_f32_16x16x32_bf16 v[140:143], a[92:95], v[188:191], v[204:207]\n\t"
    "v_exp_f32 v130, v130\n\t"
    "v_mfma_f32_16x16x32_bf16 v[140:143], a[88:91], v[184:187], v[140:143]\n\t"
    "ds_read_b128 a[100:103], v225 offset:40960\n\t"
    "ds_read_b128 a[104:107], v226 offset:40960\n\t"
    "v_mfma_f32_16x16x32_bf16 v[140:143], a[84:87], v[180:183], v[140:143]\n\t"
    "v_exp_f32 v131, v131\n\t"
    "v_mfma_f32_16x16x32_bf16 v[140:143], a[80:83], v[176:179], v[140:143]\n\t"
    "ds_read_b128 a[108:111], v227 offset:40960\n\t"
    "v_cvt_pk_bf16_f32 v153, v130, v131\n\t"
    "v_mfma_f32_16x16x32_bf16 v[32:35], a[128:131], v[148:151], v[32:35]\n\t"
    "v_exp_f32 v132, v132\n\t"
    "v_mfma_f32_16x16x32_bf16 v[32:35], a[160:163], v[164:167], v[32:35]\n\t"
    "ds_read_b128 a[112:115], v224 offset:45056\n\t"
    "ds_read_b128 a[116:119], v225 offset:45056\n\t"
    "v_mfma_f32_16x16x32_bf16 v[36:39], a[132:135], v[148:151], v[36:39]\n\t"
    "v_exp_f32 v133, v133\n\t"
    "v_mfma_f32_16x16x32_bf16 v[36:39], a[164:167], v[164:167], v[36:39]\n\t"
    "ds_read_b128 a[120:123], v226 offset:45056\n\t"
    "v_cvt_pk_bf16_f32 v154, v132, v133\n\t"
    "v_mfma_f32_16x16x32_bf16 v[40:43], a[136:139], v[148:151], v[40:43]\n\t"
    "v_exp_f32 v134, v134\n\t"
    "v_mfma_f32_16x16x32_bf16 v[40:43], a[168:171], v[164:167], v[40:43]\n\t"
    "ds_read_b128 a[124:127], v227 offset:45056\n\t"
    "v_mfma_f32_16x16x32_bf16 v[44:47], a[140:143], v[148:151], v[44:47]\n\t"
    "s_lshl1_add_u32 m0, s37, 131072\n\t"
    "v_exp_f32 v135, v135\n\t"
    "v_mfma_f32_16x16x32_bf16 v[44:47], a[172:175], v[164:167], v[44:47]\n\t"
    "s_add_u32 s76, s76, s75\n\t"
    "buffer_load_dwordx4 v244, s[44:47], s76 offen offset:3072  lds\n\t"
    "v_cvt_pk_bf16_f32 v155, v134, v135\n\t"
    "v_mfma_f32_16x16x32_bf16 v[128:131], a[64:67], a[0:3], v[192:195]\n\t"
    "v_exp_f32 v136, v136\n\t"
    "v_mfma_f32_16x16x32_bf16 v[128:131], a[68:71], a[4:7], v[128:131]\n\t"
    "v_add_f32 v221, v221, v136\n\t"
    "v_mfma_f32_16x16x32_bf16 v[128:131], a[72:75], a[8:11], v[128:131]\n\t"
    "s_waitcnt vmcnt(24) lgkmcnt(0)\n\t"
    "v_exp_f32 v137, v137\n\t"
    "v_mfma_f32_16x16x32_bf16 v[128:131], a[76:79], a[12:15], v[128:131]\n\t"
    "s_barrier\n\t"
    "v_cvt_pk_bf16_f32 v156, v136, v137\n\t"
    "v_mfma_f32_16x16x32_bf16 v[132:135], a[92:95], a[12:15], v[192:195]\n\t"
    "v_exp_f32 v138, v138\n\t"
    "v_mfma_f32_16x16x32_bf16 v[132:135], a[88:91], a[8:11], v[132:135]\n\t"
    "v_add_f32 v221, v221, v137\n\t"
    "v_add_f32 v221, v221, v138\n\t"
    "v_mfma_f32_16x16x32_bf16 v[132:135], a[84:87], a[4:7], v[132:135]\n\t"
    "v_exp_f32 v139, v139\n\t"
    "v_mfma_f32_16x16x32_bf16 v[132:135], a[80:83], a[0:3], v[132:135]\n\t"
    "ds_read_b64_tr_b16 a[128:129], v228 offset:0\n\t"
    "v_cvt_pk_bf16_f32 v157, v138, v139\n\t"
    "v_mfma_f32_16x16x32_bf16 v[208:211], a[192:195], v[160:163], v[208:211]\n\t"
    "v_exp_f32 v140, v140\n\t"
    "v_mfma_f32_16x16x32_bf16 v[16:19], a[144:147], v[144:147], v[16:19]\n\t"
    "buffer_store_dwordx4 a[236:239], v254, s[28:31], s11 offen offset:3072 sc1\n\t"
    "v_add_f32 v221, v221, v139\n\t"
    "v_add_f32 v221, v221, v140\n\t"
    "v_mfma_f32_16x16x32_bf16 v[16:19], a[176:179], v[160:163], v[16:19]\n\t"
    "v_exp_f32 v141, v141\n\t"
    "v_mfma_f32_16x16x32_bf16 v[20:23], a[148:151], v[144:147], v[20:23]\n\t"
    "ds_read_b64_tr_b16 a[130:131], v228 offset:4096\n\t"
    "v_cvt_pk_bf16_f32 v158, v140, v141\n\t"
    "v_mfma_f32_16x16x32_bf16 v[20:23], a[180:183], v[160:163], v[20:23]\n\t"
    "v_exp_f32 v142, v142\n\t"
    "v_mfma_f32_16x16x32_bf16 v[24:27], a[152:155], v[144:147], v[24:27]\n\t"
    "v_add_f32 v221, v221, v141\n\t"
    "v_add_f32 v221, v221, v142\n\t"
    "v_mfma_f32_16x16x32_bf16 v[24:27], a[184:187], v[160:163], v[24:27]\n\t"
    "v_exp_f32 v143, v143\n\t"
    "v_mfma_f32_16x16x32_bf16 v[28:31], a[156:159], v[144:147], v[28:31]\n\t"
    "ds_read_b64_tr_b16 a[132:133], v229 offset:0\n\t"
    "v_cvt_pk_bf16_f32 v159, v142, v143\n\t"
    "v_mfma_f32_16x16x32_bf16 v[28:31], a[188:191], v[160:163], v[28:31]\n\t"
    "s_add_u32 m0, s37, 32768\n\t"
    "ds_read_b64_tr_b16 a[134:135], v229 offset:4096\n\t"
    "v_add_f32 v221, v221, v143\n\t"
    "v_mfma_f32_16x16x32_bf16 v[136:139], a[64:67], a[16:19], v[196:199]\n\t"
    "v_exp_f32 v128, v128\n\t"
    "v_mfma_f32_16x16x32_bf16 v[136:139], a[68:71], a[20:23], v[136:139]\n\t"
    "buffer_load_dwordx4 v236, s[20:23], s36 offen offset:0  lds\n\t"
    "v_mfma_f32_16x16x32_bf16 v[136:139], a[72:75], a[24:27], v[136:139]\n\t"
    "v_exp_f32 v129, v129\n\t"
    "v_mfma_f32_16x16x32_bf16 v[136:139], a[76:79], a[28:31], v[136:139]\n\t"
    "ds_read_b64_tr_b16 a[136:137], v230 offset:0\n\t"
    "v_cvt_pk_bf16_f32 v144, v128, v129\n\t"
    "v_mfma_f32_16x16x32_bf16 v[140:143], a[92:95], a[28:31], v[196:199]\n\t"
    "v_exp_f32 v130, v130\n\t"
    "v_mfma_f32_16x16x32_bf16 v[140:143], a[88:91], a[24:27], v[140:143]\n\t"
    "buffer_load_dwordx4 v237, s[20:23], s36 offen offset:1024  lds\n\t"
    "v_mfma_f32_16x16x32_bf16 v[140:143], a[84:87], a[20:23], v[140:143]\n\t"
    "v_exp_f32 v131, v131\n\t"
    "v_mfma_f32_16x16x32_bf16 v[140:143], a[80:83], a[16:19], v[140:143]\n\t"
    "ds_read_b64_tr_b16 a[138:139], v230 offset:4096\n\t"
    "v_cvt_pk_bf16_f32 v145, v130, v131\n\t"
    "v_mfma_f32_16x16x32_bf16 v[48:51], a[144:147], v[148:151], v[48:51]\n\t"
    "v_exp_f32 v132, v132\n\t"
    "v_mfma_f32_16x16x32_bf16 v[48:51], a[176:179], v[164:167], v[48:51]\n\t"
    "buffer_load_dwordx4 v238, s[20:23], s36 offen offset:2048  lds\n\t"
    "v_mfma_f32_16x16x32_bf16 v[52:55], a[148:151], v[148:151], v[52:55]\n\t"
    "v_exp_f32 v133, v133\n\t"
    "v_mfma_f32_16x16x32_bf16 v[52:55], a[180:183], v[164:167], v[52:55]\n\t"
    "ds_read_b64_tr_b16 a[140:141], v231 offset:0\n\t"
    "v_cvt_pk_bf16_f32 v146, v132, v133\n\t"
    "v_mfma_f32_16x16x32_bf16 v[56:59], a[152:155], v[148:151], v[56:59]\n\t"
    "v_exp_f32 v134, v134\n\t"
    "v_mfma_f32_16x16x32_bf16 v[56:59], a[184:187], v[164:167], v[56:59]\n\t"
    "buffer_load_dwordx4 v239, s[20:23], s36 offen offset:3072  lds\n\t"
    "v_mfma_f32_16x16x32_bf16 v[60:63], a[156:159], v[148:151], v[60:63]\n\t"
    "v_exp_f32 v135, v135\n\t"
    "v_mfma_f32_16x16x32_bf16 v[60:63], a[188:191], v[164:167], v[60:63]\n\t"
    "ds_read_b64_tr_b16 a[142:143], v231 offset:4096\n\t"
    "v_cvt_pk_bf16_f32 v147, v134, v135\n\t"
    "v_mfma_f32_16x16x32_bf16 v[128:131], a[96:99], a[0:3], v[192:195]\n\t"
    "v_exp_f32 v136, v136\n\t"
    "v_mfma_f32_16x16x32_bf16 v[128:131], a[100:103], a[4:7], v[128:131]\n\t"
    "ds_read_b64_tr_b16 a[144:145], v232 offset:0\n\t"
    "v_add_f32 v213, v213, v136\n\t"
    "v_mfma_f32_16x16x32_bf16 v[128:131], a[104:107], a[8:11], v[128:131]\n\t"
    "v_exp_f32 v137, v137\n\t"
    "v_mfma_f32_16x16x32_bf16 v[128:131], a[108:111], a[12:15], v[128:131]\n\t"
    "ds_read_b64_tr_b16 a[146:147], v232 offset:4096\n\t"
    "v_cvt_pk_bf16_f32 v148, v136, v137\n\t"
    "v_mfma_f32_16x16x32_bf16 v[132:135], a[124:127], a[12:15], v[192:195]\n\t"
    "v_exp_f32 v138, v138\n\t"
    "v_mfma_f32_16x16x32_bf16 v[132:135], a[120:123], a[8:11], v[132:135]\n\t"
    "v_add_f32 v213, v213, v137\n\t"
    "v_add_f32 v213, v213, v138\n\t"
    "v_mfma_f32_16x16x32_bf16 v[132:135], a[116:119], a[4:7], v[132:135]\n\t"
    "v_exp_f32 v139, v139\n\t"
    "v_mfma_f32_16x16x32_bf16 v[132:135], a[112:115], a[0:3], v[132:135]\n\t"
    "ds_read_b64_tr_b16 a[148:149], v233 offset:0\n\t"
    "v_cvt_pk_bf16_f32 v149, v138, v139\n\t"
    "v_mfma_f32_16x16x32_bf16 v[216:219], a[192:195], v[168:171], v[216:219]\n\t"
    "v_exp_f32 v140, v140\n\t"
    "v_mfma_f32_16x16x32_bf16 v[64:67], a[160:163], v[168:171], v[64:67]\n\t"
    "s_waitcnt lgkmcnt(7)\n\t"
    "v_add_f32 v213, v213, v139\n\t"
    "v_add_f32 v213, v213, v140\n\t"
    "v_mfma_f32_16x16x32_bf16 v[64:67], a[128:131], v[152:155], v[64:67]\n\t"
    "v_exp_f32 v141, v141\n\t"
    "v_mfma_f32_16x16x32_bf16 v[68:71], a[164:167], v[168:171], v[68:71]\n\t"
    "ds_read_b64_tr_b16 a[150:151], v233 offset:4096\n\t"
    "v_cvt_pk_bf16_f32 v150, v140, v141\n\t"
    "v_mfma_f32_16x16x32_bf16 v[68:71], a[132:135], v[152:155], v[68:71]\n\t"
    "v_exp_f32 v142, v142\n\t"
    "v_mfma_f32_16x16x32_bf16 v[72:75], a[168:171], v[168:171], v[72:75]\n\t"
    "s_waitcnt lgkmcnt(4)\n\t"
    "v_add_f32 v213, v213, v141\n\t"
    "v_add_f32 v213, v213, v142\n\t"
    "v_mfma_f32_16x16x32_bf16 v[72:75], a[136:139], v[152:155], v[72:75]\n\t"
    "v_exp_f32 v143, v143\n\t"
    "v_mfma_f32_16x16x32_bf16 v[76:79], a[172:175], v[168:171], v[76:79]\n\t"
    "ds_read_b64_tr_b16 a[152:153], v234 offset:0\n\t"
    "v_cvt_pk_bf16_f32 v151, v142, v143\n\t"
    "v_mfma_f32_16x16x32_bf16 v[76:79], a[140:143], v[152:155], v[76:79]\n\t"
    "ds_read_b64_tr_b16 a[154:155], v234 offset:4096\n\t"
    "v_add_f32 v213, v213, v143\n\t"
    "v_mfma_f32_16x16x32_bf16 v[136:139], a[96:99], a[16:19], v[196:199]\n\t"
    "v_exp_f32 v128, v128\n\t"
    "v_mfma_f32_16x16x32_bf16 v[136:139], a[100:103], a[20:23], v[136:139]\n\t"
    "ds_read_b64_tr_b16 a[156:157], v235 offset:0\n\t"
    "ds_read_b64_tr_b16 a[158:159], v235 offset:4096\n\t"
    "v_mfma_f32_16x16x32_bf16 v[136:139], a[104:107], a[24:27], v[136:139]\n\t"
    "v_exp_f32 v129, v129\n\t"
    "v_mfma_f32_16x16x32_bf16 v[136:139], a[108:111], a[28:31], v[136:139]\n\t"
    "ds_read_b128 a[64:67], v224 offset:49152\n\t"
    "v_cvt_pk_bf16_f32 v160, v128, v129\n\t"
    "v_mfma_f32_16x16x32_bf16 v[140:143], a[124:127], a[28:31], v[196:199]\n\t"
    "v_exp_f32 v130, v130\n\t"
    "v_mfma_f32_16x16x32_bf16 v[140:143], a[120:123], a[24:27], v[140:143]\n\t"
    "ds_read_b128 a[68:71], v225 offset:49152\n\t"
    "ds_read_b128 a[72:75], v226 offset:49152\n\t"
    "v_mfma_f32_16x16x32_bf16 v[140:143], a[116:119], a[20:23], v[140:143]\n\t"
    "v_exp_f32 v131, v131\n\t"
    "v_mfma_f32_16x16x32_bf16 v[140:143], a[112:115], a[16:19], v[140:143]\n\t"
    "ds_read_b128 a[76:79], v227 offset:49152\n\t"
    "v_cvt_pk_bf16_f32 v161, v130, v131\n\t"
    "v_mfma_f32_16x16x32_bf16 v[96:99], a[160:163], v[172:175], v[96:99]\n\t"
    "v_exp_f32 v132, v132\n\t"
    "v_mfma_f32_16x16x32_bf16 v[96:99], a[128:131], v[156:159], v[96:99]\n\t"
    "ds_read_b128 a[80:83], v224 offset:53248\n\t"
    "ds_read_b128 a[84:87], v225 offset:53248\n\t"
    "v_mfma_f32_16x16x32_bf16 v[100:103], a[164:167], v[172:175], v[100:103]\n\t"
    "v_exp_f32 v133, v133\n\t"
    "v_mfma_f32_16x16x32_bf16 v[100:103], a[132:135], v[156:159], v[100:103]\n\t"
    "ds_read_b128 a[88:91], v226 offset:53248\n\t"
    "v_cvt_pk_bf16_f32 v162, v132, v133\n\t"
    "v_mfma_f32_16x16x32_bf16 v[104:107], a[168:171], v[172:175], v[104:107]\n\t"
    "v_exp_f32 v134, v134\n\t"
    "v_mfma_f32_16x16x32_bf16 v[104:107], a[136:139], v[156:159], v[104:107]\n\t"
    "ds_read_b128 a[92:95], v227 offset:53248\n\t"
    "v_mfma_f32_16x16x32_bf16 v[108:111], a[172:175], v[172:175], v[108:111]\n\t"
    "v_exp_f32 v135, v135\n\t"
    "v_mfma_f32_16x16x32_bf16 v[108:111], a[140:143], v[156:159], v[108:111]\n\t"
    "buffer_store_dwordx4 a[240:243], v254, s[28:31], s12 offen offset:0 sc1\n\t"
    "v_cvt_pk_bf16_f32 v163, v134, v135\n\t"
    "v_mfma_f32_16x16x32_bf16 v[128:131], a[96:99], a[32:35], v[200:203]\n\t"
    "v_exp_f32 v136, v136\n\t"
    "v_mfma_f32_16x16x32_bf16 v[128:131], a[100:103], a[36:39], v[128:131]\n\t"
    "ds_read_b64_tr_b16 a[160:161], v228 offset:8192\n\t"
    "v_add_f32 v213, v213, v136\n\t"
    "v_mfma_f32_16x16x32_bf16 v[128:131], a[104:107], a[40:43], v[128:131]\n\t"
    "v_exp_f32 v137, v137\n\t"
    "v_mfma_f32_16x16x32_bf16 v[128:131], a[108:111], a[44:47], v[128:131]\n\t"
    "ds_read_b64_tr_b16 a[162:163], v228 offset:12288\n\t"
    "v_cvt_pk_bf16_f32 v164, v136, v137\n\t"
    "v_mfma_f32_16x16x32_bf16 v[132:135], a[124:127], a[44:47], v[200:203]\n\t"
    "v_exp_f32 v138, v138\n\t"
    "v_mfma_f32_16x16x32_bf16 v[132:135], a[120:123], a[40:43], v[132:135]\n\t"
    "v_add_f32 v213, v213, v137\n\t"
    "v_add_f32 v213, v213, v138\n\t"
    "v_mfma_f32_16x16x32_bf16 v[132:135], a[116:119], a[36:39], v[132:135]\n\t"
    "v_exp_f32 v139, v139\n\t"
    "v_mfma_f32_16x16x32_bf16 v[132:135], a[112:115], a[32:35], v[132:135]\n\t"
    "ds_read_b64_tr_b16 a[164:165], v229 offset:8192\n\t"
    "v_cvt_pk_bf16_f32 v165, v138, v139\n\t"
    "v_mfma_f32_16x16x32_bf16 v[216:219], a[192:195], v[152:155], v[216:219]\n\t"
    "v_exp_f32 v140, v140\n\t"
    "v_mfma_f32_16x16x32_bf16 v[80:83], a[176:179], v[168:171], v[80:83]\n\t"
    "s_waitcnt lgkmcnt(15)\n\t"
    "v_add_f32 v213, v213, v139\n\t"
    "v_add_f32 v213, v213, v140\n\t"
    "v_mfma_f32_16x16x32_bf16 v[80:83], a[144:147], v[152:155], v[80:83]\n\t"
    "v_exp_f32 v141, v141\n\t"
    "v_mfma_f32_16x16x32_bf16 v[84:87], a[180:183], v[168:171], v[84:87]\n\t"
    "ds_read_b64_tr_b16 a[166:167], v229 offset:12288\n\t"
    "v_cvt_pk_bf16_f32 v166, v140, v141\n\t"
    "v_mfma_f32_16x16x32_bf16 v[84:87], a[148:151], v[152:155], v[84:87]\n\t"
    "v_exp_f32 v142, v142\n\t"
    "v_mfma_f32_16x16x32_bf16 v[88:91], a[184:187], v[168:171], v[88:91]\n\t"
    "s_waitcnt lgkmcnt(12)\n\t"
    "v_add_f32 v213, v213, v141\n\t"
    "v_add_f32 v213, v213, v142\n\t"
    "v_mfma_f32_16x16x32_bf16 v[88:91], a[152:155], v[152:155], v[88:91]\n\t"
    "v_exp_f32 v143, v143\n\t"
    "v_mfma_f32_16x16x32_bf16 v[92:95], a[188:191], v[168:171], v[92:95]\n\t"
    "ds_read_b64_tr_b16 a[168:169], v230 offset:8192\n\t"
    "v_cvt_pk_bf16_f32 v167, v142, v143\n\t"
    "v_mfma_f32_16x16x32_bf16 v[92:95], a[156:159], v[152:155], v[92:95]\n\t"
    "s_add_u32 m0, s37, 114688\n\t"
    "ds_read_b64_tr_b16 a[170:171], v230 offset:12288\n\t"
    "v_add_f32 v213, v213, v143\n\t"
    "v_mfma_f32_16x16x32_bf16 v[136:139], a[96:99], v[176:179], v[204:207]\n\t"
    "v_exp_f32 v128, v128\n\t"
    "v_mfma_f32_16x16x32_bf16 v[136:139], a[100:103], v[180:183], v[136:139]\n\t"
    "buffer_load_dwordx4 v240, s[24:27], s36 offen offset:0  lds\n\t"
    "v_mfma_f32_16x16x32_bf16 v[136:139], a[104:107], v[184:187], v[136:139]\n\t"
    "v_exp_f32 v129, v129\n\t"
    "v_mfma_f32_16x16x32_bf16 v[136:139], a[108:111], v[188:191], v[136:139]\n\t"
    "ds_read_b64_tr_b16 a[172:173], v231 offset:8192\n\t"
    "v_cvt_pk_bf16_f32 v168, v128, v129\n\t"
    "v_mfma_f32_16x16x32_bf16 v[140:143], a[124:127], v[188:191], v[204:207]\n\t"
    "v_exp_f32 v130, v130\n\t"
    "v_mfma_f32_16x16x32_bf16 v[140:143], a[120:123], v[184:187], v[140:143]\n\t"
    "buffer_load_dwordx4 v241, s[24:27], s36 offen offset:1024  lds\n\t"
    "v_mfma_f32_16x16x32_bf16 v[140:143], a[116:119], v[180:183], v[140:143]\n\t"
    "v_exp_f32 v131, v131\n\t"
    "v_mfma_f32_16x16x32_bf16 v[140:143], a[112:115], v[176:179], v[140:143]\n\t"
    "ds_read_b64_tr_b16 a[174:175], v231 offset:12288\n\t"
    "v_cvt_pk_bf16_f32 v169, v130, v131\n\t"
    "v_mfma_f32_16x16x32_bf16 v[112:115], a[176:179], v[172:175], v[112:115]\n\t"
    "v_exp_f32 v132, v132\n\t"
    "v_mfma_f32_16x16x32_bf16 v[112:115], a[144:147], v[156:159], v[112:115]\n\t"
    "buffer_load_dwordx4 v240, s[24:27], s36 offen offset:2048  lds\n\t"
    "v_mfma_f32_16x16x32_bf16 v[116:119], a[180:183], v[172:175], v[116:119]\n\t"
    "v_exp_f32 v133, v133\n\t"
    "v_mfma_f32_16x16x32_bf16 v[116:119], a[148:151], v[156:159], v[116:119]\n\t"
    "v_cvt_pk_bf16_f32 v170, v132, v133\n\t"
    "v_mfma_f32_16x16x32_bf16 v[120:123], a[184:187], v[172:175], v[120:123]\n\t"
    "v_exp_f32 v134, v134\n\t"
    "v_mfma_f32_16x16x32_bf16 v[120:123], a[152:155], v[156:159], v[120:123]\n\t"
    "buffer_load_dwordx4 v241, s[24:27], s36 offen offset:3072  lds\n\t"
    "v_mfma_f32_16x16x32_bf16 v[124:127], a[188:191], v[172:175], v[124:127]\n\t"
    "v_exp_f32 v135, v135\n\t"
    "v_mfma_f32_16x16x32_bf16 v[124:127], a[156:159], v[156:159], v[124:127]\n\t"
    "v_cvt_pk_bf16_f32 v171, v134, v135\n\t"
    "s_setpc_b64 s[98:99]\n\t"
    ".p2align 6\n\t"
    "L_loop%=:\n\t"
    "s_waitcnt lgkmcnt(12)\n\t"
    "v_mfma_f32_16x16x32_bf16 v[128:131], a[64:67], a[32:35], v[200:203]\n\t"
    "v_exp_f32 v136, v136\n\t"
    "v_mfma_f32_16x16x32_bf16 v[128:131], a[68:71], a[36:39], v[128:131]\n\t"
    "s_add_u32 s36, s36, 16384\n\t"
    "ds_read_b64_tr_b16 a[176:177], v232 offset:8192\n\t"
    "v_add_f32 v221, v221, v136\n\t"
    "v_mfma_f32_16x16x32_bf16 v[128:131], a[72:75], a[40:43], v[128:131]\n\t"
    "v_exp_f32 v137, v137\n\t"
    "v_mfma_f32_16x16x32_bf16 v[128:131], a[76:79], a[44:47], v[128:131]\n\t"
    "s_waitcnt lgkmcnt(9)\n\t"
    "ds_read_b64_tr_b16 a[178:179], v232 offset:12288\n\t"
    "v_cvt_pk_bf16_f32 v172, v136, v137\n\t"
    "v_mfma_f32_16x16x32_bf16 v[132:135], a[92:95], a[44:47], v[200:203]\n\t"
    "v_exp_f32 v138, v138\n\t"
    "v_mfma_f32_16x16x32_bf16 v[132:135], a[88:91], a[40:43], v[132:135]\n\t"
    "v_add_f32 v221, v221, v137\n\t"
    "v_add_f32 v221, v221, v138\n\t"
    "v_mfma_f32_16x16x32_bf16 v[132:135], a[84:87], a[36:39], v[132:135]\n\t"
    "v_exp_f32 v139, v139\n\t"
    "v_mfma_f32_16x16x32_bf16 v[132:135], a[80:83], a[32:35], v[132:135]\n\t"
    "ds_read_b64_tr_b16 a[180:181], v233 offset:8192\n\t"
    "v_cvt_pk_bf16_f32 v173, v138, v139\n\t"
    "v_mfma_f32_16x16x32_bf16 v[208:211], a[192:195], v[144:147], v[208:211]\n\t"
    "v_exp_f32 v140, v140\n\t"
    "v_mfma_f32_16x16x32_bf16 v[0:3], a[128:131], v[144:147], v[0:3]\n\t"
    "s_waitcnt lgkmcnt(7)\n\t"
    "v_add_f32 v221, v221, v139\n\t"
    "v_add_f32 v221, v221, v140\n\t"
    "v_mfma_f32_16x16x32_bf16 v[0:3], a[160:163], v[160:163], v[0:3]\n\t"
    "v_exp_f32 v141, v141\n\t"
    "v_mfma_f32_16x16x32_bf16 v[4:7], a[132:135], v[144:147], v[4:7]\n\t"
    "ds_read_b64_tr_b16 a[182:183], v233 offset:12288\n\t"
    "v_cvt_pk_bf16_f32 v174, v140, v141\n\t"
    "v_mfma_f32_16x16x32_bf16 v[4:7], a[164:167], v[160:163], v[4:7]\n\t"
    "v_exp_f32 v142, v142\n\t"
    "v_mfma_f32_16x16x32_bf16 v[8:11], a[136:139], v[144:147], v[8:11]\n\t"
    "s_waitcnt lgkmcnt(4)\n\t"
    "v_add_f32 v221, v221, v141\n\t"
    "v_add_f32 v221, v221, v142\n\t"
    "v_mfma_f32_16x16x32_bf16 v[8:11], a[168:171], v[160:163], v[8:11]\n\t"
    "v_exp_f32 v143, v143\n\t"
    "v_mfma_f32_16x16x32_bf16 v[12:15], a[140:143], v[144:147], v[12:15]\n\t"
    "ds_read_b64_tr_b16 a[184:185], v234 offset:8192\n\t"
    "v_cvt_pk_bf16_f32 v175, v142, v143\n\t"
    "v_mfma_f32_16x16x32_bf16 v[12:15], a[172:175], v[160:163], v[12:15]\n\t"
    "ds_read_b64_tr_b16 a[186:187], v234 offset:12288\n\t"
    "v_add_f32 v221, v221, v143\n\t"
    "v_mfma_f32_16x16x32_bf16 v[136:139], a[64:67], v[176:179], v[204:207]\n\t"
    "v_exp_f32 v128, v128\n\t"
    "v_mfma_f32_16x16x32_bf16 v[136:139], a[68:71], v[180:183], v[136:139]\n\t"
    "ds_read_b64_tr_b16 a[188:189], v235 offset:8192\n\t"
    "ds_read_b64_tr_b16 a[190:191], v235 offset:12288\n\t"
    "v_mfma_f32_16x16x32_bf16 v[136:139], a[72:75], v[184:187], v[136:139]\n\t"
    "v_exp_f32 v129, v129\n\t"
    "v_mfma_f32_16x16x32_bf16 v[136:139], a[76:79], v[188:191], v[136:139]\n\t"
    "ds_read_b128 a[96:99], v224 offset:57344\n\t"
    "v_cvt_pk_bf16_f32 v152, v128, v129\n\t"
    "v_mfma_f32_16x16x32_bf16 v[140:143], a[92:95], v[188:191], v[204:207]\n\t"
    "v_exp_f32 v130, v130\n\t"
    "v_mfma_f32_16x16x32_bf16 v[140:143], a[88:91], v[184:187], v[140:143]\n\t"
    "ds_read_b128 a[100:103], v225 offset:57344\n\t"
    "ds_read_b128 a[104:107], v226 offset:57344\n\t"
    "v_mfma_f32_16x16x32_bf16 v[140:143], a[84:87], v[180:183], v[140:143]\n\t"
    "v_exp_f32 v131, v131\n\t"
    "v_mfma_f32_16x16x32_bf16 v[140:143], a[80:83], v[176:179], v[140:143]\n\t"
    "ds_read_b128 a[108:111], v227 offset:57344\n\t"
    "v_cvt_pk_bf16_f32 v153, v130, v131\n\t"
    "v_mfma_f32_16x16x32_bf16 v[32:35], a[128:131], v[148:151], v[32:35]\n\t"
    "v_exp_f32 v132, v132\n\t"
    "v_mfma_f32_16x16x32_bf16 v[32:35], a[160:163], v[164:167], v[32:35]\n\t"
    "ds_read_b128 a[112:115], v224 offset:61440\n\t"
    "ds_read_b128 a[116:119], v225 offset:61440\n\t"
    "v_mfma_f32_16x16x32_bf16 v[36:39], a[132:135], v[148:151], v[36:39]\n\t"
    "v_exp_f32 v133, v133\n\t"
    "v_mfma_f32_16x16x32_bf16 v[36:39], a[164:167], v[164:167], v[36:39]\n\t"
    "ds_read_b128 a[120:123], v226 offset:61440\n\t"
    "v_cvt_pk_bf16_f32 v154, v132, v133\n\t"
    "v_mfma_f32_16x16x32_bf16 v[40:43], a[136:139], v[148:151], v[40:43]\n\t"
    "v_exp_f32 v134, v134\n\t"
    "v_mfma_f32_16x16x32_bf16 v[40:43], a[168:171], v[164:167], v[40:43]\n\t"
    "ds_read_b128 a[124:127], v227 offset:61440\n\t"
    "v_mfma_f32_16x16x32_bf16 v[44:47], a[140:143], v[148:151], v[44:47]\n\t"
    "v_exp_f32 v135, v135\n\t"
    "v_mfma_f32_16x16x32_bf16 v[44:47], a[172:175], v[164:167], v[44:47]\n\t"
    "v_cvt_pk_bf16_f32 v155, v134, v135\n\t"
    "v_mfma_f32_16x16x32_bf16 v[128:131], a[64:67], a[0:3], v[192:195]\n\t"
    "v_exp_f32 v136, v136\n\t"
    "v_mfma_f32_16x16x32_bf16 v[128:131], a[68:71], a[4:7], v[128:131]\n\t"
    "v_add_f32 v221, v221, v136\n\t"
    "v_mfma_f32_16x16x32_bf16 v[128:131], a[72:75], a[8:11], v[128:131]\n\t"
    "s_waitcnt vmcnt(16) lgkmcnt(0)\n\t"
    "v_exp_f32 v137, v137\n\t"
    "v_mfma_f32_16x16x32_bf16 v[128:131], a[76:79], a[12:15], v[128:131]\n\t"
    "s_barrier\n\t"
    "v_cvt_pk_bf16_f32 v156, v136, v137\n\t"
    "v_mfma_f32_16x16x32_bf16 v[132:135], a[92:95], a[12:15], v[192:195]\n\t"
    "v_exp_f32 v138, v138\n\t"
    "v_mfma_f32_16x16x32_bf16 v[132:135], a[88:91], a[8:11], v[132:135]\n\t"
    "v_add_f32 v221, v221, v137\n\t"
    "v_add_f32 v221, v221, v138\n\t"
    "v_mfma_f32_16x16x32_bf16 v[132:135], a[84:87], a[4:7], v[132:135]\n\t"
    "v_exp_f32 v139, v139\n\t"
    "v_mfma_f32_16x16x32_bf16 v[132:135], a[80:83], a[0:3], v[132:135]\n\t"
    "ds_read_b64_tr_b16 a[128:129], v228 offset:16384\n\t"
    "v_cvt_pk_bf16_f32 v157, v138, v139\n\t"
    "v_mfma_f32_16x16x32_bf16 v[208:211], a[192:195], v[160:163], v[208:211]\n\t"
    "v_exp_f32 v140, v140\n\t"
    "v_mfma_f32_16x16x32_bf16 v[16:19], a[144:147], v[144:147], v[16:19]\n\t"
    "v_add_f32 v221, v221, v139\n\t"
    "v_add_f32 v221, v221, v140\n\t"
    "v_mfma_f32_16x16x32_bf16 v[16:19], a[176:179], v[160:163], v[16:19]\n\t"
    "v_exp_f32 v141, v141\n\t"
    "v_mfma_f32_16x16x32_bf16 v[20:23], a[148:151], v[144:147], v[20:23]\n\t"
    "ds_read_b64_tr_b16 a[130:131], v228 offset:20480\n\t"
    "v_cvt_pk_bf16_f32 v158, v140, v141\n\t"
    "v_mfma_f32_16x16x32_bf16 v[20:23], a[180:183], v[160:163], v[20:23]\n\t"
    "v_exp_f32 v142, v142\n\t"
    "v_mfma_f32_16x16x32_bf16 v[24:27], a[152:155], v[144:147], v[24:27]\n\t"
    "v_add_f32 v221, v221, v141\n\t"
    "v_add_f32 v221, v221, v142\n\t"
    "v_mfma_f32_16x16x32_bf16 v[24:27], a[184:187], v[160:163], v[24:27]\n\t"
    "v_exp_f32 v143, v143\n\t"
    "v_mfma_f32_16x16x32_bf16 v[28:31], a[156:159], v[144:147], v[28:31]\n\t"
    "ds_read_b64_tr_b16 a[132:133], v229 offset:16384\n\t"
    "v_cvt_pk_bf16_f32 v159, v142, v143\n\t"
    "v_mfma_f32_16x16x32_bf16 v[28:31], a[188:191], v[160:163], v[28:31]\n\t"
    "s_add_u32 m0, s37, 49152\n\t"
    "ds_read_b64_tr_b16 a[134:135], v229 offset:20480\n\t"
    "v_add_f32 v221, v221, v143\n\t"
    "v_mfma_f32_16x16x32_bf16 v[136:139], a[64:67], a[16:19], v[196:199]\n\t"
    "v_exp_f32 v128, v128\n\t"
    "v_mfma_f32_16x16x32_bf16 v[136:139], a[68:71], a[20:23], v[136:139]\n\t"
    "buffer_load_dwordx4 v236, s[20:23], s36 offen offset:0  lds\n\t"
    "v_mfma_f32_16x16x32_bf16 v[136:139], a[72:75], a[24:27], v[136:139]\n\t"
    "v_exp_f32 v129, v129\n\t"
    "v_mfma_f32_16x16x32_bf16 v[136:139], a[76:79], a[28:31], v[136:139]\n\t"
    "ds_read_b64_tr_b16 a[136:137], v230 offset:16384\n\t"
    "v_cvt_pk_bf16_f32 v144, v128, v129\n\t"
    "v_mfma_f32_16x16x32_bf16 v[140:143], a[92:95], a[28:31], v[196:199]\n\t"
    "v_exp_f32 v130, v130\n\t"
    "v_mfma_f32_16x16x32_bf16 v[140:143], a[88:91], a[24:27], v[140:143]\n\t"
    "buffer_load_dwordx4 v237, s[20:23], s36 offen offset:1024  lds\n\t"
    "v_mfma_f32_16x16x32_bf16 v[140:143], a[84:87], a[20:23], v[140:143]\n\t"
    "v_exp_f32 v131, v131\n\t"
    "v_mfma_f32_16x16x32_bf16 v[140:143], a[80:83], a[16:19], v[140:143]\n\t"
    "ds_read_b64_tr_b16 a[138:139], v230 offset:20480\n\t"
    "v_cvt_pk_bf16_f32 v145, v130, v131\n\t"
    "v_mfma_f32_16x16x32_bf16 v[48:51], a[144:147], v[148:151], v[48:51]\n\t"
    "v_exp_f32 v132, v132\n\t"
    "v_mfma_f32_16x16x32_bf16 v[48:51], a[176:179], v[164:167], v[48:51]\n\t"
    "buffer_load_dwordx4 v238, s[20:23], s36 offen offset:2048  lds\n\t"
    "v_mfma_f32_16x16x32_bf16 v[52:55], a[148:151], v[148:151], v[52:55]\n\t"
    "v_exp_f32 v133, v133\n\t"
    "v_mfma_f32_16x16x32_bf16 v[52:55], a[180:183], v[164:167], v[52:55]\n\t"
    "ds_read_b64_tr_b16 a[140:141], v231 offset:16384\n\t"
    "v_cvt_pk_bf16_f32 v146, v132, v133\n\t"
    "v_mfma_f32_16x16x32_bf16 v[56:59], a[152:155], v[148:151], v[56:59]\n\t"
    "v_exp_f32 v134, v134\n\t"
    "v_mfma_f32_16x16x32_bf16 v[56:59], a[184:187], v[164:167], v[56:59]\n\t"
    "buffer_load_dwordx4 v239, s[20:23], s36 offen offset:3072  lds\n\t"
    "v_mfma_f32_16x16x32_bf16 v[60:63], a[156:159], v[148:151], v[60:63]\n\t"
    "v_exp_f32 v135, v135\n\t"
    "v_mfma_f32_16x16x32_bf16 v[60:63], a[188:191], v[164:167], v[60:63]\n\t"
    "ds_read_b64_tr_b16 a[142:143], v231 offset:20480\n\t"
    "v_cvt_pk_bf16_f32 v147, v134, v135\n\t"
    "v_mfma_f32_16x16x32_bf16 v[128:131], a[96:99], a[0:3], v[192:195]\n\t"
    "v_exp_f32 v136, v136\n\t"
    "v_mfma_f32_16x16x32_bf16 v[128:131], a[100:103], a[4:7], v[128:131]\n\t"
    "ds_read_b64_tr_b16 a[144:145], v232 offset:16384\n\t"
    "v_add_f32 v213, v213, v136\n\t"
    "v_mfma_f32_16x16x32_bf16 v[128:131], a[104:107], a[8:11], v[128:131]\n\t"
    "v_exp_f32 v137, v137\n\t"
    "v_mfma_f32_16x16x32_bf16 v[128:131], a[108:111], a[12:15], v[128:131]\n\t"
    "ds_read_b64_tr_b16 a[146:147], v232 offset:20480\n\t"
    "v_cvt_pk_bf16_f32 v148, v136, v137\n\t"
    "v_mfma_f32_16x16x32_bf16 v[132:135], a[124:127], a[12:15], v[192:195]\n\t"
    "v_exp_f32 v138, v138\n\t"
    "v_mfma_f32_16x16x32_bf16 v[132:135], a[120:123], a[8:11], v[132:135]\n\t"
    "v_add_f32 v213, v213, v137\n\t"
    "v_add_f32 v213, v213, v138\n\t"
    "v_mfma_f32_16x16x32_bf16 v[132:135], a[116:119], a[4:7], v[132:135]\n\t"
    "v_exp_f32 v139, v139\n\t"
    "v_mfma_f32_16x16x32_bf16 v[132:135], a[112:115], a[0:3], v[132:135]\n\t"
    "ds_read_b64_tr_b16 a[148:149], v233 offset:16384\n\t"
    "v_cvt_pk_bf16_f32 v149, v138, v139\n\t"
    "v_mfma_f32_16x16x32_bf16 v[216:219], a[192:195], v[168:171], v[216:219]\n\t"
    "v_exp_f32 v140, v140\n\t"
    "v_mfma_f32_16x16x32_bf16 v[64:67], a[160:163], v[168:171], v[64:67]\n\t"
    "s_waitcnt lgkmcnt(7)\n\t"
    "v_add_f32 v213, v213, v139\n\t"
    "v_add_f32 v213, v213, v140\n\t"
    "v_mfma_f32_16x16x32_bf16 v[64:67], a[128:131], v[152:155], v[64:67]\n\t"
    "v_exp_f32 v141, v141\n\t"
    "v_mfma_f32_16x16x32_bf16 v[68:71], a[164:167], v[168:171], v[68:71]\n\t"
    "ds_read_b64_tr_b16 a[150:151], v233 offset:20480\n\t"
    "v_cvt_pk_bf16_f32 v150, v140, v141\n\t"
    "v_mfma_f32_16x16x32_bf16 v[68:71], a[132:135], v[152:155], v[68:71]\n\t"
    "v_exp_f32 v142, v142\n\t"
    "v_mfma_f32_16x16x32_bf16 v[72:75], a[168:171], v[168:171], v[72:75]\n\t"
    "s_waitcnt lgkmcnt(4)\n\t"
    "v_add_f32 v213, v213, v141\n\t"
    "v_add_f32 v213, v213, v142\n\t"
    "v_mfma_f32_16x16x32_bf16 v[72:75], a[136:139], v[152:155], v[72:75]\n\t"
    "v_exp_f32 v143, v143\n\t"
    "v_mfma_f32_16x16x32_bf16 v[76:79], a[172:175], v[168:171], v[76:79]\n\t"
    "ds_read_b64_tr_b16 a[152:153], v234 offset:16384\n\t"
    "v_cvt_pk_bf16_f32 v151, v142, v143\n\t"
    "v_mfma_f32_16x16x32_bf16 v[76:79], a[140:143], v[152:155], v[76:79]\n\t"
    "ds_read_b64_tr_b16 a[154:155], v234 offset:20480\n\t"
    "v_add_f32 v213, v213, v143\n\t"
    "v_mfma_f32_16x16x32_bf16 v[136:139], a[96:99], a[16:19], v[196:199]\n\t"
    "v_exp_f32 v128, v128\n\t"
    "v_mfma_f32_16x16x32_bf16 v[136:139], a[100:103], a[20:23], v[136:139]\n\t"
    "ds_read_b64_tr_b16 a[156:157], v235 offset:16384\n\t"
    "ds_read_b64_tr_b16 a[158:159], v235 offset:20480\n\t"
    "v_mfma_f32_16x16x32_bf16 v[136:139], a[104:107], a[24:27], v[136:139]\n\t"
    "v_exp_f32 v129, v129\n\t"
    "v_mfma_f32_16x16x32_bf16 v[136:139], a[108:111], a[28:31], v[136:139]\n\t"
    "ds_read_b128 a[64:67], v224 offset:0\n\t"
    "v_cvt_pk_bf16_f32 v160, v128, v129\n\t"
    "v_mfma_f32_16x16x32_bf16 v[140:143], a[124:127], a[28:31], v[196:199]\n\t"
    "v_exp_f32 v130, v130\n\t"
    "v_mfma_f32_16x16x32_bf16 v[140:143], a[120:123], a[24:27], v[140:143]\n\t"
    "ds_read_b128 a[68:71], v225 offset:0\n\t"
    "ds_read_b128 a[72:75], v226 offset:0\n\t"
    "v_mfma_f32_16x16x32_bf16 v[140:143], a[116:119], a[20:23], v[140:143]\n\t"
    "v_exp_f32 v131, v131\n\t"
    "v_mfma_f32_16x16x32_bf16 v[140:143], a[112:115], a[16:19], v[140:143]\n\t"
    "ds_read_b128 a[76:79], v227 offset:0\n\t"
    "v_cvt_pk_bf16_f32 v161, v130, v131\n\t"
    "v_mfma_f32_16x16x32_bf16 v[96:99], a[160:163], v[172:175], v[96:99]\n\t"
    "v_exp_f32 v132, v132\n\t"
    "v_mfma_f32_16x16x32_bf16 v[96:99], a[128:131], v[156:159], v[96:99]\n\t"
    "ds_read_b128 a[80:83], v224 offset:4096\n\t"
    "ds_read_b128 a[84:87], v225 offset:4096\n\t"
    "v_mfma_f32_16x16x32_bf16 v[100:103], a[164:167], v[172:175], v[100:103]\n\t"
    "v_exp_f32 v133, v133\n\t"
    "v_mfma_f32_16x16x32_bf16 v[100:103], a[132:135], v[156:159], v[100:103]\n\t"
    "ds_read_b128 a[88:91], v226 offset:4096\n\t"
    "v_cvt_pk_bf16_f32 v162, v132, v133\n\t"
    "v_mfma_f32_16x16x32_bf16 v[104:107], a[168:171], v[172:175], v[104:107]\n\t"
    "v_exp_f32 v134, v134\n\t"
    "v_mfma_f32_16x16x32_bf16 v[104:107], a[136:139], v[156:159], v[104:107]\n\t"
    "ds_read_b128 a[92:95], v227 offset:4096\n\t"
    "v_mfma_f32_16x16x32_bf16 v[108:111], a[172:175], v[172:175], v[108:111]\n\t"
    "v_exp_f32 v135, v135\n\t"
    "v_mfma_f32_16x16x32_bf16 v[108:111], a[140:143], v[156:159], v[108:111]\n\t"
    "v_cvt_pk_bf16_f32 v163, v134, v135\n\t"
    "v_mfma_f32_16x16x32_bf16 v[128:131], a[96:99], a[32:35], v[200:203]\n\t"
    "v_exp_f32 v136, v136\n\t"
    "v_mfma_f32_16x16x32_bf16 v[128:131], a[100:103], a[36:39], v[128:131]\n\t"
    "ds_read_b64_tr_b16 a[160:161], v228 offset:24576\n\t"
    "v_add_f32 v213, v213, v136\n\t"
    "v_mfma_f32_16x16x32_bf16 v[128:131], a[104:107], a[40:43], v[128:131]\n\t"
    "v_exp_f32 v137, v137\n\t"
    "v_mfma_f32_16x16x32_bf16 v[128:131], a[108:111], a[44:47], v[128:131]\n\t"
    "ds_read_b64_tr_b16 a[162:163], v228 offset:28672\n\t"
    "v_cvt_pk_bf16_f32 v164, v136, v137\n\t"
    "v_mfma_f32_16x16x32_bf16 v[132:135], a[124:127], a[44:47], v[200:203]\n\t"
    "v_exp_f32 v138, v138\n\t"
    "v_mfma_f32_16x16x32_bf16 v[132:135], a[120:123], a[40:43], v[132:135]\n\t"
    "v_add_f32 v213, v213, v137\n\t"
    "v_add_f32 v213, v213, v138\n\t"
    "v_mfma_f32_16x16x32_bf16 v[132:135], a[116:119], a[36:39], v[132:135]\n\t"
    "v_exp_f32 v139, v139\n\t"
    "v_mfma_f32_16x16x32_bf16 v[132:135], a[112:115], a[32:35], v[132:135]\n\t"
    "ds_read_b64_tr_b16 a[164:165], v229 offset:24576\n\t"
    "v_cvt_pk_bf16_f32 v165, v138, v139\n\t"
    "v_mfma_f32_16x16x32_bf16 v[216:219], a[192:195], v[152:155], v[216:219]\n\t"
    "v_exp_f32 v140, v140\n\t"
    "v_mfma_f32_16x16x32_bf16 v[80:83], a[176:179], v[168:171], v[80:83]\n\t"
    "s_waitcnt lgkmcnt(15)\n\t"
    "v_add_f32 v213, v213, v139\n\t"
    "v_add_f32 v213, v213, v140\n\t"
    "v_mfma_f32_16x16x32_bf16 v[80:83], a[144:147], v[152:155], v[80:83]\n\t"
    "v_exp_f32 v141, v141\n\t"
    "v_mfma_f32_16x16x32_bf16 v[84:87], a[180:183], v[168:171], v[84:87]\n\t"
    "ds_read_b64_tr_b16 a[166:167], v229 offset:28672\n\t"
    "v_cvt_pk_bf16_f32 v166, v140, v141\n\t"
    "v_mfma_f32_16x16x32_bf16 v[84:87], a[148:151], v[152:155], v[84:87]\n\t"
    "v_exp_f32 v142, v142\n\t"
    "v_mfma_f32_16x16x32_bf16 v[88:91], a[184:187], v[168:171], v[88:91]\n\t"
    "s_waitcnt lgkmcnt(12)\n\t"
    "v_add_f32 v213, v213, v141\n\t"
    "v_add_f32 v213, v213, v142\n\t"
    "v_mfma_f32_16x16x32_bf16 v[88:91], a[152:155], v[152:155], v[88:91]\n\t"
    "v_exp_f32 v143, v143\n\t"
    "v_mfma_f32_16x16x32_bf16 v[92:95], a[188:191], v[168:171], v[92:95]\n\t"
    "ds_read_b64_tr_b16 a[168:169], v230 offset:24576\n\t"
    "v_cvt_pk_bf16_f32 v167, v142, v143\n\t"
    "v_mfma_f32_16x16x32_bf16 v[92:95], a[156:159], v[152:155], v[92:95]\n\t"
    "s_add_u32 m0, s37, 65536\n\t"
    "ds_read_b64_tr_b16 a[170:171], v230 offset:28672\n\t"
    "v_add_f32 v213, v213, v143\n\t"
    "v_mfma_f32_16x16x32_bf16 v[136:139], a[96:99], v[176:179], v[204:207]\n\t"
    "v_exp_f32 v128, v128\n\t"
    "v_mfma_f32_16x16x32_bf16 v[136:139], a[100:103], v[180:183], v[136:139]\n\t"
    "buffer_load_dwordx4 v240, s[24:27], s36 offen offset:0  lds\n\t"
    "v_mfma_f32_16x16x32_bf16 v[136:139], a[104:107], v[184:187], v[136:139]\n\t"
    "v_exp_f32 v129, v129\n\t"
    "v_mfma_f32_16x16x32_bf16 v[136:139], a[108:111], v[188:191], v[136:139]\n\t"
    "ds_read_b64_tr_b16 a[172:173], v231 offset:24576\n\t"
    "v_cvt_pk_bf16_f32 v168, v128, v129\n\t"
    "v_mfma_f32_16x16x32_bf16 v[140:143], a[124:127], v[188:191], v[204:207]\n\t"
    "v_exp_f32 v130, v130\n\t"
    "v_mfma_f32_16x16x32_bf16 v[140:143], a[120:123], v[184:187], v[140:143]\n\t"
    "buffer_load_dwordx4 v241, s[24:27], s36 offen offset:1024  lds\n\t"
    "v_mfma_f32_16x16x32_bf16 v[140:143], a[116:119], v[180:183], v[140:143]\n\t"
    "v_exp_f32 v131, v131\n\t"
    "v_mfma_f32_16x16x32_bf16 v[140:143], a[112:115], v[176:179], v[140:143]\n\t"
    "ds_read_b64_tr_b16 a[174:175], v231 offset:28672\n\t"
    "v_cvt_pk_bf16_f32 v169, v130, v131\n\t"
    "v_mfma_f32_16x16x32_bf16 v[112:115], a[176:179], v[172:175], v[112:115]\n\t"
    "v_exp_f32 v132, v132\n\t"
    "v_mfma_f32_16x16x32_bf16 v[112:115], a[144:147], v[156:159], v[112:115]\n\t"
    "buffer_load_dwordx4 v240, s[24:27], s36 offen offset:2048  lds\n\t"
    "v_mfma_f32_16x16x32_bf16 v[116:119], a[180:183], v[172:175], v[116:119]\n\t"
    "v_exp_f32 v133, v133\n\t"
    "v_mfma_f32_16x16x32_bf16 v[116:119], a[148:151], v[156:159], v[116:119]\n\t"
    "v_cvt_pk_bf16_f32 v170, v132, v133\n\t"
    "v_mfma_f32_16x16x32_bf16 v[120:123], a[184:187], v[172:175], v[120:123]\n\t"
    "v_exp_f32 v134, v134\n\t"
    "v_mfma_f32_16x16x32_bf16 v[120:123], a[152:155], v[156:159], v[120:123]\n\t"
    "buffer_load_dwordx4 v241, s[24:27], s36 offen offset:3072  lds\n\t"
    "v_mfma_f32_16x16x32_bf16 v[124:127], a[188:191], v[172:175], v[124:127]\n\t"
    "v_exp_f32 v135, v135\n\t"
    "v_mfma_f32_16x16x32_bf16 v[124:127], a[156:159], v[156:159], v[124:127]\n\t"
    "s_waitcnt lgkmcnt(12)\n\t"
    "v_cvt_pk_bf16_f32 v171, v134, v135\n\t"
    "v_mfma_f32_16x16x32_bf16 v[128:131], a[64:67], a[32:35], v[200:203]\n\t"
    "v_exp_f32 v136, v136\n\t"
    "v_mfma_f32_16x16x32_bf16 v[128:131], a[68:71], a[36:39], v[128:131]\n\t"
    "s_add_u32 s36, s36, 16384\n\t"
    "ds_read_b64_tr_b16 a[176:177], v232 offset:24576\n\t"
    "v_add_f32 v221, v221, v136\n\t"
    "v_mfma_f32_16x16x32_bf16 v[128:131], a[72:75], a[40:43], v[128:131]\n\t"
    "v_exp_f32 v137, v137\n\t"
    "v_mfma_f32_16x16x32_bf16 v[128:131], a[76:79], a[44:47], v[128:131]\n\t"
    "s_waitcnt lgkmcnt(9)\n\t"
    "ds_read_b64_tr_b16 a[178:179], v232 offset:28672\n\t"
    "v_cvt_pk_bf16_f32 v172, v136, v137\n\t"
    "v_mfma_f32_16x16x32_bf16 v[132:135], a[92:95], a[44:47], v[200:203]\n\t"
    "v_exp_f32 v138, v138\n\t"
    "v_mfma_f32_16x16x32_bf16 v[132:135], a[88:91], a[40:43], v[132:135]\n\t"
    "v_add_f32 v221, v221, v137\n\t"
    "v_add_f32 v221, v221, v138\n\t"
    "v_mfma_f32_16x16x32_bf16 v[132:135], a[84:87], a[36:39], v[132:135]\n\t"
    "v_exp_f32 v139, v139\n\t"
    "v_mfma_f32_16x16x32_bf16 v[132:135], a[80:83], a[32:35], v[132:135]\n\t"
    "ds_read_b64_tr_b16 a[180:181], v233 offset:24576\n\t"
    "v_cvt_pk_bf16_f32 v173, v138, v139\n\t"
    "v_mfma_f32_16x16x32_bf16 v[208:211], a[192:195], v[144:147], v[208:211]\n\t"
    "v_exp_f32 v140, v140\n\t"
    "v_mfma_f32_16x16x32_bf16 v[0:3], a[128:131], v[144:147], v[0:3]\n\t"
    "s_waitcnt lgkmcnt(7)\n\t"
    "v_add_f32 v221, v221, v139\n\t"
    "v_add_f32 v221, v221, v140\n\t"
    "v_mfma_f32_16x16x32_bf16 v[0:3], a[160:163], v[160:163], v[0:3]\n\t"
    "v_exp_f32 v141, v141\n\t"
    "v_mfma_f32_16x16x32_bf16 v[4:7], a[132:135], v[144:147], v[4:7]\n\t"
    "ds_read_b64_tr_b16 a[182:183], v233 offset:28672\n\t"
    "v_cvt_pk_bf16_f32 v174, v140, v141\n\t"
    "v_mfma_f32_16x16x32_bf16 v[4:7], a[164:167], v[160:163], v[4:7]\n\t"
    "v_exp_f32 v142, v142\n\t"
    "v_mfma_f32_16x16x32_bf16 v[8:11], a[136:139], v[144:147], v[8:11]\n\t"
    "s_waitcnt lgkmcnt(4)\n\t"
    "v_add_f32 v221, v221, v141\n\t"
    "v_add_f32 v221, v221, v142\n\t"
    "v_mfma_f32_16x16x32_bf16 v[8:11], a[168:171], v[160:163], v[8:11]\n\t"
    "v_exp_f32 v143, v143\n\t"
    "v_mfma_f32_16x16x32_bf16 v[12:15], a[140:143], v[144:147], v[12:15]\n\t"
    "ds_read_b64_tr_b16 a[184:185], v234 offset:24576\n\t"
    "v_cvt_pk_bf16_f32 v175, v142, v143\n\t"
    "v_mfma_f32_16x16x32_bf16 v[12:15], a[172:175], v[160:163], v[12:15]\n\t"
    "ds_read_b64_tr_b16 a[186:187], v234 offset:28672\n\t"
    "v_add_f32 v221, v221, v143\n\t"
    "v_mfma_f32_16x16x32_bf16 v[136:139], a[64:67], v[176:179], v[204:207]\n\t"
    "v_exp_f32 v128, v128\n\t"
    "v_mfma_f32_16x16x32_bf16 v[136:139], a[68:71], v[180:183], v[136:139]\n\t"
    "ds_read_b64_tr_b16 a[188:189], v235 offset:24576\n\t"
    "ds_read_b64_tr_b16 a[190:191], v235 offset:28672\n\t"
    "v_mfma_f32_16x16x32_bf16 v[136:139], a[72:75], v[184:187], v[136:139]\n\t"
    "v_exp_f32 v129, v129\n\t"
    "v_mfma_f32_16x16x32_bf16 v[136:139], a[76:79], v[188:191], v[136:139]\n\t"
    "ds_read_b128 a[96:99], v224 offset:8192\n\t"
    "v_cvt_pk_bf16_f32 v152, v128, v129\n\t"
    "v_mfma_f32_16x16x32_bf16 v[140:143], a[92:95], v[188:191], v[204:207]\n\t"
    "v_exp_f32 v130, v130\n\t"
    "v_mfma_f32_16x16x32_bf16 v[140:143], a[88:91], v[184:187], v[140:143]\n\t"
    "ds_read_b128 a[100:103], v225 offset:8192\n\t"
    "ds_read_b128 a[104:107], v226 offset:8192\n\t"
    "v_mfma_f32_16x16x32_bf16 v[140:143], a[84:87], v[180:183], v[140:143]\n\t"
    "v_exp_f32 v131, v131\n\t"
    "v_mfma_f32_16x16x32_bf16 v[140:143], a[80:83], v[176:179], v[140:143]\n\t"
    "ds_read_b128 a[108:111], v227 offset:8192\n\t"
    "v_cvt_pk_bf16_f32 v153, v130, v131\n\t"
    "v_mfma_f32_16x16x32_bf16 v[32:35], a[128:131], v[148:151], v[32:35]\n\t"
    "v_exp_f32 v132, v132\n\t"
    "v_mfma_f32_16x16x32_bf16 v[32:35], a[160:163], v[164:167], v[32:35]\n\t"
    "ds_read_b128 a[112:115], v224 offset:12288\n\t"
    "ds_read_b128 a[116:119], v225 offset:12288\n\t"
    "v_mfma_f32_16x16x32_bf16 v[36:39], a[132:135], v[148:151], v[36:39]\n\t"
    "v_exp_f32 v133, v133\n\t"
    "v_mfma_f32_16x16x32_bf16 v[36:39], a[164:167], v[164:167], v[36:39]\n\t"
    "ds_read_b128 a[120:123], v226 offset:12288\n\t"
    "v_cvt_pk_bf16_f32 v154, v132, v133\n\t"
    "v_mfma_f32_16x16x32_bf16 v[40:43], a[136:139], v[148:151], v[40:43]\n\t"
    "v_exp_f32 v134, v134\n\t"
    "v_mfma_f32_16x16x32_bf16 v[40:43], a[168:171], v[164:167], v[40:43]\n\t"
    "ds_read_b128 a[124:127], v227 offset:12288\n\t"
    "v_mfma_f32_16x16x32_bf16 v[44:47], a[140:143], v[148:151], v[44:47]\n\t"
    "v_exp_f32 v135, v135\n\t"
    "v_mfma_f32_16x16x32_bf16 v[44:47], a[172:175], v[164:167], v[44:47]\n\t"
    "v_cvt_pk_bf16_f32 v155, v134, v135\n\t"
    "v_mfma_f32_16x16x32_bf16 v[128:131], a[64:67], a[0:3], v[192:195]\n\t"
    "v_exp_f32 v136, v136\n\t"
    "v_mfma_f32_16x16x32_bf16 v[128:131], a[68:71], a[4:7], v[128:131]\n\t"
    "v_add_f32 v221, v221, v136\n\t"
    "v_mfma_f32_16x16x32_bf16 v[128:131], a[72:75], a[8:11], v[128:131]\n\t"
    "s_waitcnt vmcnt(16) lgkmcnt(0)\n\t"
    "v_exp_f32 v137, v137\n\t"
    "v_mfma_f32_16x16x32_bf16 v[128:131], a[76:79], a[12:15], v[128:131]\n\t"
    "s_barrier\n\t"
    "v_cvt_pk_bf16_f32 v156, v136, v137\n\t"
    "v_mfma_f32_16x16x32_bf16 v[132:135], a[92:95], a[12:15], v[192:195]\n\t"
    "v_exp_f32 v138, v138\n\t"
    "v_mfma_f32_16x16x32_bf16 v[132:135], a[88:91], a[8:11], v[132:135]\n\t"
    "v_add_f32 v221, v221, v137\n\t"
    "v_add_f32 v221, v221, v138\n\t"
    "v_mfma_f32_16x16x32_bf16 v[132:135], a[84:87], a[4:7], v[132:135]\n\t"
    "v_exp_f32 v139, v139\n\t"
    "v_mfma_f32_16x16x32_bf16 v[132:135], a[80:83], a[0:3], v[132:135]\n\t"
    "ds_read_b64_tr_b16 a[128:129], v228 offset:32768\n\t"
    "v_cvt_pk_bf16_f32 v157, v138, v139\n\t"
    "v_mfma_f32_16x16x32_bf16 v[208:211], a[192:195], v[160:163], v[208:211]\n\t"
    "v_exp_f32 v140, v140\n\t"
    "v_mfma_f32_16x16x32_bf16 v[16:19], a[144:147], v[144:147], v[16:19]\n\t"
    "v_add_f32 v221, v221, v139\n\t"
    "v_add_f32 v221, v221, v140\n\t"
    "v_mfma_f32_16x16x32_bf16 v[16:19], a[176:179], v[160:163], v[16:19]\n\t"
    "v_exp_f32 v141, v141\n\t"
    "v_mfma_f32_16x16x32_bf16 v[20:23], a[148:151], v[144:147], v[20:23]\n\t"
    "ds_read_b64_tr_b16 a[130:131], v228 offset:36864\n\t"
    "v_cvt_pk_bf16_f32 v158, v140, v141\n\t"
    "v_mfma_f32_16x16x32_bf16 v[20:23], a[180:183], v[160:163], v[20:23]\n\t"
    "v_exp_f32 v142, v142\n\t"
    "v_mfma_f32_16x16x32_bf16 v[24:27], a[152:155], v[144:147], v[24:27]\n\t"
    "v_add_f32 v221, v221, v141\n\t"
    "v_add_f32 v221, v221, v142\n\t"
    "v_mfma_f32_16x16x32_bf16 v[24:27], a[184:187], v[160:163], v[24:27]\n\t"
    "v_exp_f32 v143, v143\n\t"
    "v_mfma_f32_16x16x32_bf16 v[28:31], a[156:159], v[144:147], v[28:31]\n\t"
    "ds_read_b64_tr_b16 a[132:133], v229 offset:32768\n\t"
    "v_cvt_pk_bf16_f32 v159, v142, v143\n\t"
    "v_mfma_f32_16x16x32_bf16 v[28:31], a[188:191], v[160:163], v[28:31]\n\t"
    "s_add_u32 m0, s37, 0\n\t"
    "ds_read_b64_tr_b16 a[134:135], v229 offset:36864\n\t"
    "v_add_f32 v221, v221, v143\n\t"
    "v_mfma_f32_16x16x32_bf16 v[136:139], a[64:67], a[16:19], v[196:199]\n\t"
    "v_exp_f32 v128, v128\n\t"
    "v_mfma_f32_16x16x32_bf16 v[136:139], a[68:71], a[20:23], v[136:139]\n\t"
    "buffer_load_dwordx4 v236, s[20:23], s36 offen offset:0  lds\n\t"
    "v_mfma_f32_16x16x32_bf16 v[136:139], a[72:75], a[24:27], v[136:139]\n\t"
    "v_exp_f32 v129, v129\n\t"
    "v_mfma_f32_16x16x32_bf16 v[136:139], a[76:79], a[28:31], v[136:139]\n\t"
    "ds_read_b64_tr_b16 a[136:137], v230 offset:32768\n\t"
    "v_cvt_pk_bf16_f32 v144, v128, v129\n\t"
    "v_mfma_f32_16x16x32_bf16 v[140:143], a[92:95], a[28:31], v[196:199]\n\t"
    "v_exp_f32 v130, v130\n\t"
    "v_mfma_f32_16x16x32_bf16 v[140:143], a[88:91], a[24:27], v[140:143]\n\t"
    "buffer_load_dwordx4 v237, s[20:23], s36 offen offset:1024  lds\n\t"
    "v_mfma_f32_16x16x32_bf16 v[140:143], a[84:87], a[20:23], v[140:143]\n\t"
    "v_exp_f32 v131, v131\n\t"
    "v_mfma_f32_16x16x32_bf16 v[140:143], a[80:83], a[16:19], v[140:143]\n\t"
    "ds_read_b64_tr_b16 a[138:139], v230 offset:36864\n\t"
    "v_cvt_pk_bf16_f32 v145, v130, v131\n\t"
    "v_mfma_f32_16x16x32_bf16 v[48:51], a[144:147], v[148:151], v[48:51]\n\t"
    "v_exp_f32 v132, v132\n\t"
    "v_mfma_f32_16x16x32_bf16 v[48:51], a[176:179], v[164:167], v[48:51]\n\t"
    "buffer_load_dwordx4 v238, s[20:23], s36 offen offset:2048  lds\n\t"
    "v_mfma_f32_16x16x32_bf16 v[52:55], a[148:151], v[148:151], v[52:55]\n\t"
    "v_exp_f32 v133, v133\n\t"
    "v_mfma_f32_16x16x32_bf16 v[52:55], a[180:183], v[164:167], v[52:55]\n\t"
    "ds_read_b64_tr_b16 a[140:141], v231 offset:32768\n\t"
    "v_cvt_pk_bf16_f32 v146, v132, v133\n\t"
    "v_mfma_f32_16x16x32_bf16 v[56:59], a[152:155], v[148:151], v[56:59]\n\t"
    "v_exp_f32 v134, v134\n\t"
    "v_mfma_f32_16x16x32_bf16 v[56:59], a[184:187], v[164:167], v[56:59]\n\t"
    "buffer_load_dwordx4 v239, s[20:23], s36 offen offset:3072  lds\n\t"
    "v_mfma_f32_16x16x32_bf16 v[60:63], a[156:159], v[148:151], v[60:63]\n\t"
    "v_exp_f32 v135, v135\n\t"
    "v_mfma_f32_16x16x32_bf16 v[60:63], a[188:191], v[164:167], v[60:63]\n\t"
    "ds_read_b64_tr_b16 a[142:143], v231 offset:36864\n\t"
    "v_cvt_pk_bf16_f32 v147, v134, v135\n\t"
    "v_mfma_f32_16x16x32_bf16 v[128:131], a[96:99], a[0:3], v[192:195]\n\t"
    "v_exp_f32 v136, v136\n\t"
    "v_mfma_f32_16x16x32_bf16 v[128:131], a[100:103], a[4:7], v[128:131]\n\t"
    "ds_read_b64_tr_b16 a[144:145], v232 offset:32768\n\t"
    "v_add_f32 v213, v213, v136\n\t"
    "v_mfma_f32_16x16x32_bf16 v[128:131], a[104:107], a[8:11], v[128:131]\n\t"
    "v_exp_f32 v137, v137\n\t"
    "v_mfma_f32_16x16x32_bf16 v[128:131], a[108:111], a[12:15], v[128:131]\n\t"
    "ds_read_b64_tr_b16 a[146:147], v232 offset:36864\n\t"
    "v_cvt_pk_bf16_f32 v148, v136, v137\n\t"
    "v_mfma_f32_16x16x32_bf16 v[132:135], a[124:127], a[12:15], v[192:195]\n\t"
    "v_exp_f32 v138, v138\n\t"
    "v_mfma_f32_16x16x32_bf16 v[132:135], a[120:123], a[8:11], v[132:135]\n\t"
    "v_add_f32 v213, v213, v137\n\t"
    "v_add_f32 v213, v213, v138\n\t"
    "v_mfma_f32_16x16x32_bf16 v[132:135], a[116:119], a[4:7], v[132:135]\n\t"
    "v_exp_f32 v139, v139\n\t"
    "v_mfma_f32_16x16x32_bf16 v[132:135], a[112:115], a[0:3], v[132:135]\n\t"
    "ds_read_b64_tr_b16 a[148:149], v233 offset:32768\n\t"
    "v_cvt_pk_bf16_f32 v149, v138, v139\n\t"
    "v_mfma_f32_16x16x32_bf16 v[216:219], a[192:195], v[168:171], v[216:219]\n\t"
    "v_exp_f32 v140, v140\n\t"
    "v_mfma_f32_16x16x32_bf16 v[64:67], a[160:163], v[168:171], v[64:67]\n\t"
    "s_waitcnt lgkmcnt(7)\n\t"
    "v_add_f32 v213, v213, v139\n\t"
    "v_add_f32 v213, v213, v140\n\t"
    "v_mfma_f32_16x16x32_bf16 v[64:67], a[128:131], v[152:155], v[64:67]\n\t"
    "v_exp_f32 v141, v141\n\t"
    "v_mfma_f32_16x16x32_bf16 v[68:71], a[164:167], v[168:171], v[68:71]\n\t"
    "ds_read_b64_tr_b16 a[150:151], v233 offset:36864\n\t"
    "v_cvt_pk_bf16_f32 v150, v140, v141\n\t"
    "v_mfma_f32_16x16x32_bf16 v[68:71], a[132:135], v[152:155], v[68:71]\n\t"
    "v_exp_f32 v142, v142\n\t"
    "v_mfma_f32_16x16x32_bf16 v[72:75], a[168:171], v[168:171], v[72:75]\n\t"
    "s_waitcnt lgkmcnt(4)\n\t"
    "v_add_f32 v213, v213, v141\n\t"
    "v_add_f32 v213, v213, v142\n\t"
    "v_mfma_f32_16x16x32_bf16 v[72:75], a[136:139], v[152:155], v[72:75]\n\t"
    "v_exp_f32 v143, v143\n\t"
    "v_mfma_f32_16x16x32_bf16 v[76:79], a[172:175], v[168:171], v[76:79]\n\t"
    "ds_read_b64_tr_b16 a[152:153], v234 offset:32768\n\t"
    "v_cvt_pk_bf16_f32 v151, v142, v143\n\t"
    "v_mfma_f32_16x16x32_bf16 v[76:79], a[140:143], v[152:155], v[76:79]\n\t"
    "ds_read_b64_tr_b16 a[154:155], v234 offset:36864\n\t"
    "v_add_f32 v213, v213, v143\n\t"
    "v_mfma_f32_16x16x32_bf16 v[136:139], a[96:99], a[16:19], v[196:199]\n\t"
    "v_exp_f32 v128, v128\n\t"
    "v_mfma_f32_16x16x32_bf16 v[136:139], a[100:103], a[20:23], v[136:139]\n\t"
    "ds_read_b64_tr_b16 a[156:157], v235 offset:32768\n\t"
    "ds_read_b64_tr_b16 a[158:159], v235 offset:36864\n\t"
    "v_mfma_f32_16x16x32_bf16 v[136:139], a[104:107], a[24:27], v[136:139]\n\t"
    "v_exp_f32 v129, v129\n\t"
    "v_mfma_f32_16x16x32_bf16 v[136:139], a[108:111], a[28:31], v[136:139]\n\t"
    "ds_read_b128 a[64:67], v224 offset:16384\n\t"
    "v_cvt_pk_bf16_f32 v160, v128, v129\n\t"
    "v_mfma_f32_16x16x32_bf16 v[140:143], a[124:127], a[28:31], v[196:199]\n\t"
    "v_exp_f32 v130, v130\n\t"
    "v_mfma_f32_16x16x32_bf16 v[140:143], a[120:123], a[24:27], v[140:143]\n\t"
    "ds_read_b128 a[68:71], v225 offset:16384\n\t"
    "ds_read_b128 a[72:75], v226 offset:16384\n\t"
    "v_mfma_f32_16x16x32_bf16 v[140:143], a[116:119], a[20:23], v[140:143]\n\t"
    "v_exp_f32 v131, v131\n\t"
    "v_mfma_f32_16x16x32_bf16 v[140:143], a[112:115], a[16:19], v[140:143]\n\t"
    "ds_read_b128 a[76:79], v227 offset:16384\n\t"
    "v_cvt_pk_bf16_f32 v161, v130, v131\n\t"
    "v_mfma_f32_16x16x32_bf16 v[96:99], a[160:163], v[172:175], v[96:99]\n\t"
    "v_exp_f32 v132, v132\n\t"
    "v_mfma_f32_16x16x32_bf16 v[96:99], a[128:131], v[156:159], v[96:99]\n\t"
    "ds_read_b128 a[80:83], v224 offset:20480\n\t"
    "ds_read_b128 a[84:87], v225 offset:20480\n\t"
    "v_mfma_f32_16x16x32_bf16 v[100:103], a[164:167], v[172:175], v[100:103]\n\t"
    "v_exp_f32 v133, v133\n\t"
    "v_mfma_f32_16x16x32_bf16 v[100:103], a[132:135], v[156:159], v[100:103]\n\t"
    "ds_read_b128 a[88:91], v226 offset:20480\n\t"
    "v_cvt_pk_bf16_f32 v162, v132, v133\n\t"
    "v_mfma_f32_16x16x32_bf16 v[104:107], a[168:171], v[172:175], v[104:107]\n\t"
    "v_exp_f32 v134, v134\n\t"
    "v_mfma_f32_16x16x32_bf16 v[104:107], a[136:139], v[156:159], v[104:107]\n\t"
    "ds_read_b128 a[92:95], v227 offset:20480\n\t"
    "v_mfma_f32_16x16x32_bf16 v[108:111], a[172:175], v[172:175], v[108:111]\n\t"
    "v_exp_f32 v135, v135\n\t"
    "v_mfma_f32_16x16x32_bf16 v[108:111], a[140:143], v[156:159], v[108:111]\n\t"
    "v_cvt_pk_bf16_f32 v163, v134, v135\n\t"
    "v_mfma_f32_16x16x32_bf16 v[128:131], a[96:99], a[32:35], v[200:203]\n\t"
    "v_exp_f32 v136, v136\n\t"
    "v_mfma_f32_16x16x32_bf16 v[128:131], a[100:103], a[36:39], v[128:131]\n\t"
    "ds_read_b64_tr_b16 a[160:161], v228 offset:40960\n\t"
    "v_add_f32 v213, v213, v136\n\t"
    "v_mfma_f32_16x16x32_bf16 v[128:131], a[104:107], a[40:43], v[128:131]\n\t"
    "v_exp_f32 v137, v137\n\t"
    "v_mfma_f32_16x16x32_bf16 v[128:131], a[108:111], a[44:47], v[128:131]\n\t"
    "ds_read_b64_tr_b16 a[162:163], v228 offset:45056\n\t"
    "v_cvt_pk_bf16_f32 v164, v136, v137\n\t"
    "v_mfma_f32_16x16x32_bf16 v[132:135], a[124:127], a[44:47], v[200:203]\n\t"
    "v_exp_f32 v138, v138\n\t"
    "v_mfma_f32_16x16x32_bf16 v[132:135], a[120:123], a[40:43], v[132:135]\n\t"
    "v_add_f32 v213, v213, v137\n\t"
    "v_add_f32 v213, v213, v138\n\t"
    "v_mfma_f32_16x16x32_bf16 v[132:135], a[116:119], a[36:39], v[132:135]\n\t"
    "v_exp_f32 v139, v139\n\t"
    "v_mfma_f32_16x16x32_bf16 v[132:135], a[112:115], a[32:35], v[132:135]\n\t"
    "ds_read_b64_tr_b16 a[164:165], v229 offset:40960\n\t"
    "v_cvt_pk_bf16_f32 v165, v138, v139\n\t"
    "v_mfma_f32_16x16x32_bf16 v[216:219], a[192:195], v[152:155], v[216:219]\n\t"
    "v_exp_f32 v140, v140\n\t"
    "v_mfma_f32_16x16x32_bf16 v[80:83], a[176:179], v[168:171], v[80:83]\n\t"
    "s_waitcnt lgkmcnt(15)\n\t"
    "v_add_f32 v213, v213, v139\n\t"
    "v_add_f32 v213, v213, v140\n\t"
    "v_mfma_f32_16x16x32_bf16 v[80:83], a[144:147], v[152:155], v[80:83]\n\t"
    "v_exp_f32 v141, v141\n\t"
    "v_mfma_f32_16x16x32_bf16 v[84:87], a[180:183], v[168:171], v[84:87]\n\t"
    "ds_read_b64_tr_b16 a[166:167], v229 offset:45056\n\t"
    "v_cvt_pk_bf16_f32 v166, v140, v141\n\t"
    "v_mfma_f32_16x16x32_bf16 v[84:87], a[148:151], v[152:155], v[84:87]\n\t"
    "v_exp_f32 v142, v142\n\t"
    "v_mfma_f32_16x16x32_bf16 v[88:91], a[184:187], v[168:171], v[88:91]\n\t"
    "s_waitcnt lgkmcnt(12)\n\t"
    "v_add_f32 v213, v213, v141\n\t"
    "v_add_f32 v213, v213, v142\n\t"
    "v_mfma_f32_16x16x32_bf16 v[88:91], a[152:155], v[152:155], v[88:91]\n\t"
    "v_exp_f32 v143, v143\n\t"
    "v_mfma_f32_16x16x32_bf16 v[92:95], a[188:191], v[168:171], v[92:95]\n\t"
    "ds_read_b64_tr_b16 a[168:169], v230 offset:40960\n\t"
    "v_cvt_pk_bf16_f32 v167, v142, v143\n\t"
    "v_mfma_f32_16x16x32_bf16 v[92:95], a[156:159], v[152:155], v[92:95]\n\t"
    "s_add_u32 m0, s37, 81920\n\t"
    "ds_read_b64_tr_b16 a[170:171], v230 offset:45056\n\t"
    "v_add_f32 v213, v213, v143\n\t"
    "v_mfma_f32_16x16x32_bf16 v[136:139], a[96:99], v[176:179], v[204:207]\n\t"
    "v_exp_f32 v128, v128\n\t"
    "v_mfma_f32_16x16x32_bf16 v[136:139], a[100:103], v[180:183], v[136:139]\n\t"
    "buffer_load_dwordx4 v240, s[24:27], s36 offen offset:0  lds\n\t"
    "v_mfma_f32_16x16x32_bf16 v[136:139], a[104:107], v[184:187], v[136:139]\n\t"
    "v_exp_f32 v129, v129\n\t"
    "v_mfma_f32_16x16x32_bf16 v[136:139], a[108:111], v[188:191], v[136:139]\n\t"
    "ds_read_b64_tr_b16 a[172:173], v231 offset:40960\n\t"
    "v_cvt_pk_bf16_f32 v168, v128, v129\n\t"
    "v_mfma_f32_16x16x32_bf16 v[140:143], a[124:127], v[188:191], v[204:207]\n\t"
    "v_exp_f32 v130, v130\n\t"
    "v_mfma_f32_16x16x32_bf16 v[140:143], a[120:123], v[184:187], v[140:143]\n\t"
    "buffer_load_dwordx4 v241, s[24:27], s36 offen offset:1024  lds\n\t"
    "v_mfma_f32_16x16x32_bf16 v[140:143], a[116:119], v[180:183], v[140:143]\n\t"
    "v_exp_f32 v131, v131\n\t"
    "v_mfma_f32_16x16x32_bf16 v[140:143], a[112:115], v[176:179], v[140:143]\n\t"
    "ds_read_b64_tr_b16 a[174:175], v231 offset:45056\n\t"
    "v_cvt_pk_bf16_f32 v169, v130, v131\n\t"
    "v_mfma_f32_16x16x32_bf16 v[112:115], a[176:179], v[172:175], v[112:115]\n\t"
    "v_exp_f32 v132, v132\n\t"
    "v_mfma_f32_16x16x32_bf16 v[112:115], a[144:147], v[156:159], v[112:115]\n\t"
    "buffer_load_dwordx4 v240, s[24:27], s36 offen offset:2048  lds\n\t"
    "v_mfma_f32_16x16x32_bf16 v[116:119], a[180:183], v[172:175], v[116:119]\n\t"
    "v_exp_f32 v133, v133\n\t"
    "v_mfma_f32_16x16x32_bf16 v[116:119], a[148:151], v[156:159], v[116:119]\n\t"
    "v_cvt_pk_bf16_f32 v170, v132, v133\n\t"
    "v_mfma_f32_16x16x32_bf16 v[120:123], a[184:187], v[172:175], v[120:123]\n\t"
    "v_exp_f32 v134, v134\n\t"
    "v_mfma_f32_16x16x32_bf16 v[120:123], a[152:155], v[156:159], v[120:123]\n\t"
    "buffer_load_dwordx4 v241, s[24:27], s36 offen offset:3072  lds\n\t"
    "v_mfma_f32_16x16x32_bf16 v[124:127], a[188:191], v[172:175], v[124:127]\n\t"
    "v_exp_f32 v135, v135\n\t"
    "v_mfma_f32_16x16x32_bf16 v[124:127], a[156:159], v[156:159], v[124:127]\n\t"
    "s_waitcnt lgkmcnt(12)\n\t"
    "v_cvt_pk_bf16_f32 v171, v134, v135\n\t"
    "v_mfma_f32_16x16x32_bf16 v[128:131], a[64:67], a[32:35], v[200:203]\n\t"
    "v_exp_f32 v136, v136\n\t"
    "v_mfma_f32_16x16x32_bf16 v[128:131], a[68:71], a[36:39], v[128:131]\n\t"
    "s_add_u32 s36, s36, 16384\n\t"
    "ds_read_b64_tr_b16 a[176:177], v232 offset:40960\n\t"
    "v_add_f32 v221, v221, v136\n\t"
    "v_mfma_f32_16x16x32_bf16 v[128:131], a[72:75], a[40:43], v[128:131]\n\t"
    "v_exp_f32 v137, v137\n\t"
    "v_mfma_f32_16x16x32_bf16 v[128:131], a[76:79], a[44:47], v[128:131]\n\t"
    "s_waitcnt lgkmcnt(9)\n\t"
    "ds_read_b64_tr_b16 a[178:179], v232 offset:45056\n\t"
    "v_cvt_pk_bf16_f32 v172, v136, v137\n\t"
    "v_mfma_f32_16x16x32_bf16 v[132:135], a[92:95], a[44:47], v[200:203]\n\t"
    "v_exp_f32 v138, v138\n\t"
    "v_mfma_f32_16x16x32_bf16 v[132:135], a[88:91], a[40:43], v[132:135]\n\t"
    "v_add_f32 v221, v221, v137\n\t"
    "v_add_f32 v221, v221, v138\n\t"
    "v_mfma_f32_16x16x32_bf16 v[132:135], a[84:87], a[36:39], v[132:135]\n\t"
    "v_exp_f32 v139, v139\n\t"
    "v_mfma_f32_16x16x32_bf16 v[132:135], a[80:83], a[32:35], v[132:135]\n\t"
    "ds_read_b64_tr_b16 a[180:181], v233 offset:40960\n\t"
    "v_cvt_pk_bf16_f32 v173, v138, v139\n\t"
    "v_mfma_f32_16x16x32_bf16 v[208:211], a[192:195], v[144:147], v[208:211]\n\t"
    "v_exp_f32 v140, v140\n\t"
    "v_mfma_f32_16x16x32_bf16 v[0:3], a[128:131], v[144:147], v[0:3]\n\t"
    "s_waitcnt lgkmcnt(7)\n\t"
    "v_add_f32 v221, v221, v139\n\t"
    "v_add_f32 v221, v221, v140\n\t"
    "v_mfma_f32_16x16x32_bf16 v[0:3], a[160:163], v[160:163], v[0:3]\n\t"
    "v_exp_f32 v141, v141\n\t"
    "v_mfma_f32_16x16x32_bf16 v[4:7], a[132:135], v[144:147], v[4:7]\n\t"
    "ds_read_b64_tr_b16 a[182:183], v233 offset:45056\n\t"
    "v_cvt_pk_bf16_f32 v174, v140, v141\n\t"
    "v_mfma_f32_16x16x32_bf16 v[4:7], a[164:167], v[160:163], v[4:7]\n\t"
    "v_exp_f32 v142, v142\n\t"
    "v_mfma_f32_16x16x32_bf16 v[8:11], a[136:139], v[144:147], v[8:11]\n\t"
    "s_waitcnt lgkmcnt(4)\n\t"
    "v_add_f32 v221, v221, v141\n\t"
    "v_add_f32 v221, v221, v142\n\t"
    "v_mfma_f32_16x16x32_bf16 v[8:11], a[168:171], v[160:163], v[8:11]\n\t"
    "v_exp_f32 v143, v143\n\t"
    "v_mfma_f32_16x16x32_bf16 v[12:15], a[140:143], v[144:147], v[12:15]\n\t"
    "ds_read_b64_tr_b16 a[184:185], v234 offset:40960\n\t"
    "v_cvt_pk_bf16_f32 v175, v142, v143\n\t"
    "v_mfma_f32_16x16x32_bf16 v[12:15], a[172:175], v[160:163], v[12:15]\n\t"
    "ds_read_b64_tr_b16 a[186:187], v234 offset:45056\n\t"
    "v_add_f32 v221, v221, v143\n\t"
    "v_mfma_f32_16x16x32_bf16 v[136:139], a[64:67], v[176:179], v[204:207]\n\t"
    "v_exp_f32 v128, v128\n\t"
    "v_mfma_f32_16x16x32_bf16 v[136:139], a[68:71], v[180:183], v[136:139]\n\t"
    "ds_read_b64_tr_b16 a[188:189], v235 offset:40960\n\t"
    "ds_read_b64_tr_b16 a[190:191], v235 offset:45056\n\t"
    "v_mfma_f32_16x16x32_bf16 v[136:139], a[72:75], v[184:187], v[136:139]\n\t"
    "v_exp_f32 v129, v129\n\t"
    "v_mfma_f32_16x16x32_bf16 v[136:139], a[76:79], v[188:191], v[136:139]\n\t"
    "ds_read_b128 a[96:99], v224 offset:24576\n\t"
    "v_cvt_pk_bf16_f32 v152, v128, v129\n\t"
    "v_mfma_f32_16x16x32_bf16 v[140:143], a[92:95], v[188:191], v[204:207]\n\t"
    "v_exp_f32 v130, v130\n\t"
    "v_mfma_f32_16x16x32_bf16 v[140:143], a[88:91], v[184:187], v[140:143]\n\t"
    "ds_read_b128 a[100:103], v225 offset:24576\n\t"
    "ds_read_b128 a[104:107], v226 offset:24576\n\t"
    "v_mfma_f32_16x16x32_bf16 v[140:143], a[84:87], v[180:183], v[140:143]\n\t"
    "v_exp_f32 v131, v131\n\t"
    "v_mfma_f32_16x16x32_bf16 v[140:143], a[80:83], v[176:179], v[140:143]\n\t"
    "ds_read_b128 a[108:111], v227 offset:24576\n\t"
    "v_cvt_pk_bf16_f32 v153, v130, v131\n\t"
    "v_mfma_f32_16x16x32_bf16 v[32:35], a[128:131], v[148:151], v[32:35]\n\t"
    "v_exp_f32 v132, v132\n\t"
    "v_mfma_f32_16x16x32_bf16 v[32:35], a[160:163], v[164:167], v[32:35]\n\t"
    "ds_read_b128 a[112:115], v224 offset:28672\n\t"
    "ds_read_b128 a[116:119], v225 offset:28672\n\t"
    "v_mfma_f32_16x16x32_bf16 v[36:39], a[132:135], v[148:151], v[36:39]\n\t"
    "v_exp_f32 v133, v133\n\t"
    "v_mfma_f32_16x16x32_bf16 v[36:39], a[164:167], v[164:167], v[36:39]\n\t"
    "ds_read_b128 a[120:123], v226 offset:28672\n\t"
    "v_cvt_pk_bf16_f32 v154, v132, v133\n\t"
    "v_mfma_f32_16x16x32_bf16 v[40:43], a[136:139], v[148:151], v[40:43]\n\t"
    "v_exp_f32 v134, v134\n\t"
    "v_mfma_f32_16x16x32_bf16 v[40:43], a[168:171], v[164:167], v[40:43]\n\t"
    "ds_read_b128 a[124:127], v227 offset:28672\n\t"
    "v_mfma_f32_16x16x32_bf16 v[44:47], a[140:143], v[148:151], v[44:47]\n\t"
    "v_exp_f32 v135, v135\n\t"
    "v_mfma_f32_16x16x32_bf16 v[44:47], a[172:175], v[164:167], v[44:47]\n\t"
    "v_cvt_pk_bf16_f32 v155, v134, v135\n\t"
    "v_mfma_f32_16x16x32_bf16 v[128:131], a[64:67], a[0:3], v[192:195]\n\t"
    "v_exp_f32 v136, v136\n\t"
    "v_mfma_f32_16x16x32_bf16 v[128:131], a[68:71], a[4:7], v[128:131]\n\t"
    "v_add_f32 v221, v221, v136\n\t"
    "v_mfma_f32_16x16x32_bf16 v[128:131], a[72:75], a[8:11], v[128:131]\n\t"
    "s_waitcnt vmcnt(16) lgkmcnt(0)\n\t"
    "v_exp_f32 v137, v137\n\t"
    "v_mfma_f32_16x16x32_bf16 v[128:131], a[76:79], a[12:15], v[128:131]\n\t"
    "s_barrier\n\t"
    "v_cvt_pk_bf16_f32 v156, v136, v137\n\t"
    "v_mfma_f32_16x16x32_bf16 v[132:135], a[92:95], a[12:15], v[192:195]\n\t"
    "v_exp_f32 v138, v138\n\t"
    "v_mfma_f32_16x16x32_bf16 v[132:135], a[88:91], a[8:11], v[132:135]\n\t"
    "v_add_f32 v221, v221, v137\n\t"
    "v_add_f32 v221, v221, v138\n\t"
    "v_mfma_f32_16x16x32_bf16 v[132:135], a[84:87], a[4:7], v[132:135]\n\t"
    "v_exp_f32 v139, v139\n\t"
    "v_mfma_f32_16x16x32_bf16 v[132:135], a[80:83], a[0:3], v[132:135]\n\t"
    "ds_read_b64_tr_b16 a[128:129], v228 offset:49152\n\t"
    "v_cvt_pk_bf16_f32 v157, v138, v139\n\t"
    "v_mfma_f32_16x16x32_bf16 v[208:211], a[192:195], v[160:163], v[208:211]\n\t"
    "v_exp_f32 v140, v140\n\t"
    "v_mfma_f32_16x16x32_bf16 v[16:19], a[144:147], v[144:147], v[16:19]\n\t"
    "v_add_f32 v221, v221, v139\n\t"
    "v_add_f32 v221, v221, v140\n\t"
    "v_mfma_f32_16x16x32_bf16 v[16:19], a[176:179], v[160:163], v[16:19]\n\t"
    "v_exp_f32 v141, v141\n\t"
    "v_mfma_f32_16x16x32_bf16 v[20:23], a[148:151], v[144:147], v[20:23]\n\t"
    "ds_read_b64_tr_b16 a[130:131], v228 offset:53248\n\t"
    "v_cvt_pk_bf16_f32 v158, v140, v141\n\t"
    "v_mfma_f32_16x16x32_bf16 v[20:23], a[180:183], v[160:163], v[20:23]\n\t"
    "v_exp_f32 v142, v142\n\t"
    "v_mfma_f32_16x16x32_bf16 v[24:27], a[152:155], v[144:147], v[24:27]\n\t"
    "v_add_f32 v221, v221, v141\n\t"
    "v_add_f32 v221, v221, v142\n\t"
    "v_mfma_f32_16x16x32_bf16 v[24:27], a[184:187], v[160:163], v[24:27]\n\t"
    "v_exp_f32 v143, v143\n\t"
    "v_mfma_f32_16x16x32_bf16 v[28:31], a[156:159], v[144:147], v[28:31]\n\t"
    "ds_read_b64_tr_b16 a[132:133], v229 offset:49152\n\t"
    "v_cvt_pk_bf16_f32 v159, v142, v143\n\t"
    "v_mfma_f32_16x16x32_bf16 v[28:31], a[188:191], v[160:163], v[28:31]\n\t"
    "s_add_u32 m0, s37, 16384\n\t"
    "ds_read_b64_tr_b16 a[134:135], v229 offset:53248\n\t"
    "v_add_f32 v221, v221, v143\n\t"
    "v_mfma_f32_16x16x32_bf16 v[136:139], a[64:67], a[16:19], v[196:199]\n\t"
    "v_exp_f32 v128, v128\n\t"
    "v_mfma_f32_16x16x32_bf16 v[136:139], a[68:71], a[20:23], v[136:139]\n\t"
    "buffer_load_dwordx4 v236, s[20:23], s36 offen offset:0  lds\n\t"
    "v_mfma_f32_16x16x32_bf16 v[136:139], a[72:75], a[24:27], v[136:139]\n\t"
    "v_exp_f32 v129, v129\n\t"
    "v_mfma_f32_16x16x32_bf16 v[136:139], a[76:79], a[28:31], v[136:139]\n\t"
    "ds_read_b64_tr_b16 a[136:137], v230 offset:49152\n\t"
    "v_cvt_pk_bf16_f32 v144, v128, v129\n\t"
    "v_mfma_f32_16x16x32_bf16 v[140:143], a[92:95], a[28:31], v[196:199]\n\t"
    "v_exp_f32 v130, v130\n\t"
    "v_mfma_f32_16x16x32_bf16 v[140:143], a[88:91], a[24:27], v[140:143]\n\t"
    "buffer_load_dwordx4 v237, s[20:23], s36 offen offset:1024  lds\n\t"
    "v_mfma_f32_16x16x32_bf16 v[140:143], a[84:87], a[20:23], v[140:143]\n\t"
    "v_exp_f32 v131, v131\n\t"
    "v_mfma_f32_16x16x32_bf16 v[140:143], a[80:83], a[16:19], v[140:143]\n\t"
    "ds_read_b64_tr_b16 a[138:139], v230 offset:53248\n\t"
    "v_cvt_pk_bf16_f32 v145, v130, v131\n\t"
    "v_mfma_f32_16x16x32_bf16 v[48:51], a[144:147], v[148:151], v[48:51]\n\t"
    "v_exp_f32 v132, v132\n\t"
    "v_mfma_f32_16x16x32_bf16 v[48:51], a[176:179], v[164:167], v[48:51]\n\t"
    "buffer_load_dwordx4 v238, s[20:23], s36 offen offset:2048  lds\n\t"
    "v_mfma_f32_16x16x32_bf16 v[52:55], a[148:151], v[148:151], v[52:55]\n\t"
    "v_exp_f32 v133, v133\n\t"
    "v_mfma_f32_16x16x32_bf16 v[52:55], a[180:183], v[164:167], v[52:55]\n\t"
    "ds_read_b64_tr_b16 a[140:141], v231 offset:49152\n\t"
    "v_cvt_pk_bf16_f32 v146, v132, v133\n\t"
    "v_mfma_f32_16x16x32_bf16 v[56:59], a[152:155], v[148:151], v[56:59]\n\t"
    "v_exp_f32 v134, v134\n\t"
    "v_mfma_f32_16x16x32_bf16 v[56:59], a[184:187], v[164:167], v[56:59]\n\t"
    "buffer_load_dwordx4 v239, s[20:23], s36 offen offset:3072  lds\n\t"
    "v_mfma_f32_16x16x32_bf16 v[60:63], a[156:159], v[148:151], v[60:63]\n\t"
    "v_exp_f32 v135, v135\n\t"
    "v_mfma_f32_16x16x32_bf16 v[60:63], a[188:191], v[164:167], v[60:63]\n\t"
    "ds_read_b64_tr_b16 a[142:143], v231 offset:53248\n\t"
    "v_cvt_pk_bf16_f32 v147, v134, v135\n\t"
    "v_mfma_f32_16x16x32_bf16 v[128:131], a[96:99], a[0:3], v[192:195]\n\t"
    "v_exp_f32 v136, v136\n\t"
    "v_mfma_f32_16x16x32_bf16 v[128:131], a[100:103], a[4:7], v[128:131]\n\t"
    "ds_read_b64_tr_b16 a[144:145], v232 offset:49152\n\t"
    "v_add_f32 v213, v213, v136\n\t"
    "v_mfma_f32_16x16x32_bf16 v[128:131], a[104:107], a[8:11], v[128:131]\n\t"
    "v_exp_f32 v137, v137\n\t"
    "v_mfma_f32_16x16x32_bf16 v[128:131], a[108:111], a[12:15], v[128:131]\n\t"
    "ds_read_b64_tr_b16 a[146:147], v232 offset:53248\n\t"
    "v_cvt_pk_bf16_f32 v148, v136, v137\n\t"
    "v_mfma_f32_16x16x32_bf16 v[132:135], a[124:127], a[12:15], v[192:195]\n\t"
    "v_exp_f32 v138, v138\n\t"
    "v_mfma_f32_16x16x32_bf16 v[132:135], a[120:123], a[8:11], v[132:135]\n\t"
    "v_add_f32 v213, v213, v137\n\t"
    "v_add_f32 v213, v213, v138\n\t"
    "v_mfma_f32_16x16x32_bf16 v[132:135], a[116:119], a[4:7], v[132:135]\n\t"
    "v_exp_f32 v139, v139\n\t"
    "v_mfma_f32_16x16x32_bf16 v[132:135], a[112:115], a[0:3], v[132:135]\n\t"
    "ds_read_b64_tr_b16 a[148:149], v233 offset:49152\n\t"
    "v_cvt_pk_bf16_f32 v149, v138, v139\n\t"
    "v_mfma_f32_16x16x32_bf16 v[216:219], a[192:195], v[168:171], v[216:219]\n\t"
    "v_exp_f32 v140, v140\n\t"
    "v_mfma_f32_16x16x32_bf16 v[64:67], a[160:163], v[168:171], v[64:67]\n\t"
    "s_waitcnt lgkmcnt(7)\n\t"
    "v_add_f32 v213, v213, v139\n\t"
    "v_add_f32 v213, v213, v140\n\t"
    "v_mfma_f32_16x16x32_bf16 v[64:67], a[128:131], v[152:155], v[64:67]\n\t"
    "v_exp_f32 v141, v141\n\t"
    "v_mfma_f32_16x16x32_bf16 v[68:71], a[164:167], v[168:171], v[68:71]\n\t"
    "ds_read_b64_tr_b16 a[150:151], v233 offset:53248\n\t"
    "v_cvt_pk_bf16_f32 v150, v140, v141\n\t"
    "v_mfma_f32_16x16x32_bf16 v[68:71], a[132:135], v[152:155], v[68:71]\n\t"
    "v_exp_f32 v142, v142\n\t"
    "v_mfma_f32_16x16x32_bf16 v[72:75], a[168:171], v[168:171], v[72:75]\n\t"
    "s_waitcnt lgkmcnt(4)\n\t"
    "v_add_f32 v213, v213, v141\n\t"
    "v_add_f32 v213, v213, v142\n\t"
    "v_mfma_f32_16x16x32_bf16 v[72:75], a[136:139], v[152:155], v[72:75]\n\t"
    "v_exp_f32 v143, v143\n\t"
    "v_mfma_f32_16x16x32_bf16 v[76:79], a[172:175], v[168:171], v[76:79]\n\t"
    "ds_read_b64_tr_b16 a[152:153], v234 offset:49152\n\t"
    "v_cvt_pk_bf16_f32 v151, v142, v143\n\t"
    "v_mfma_f32_16x16x32_bf16 v[76:79], a[140:143], v[152:155], v[76:79]\n\t"
    "ds_read_b64_tr_b16 a[154:155], v234 offset:53248\n\t"
    "v_add_f32 v213, v213, v143\n\t"
    "v_mfma_f32_16x16x32_bf16 v[136:139], a[96:99], a[16:19], v[196:199]\n\t"
    "v_exp_f32 v128, v128\n\t"
    "v_mfma_f32_16x16x32_bf16 v[136:139], a[100:103], a[20:23], v[136:139]\n\t"
    "ds_read_b64_tr_b16 a[156:157], v235 offset:49152\n\t"
    "ds_read_b64_tr_b16 a[158:159], v235 offset:53248\n\t"
    "v_mfma_f32_16x16x32_bf16 v[136:139], a[104:107], a[24:27], v[136:139]\n\t"
    "v_exp_f32 v129, v129\n\t"
    "v_mfma_f32_16x16x32_bf16 v[136:139], a[108:111], a[28:31], v[136:139]\n\t"
    "ds_read_b128 a[64:67], v224 offset:32768\n\t"
    "v_cvt_pk_bf16_f32 v160, v128, v129\n\t"
    "v_mfma_f32_16x16x32_bf16 v[140:143], a[124:127], a[28:31], v[196:199]\n\t"
    "v_exp_f32 v130, v130\n\t"
    "v_mfma_f32_16x16x32_bf16 v[140:143], a[120:123], a[24:27], v[140:143]\n\t"
    "ds_read_b128 a[68:71], v225 offset:32768\n\t"
    "ds_read_b128 a[72:75], v226 offset:32768\n\t"
    "v_mfma_f32_16x16x32_bf16 v[140:143], a[116:119], a[20:23], v[140:143]\n\t"
    "v_exp_f32 v131, v131\n\t"
    "v_mfma_f32_16x16x32_bf16 v[140:143], a[112:115], a[16:19], v[140:143]\n\t"
    "ds_read_b128 a[76:79], v227 offset:32768\n\t"
    "v_cvt_pk_bf16_f32 v161, v130, v131\n\t"
    "v_mfma_f32_16x16x32_bf16 v[96:99], a[160:163], v[172:175], v[96:99]\n\t"
    "v_exp_f32 v132, v132\n\t"
    "v_mfma_f32_16x16x32_bf16 v[96:99], a[128:131], v[156:159], v[96:99]\n\t"
    "ds_read_b128 a[80:83], v224 offset:36864\n\t"
    "ds_read_b128 a[84:87], v225 offset:36864\n\t"
    "v_mfma_f32_16x16x32_bf16 v[100:103], a[164:167], v[172:175], v[100:103]\n\t"
    "v_exp_f32 v133, v133\n\t"
    "v_mfma_f32_16x16x32_bf16 v[100:103], a[132:135], v[156:159], v[100:103]\n\t"
    "ds_read_b128 a[88:91], v226 offset:36864\n\t"
    "v_cvt_pk_bf16_f32 v162, v132, v133\n\t"
    "v_mfma_f32_16x16x32_bf16 v[104:107], a[168:171], v[172:175], v[104:107]\n\t"
    "v_exp_f32 v134, v134\n\t"
    "v_mfma_f32_16x16x32_bf16 v[104:107], a[136:139], v[156:159], v[104:107]\n\t"
    "ds_read_b128 a[92:95], v227 offset:36864\n\t"
    "v_mfma_f32_16x16x32_bf16 v[108:111], a[172:175], v[172:175], v[108:111]\n\t"
    "v_exp_f32 v135, v135\n\t"
    "v_mfma_f32_16x16x32_bf16 v[108:111], a[140:143], v[156:159], v[108:111]\n\t"
    "v_cvt_pk_bf16_f32 v163, v134, v135\n\t"
    "v_mfma_f32_16x16x32_bf16 v[128:131], a[96:99], a[32:35], v[200:203]\n\t"
    "v_exp_f32 v136, v136\n\t"
    "v_mfma_f32_16x16x32_bf16 v[128:131], a[100:103], a[36:39], v[128:131]\n\t"
    "ds_read_b64_tr_b16 a[160:161], v228 offset:57344\n\t"
    "v_add_f32 v213, v213, v136\n\t"
    "v_mfma_f32_16x16x32_bf16 v[128:131], a[104:107], a[40:43], v[128:131]\n\t"
    "v_exp_f32 v137, v137\n\t"
    "v_mfma_f32_16x16x32_bf16 v[128:131], a[108:111], a[44:47], v[128:131]\n\t"
    "ds_read_b64_tr_b16 a[162:163], v228 offset:61440\n\t"
    "v_cvt_pk_bf16_f32 v164, v136, v137\n\t"
    "v_mfma_f32_16x16x32_bf16 v[132:135], a[124:127], a[44:47], v[200:203]\n\t"
    "v_exp_f32 v138, v138\n\t"
    "v_mfma_f32_16x16x32_bf16 v[132:135], a[120:123], a[40:43], v[132:135]\n\t"
    "v_add_f32 v213, v213, v137\n\t"
    "v_add_f32 v213, v213, v138\n\t"
    "v_mfma_f32_16x16x32_bf16 v[132:135], a[116:119], a[36:39], v[132:135]\n\t"
    "v_exp_f32 v139, v139\n\t"
    "v_mfma_f32_16x16x32_bf16 v[132:135], a[112:115], a[32:35], v[132:135]\n\t"
    "ds_read_b64_tr_b16 a[164:165], v229 offset:57344\n\t"
    "v_cvt_pk_bf16_f32 v165, v138, v139\n\t"
    "v_mfma_f32_16x16x32_bf16 v[216:219], a[192:195], v[152:155], v[216:219]\n\t"
    "v_exp_f32 v140, v140\n\t"
    "v_mfma_f32_16x16x32_bf16 v[80:83], a[176:179], v[168:171], v[80:83]\n\t"
    "s_waitcnt lgkmcnt(15)\n\t"
    "v_add_f32 v213, v213, v139\n\t"
    "v_add_f32 v213, v213, v140\n\t"
    "v_mfma_f32_16x16x32_bf16 v[80:83], a[144:147], v[152:155], v[80:83]\n\t"
    "v_exp_f32 v141, v141\n\t"
    "v_mfma_f32_16x16x32_bf16 v[84:87], a[180:183], v[168:171], v[84:87]\n\t"
    "ds_read_b64_tr_b16 a[166:167], v229 offset:61440\n\t"
    "v_cvt_pk_bf16_f32 v166, v140, v141\n\t"
    "v_mfma_f32_16x16x32_bf16 v[84:87], a[148:151], v[152:155], v[84:87]\n\t"
    "v_exp_f32 v142, v142\n\t"
    "v_mfma_f32_16x16x32_bf16 v[88:91], a[184:187], v[168:171], v[88:91]\n\t"
    "s_waitcnt lgkmcnt(12)\n\t"
    "v_add_f32 v213, v213, v141\n\t"
    "v_add_f32 v213, v213, v142\n\t"
    "v_mfma_f32_16x16x32_bf16 v[88:91], a[152:155], v[152:155], v[88:91]\n\t"
    "v_exp_f32 v143, v143\n\t"
    "v_mfma_f32_16x16x32_bf16 v[92:95], a[188:191], v[168:171], v[92:95]\n\t"
    "ds_read_b64_tr_b16 a[168:169], v230 offset:57344\n\t"
    "v_cvt_pk_bf16_f32 v167, v142, v143\n\t"
    "v_mfma_f32_16x16x32_bf16 v[92:95], a[156:159], v[152:155], v[92:95]\n\t"
    "s_add_u32 m0, s37, 98304\n\t"
    "ds_read_b64_tr_b16 a[170:171], v230 offset:61440\n\t"
    "v_add_f32 v213, v213, v143\n\t"
    "v_mfma_f32_16x16x32_bf16 v[136:139], a[96:99], v[176:179], v[204:207]\n\t"
    "v_exp_f32 v128, v128\n\t"
    "v_mfma_f32_16x16x32_bf16 v[136:139], a[100:103], v[180:183], v[136:139]\n\t"
    "buffer_load_dwordx4 v240, s[24:27], s36 offen offset:0  lds\n\t"
    "v_mfma_f32_16x16x32_bf16 v[136:139], a[104:107], v[184:187], v[136:139]\n\t"
    "v_exp_f32 v129, v129\n\t"
    "v_mfma_f32_16x16x32_bf16 v[136:139], a[108:111], v[188:191], v[136:139]\n\t"
    "ds_read_b64_tr_b16 a[172:173], v231 offset:57344\n\t"
    "v_cvt_pk_bf16_f32 v168, v128, v129\n\t"
    "v_mfma_f32_16x16x32_bf16 v[140:143], a[124:127], v[188:191], v[204:207]\n\t"
    "v_exp_f32 v130, v130\n\t"
    "v_mfma_f32_16x16x32_bf16 v[140:143], a[120:123], v[184:187], v[140:143]\n\t"
    "buffer_load_dwordx4 v241, s[24:27], s36 offen offset:1024  lds\n\t"
    "v_mfma_f32_16x16x32_bf16 v[140:143], a[116:119], v[180:183], v[140:143]\n\t"
    "v_exp_f32 v131, v131\n\t"
    "v_mfma_f32_16x16x32_bf16 v[140:143], a[112:115], v[176:179], v[140:143]\n\t"
    "ds_read_b64_tr_b16 a[174:175], v231 offset:61440\n\t"
    "v_cvt_pk_bf16_f32 v169, v130, v131\n\t"
    "v_mfma_f32_16x16x32_bf16 v[112:115], a[176:179], v[172:175], v[112:115]\n\t"
    "v_exp_f32 v132, v132\n\t"
    "v_mfma_f32_16x16x32_bf16 v[112:115], a[144:147], v[156:159], v[112:115]\n\t"
    "buffer_load_dwordx4 v240, s[24:27], s36 offen offset:2048  lds\n\t"
    "v_mfma_f32_16x16x32_bf16 v[116:119], a[180:183], v[172:175], v[116:119]\n\t"
    "v_exp_f32 v133, v133\n\t"
    "v_mfma_f32_16x16x32_bf16 v[116:119], a[148:151], v[156:159], v[116:119]\n\t"
    "v_cvt_pk_bf16_f32 v170, v132, v133\n\t"
    "v_mfma_f32_16x16x32_bf16 v[120:123], a[184:187], v[172:175], v[120:123]\n\t"
    "v_exp_f32 v134, v134\n\t"
    "v_mfma_f32_16x16x32_bf16 v[120:123], a[152:155], v[156:159], v[120:123]\n\t"
    "buffer_load_dwordx4 v241, s[24:27], s36 offen offset:3072  lds\n\t"
    "v_mfma_f32_16x16x32_bf16 v[124:127], a[188:191], v[172:175], v[124:127]\n\t"
    "v_exp_f32 v135, v135\n\t"
    "v_mfma_f32_16x16x32_bf16 v[124:127], a[156:159], v[156:159], v[124:127]\n\t"
    "v_cvt_pk_bf16_f32 v171, v134, v135\n\t"
    "L_entry%=:\n\t"
    "s_waitcnt lgkmcnt(12)\n\t"
    "v_mfma_f32_16x16x32_bf16 v[128:131], a[64:67], a[32:35], v[200:203]\n\t"
    "v_exp_f32 v136, v136\n\t"
    "v_mfma_f32_16x16x32_bf16 v[128:131], a[68:71], a[36:39], v[128:131]\n\t"
    "s_add_u32 s36, s36, 16384\n\t"
    "ds_read_b64_tr_b16 a[176:177], v232 offset:57344\n\t"
    "v_add_f32 v221, v221, v136\n\t"
    "v_mfma_f32_16x16x32_bf16 v[128:131], a[72:75], a[40:43], v[128:131]\n\t"
    "v_exp_f32 v137, v137\n\t"
    "v_mfma_f32_16x16x32_bf16 v[128:131], a[76:79], a[44:47], v[128:131]\n\t"
    "s_waitcnt lgkmcnt(9)\n\t"
    "ds_read_b64_tr_b16 a[178:179], v232 offset:61440\n\t"
    "v_cvt_pk_bf16_f32 v172, v136, v137\n\t"
    "v_mfma_f32_16x16x32_bf16 v[132:135], a[92:95], a[44:47], v[200:203]\n\t"
    "v_exp_f32 v138, v138\n\t"
    "v_mfma_f32_16x16x32_bf16 v[132:135], a[88:91], a[40:43], v[132:135]\n\t"
    "v_add_f32 v221, v221, v137\n\t"
    "v_add_f32 v221, v221, v138\n\t"
    "v_mfma_f32_16x16x32_bf16 v[132:135], a[84:87], a[36:39], v[132:135]\n\t"
    "v_exp_f32 v139, v139\n\t"
    "v_mfma_f32_16x16x32_bf16 v[132:135], a[80:83], a[32:35], v[132:135]\n\t"
    "ds_read_b64_tr_b16 a[180:181], v233 offset:57344\n\t"
    "v_cvt_pk_bf16_f32 v173, v138, v139\n\t"
    "v_mfma_f32_16x16x32_bf16 v[208:211], a[192:195], v[144:147], v[208:211]\n\t"
    "v_exp_f32 v140, v140\n\t"
    "v_mfma_f32_16x16x32_bf16 v[0:3], a[128:131], v[144:147], v[0:3]\n\t"
    "s_waitcnt lgkmcnt(7)\n\t"
    "v_add_f32 v221, v221, v139\n\t"
    "v_add_f32 v221, v221, v140\n\t"
    "v_mfma_f32_16x16x32_bf16 v[0:3], a[160:163], v[160:163], v[0:3]\n\t"
    "v_exp_f32 v141, v141\n\t"
    "v_mfma_f32_16x16x32_bf16 v[4:7], a[132:135], v[144:147], v[4:7]\n\t"
    "ds_read_b64_tr_b16 a[182:183], v233 offset:61440\n\t"
    "v_cvt_pk_bf16_f32 v174, v140, v141\n\t"
    "v_mfma_f32_16x16x32_bf16 v[4:7], a[164:167], v[160:163], v[4:7]\n\t"
    "v_exp_f32 v142, v142\n\t"
    "v_mfma_f32_16x16x32_bf16 v[8:11], a[136:139], v[144:147], v[8:11]\n\t"
    "s_waitcnt lgkmcnt(4)\n\t"
    "v_add_f32 v221, v221, v141\n\t"
    "v_add_f32 v221, v221, v142\n\t"
    "v_mfma_f32_16x16x32_bf16 v[8:11], a[168:171], v[160:163], v[8:11]\n\t"
    "v_exp_f32 v143, v143\n\t"
    "v_mfma_f32_16x16x32_bf16 v[12:15], a[140:143], v[144:147], v[12:15]\n\t"
    "ds_read_b64_tr_b16 a[184:185], v234 offset:57344\n\t"
    "v_cvt_pk_bf16_f32 v175, v142, v143\n\t"
    "v_mfma_f32_16x16x32_bf16 v[12:15], a[172:175], v[160:163], v[12:15]\n\t"
    "ds_read_b64_tr_b16 a[186:187], v234 offset:61440\n\t"
    "v_add_f32 v221, v221, v143\n\t"
    "v_mfma_f32_16x16x32_bf16 v[136:139], a[64:67], v[176:179], v[204:207]\n\t"
    "v_exp_f32 v128, v128\n\t"
    "v_mfma_f32_16x16x32_bf16 v[136:139], a[68:71], v[180:183], v[136:139]\n\t"
    "ds_read_b64_tr_b16 a[188:189], v235 offset:57344\n\t"
    "ds_read_b64_tr_b16 a[190:191], v235 offset:61440\n\t"
    "v_mfma_f32_16x16x32_bf16 v[136:139], a[72:75], v[184:187], v[136:139]\n\t"
    "v_exp_f32 v129, v129\n\t"
    "v_mfma_f32_16x16x32_bf16 v[136:139], a[76:79], v[188:191], v[136:139]\n\t"
    "ds_read_b128 a[96:99], v224 offset:40960\n\t"
    "v_cvt_pk_bf16_f32 v152, v128, v129\n\t"
    "v_mfma_f32_16x16x32_bf16 v[140:143], a[92:95], v[188:191], v[204:207]\n\t"
    "v_exp_f32 v130, v130\n\t"
    "v_mfma_f32_16x16x32_bf16 v[140:143], a[88:91], v[184:187], v[140:143]\n\t"
    "ds_read_b128 a[100:103], v225 offset:40960\n\t"
    "ds_read_b128 a[104:107], v226 offset:40960\n\t"
    "v_mfma_f32_16x16x32_bf16 v[140:143], a[84:87], v[180:183], v[140:143]\n\t"
    "v_exp_f32 v131, v131\n\t"
    "v_mfma_f32_16x16x32_bf16 v[140:143], a[80:83], v[176:179], v[140:143]\n\t"
    "ds_read_b128 a[108:111], v227 offset:40960\n\t"
    "v_cvt_pk_bf16_f32 v153, v130, v131\n\t"
    "v_mfma_f32_16x16x32_bf16 v[32:35], a[128:131], v[148:151], v[32:35]\n\t"
    "v_exp_f32 v132, v132\n\t"
    "v_mfma_f32_16x16x32_bf16 v[32:35], a[160:163], v[164:167], v[32:35]\n\t"
    "ds_read_b128 a[112:115], v224 offset:45056\n\t"
    "ds_read_b128 a[116:119], v225 offset:45056\n\t"
    "v_mfma_f32_16x16x32_bf16 v[36:39], a[132:135], v[148:151], v[36:39]\n\t"
    "v_exp_f32 v133, v133\n\t"
    "v_mfma_f32_16x16x32_bf16 v[36:39], a[164:167], v[164:167], v[36:39]\n\t"
    "ds_read_b128 a[120:123], v226 offset:45056\n\t"
    "v_cvt_pk_bf16_f32 v154, v132, v133\n\t"
    "v_mfma_f32_16x16x32_bf16 v[40:43], a[136:139], v[148:151], v[40:43]\n\t"
    "v_exp_f32 v134, v134\n\t"
    "v_mfma_f32_16x16x32_bf16 v[40:43], a[168:171], v[164:167], v[40:43]\n\t"
    "ds_read_b128 a[124:127], v227 offset:45056\n\t"
    "v_mfma_f32_16x16x32_bf16 v[44:47], a[140:143], v[148:151], v[44:47]\n\t"
    "v_exp_f32 v135, v135\n\t"
    "v_mfma_f32_16x16x32_bf16 v[44:47], a[172:175], v[164:167], v[44:47]\n\t"
    "v_cvt_pk_bf16_f32 v155, v134, v135\n\t"
    "v_mfma_f32_16x16x32_bf16 v[128:131], a[64:67], a[0:3], v[192:195]\n\t"
    "v_exp_f32 v136, v136\n\t"
    "v_mfma_f32_16x16x32_bf16 v[128:131], a[68:71], a[4:7], v[128:131]\n\t"
    "v_add_f32 v221, v221, v136\n\t"
    "v_mfma_f32_16x16x32_bf16 v[128:131], a[72:75], a[8:11], v[128:131]\n\t"
    "s_waitcnt vmcnt(16) lgkmcnt(0)\n\t"
    "v_exp_f32 v137, v137\n\t"
    "v_mfma_f32_16x16x32_bf16 v[128:131], a[76:79], a[12:15], v[128:131]\n\t"
    "s_barrier\n\t"
    "v_cvt_pk_bf16_f32 v156, v136, v137\n\t"
    "v_mfma_f32_16x16x32_bf16 v[132:135], a[92:95], a[12:15], v[192:195]\n\t"
    "v_exp_f32 v138, v138\n\t"
    "v_mfma_f32_16x16x32_bf16 v[132:135], a[88:91], a[8:11], v[132:135]\n\t"
    "v_add_f32 v221, v221, v137\n\t"
    "v_add_f32 v221, v221, v138\n\t"
    "v_mfma_f32_16x16x32_bf16 v[132:135], a[84:87], a[4:7], v[132:135]\n\t"
    "v_exp_f32 v139, v139\n\t"
    "v_mfma_f32_16x16x32_bf16 v[132:135], a[80:83], a[0:3], v[132:135]\n\t"
    "ds_read_b64_tr_b16 a[128:129], v228 offset:0\n\t"
    "v_cvt_pk_bf16_f32 v157, v138, v139\n\t"
    "v_mfma_f32_16x16x32_bf16 v[208:211], a[192:195], v[160:163], v[208:211]\n\t"
    "v_exp_f32 v140, v140\n\t"
    "v_mfma_f32_16x16x32_bf16 v[16:19], a[144:147], v[144:147], v[16:19]\n\t"
    "v_add_f32 v221, v221, v139\n\t"
    "v_add_f32 v221, v221, v140\n\t"
    "v_mfma_f32_16x16x32_bf16 v[16:19], a[176:179], v[160:163], v[16:19]\n\t"
    "v_exp_f32 v141, v141\n\t"
    "v_mfma_f32_16x16x32_bf16 v[20:23], a[148:151], v[144:147], v[20:23]\n\t"
    "ds_read_b64_tr_b16 a[130:131], v228 offset:4096\n\t"
    "v_cvt_pk_bf16_f32 v158, v140, v141\n\t"
    "v_mfma_f32_16x16x32_bf16 v[20:23], a[180:183], v[160:163], v[20:23]\n\t"
    "v_exp_f32 v142, v142\n\t"
    "v_mfma_f32_16x16x32_bf16 v[24:27], a[152:155], v[144:147], v[24:27]\n\t"
    "v_add_f32 v221, v221, v141\n\t"
    "v_add_f32 v221, v221, v142\n\t"
    "v_mfma_f32_16x16x32_bf16 v[24:27], a[184:187], v[160:163], v[24:27]\n\t"
    "v_exp_f32 v143, v143\n\t"
    "v_mfma_f32_16x16x32_bf16 v[28:31], a[156:159], v[144:147], v[28:31]\n\t"
    "ds_read_b64_tr_b16 a[132:133], v229 offset:0\n\t"
    "v_cvt_pk_bf16_f32 v159, v142, v143\n\t"
    "v_mfma_f32_16x16x32_bf16 v[28:31], a[188:191], v[160:163], v[28:31]\n\t"
    "s_add_u32 m0, s37, 32768\n\t"
    "ds_read_b64_tr_b16 a[134:135], v229 offset:4096\n\t"
    "v_add_f32 v221, v221, v143\n\t"
    "v_mfma_f32_16x16x32_bf16 v[136:139], a[64:67], a[16:19], v[196:199]\n\t"
    "v_exp_f32 v128, v128\n\t"
    "v_mfma_f32_16x16x32_bf16 v[136:139], a[68:71], a[20:23], v[136:139]\n\t"
    "buffer_load_dwordx4 v236, s[20:23], s36 offen offset:0  lds\n\t"
    "v_mfma_f32_16x16x32_bf16 v[136:139], a[72:75], a[24:27], v[136:139]\n\t"
    "v_exp_f32 v129, v129\n\t"
    "v_mfma_f32_16x16x32_bf16 v[136:139], a[76:79], a[28:31], v[136:139]\n\t"
    "ds_read_b64_tr_b16 a[136:137], v230 offset:0\n\t"
    "v_cvt_pk_bf16_f32 v144, v128, v129\n\t"
    "v_mfma_f32_16x16x32_bf16 v[140:143], a[92:95], a[28:31], v[196:199]\n\t"
    "v_exp_f32 v130, v130\n\t"
    "v_mfma_f32_16x16x32_bf16 v[140:143], a[88:91], a[24:27], v[140:143]\n\t"
    "buffer_load_dwordx4 v237, s[20:23], s36 offen offset:1024  lds\n\t"
    "v_mfma_f32_16x16x32_bf16 v[140:143], a[84:87], a[20:23], v[140:143]\n\t"
    "v_exp_f32 v131, v131\n\t"
    "v_mfma_f32_16x16x32_bf16 v[140:143], a[80:83], a[16:19], v[140:143]\n\t"
    "ds_read_b64_tr_b16 a[138:139], v230 offset:4096\n\t"
    "v_cvt_pk_bf16_f32 v145, v130, v131\n\t"
    "v_mfma_f32_16x16x32_bf16 v[48:51], a[144:147], v[148:151], v[48:51]\n\t"
    "v_exp_f32 v132, v132\n\t"
    "v_mfma_f32_16x16x32_bf16 v[48:51], a[176:179], v[164:167], v[48:51]\n\t"
    "buffer_load_dwordx4 v238, s[20:23], s36 offen offset:2048  lds\n\t"
    "v_mfma_f32_16x16x32_bf16 v[52:55], a[148:151], v[148:151], v[52:55]\n\t"
    "v_exp_f32 v133, v133\n\t"
    "v_mfma_f32_16x16x32_bf16 v[52:55], a[180:183], v[164:167], v[52:55]\n\t"
    "ds_read_b64_tr_b16 a[140:141], v231 offset:0\n\t"
    "v_cvt_pk_bf16_f32 v146, v132, v133\n\t"
    "v_mfma_f32_16x16x32_bf16 v[56:59], a[152:155], v[148:151], v[56:59]\n\t"
    "v_exp_f32 v134, v134\n\t"
    "v_mfma_f32_16x16x32_bf16 v[56:59], a[184:187], v[164:167], v[56:59]\n\t"
    "buffer_load_dwordx4 v239, s[20:23], s36 offen offset:3072  lds\n\t"
    "v_mfma_f32_16x16x32_bf16 v[60:63], a[156:159], v[148:151], v[60:63]\n\t"
    "v_exp_f32 v135, v135\n\t"
    "v_mfma_f32_16x16x32_bf16 v[60:63], a[188:191], v[164:167], v[60:63]\n\t"
    "ds_read_b64_tr_b16 a[142:143], v231 offset:4096\n\t"
    "v_cvt_pk_bf16_f32 v147, v134, v135\n\t"
    "v_mfma_f32_16x16x32_bf16 v[128:131], a[96:99], a[0:3], v[192:195]\n\t"
    "v_exp_f32 v136, v136\n\t"
    "v_mfma_f32_16x16x32_bf16 v[128:131], a[100:103], a[4:7], v[128:131]\n\t"
    "ds_read_b64_tr_b16 a[144:145], v232 offset:0\n\t"
    "v_add_f32 v213, v213, v136\n\t"
    "v_mfma_f32_16x16x32_bf16 v[128:131], a[104:107], a[8:11], v[128:131]\n\t"
    "v_exp_f32 v137, v137\n\t"
    "v_mfma_f32_16x16x32_bf16 v[128:131], a[108:111], a[12:15], v[128:131]\n\t"
    "ds_read_b64_tr_b16 a[146:147], v232 offset:4096\n\t"
    "v_cvt_pk_bf16_f32 v148, v136, v137\n\t"
    "v_mfma_f32_16x16x32_bf16 v[132:135], a[124:127], a[12:15], v[192:195]\n\t"
    "v_exp_f32 v138, v138\n\t"
    "v_mfma_f32_16x16x32_bf16 v[132:135], a[120:123], a[8:11], v[132:135]\n\t"
    "v_add_f32 v213, v213, v137\n\t"
    "v_add_f32 v213, v213, v138\n\t"
    "v_mfma_f32_16x16x32_bf16 v[132:135], a[116:119], a[4:7], v[132:135]\n\t"
    "v_exp_f32 v139, v139\n\t"
    "v_mfma_f32_16x16x32_bf16 v[132:135], a[112:115], a[0:3], v[132:135]\n\t"
    "ds_read_b64_tr_b16 a[148:149], v233 offset:0\n\t"
    "v_cvt_pk_bf16_f32 v149, v138, v139\n\t"
    "v_mfma_f32_16x16x32_bf16 v[216:219], a[192:195], v[168:171], v[216:219]\n\t"
    "v_exp_f32 v140, v140\n\t"
    "v_mfma_f32_16x16x32_bf16 v[64:67], a[160:163], v[168:171], v[64:67]\n\t"
    "s_waitcnt lgkmcnt(7)\n\t"
    "v_add_f32 v213, v213, v139\n\t"
    "v_add_f32 v213, v213, v140\n\t"
    "v_mfma_f32_16x16x32_bf16 v[64:67], a[128:131], v[152:155], v[64:67]\n\t"
    "v_exp_f32 v141, v141\n\t"
    "v_mfma_f32_16x16x32_bf16 v[68:71], a[164:167], v[168:171], v[68:71]\n\t"
    "ds_read_b64_tr_b16 a[150:151], v233 offset:4096\n\t"
    "v_cvt_pk_bf16_f32 v150, v140, v141\n\t"
    "v_mfma_f32_16x16x32_bf16 v[68:71], a[132:135], v[152:155], v[68:71]\n\t"
    "v_exp_f32 v142, v142\n\t"
    "v_mfma_f32_16x16x32_bf16 v[72:75], a[168:171], v[168:171], v[72:75]\n\t"
    "s_waitcnt lgkmcnt(4)\n\t"
    "v_add_f32 v213, v213, v141\n\t"
    "v_add_f32 v213, v213, v142\n\t"
    "v_mfma_f32_16x16x32_bf16 v[72:75], a[136:139], v[152:155], v[72:75]\n\t"
    "v_exp_f32 v143, v143\n\t"
    "v_mfma_f32_16x16x32_bf16 v[76:79], a[172:175], v[168:171], v[76:79]\n\t"
    "ds_read_b64_tr_b16 a[152:153], v234 offset:0\n\t"
    "v_cvt_pk_bf16_f32 v151, v142, v143\n\t"
    "v_mfma_f32_16x16x32_bf16 v[76:79], a[140:143], v[152:155], v[76:79]\n\t"
    "ds_read_b64_tr_b16 a[154:155], v234 offset:4096\n\t"
    "v_add_f32 v213, v213, v143\n\t"
    "v_mfma_f32_16x16x32_bf16 v[136:139], a[96:99], a[16:19], v[196:199]\n\t"
    "v_exp_f32 v128, v128\n\t"
    "v_mfma_f32_16x16x32_bf16 v[136:139], a[100:103], a[20:23], v[136:139]\n\t"
    "ds_read_b64_tr_b16 a[156:157], v235 offset:0\n\t"
    "ds_read_b64_tr_b16 a[158:159], v235 offset:4096\n\t"
    "v_mfma_f32_16x16x32_bf16 v[136:139], a[104:107], a[24:27], v[136:139]\n\t"
    "v_exp_f32 v129, v129\n\t"
    "v_mfma_f32_16x16x32_bf16 v[136:139], a[108:111], a[28:31], v[136:139]\n\t"
    "ds_read_b128 a[64:67], v224 offset:49152\n\t"
    "v_cvt_pk_bf16_f32 v160, v128, v129\n\t"
    "v_mfma_f32_16x16x32_bf16 v[140:143], a[124:127], a[28:31], v[196:199]\n\t"
    "v_exp_f32 v130, v130\n\t"
    "v_mfma_f32_16x16x32_bf16 v[140:143], a[120:123], a[24:27], v[140:143]\n\t"
    "ds_read_b128 a[68:71], v225 offset:49152\n\t"
    "ds_read_b128 a[72:75], v226 offset:49152\n\t"
    "v_mfma_f32_16x16x32_bf16 v[140:143], a[116:119], a[20:23], v[140:143]\n\t"
    "v_exp_f32 v131, v131\n\t"
    "v_mfma_f32_16x16x32_bf16 v[140:143], a[112:115], a[16:19], v[140:143]\n\t"
    "ds_read_b128 a[76:79], v227 offset:49152\n\t"
    "v_cvt_pk_bf16_f32 v161, v130, v131\n\t"
    "v_mfma_f32_16x16x32_bf16 v[96:99], a[160:163], v[172:175], v[96:99]\n\t"
    "v_exp_f32 v132, v132\n\t"
    "v_mfma_f32_16x16x32_bf16 v[96:99], a[128:131], v[156:159], v[96:99]\n\t"
    "ds_read_b128 a[80:83], v224 offset:53248\n\t"
    "ds_read_b128 a[84:87], v225 offset:53248\n\t"
    "v_mfma_f32_16x16x32_bf16 v[100:103], a[164:167], v[172:175], v[100:103]\n\t"
    "v_exp_f32 v133, v133\n\t"
    "v_mfma_f32_16x16x32_bf16 v[100:103], a[132:135], v[156:159], v[100:103]\n\t"
    "ds_read_b128 a[88:91], v226 offset:53248\n\t"
    "v_cvt_pk_bf16_f32 v162, v132, v133\n\t"
    "v_mfma_f32_16x16x32_bf16 v[104:107], a[168:171], v[172:175], v[104:107]\n\t"
    "v_exp_f32 v134, v134\n\t"
    "v_mfma_f32_16x16x32_bf16 v[104:107], a[136:139], v[156:159], v[104:107]\n\t"
    "ds_read_b128 a[92:95], v227 offset:53248\n\t"
    "v_mfma_f32_16x16x32_bf16 v[108:111], a[172:175], v[172:175], v[108:111]\n\t"
    "v_exp_f32 v135, v135\n\t"
    "v_mfma_f32_16x16x32_bf16 v[108:111], a[140:143], v[156:159], v[108:111]\n\t"
    "v_cvt_pk_bf16_f32 v163, v134, v135\n\t"
    "v_mfma_f32_16x16x32_bf16 v[128:131], a[96:99], a[32:35], v[200:203]\n\t"
    "v_exp_f32 v136, v136\n\t"
    "v_mfma_f32_16x16x32_bf16 v[128:131], a[100:103], a[36:39], v[128:131]\n\t"
    "ds_read_b64_tr_b16 a[160:161], v228 offset:8192\n\t"
    "v_add_f32 v213, v213, v136\n\t"
    "v_mfma_f32_16x16x32_bf16 v[128:131], a[104:107], a[40:43], v[128:131]\n\t"
    "v_exp_f32 v137, v137\n\t"
    "v_mfma_f32_16x16x32_bf16 v[128:131], a[108:111], a[44:47], v[128:131]\n\t"
    "ds_read_b64_tr_b16 a[162:163], v228 offset:12288\n\t"
    "v_cvt_pk_bf16_f32 v164, v136, v137\n\t"
    "v_mfma_f32_16x16x32_bf16 v[132:135], a[124:127], a[44:47], v[200:203]\n\t"
    "v_exp_f32 v138, v138\n\t"
    "v_mfma_f32_16x16x32_bf16 v[132:135], a[120:123], a[40:43], v[132:135]\n\t"
    "v_add_f32 v213, v213, v137\n\t"
    "v_add_f32 v213, v213, v138\n\t"
    "v_mfma_f32_16x16x32_bf16 v[132:135], a[116:119], a[36:39], v[132:135]\n\t"
    "v_exp_f32 v139, v139\n\t"
    "v_mfma_f32_16x16x32_bf16 v[132:135], a[112:115], a[32:35], v[132:135]\n\t"
    "ds_read_b64_tr_b16 a[164:165], v229 offset:8192\n\t"
    "v_cvt_pk_bf16_f32 v165, v138, v139\n\t"
    "v_mfma_f32_16x16x32_bf16 v[216:219], a[192:195], v[152:155], v[216:219]\n\t"
    "v_exp_f32 v140, v140\n\t"
    "v_mfma_f32_16x16x32_bf16 v[80:83], a[176:179], v[168:171], v[80:83]\n\t"
    "s_waitcnt lgkmcnt(15)\n\t"
    "v_add_f32 v213, v213, v139\n\t"
    "v_add_f32 v213, v213, v140\n\t"
    "v_mfma_f32_16x16x32_bf16 v[80:83], a[144:147], v[152:155], v[80:83]\n\t"
    "v_exp_f32 v141, v141\n\t"
    "v_mfma_f32_16x16x32_bf16 v[84:87], a[180:183], v[168:171], v[84:87]\n\t"
    "ds_read_b64_tr_b16 a[166:167], v229 offset:12288\n\t"
    "v_cvt_pk_bf16_f32 v166, v140, v141\n\t"
    "v_mfma_f32_16x16x32_bf16 v[84:87], a[148:151], v[152:155], v[84:87]\n\t"
    "v_exp_f32 v142, v142\n\t"
    "v_mfma_f32_16x16x32_bf16 v[88:91], a[184:187], v[168:171], v[88:91]\n\t"
    "s_waitcnt lgkmcnt(12)\n\t"
    "v_add_f32 v213, v213, v141\n\t"
    "v_add_f32 v213, v213, v142\n\t"
    "v_mfma_f32_16x16x32_bf16 v[88:91], a[152:155], v[152:155], v[88:91]\n\t"
    "v_exp_f32 v143, v143\n\t"
    "v_mfma_f32_16x16x32_bf16 v[92:95], a[188:191], v[168:171], v[92:95]\n\t"
    "ds_read_b64_tr_b16 a[168:169], v230 offset:8192\n\t"
    "v_cvt_pk_bf16_f32 v167, v142, v143\n\t"
    "v_mfma_f32_16x16x32_bf16 v[92:95], a[156:159], v[152:155], v[92:95]\n\t"
    "s_add_u32 m0, s37, 114688\n\t"
    "ds_read_b64_tr_b16 a[170:171], v230 offset:12288\n\t"
    "v_add_f32 v213, v213, v143\n\t"
    "v_mfma_f32_16x16x32_bf16 v[136:139], a[96:99], v[176:179], v[204:207]\n\t"
    "v_exp_f32 v128, v128\n\t"
    "v_mfma_f32_16x16x32_bf16 v[136:139], a[100:103], v[180:183], v[136:139]\n\t"
    "buffer_load_dwordx4 v240, s[24:27], s36 offen offset:0  lds\n\t"
    "v_mfma_f32_16x16x32_bf16 v[136:139], a[104:107], v[184:187], v[136:139]\n\t"
    "v_exp_f32 v129, v129\n\t"
    "v_mfma_f32_16x16x32_bf16 v[136:139], a[108:111], v[188:191], v[136:139]\n\t"
    "ds_read_b64_tr_b16 a[172:173], v231 offset:8192\n\t"
    "v_cvt_pk_bf16_f32 v168, v128, v129\n\t"
    "v_mfma_f32_16x16x32_bf16 v[140:143], a[124:127], v[188:191], v[204:207]\n\t"
    "v_exp_f32 v130, v130\n\t"
    "v_mfma_f32_16x16x32_bf16 v[140:143], a[120:123], v[184:187], v[140:143]\n\t"
    "buffer_load_dwordx4 v241, s[24:27], s36 offen offset:1024  lds\n\t"
    "v_mfma_f32_16x16x32_bf16 v[140:143], a[116:119], v[180:183], v[140:143]\n\t"
    "v_exp_f32 v131, v131\n\t"
    "v_mfma_f32_16x16x32_bf16 v[140:143], a[112:115], v[176:179], v[140:143]\n\t"
    "ds_read_b64_tr_b16 a[174:175], v231 offset:12288\n\t"
    "v_cvt_pk_bf16_f32 v169, v130, v131\n\t"
    "v_mfma_f32_16x16x32_bf16 v[112:115], a[176:179], v[172:175], v[112:115]\n\t"
    "v_exp_f32 v132, v132\n\t"
    "v_mfma_f32_16x16x32_bf16 v[112:115], a[144:147], v[156:159], v[112:115]\n\t"
    "buffer_load_dwordx4 v240, s[24:27], s36 offen offset:2048  lds\n\t"
    "v_mfma_f32_16x16x32_bf16 v[116:119], a[180:183], v[172:175], v[116:119]\n\t"
    "v_exp_f32 v133, v133\n\t"
    "v_mfma_f32_16x16x32_bf16 v[116:119], a[148:151], v[156:159], v[116:119]\n\t"
    "v_cvt_pk_bf16_f32 v170, v132, v133\n\t"
    "v_mfma_f32_16x16x32_bf16 v[120:123], a[184:187], v[172:175], v[120:123]\n\t"
    "v_exp_f32 v134, v134\n\t"
    "v_mfma_f32_16x16x32_bf16 v[120:123], a[152:155], v[156:159], v[120:123]\n\t"
    "buffer_load_dwordx4 v241, s[24:27], s36 offen offset:3072  lds\n\t"
    "v_mfma_f32_16x16x32_bf16 v[124:127], a[188:191], v[172:175], v[124:127]\n\t"
    "v_exp_f32 v135, v135\n\t"
    "v_mfma_f32_16x16x32_bf16 v[124:127], a[156:159], v[156:159], v[124:127]\n\t"
    "v_cvt_pk_bf16_f32 v171, v134, v135\n\t"
    "s_setpc_b64 s[98:99]\n\t"
    "L_drain%=:\n\t"
    "s_waitcnt lgkmcnt(12)\n\t"
    "v_mfma_f32_16x16x32_bf16 v[128:131], a[64:67], a[32:35], v[200:203]\n\t"
    "v_exp_f32 v136, v136\n\t"
    "v_mfma_f32_16x16x32_bf16 v[128:131], a[68:71], a[36:39], v[128:131]\n\t"
    "ds_read_b64_tr_b16 a[176:177], v232 offset:8192\n\t"
    "ds_read_b64_tr_b16 a[178:179], v232 offset:12288\n\t"
    "v_mfma_f32_16x16x32_bf16 v[128:131], a[72:75], a[40:43], v[128:131]\n\t"
    "v_exp_f32 v137, v137\n\t"
    "v_mfma_f32_16x16x32_bf16 v[128:131], a[76:79], a[44:47], v[128:131]\n\t"
    "s_waitcnt lgkmcnt(10)\n\t"
    "ds_read_b64_tr_b16 a[180:181], v233 offset:8192\n\t"
    "v_cvt_pk_bf16_f32 v172, v136, v137\n\t"
    "v_mfma_f32_16x16x32_bf16 v[132:135], a[92:95], a[44:47], v[200:203]\n\t"
    "v_exp_f32 v138, v138\n\t"
    "v_mfma_f32_16x16x32_bf16 v[132:135], a[88:91], a[40:43], v[132:135]\n\t"
    "ds_read_b64_tr_b16 a[182:183], v233 offset:12288\n\t"
    "ds_read_b64_tr_b16 a[184:185], v234 offset:8192\n\t"
    "v_mfma_f32_16x16x32_bf16 v[132:135], a[84:87], a[36:39], v[132:135]\n\t"
    "v_exp_f32 v139, v139\n\t"
    "v_mfma_f32_16x16x32_bf16 v[132:135], a[80:83], a[32:35], v[132:135]\n\t"
    "ds_read_b64_tr_b16 a[186:187], v234 offset:12288\n\t"
    "v_cvt_pk_bf16_f32 v173, v138, v139\n\t"
    "v_mfma_f32_16x16x32_bf16 v[208:211], a[192:195], v[144:147], v[208:211]\n\t"
    "v_exp_f32 v140, v140\n\t"
    "v_mfma_f32_16x16x32_bf16 v[0:3], a[128:131], v[144:147], v[0:3]\n\t"
    "s_waitcnt lgkmcnt(10)\n\t"
    "ds_read_b64_tr_b16 a[188:189], v235 offset:8192\n\t"
    "ds_read_b64_tr_b16 a[190:191], v235 offset:12288\n\t"
    "v_mfma_f32_16x16x32_bf16 v[0:3], a[160:163], v[160:163], v[0:3]\n\t"
    "v_exp_f32 v141, v141\n\t"
    "v_mfma_f32_16x16x32_bf16 v[4:7], a[132:135], v[144:147], v[4:7]\n\t"
    "ds_read_b128 a[96:99], v224 offset:57344\n\t"
    "v_cvt_pk_bf16_f32 v174, v140, v141\n\t"
    "v_mfma_f32_16x16x32_bf16 v[4:7], a[164:167], v[160:163], v[4:7]\n\t"
    "v_exp_f32 v142, v142\n\t"
    "v_mfma_f32_16x16x32_bf16 v[8:11], a[136:139], v[144:147], v[8:11]\n\t"
    "s_waitcnt lgkmcnt(9)\n\t"
    "ds_read_b128 a[100:103], v225 offset:57344\n\t"
    "v_mov_b32 v214, v213\n\t"
    "v_mfma_f32_16x16x32_bf16 v[8:11], a[168:171], v[160:163], v[8:11]\n\t"
    "v_exp_f32 v143, v143\n\t"
    "v_mfma_f32_16x16x32_bf16 v[12:15], a[140:143], v[144:147], v[12:15]\n\t"
    "ds_read_b128 a[104:107], v226 offset:57344\n\t"
    "v_cvt_pk_bf16_f32 v175, v142, v143\n\t"
    "v_mfma_f32_16x16x32_bf16 v[12:15], a[172:175], v[160:163], v[12:15]\n\t"
    "ds_read_b128 a[108:111], v227 offset:57344\n\t"
    "v_mov_b32 v222, v221\n\t"
    "v_mfma_f32_16x16x32_bf16 v[136:139], a[64:67], v[176:179], v[204:207]\n\t"
    "v_exp_f32 v128, v128\n\t"
    "v_mfma_f32_16x16x32_bf16 v[136:139], a[68:71], v[180:183], v[136:139]\n\t"
    "ds_read_b128 a[112:115], v224 offset:61440\n\t"
    "v_permlane16_swap_b32 v213, v214\n\t"
    "v_mfma_f32_16x16x32_bf16 v[136:139], a[72:75], v[184:187], v[136:139]\n\t"
    "v_exp_f32 v129, v129\n\t"
    "v_mfma_f32_16x16x32_bf16 v[136:139], a[76:79], v[188:191], v[136:139]\n\t"
    "ds_read_b128 a[116:119], v225 offset:61440\n\t"
    "v_cvt_pk_bf16_f32 v152, v128, v129\n\t"
    "v_mfma_f32_16x16x32_bf16 v[140:143], a[92:95], v[188:191], v[204:207]\n\t"
    "v_exp_f32 v130, v130\n\t"
    "v_mfma_f32_16x16x32_bf16 v[140:143], a[88:91], v[184:187], v[140:143]\n\t"
    "ds_read_b128 a[120:123], v226 offset:61440\n\t"
    "v_permlane16_swap_b32 v221, v222\n\t"
    "v_mfma_f32_16x16x32_bf16 v[140:143], a[84:87], v[180:183], v[140:143]\n\t"
    "v_exp_f32 v131, v131\n\t"
    "v_mfma_f32_16x16x32_bf16 v[140:143], a[80:83], v[176:179], v[140:143]\n\t"
    "ds_read_b128 a[124:127], v227 offset:61440\n\t"
    "v_cvt_pk_bf16_f32 v153, v130, v131\n\t"
    "v_mfma_f32_16x16x32_bf16 v[32:35], a[128:131], v[148:151], v[32:35]\n\t"
    "v_exp_f32 v132, v132\n\t"
    "v_mfma_f32_16x16x32_bf16 v[32:35], a[160:163], v[164:167], v[32:35]\n\t"
    "v_add_f32 v213, v213, v214\n\t"
    "v_add_f32 v221, v221, v222\n\t"
    "v_mfma_f32_16x16x32_bf16 v[36:39], a[132:135], v[148:151], v[36:39]\n\t"
    "v_exp_f32 v133, v133\n\t"
    "v_mfma_f32_16x16x32_bf16 v[36:39], a[164:167], v[164:167], v[36:39]\n\t"
    "v_cvt_pk_bf16_f32 v154, v132, v133\n\t"
    "v_mov_b32 v214, v213\n\t"
    "v_mfma_f32_16x16x32_bf16 v[40:43], a[136:139], v[148:151], v[40:43]\n\t"
    "v_exp_f32 v134, v134\n\t"
    "v_mfma_f32_16x16x32_bf16 v[40:43], a[168:171], v[164:167], v[40:43]\n\t"
    "v_mov_b32 v222, v221\n\t"
    "v_permlane32_swap_b32 v213, v214\n\t"
    "v_mfma_f32_16x16x32_bf16 v[44:47], a[140:143], v[148:151], v[44:47]\n\t"
    "v_exp_f32 v135, v135\n\t"
    "v_mfma_f32_16x16x32_bf16 v[44:47], a[172:175], v[164:167], v[44:47]\n\t"
    "v_cvt_pk_bf16_f32 v155, v134, v135\n\t"
    "v_permlane32_swap_b32 v221, v222\n\t"
    "v_mfma_f32_16x16x32_bf16 v[128:131], a[64:67], a[0:3], v[192:195]\n\t"
    "v_exp_f32 v136, v136\n\t"
    "v_mfma_f32_16x16x32_bf16 v[128:131], a[68:71], a[4:7], v[128:131]\n\t"
    "s_waitcnt vmcnt(16) lgkmcnt(0)\n\t"
    "v_add_f32 v213, v213, v214\n\t"
    "v_add_f32 v221, v221, v222\n\t"
    "v_mfma_f32_16x16x32_bf16 v[128:131], a[72:75], a[8:11], v[128:131]\n\t"
    "s_barrier\n\t"
    "v_exp_f32 v137, v137\n\t"
    "v_mfma_f32_16x16x32_bf16 v[128:131], a[76:79], a[12:15], v[128:131]\n\t"
    "ds_read_b64_tr_b16 a[128:129], v228 offset:16384\n\t"
    "v_cvt_pk_bf16_f32 v156, v136, v137\n\t"
    "v_mfma_f32_16x16x32_bf16 v[132:135], a[92:95], a[12:15], v[192:195]\n\t"
    "s_add_u32 m0, s37, 32768\n\t"
    "v_exp_f32 v138, v138\n\t"
    "v_mfma_f32_16x16x32_bf16 v[132:135], a[88:91], a[8:11], v[132:135]\n\t"
    "s_add_u32 s97, s61, 0\n\t"
    "buffer_load_dwordx4 v254, s[28:31], s97 offen offset:0 sc1 lds\n\t"
    "v_mfma_f32_16x16x32_bf16 v[132:135], a[84:87], a[4:7], v[132:135]\n\t"
    "v_exp_f32 v139, v139\n\t"
    "v_mfma_f32_16x16x32_bf16 v[132:135], a[80:83], a[0:3], v[132:135]\n\t"
    "ds_read_b64_tr_b16 a[130:131], v228 offset:20480\n\t"
    "v_cvt_pk_bf16_f32 v157, v138, v139\n\t"
    "v_mfma_f32_16x16x32_bf16 v[208:211], a[192:195], v[160:163], v[208:211]\n\t"
    "v_exp_f32 v140, v140\n\t"
    "v_mfma_f32_16x16x32_bf16 v[16:19], a[144:147], v[144:147], v[16:19]\n\t"
    "buffer_load_dwordx4 v254, s[28:31], s97 offen offset:1024 sc1 lds\n\t"
    "v_mfma_f32_16x16x32_bf16 v[16:19], a[176:179], v[160:163], v[16:19]\n\t"
    "v_exp_f32 v141, v141\n\t"
    "v_mfma_f32_16x16x32_bf16 v[20:23], a[148:151], v[144:147], v[20:23]\n\t"
    "ds_read_b64_tr_b16 a[132:133], v229 offset:16384\n\t"
    "v_cvt_pk_bf16_f32 v158, v140, v141\n\t"
    "v_mfma_f32_16x16x32_bf16 v[20:23], a[180:183], v[160:163], v[20:23]\n\t"
    "v_exp_f32 v142, v142\n\t"
    "v_mfma_f32_16x16x32_bf16 v[24:27], a[152:155], v[144:147], v[24:27]\n\t"
    "buffer_load_dwordx4 v254, s[28:31], s97 offen offset:2048 sc1 lds\n\t"
    "v_mfma_f32_16x16x32_bf16 v[24:27], a[184:187], v[160:163], v[24:27]\n\t"
    "v_exp_f32 v143, v143\n\t"
    "v_mfma_f32_16x16x32_bf16 v[28:31], a[156:159], v[144:147], v[28:31]\n\t"
    "ds_read_b64_tr_b16 a[134:135], v229 offset:20480\n\t"
    "v_cvt_pk_bf16_f32 v159, v142, v143\n\t"
    "v_mfma_f32_16x16x32_bf16 v[28:31], a[188:191], v[160:163], v[28:31]\n\t"
    "buffer_load_dwordx4 v254, s[28:31], s97 offen offset:3072 sc1 lds\n\t"
    "v_mfma_f32_16x16x32_bf16 v[136:139], a[64:67], a[16:19], v[196:199]\n\t"
    "v_exp_f32 v128, v128\n\t"
    "v_mfma_f32_16x16x32_bf16 v[136:139], a[68:71], a[20:23], v[136:139]\n\t"
    "ds_read_b64_tr_b16 a[136:137], v230 offset:16384\n\t"
    "v_add_f32 v212, v212, v213\n\t"
    "v_mfma_f32_16x16x32_bf16 v[136:139], a[72:75], a[24:27], v[136:139]\n\t"
    "v_exp_f32 v129, v129\n\t"
    "v_mfma_f32_16x16x32_bf16 v[136:139], a[76:79], a[28:31], v[136:139]\n\t"
    "ds_read_b64_tr_b16 a[138:139], v230 offset:20480\n\t"
    "v_cvt_pk_bf16_f32 v144, v128, v129\n\t"
    "v_mfma_f32_16x16x32_bf16 v[140:143], a[92:95], a[28:31], v[196:199]\n\t"
    "v_exp_f32 v130, v130\n\t"
    "v_mfma_f32_16x16x32_bf16 v[140:143], a[88:91], a[24:27], v[140:143]\n\t"
    "s_add_u32 s43, s61, 65536\n\t"
    "buffer_load_dwordx4 a[196:199], v254, s[28:31], s43 offen offset:1024 sc1\n\t"
    "v_mfma_f32_16x16x32_bf16 v[140:143], a[84:87], a[20:23], v[140:143]\n\t"
    "v_exp_f32 v131, v131\n\t"
    "v_mfma_f32_16x16x32_bf16 v[140:143], a[80:83], a[16:19], v[140:143]\n\t"
    "ds_read_b64_tr_b16 a[140:141], v231 offset:16384\n\t"
    "v_cvt_pk_bf16_f32 v145, v130, v131\n\t"
    "v_mfma_f32_16x16x32_bf16 v[48:51], a[144:147], v[148:151], v[48:51]\n\t"
    "v_exp_f32 v132, v132\n\t"
    "v_mfma_f32_16x16x32_bf16 v[48:51], a[176:179], v[164:167], v[48:51]\n\t"
    "buffer_load_dwordx4 a[200:203], v254, s[28:31], s43 offen offset:2048 sc1\n\t"
    "v_mfma_f32_16x16x32_bf16 v[52:55], a[148:151], v[148:151], v[52:55]\n\t"
    "v_exp_f32 v133, v133\n\t"
    "v_mfma_f32_16x16x32_bf16 v[52:55], a[180:183], v[164:167], v[52:55]\n\t"
    "ds_read_b64_tr_b16 a[142:143], v231 offset:20480\n\t"
    "v_cvt_pk_bf16_f32 v146, v132, v133\n\t"
    "v_mfma_f32_16x16x32_bf16 v[56:59], a[152:155], v[148:151], v[56:59]\n\t"
    "v_exp_f32 v134, v134\n\t"
    "v_mfma_f32_16x16x32_bf16 v[56:59], a[184:187], v[164:167], v[56:59]\n\t"
    "v_add_f32 v220, v220, v221\n\t"
    "v_mfma_f32_16x16x32_bf16 v[60:63], a[156:159], v[148:151], v[60:63]\n\t"
    "v_exp_f32 v135, v135\n\t"
    "v_mfma_f32_16x16x32_bf16 v[60:63], a[188:191], v[164:167], v[60:63]\n\t"
    "v_cvt_pk_bf16_f32 v147, v134, v135\n\t"
    "v_mfma_f32_16x16x32_bf16 v[128:131], a[96:99], a[0:3], v[192:195]\n\t"
    "v_exp_f32 v136, v136\n\t"
    "v_mfma_f32_16x16x32_bf16 v[128:131], a[100:103], a[4:7], v[128:131]\n\t"
    "ds_read_b64_tr_b16 a[144:145], v232 offset:16384\n\t"
    "ds_read_b64_tr_b16 a[146:147], v232 offset:20480\n\t"
    "v_mfma_f32_16x16x32_bf16 v[128:131], a[104:107], a[8:11], v[128:131]\n\t"
    "v_exp_f32 v137, v137\n\t"
    "v_mfma_f32_16x16x32_bf16 v[128:131], a[108:111], a[12:15], v[128:131]\n\t"
    "ds_read_b64_tr_b16 a[148:149], v233 offset:16384\n\t"
    "v_cvt_pk_bf16_f32 v148, v136, v137\n\t"
    "v_mfma_f32_16x16x32_bf16 v[132:135], a[124:127], a[12:15], v[192:195]\n\t"
    "v_exp_f32 v138, v138\n\t"
    "v_mfma_f32_16x16x32_bf16 v[132:135], a[120:123], a[8:11], v[132:135]\n\t"
    "ds_read_b64_tr_b16 a[150:151], v233 offset:20480\n\t"
    "ds_read_b64_tr_b16 a[152:153], v234 offset:16384\n\t"
    "v_mfma_f32_16x16x32_bf16 v[132:135], a[116:119], a[4:7], v[132:135]\n\t"
    "v_exp_f32 v139, v139\n\t"
    "v_mfma_f32_16x16x32_bf16 v[132:135], a[112:115], a[0:3], v[132:135]\n\t"
    "ds_read_b64_tr_b16 a[154:155], v234 offset:20480\n\t"
    "v_cvt_pk_bf16_f32 v149, v138, v139\n\t"
    "v_mfma_f32_16x16x32_bf16 v[216:219], a[192:195], v[168:171], v[216:219]\n\t"
    "v_exp_f32 v140, v140\n\t"
    "v_mfma_f32_16x16x32_bf16 v[64:67], a[160:163], v[168:171], v[64:67]\n\t"
    "s_waitcnt lgkmcnt(10)\n\t"
    "ds_read_b64_tr_b16 a[156:157], v235 offset:16384\n\t"
    "ds_read_b64_tr_b16 a[158:159], v235 offset:20480\n\t"
    "v_mfma_f32_16x16x32_bf16 v[64:67], a[128:131], v[152:155], v[64:67]\n\t"
    "v_exp_f32 v141, v141\n\t"
    "v_mfma_f32_16x16x32_bf16 v[68:71], a[164:167], v[168:171], v[68:71]\n\t"
    "ds_read_b128 a[64:67], v224 offset:0\n\t"
    "v_cvt_pk_bf16_f32 v150, v140, v141\n\t"
    "v_mfma_f32_16x16x32_bf16 v[68:71], a[132:135], v[152:155], v[68:71]\n\t"
    "v_exp_f32 v142, v142\n\t"
    "v_mfma_f32_16x16x32_bf16 v[72:75], a[168:171], v[168:171], v[72:75]\n\t"
    "s_waitcnt lgkmcnt(9)\n\t"
    "ds_read_b128 a[68:71], v225 offset:0\n\t"
    "v_mfma_f32_16x16x32_bf16 v[72:75], a[136:139], v[152:155], v[72:75]\n\t"
    "v_exp_f32 v143, v143\n\t"
    "v_mfma_f32_16x16x32_bf16 v[76:79], a[172:175], v[168:171], v[76:79]\n\t"
    "ds_read_b128 a[72:75], v226 offset:0\n\t"
    "v_cvt_pk_bf16_f32 v151, v142, v143\n\t"
    "v_mfma_f32_16x16x32_bf16 v[76:79], a[140:143], v[152:155], v[76:79]\n\t"
    "ds_read_b128 a[76:79], v227 offset:0\n\t"
    "v_mfma_f32_16x16x32_bf16 v[136:139], a[96:99], a[16:19], v[196:199]\n\t"
    "v_exp_f32 v128, v128\n\t"
    "v_mfma_f32_16x16x32_bf16 v[136:139], a[100:103], a[20:23], v[136:139]\n\t"
    "ds_read_b128 a[80:83], v224 offset:4096\n\t"
    "v_mfma_f32_16x16x32_bf16 v[136:139], a[104:107], a[24:27], v[136:139]\n\t"
    "v_exp_f32 v129, v129\n\t"
    "v_mfma_f32_16x16x32_bf16 v[136:139], a[108:111], a[28:31], v[136:139]\n\t"
    "ds_read_b128 a[84:87], v225 offset:4096\n\t"
    "v_cvt_pk_bf16_f32 v160, v128, v129\n\t"
    "v_mfma_f32_16x16x32_bf16 v[140:143], a[124:127], a[28:31], v[196:199]\n\t"
    "v_exp_f32 v130, v130\n\t"
    "v_mfma_f32_16x16x32_bf16 v[140:143], a[120:123], a[24:27], v[140:143]\n\t"
    "ds_read_b128 a[88:91], v226 offset:4096\n\t"
    "v_mfma_f32_16x16x32_bf16 v[140:143], a[116:119], a[20:23], v[140:143]\n\t"
    "v_exp_f32 v131, v131\n\t"
    "v_mfma_f32_16x16x32_bf16 v[140:143], a[112:115], a[16:19], v[140:143]\n\t"
    "ds_read_b128 a[92:95], v227 offset:4096\n\t"
    "v_cvt_pk_bf16_f32 v161, v130, v131\n\t"
    "v_mfma_f32_16x16x32_bf16 v[220:223], a[192:195], v[172:175], v[220:223]\n\t"
    "v_exp_f32 v132, v132\n\t"
    "v_mfma_f32_16x16x32_bf16 v[96:99], a[160:163], v[172:175], v[96:99]\n\t"
    "buffer_load_dwordx4 a[204:207], v254, s[28:31], s43 offen offset:3072 sc1\n\t"
    "v_mfma_f32_16x16x32_bf16 v[96:99], a[128:131], v[156:159], v[96:99]\n\t"
    "v_exp_f32 v133, v133\n\t"
    "v_mfma_f32_16x16x32_bf16 v[100:103], a[164:167], v[172:175], v[100:103]\n\t"
    "v_cvt_pk_bf16_f32 v162, v132, v133\n\t"
    "v_mfma_f32_16x16x32_bf16 v[100:103], a[132:135], v[156:159], v[100:103]\n\t"
    "v_exp_f32 v134, v134\n\t"
    "v_mfma_f32_16x16x32_bf16 v[104:107], a[168:171], v[172:175], v[104:107]\n\t"
    "s_add_u32 s43, s61, 69632\n\t"
    "buffer_load_dwordx4 a[208:211], v254, s[28:31], s43 offen offset:0 sc1\n\t"
    "v_mfma_f32_16x16x32_bf16 v[104:107], a[136:139], v[156:159], v[104:107]\n\t"
    "v_exp_f32 v135, v135\n\t"
    "v_mfma_f32_16x16x32_bf16 v[108:111], a[172:175], v[172:175], v[108:111]\n\t"
    "v_cvt_pk_bf16_f32 v163, v134, v135\n\t"
    "v_mfma_f32_16x16x32_bf16 v[108:111], a[140:143], v[156:159], v[108:111]\n\t"
    "v_mfma_f32_16x16x32_bf16 v[128:131], a[96:99], a[32:35], v[200:203]\n\t"
    "v_exp_f32 v136, v136\n\t"
    "v_mfma_f32_16x16x32_bf16 v[128:131], a[100:103], a[36:39], v[128:131]\n\t"
    "v_mfma_f32_16x16x32_bf16 v[128:131], a[104:107], a[40:43], v[128:131]\n\t"
    "v_exp_f32 v137, v137\n\t"
    "v_mfma_f32_16x16x32_bf16 v[128:131], a[108:111], a[44:47], v[128:131]\n\t"
    "ds_read_b64_tr_b16 a[160:161], v228 offset:24576\n\t"
    "v_cvt_pk_bf16_f32 v164, v136, v137\n\t"
    "v_mfma_f32_16x16x32_bf16 v[132:135], a[124:127], a[44:47], v[200:203]\n\t"
    "s_lshl1_add_u32 m0, s37, 131072\n\t"
    "v_exp_f32 v138, v138\n\t"
    "v_mfma_f32_16x16x32_bf16 v[132:135], a[120:123], a[40:43], v[132:135]\n\t"
    "s_add_u32 s97, s61, 4096\n\t"
    "buffer_load_dwordx4 v254, s[28:31], s97 offen offset:0 sc1 lds\n\t"
    "v_mfma_f32_16x16x32_bf16 v[132:135], a[116:119], a[36:39], v[132:135]\n\t"
    "v_exp_f32 v139, v139\n\t"
    "v_mfma_f32_16x16x32_bf16 v[132:135], a[112:115], a[32:35], v[132:135]\n\t"
    "ds_read_b64_tr_b16 a[162:163], v228 offset:28672\n\t"
    "v_cvt_pk_bf16_f32 v165, v138, v139\n\t"
    "v_mfma_f32_16x16x32_bf16 v[216:219], a[192:195], v[152:155], v[216:219]\n\t"
    "v_exp_f32 v140, v140\n\t"
    "v_mfma_f32_16x16x32_bf16 v[80:83], a[176:179], v[168:171], v[80:83]\n\t"
    "s_waitcnt lgkmcnt(14)\n\t"
    "buffer_load_dwordx4 v254, s[28:31], s97 offen offset:1024 sc1 lds\n\t"
    "v_mfma_f32_16x16x32_bf16 v[80:83], a[144:147], v[152:155], v[80:83]\n\t"
    "v_exp_f32 v141, v141\n\t"
    "v_mfma_f32_16x16x32_bf16 v[84:87], a[180:183], v[168:171], v[84:87]\n\t"
    "ds_read_b64_tr_b16 a[164:165], v229 offset:24576\n\t"
    "v_cvt_pk_bf16_f32 v166, v140, v141\n\t"
    "v_mfma_f32_16x16x32_bf16 v[84:87], a[148:151], v[152:155], v[84:87]\n\t"
    "v_exp_f32 v142, v142\n\t"
    "v_mfma_f32_16x16x32_bf16 v[88:91], a[184:187], v[168:171], v[88:91]\n\t"
    "s_waitcnt lgkmcnt(11)\n\t"
    "buffer_load_dwordx4 v254, s[28:31], s97 offen offset:2048 sc1 lds\n\t"
    "v_mfma_f32_16x16x32_bf16 v[88:91], a[152:155], v[152:155], v[88:91]\n\t"
    "v_exp_f32 v143, v143\n\t"
    "v_mfma_f32_16x16x32_bf16 v[92:95], a[188:191], v[168:171], v[92:95]\n\t"
    "ds_read_b64_tr_b16 a[166:167], v229 offset:28672\n\t"
    "v_cvt_pk_bf16_f32 v167, v142, v143\n\t"
    "v_mfma_f32_16x16x32_bf16 v[92:95], a[156:159], v[152:155], v[92:95]\n\t"
    "buffer_load_dwordx4 v254, s[28:31], s97 offen offset:3072 sc1 lds\n\t"
    "v_mfma_f32_16x16x32_bf16 v[136:139], a[96:99], v[176:179], v[204:207]\n\t"
    "v_exp_f32 v128, v128\n\t"
    "v_mfma_f32_16x16x32_bf16 v[136:139], a[100:103], v[180:183], v[136:139]\n\t"
    "ds_read_b64_tr_b16 a[168:169], v230 offset:24576\n\t"
    "v_mfma_f32_16x16x32_bf16 v[136:139], a[104:107], v[184:187], v[136:139]\n\t"
    "v_exp_f32 v129, v129\n\t"
    "v_mfma_f32_16x16x32_bf16 v[136:139], a[108:111], v[188:191], v[136:139]\n\t"
    "ds_read_b64_tr_b16 a[170:171], v230 offset:28672\n\t"
    "v_cvt_pk_bf16_f32 v168, v128, v129\n\t"
    "v_mfma_f32_16x16x32_bf16 v[140:143], a[124:127], v[188:191], v[204:207]\n\t"
    "s_add_u32 m0, s37, 49152\n\t"
    "v_exp_f32 v130, v130\n\t"
    "v_mfma_f32_16x16x32_bf16 v[140:143], a[120:123], v[184:187], v[140:143]\n\t"
    "s_add_u32 s97, s61, 12288\n\t"
    "buffer_load_dwordx4 v254, s[28:31], s97 offen offset:0 sc1 lds\n\t"
    "v_mfma_f32_16x16x32_bf16 v[140:143], a[116:119], v[180:183], v[140:143]\n\t"
    "v_exp_f32 v131, v131\n\t"
    "v_mfma_f32_16x16x32_bf16 v[140:143], a[112:115], v[176:179], v[140:143]\n\t"
    "ds_read_b64_tr_b16 a[172:173], v231 offset:24576\n\t"
    "v_cvt_pk_bf16_f32 v169, v130, v131\n\t"
    "v_mfma_f32_16x16x32_bf16 v[220:223], a[192:195], v[156:159], v[220:223]\n\t"
    "v_exp_f32 v132, v132\n\t"
    "v_mfma_f32_16x16x32_bf16 v[112:115], a[176:179], v[172:175], v[112:115]\n\t"
    "buffer_load_dwordx4 v254, s[28:31], s97 offen offset:1024 sc1 lds\n\t"
    "v_mfma_f32_16x16x32_bf16 v[112:115], a[144:147], v[156:159], v[112:115]\n\t"
    "v_exp_f32 v133, v133\n\t"
    "v_mfma_f32_16x16x32_bf16 v[116:119], a[180:183], v[172:175], v[116:119]\n\t"
    "ds_read_b64_tr_b16 a[174:175], v231 offset:28672\n\t"
    "v_cvt_pk_bf16_f32 v170, v132, v133\n\t"
    "v_mfma_f32_16x16x32_bf16 v[116:119], a[148:151], v[156:159], v[116:119]\n\t"
    "v_exp_f32 v134, v134\n\t"
    "v_mfma_f32_16x16x32_bf16 v[120:123], a[184:187], v[172:175], v[120:123]\n\t"
    "buffer_load_dwordx4 v254, s[28:31], s97 offen offset:2048 sc1 lds\n\t"
    "v_mfma_f32_16x16x32_bf16 v[120:123], a[152:155], v[156:159], v[120:123]\n\t"
    "v_exp_f32 v135, v135\n\t"
    "v_mfma_f32_16x16x32_bf16 v[124:127], a[188:191], v[172:175], v[124:127]\n\t"
    "v_cvt_pk_bf16_f32 v171, v134, v135\n\t"
    "v_mfma_f32_16x16x32_bf16 v[124:127], a[156:159], v[156:159], v[124:127]\n\t"
    "s_waitcnt lgkmcnt(12)\n\t"
    "buffer_load_dwordx4 v254, s[28:31], s97 offen offset:3072 sc1 lds\n\t"
    "v_mfma_f32_16x16x32_bf16 v[128:131], a[64:67], a[32:35], v[200:203]\n\t"
    "v_exp_f32 v136, v136\n\t"
    "v_mfma_f32_16x16x32_bf16 v[128:131], a[68:71], a[36:39], v[128:131]\n\t"
    "ds_read_b64_tr_b16 a[176:177], v232 offset:24576\n\t"
    "ds_read_b64_tr_b16 a[178:179], v232 offset:28672\n\t"
    "v_mfma_f32_16x16x32_bf16 v[128:131], a[72:75], a[40:43], v[128:131]\n\t"
    "v_exp_f32 v137, v137\n\t"
    "v_mfma_f32_16x16x32_bf16 v[128:131], a[76:79], a[44:47], v[128:131]\n\t"
    "s_waitcnt lgkmcnt(10)\n\t"
    "ds_read_b64_tr_b16 a[180:181], v233 offset:24576\n\t"
    "v_cvt_pk_bf16_f32 v172, v136, v137\n\t"
    "v_mfma_f32_16x16x32_bf16 v[132:135], a[92:95], a[44:47], v[200:203]\n\t"
    "v_exp_f32 v138, v138\n\t"
    "v_mfma_f32_16x16x32_bf16 v[132:135], a[88:91], a[40:43], v[132:135]\n\t"
    "ds_read_b64_tr_b16 a[182:183], v233 offset:28672\n\t"
    "ds_read_b64_tr_b16 a[184:185], v234 offset:24576\n\t"
    "v_mfma_f32_16x16x32_bf16 v[132:135], a[84:87], a[36:39], v[132:135]\n\t"
    "v_exp_f32 v139, v139\n\t"
    "v_mfma_f32_16x16x32_bf16 v[132:135], a[80:83], a[32:35], v[132:135]\n\t"
    "ds_read_b64_tr_b16 a[186:187], v234 offset:28672\n\t"
    "v_cvt_pk_bf16_f32 v173, v138, v139\n\t"
    "v_mfma_f32_16x16x32_bf16 v[208:211], a[192:195], v[144:147], v[208:211]\n\t"
    "v_exp_f32 v140, v140\n\t"
    "v_mfma_f32_16x16x32_bf16 v[0:3], a[128:131], v[144:147], v[0:3]\n\t"
    "s_waitcnt lgkmcnt(10)\n\t"
    "ds_read_b64_tr_b16 a[188:189], v235 offset:24576\n\t"
    "ds_read_b64_tr_b16 a[190:191], v235 offset:28672\n\t"
    "v_mfma_f32_16x16x32_bf16 v[0:3], a[160:163], v[160:163], v[0:3]\n\t"
    "v_exp_f32 v141, v141\n\t"
    "v_mfma_f32_16x16x32_bf16 v[4:7], a[132:135], v[144:147], v[4:7]\n\t"
    "ds_read_b128 a[96:99], v224 offset:8192\n\t"
    "v_cvt_pk_bf16_f32 v174, v140, v141\n\t"
    "v_mfma_f32_16x16x32_bf16 v[4:7], a[164:167], v[160:163], v[4:7]\n\t"
    "v_exp_f32 v142, v142\n\t"
    "v_mfma_f32_16x16x32_bf16 v[8:11], a[136:139], v[144:147], v[8:11]\n\t"
    "s_waitcnt lgkmcnt(9)\n\t"
    "ds_read_b128 a[100:103], v225 offset:8192\n\t"
    "v_mfma_f32_16x16x32_bf16 v[8:11], a[168:171], v[160:163], v[8:11]\n\t"
    "v_exp_f32 v143, v143\n\t"
    "v_mfma_f32_16x16x32_bf16 v[12:15], a[140:143], v[144:147], v[12:15]\n\t"
    "ds_read_b128 a[104:107], v226 offset:8192\n\t"
    "v_cvt_pk_bf16_f32 v175, v142, v143\n\t"
    "v_mfma_f32_16x16x32_bf16 v[12:15], a[172:175], v[160:163], v[12:15]\n\t"
    "ds_read_b128 a[108:111], v227 offset:8192\n\t"
    "v_mfma_f32_16x16x32_bf16 v[136:139], a[64:67], v[176:179], v[204:207]\n\t"
    "v_exp_f32 v128, v128\n\t"
    "v_mfma_f32_16x16x32_bf16 v[136:139], a[68:71], v[180:183], v[136:139]\n\t"
    "ds_read_b128 a[112:115], v224 offset:12288\n\t"
    "v_mfma_f32_16x16x32_bf16 v[136:139], a[72:75], v[184:187], v[136:139]\n\t"
    "v_exp_f32 v129, v129\n\t"
    "v_mfma_f32_16x16x32_bf16 v[136:139], a[76:79], v[188:191], v[136:139]\n\t"
    "ds_read_b128 a[116:119], v225 offset:12288\n\t"
    "v_cvt_pk_bf16_f32 v152, v128, v129\n\t"
    "v_mfma_f32_16x16x32_bf16 v[140:143], a[92:95], v[188:191], v[204:207]\n\t"
    "v_exp_f32 v130, v130\n\t"
    "v_mfma_f32_16x16x32_bf16 v[140:143], a[88:91], v[184:187], v[140:143]\n\t"
    "ds_read_b128 a[120:123], v226 offset:12288\n\t"
    "v_mfma_f32_16x16x32_bf16 v[140:143], a[84:87], v[180:183], v[140:143]\n\t"
    "v_exp_f32 v131, v131\n\t"
    "v_mfma_f32_16x16x32_bf16 v[140:143], a[80:83], v[176:179], v[140:143]\n\t"
    "ds_read_b128 a[124:127], v227 offset:12288\n\t"
    "v_cvt_pk_bf16_f32 v153, v130, v131\n\t"
    "v_mfma_f32_16x16x32_bf16 v[212:215], a[192:195], v[148:151], v[212:215]\n\t"
    "v_exp_f32 v132, v132\n\t"
    "v_mfma_f32_16x16x32_bf16 v[32:35], a[128:131], v[148:151], v[32:35]\n\t"
    "buffer_load_dwordx4 a[212:215], v254, s[28:31], s43 offen offset:1024 sc1\n\t"
    "v_mfma_f32_16x16x32_bf16 v[32:35], a[160:163], v[164:167], v[32:35]\n\t"
    "v_exp_f32 v133, v133\n\t"
    "v_mfma_f32_16x16x32_bf16 v[36:39], a[132:135], v[148:151], v[36:39]\n\t"
    "v_cvt_pk_bf16_f32 v154, v132, v133\n\t"
    "v_mfma_f32_16x16x32_bf16 v[36:39], a[164:167], v[164:167], v[36:39]\n\t"
    "v_exp_f32 v134, v134\n\t"
    "v_mfma_f32_16x16x32_bf16 v[40:43], a[136:139], v[148:151], v[40:43]\n\t"
    "buffer_load_dwordx4 a[216:219], v254, s[28:31], s43 offen offset:2048 sc1\n\t"
    "v_mfma_f32_16x16x32_bf16 v[40:43], a[168:171], v[164:167], v[40:43]\n\t"
    "v_exp_f32 v135, v135\n\t"
    "v_mfma_f32_16x16x32_bf16 v[44:47], a[140:143], v[148:151], v[44:47]\n\t"
    "v_cvt_pk_bf16_f32 v155, v134, v135\n\t"
    "v_mfma_f32_16x16x32_bf16 v[44:47], a[172:175], v[164:167], v[44:47]\n\t"
    "v_mfma_f32_16x16x32_bf16 v[128:131], a[64:67], a[0:3], v[192:195]\n\t"
    "v_exp_f32 v136, v136\n\t"
    "v_mfma_f32_16x16x32_bf16 v[128:131], a[68:71], a[4:7], v[128:131]\n\t"
    "s_waitcnt vmcnt(26) lgkmcnt(0)\n\t"
    "v_mfma_f32_16x16x32_bf16 v[128:131], a[72:75], a[8:11], v[128:131]\n\t"
    "s_barrier\n\t"
    "v_exp_f32 v137, v137\n\t"
    "v_mfma_f32_16x16x32_bf16 v[128:131], a[76:79], a[12:15], v[128:131]\n\t"
    "ds_read_b64_tr_b16 a[128:129], v228 offset:32768\n\t"
    "v_cvt_pk_bf16_f32 v156, v136, v137\n\t"
    "v_mfma_f32_16x16x32_bf16 v[132:135], a[92:95], a[12:15], v[192:195]\n\t"
    "s_lshl1_add_u32 m0, s37, 135168\n\t"
    "v_exp_f32 v138, v138\n\t"
    "v_mfma_f32_16x16x32_bf16 v[132:135], a[88:91], a[8:11], v[132:135]\n\t"
    "s_add_u32 s97, s61, 8192\n\t"
    "buffer_load_dwordx4 v254, s[28:31], s97 offen offset:0 sc1 lds\n\t"
    "v_mfma_f32_16x16x32_bf16 v[132:135], a[84:87], a[4:7], v[132:135]\n\t"
    "v_exp_f32 v139, v139\n\t"
    "v_mfma_f32_16x16x32_bf16 v[132:135], a[80:83], a[0:3], v[132:135]\n\t"
    "ds_read_b64_tr_b16 a[130:131], v228 offset:36864\n\t"
    "v_cvt_pk_bf16_f32 v157, v138, v139\n\t"
    "v_mfma_f32_16x16x32_bf16 v[208:211], a[192:195], v[160:163], v[208:211]\n\t"
    "v_exp_f32 v140, v140\n\t"
    "v_mfma_f32_16x16x32_bf16 v[16:19], a[144:147], v[144:147], v[16:19]\n\t"
    "buffer_load_dwordx4 v254, s[28:31], s97 offen offset:1024 sc1 lds\n\t"
    "v_mfma_f32_16x16x32_bf16 v[16:19], a[176:179], v[160:163], v[16:19]\n\t"
    "v_exp_f32 v141, v141\n\t"
    "v_mfma_f32_16x16x32_bf16 v[20:23], a[148:151], v[144:147], v[20:23]\n\t"
    "ds_read_b64_tr_b16 a[132:133], v229 offset:32768\n\t"
    "v_cvt_pk_bf16_f32 v158, v140, v141\n\t"
    "v_mfma_f32_16x16x32_bf16 v[20:23], a[180:183], v[160:163], v[20:23]\n\t"
    "v_exp_f32 v142, v142\n\t"
    "v_mfma_f32_16x16x32_bf16 v[24:27], a[152:155], v[144:147], v[24:27]\n\t"
    "buffer_load_dwordx4 v254, s[28:31], s97 offen offset:2048 sc1 lds\n\t"
    "v_mfma_f32_16x16x32_bf16 v[24:27], a[184:187], v[160:163], v[24:27]\n\t"
    "v_exp_f32 v143, v143\n\t"
    "v_mfma_f32_16x16x32_bf16 v[28:31], a[156:159], v[144:147], v[28:31]\n\t"
    "ds_read_b64_tr_b16 a[134:135], v229 offset:36864\n\t"
    "v_cvt_pk_bf16_f32 v159, v142, v143\n\t"
    "v_mfma_f32_16x16x32_bf16 v[28:31], a[188:191], v[160:163], v[28:31]\n\t"
    "buffer_load_dwordx4 v254, s[28:31], s97 offen offset:3072 sc1 lds\n\t"
    "v_mfma_f32_16x16x32_bf16 v[136:139], a[64:67], a[16:19], v[196:199]\n\t"
    "v_exp_f32 v128, v128\n\t"
    "v_mfma_f32_16x16x32_bf16 v[136:139], a[68:71], a[20:23], v[136:139]\n\t"
    "ds_read_b64_tr_b16 a[136:137], v230 offset:32768\n\t"
    "v_mfma_f32_16x16x32_bf16 v[136:139], a[72:75], a[24:27], v[136:139]\n\t"
    "v_exp_f32 v129, v129\n\t"
    "v_mfma_f32_16x16x32_bf16 v[136:139], a[76:79], a[28:31], v[136:139]\n\t"
    "ds_read_b64_tr_b16 a[138:139], v230 offset:36864\n\t"
    "v_cvt_pk_bf16_f32 v144, v128, v129\n\t"
    "v_mfma_f32_16x16x32_bf16 v[140:143], a[92:95], a[28:31], v[196:199]\n\t"
    "s_add_u32 m0, s37, 65536\n\t"
    "v_exp_f32 v130, v130\n\t"
    "v_mfma_f32_16x16x32_bf16 v[140:143], a[88:91], a[24:27], v[140:143]\n\t"
    "s_add_u32 s97, s61, 16384\n\t"
    "buffer_load_dwordx4 v254, s[28:31], s97 offen offset:0 sc1 lds\n\t"
    "v_mfma_f32_16x16x32_bf16 v[140:143], a[84:87], a[20:23], v[140:143]\n\t"
    "v_exp_f32 v131, v131\n\t"
    "v_mfma_f32_16x16x32_bf16 v[140:143], a[80:83], a[16:19], v[140:143]\n\t"
    "ds_read_b64_tr_b16 a[140:141], v231 offset:32768\n\t"
    "v_cvt_pk_bf16_f32 v145, v130, v131\n\t"
    "v_mfma_f32_16x16x32_bf16 v[212:215], a[192:195], v[164:167], v[212:215]\n\t"
    "v_exp_f32 v132, v132\n\t"
    "v_mfma_f32_16x16x32_bf16 v[48:51], a[144:147], v[148:151], v[48:51]\n\t"
    "buffer_load_dwordx4 v254, s[28:31], s97 offen offset:1024 sc1 lds\n\t"
    "v_mfma_f32_16x16x32_bf16 v[48:51], a[176:179], v[164:167], v[48:51]\n\t"
    "v_exp_f32 v133, v133\n\t"
    "v_mfma_f32_16x16x32_bf16 v[52:55], a[148:151], v[148:151], v[52:55]\n\t"
    "ds_read_b64_tr_b16 a[142:143], v231 offset:36864\n\t"
    "v_cvt_pk_bf16_f32 v146, v132, v133\n\t"
    "v_mfma_f32_16x16x32_bf16 v[52:55], a[180:183], v[164:167], v[52:55]\n\t"
    "v_exp_f32 v134, v134\n\t"
    "v_mfma_f32_16x16x32_bf16 v[56:59], a[152:155], v[148:151], v[56:59]\n\t"
    "buffer_load_dwordx4 v254, s[28:31], s97 offen offset:2048 sc1 lds\n\t"
    "v_mfma_f32_16x16x32_bf16 v[56:59], a[184:187], v[164:167], v[56:59]\n\t"
    "v_exp_f32 v135, v135\n\t"
    "v_mfma_f32_16x16x32_bf16 v[60:63], a[156:159], v[148:151], v[60:63]\n\t"
    "v_cvt_pk_bf16_f32 v147, v134, v135\n\t"
    "v_mfma_f32_16x16x32_bf16 v[60:63], a[188:191], v[164:167], v[60:63]\n\t"
    "buffer_load_dwordx4 v254, s[28:31], s97 offen offset:3072 sc1 lds\n\t"
    "v_mfma_f32_16x16x32_bf16 v[128:131], a[96:99], a[0:3], v[192:195]\n\t"
    "v_exp_f32 v136, v136\n\t"
    "v_mfma_f32_16x16x32_bf16 v[128:131], a[100:103], a[4:7], v[128:131]\n\t"
    "ds_read_b64_tr_b16 a[144:145], v232 offset:32768\n\t"
    "ds_read_b64_tr_b16 a[146:147], v232 offset:36864\n\t"
    "v_mfma_f32_16x16x32_bf16 v[128:131], a[104:107], a[8:11], v[128:131]\n\t"
    "s_waitcnt vmcnt(20)\n\t"
    "v_exp_f32 v137, v137\n\t"
    "v_mfma_f32_16x16x32_bf16 v[128:131], a[108:111], a[12:15], v[128:131]\n\t"
    "ds_read_b64_tr_b16 a[148:149], v233 offset:32768\n\t"
    "v_cvt_pk_bf16_f32 v148, v136, v137\n\t"
    "v_mfma_f32_16x16x32_bf16 v[132:135], a[124:127], a[12:15], v[192:195]\n\t"
    "v_exp_f32 v138, v138\n\t"
    "v_mfma_f32_16x16x32_bf16 v[132:135], a[120:123], a[8:11], v[132:135]\n\t"
    "ds_read_b64_tr_b16 a[150:151], v233 offset:36864\n\t"
    "ds_read_b64_tr_b16 a[152:153], v234 offset:32768\n\t"
    "v_mfma_f32_16x16x32_bf16 v[132:135], a[116:119], a[4:7], v[132:135]\n\t"
    "v_exp_f32 v139, v139\n\t"
    "v_mfma_f32_16x16x32_bf16 v[132:135], a[112:115], a[0:3], v[132:135]\n\t"
    "ds_read_b64_tr_b16 a[154:155], v234 offset:36864\n\t"
    "v_cvt_pk_bf16_f32 v149, v138, v139\n\t"
    "v_mfma_f32_16x16x32_bf16 v[216:219], a[192:195], v[168:171], v[216:219]\n\t"
    "v_exp_f32 v140, v140\n\t"
    "v_mfma_f32_16x16x32_bf16 v[64:67], a[160:163], v[168:171], v[64:67]\n\t"
    "s_waitcnt lgkmcnt(10)\n\t"
    "ds_read_b64_tr_b16 a[156:157], v235 offset:32768\n\t"
    "ds_read_b64_tr_b16 a[158:159], v235 offset:36864\n\t"
    "v_mfma_f32_16x16x32_bf16 v[64:67], a[128:131], v[152:155], v[64:67]\n\t"
    "v_exp_f32 v141, v141\n\t"
    "v_mfma_f32_16x16x32_bf16 v[68:71], a[164:167], v[168:171], v[68:71]\n\t"
    "ds_read_b128 a[64:67], v224 offset:16384\n\t"
    "v_cvt_pk_bf16_f32 v150, v140, v141\n\t"
    "v_mfma_f32_16x16x32_bf16 v[68:71], a[132:135], v[152:155], v[68:71]\n\t"
    "v_exp_f32 v142, v142\n\t"
    "v_mfma_f32_16x16x32_bf16 v[72:75], a[168:171], v[168:171], v[72:75]\n\t"
    "s_waitcnt lgkmcnt(9)\n\t"
    "ds_read_b128 a[68:71], v225 offset:16384\n\t"
    "v_mfma_f32_16x16x32_bf16 v[72:75], a[136:139], v[152:155], v[72:75]\n\t"
    "v_exp_f32 v143, v143\n\t"
    "v_mfma_f32_16x16x32_bf16 v[76:79], a[172:175], v[168:171], v[76:79]\n\t"
    "ds_read_b128 a[72:75], v226 offset:16384\n\t"
    "v_cvt_pk_bf16_f32 v151, v142, v143\n\t"
    "v_mfma_f32_16x16x32_bf16 v[76:79], a[140:143], v[152:155], v[76:79]\n\t"
    "ds_read_b128 a[76:79], v227 offset:16384\n\t"
    "v_mfma_f32_16x16x32_bf16 v[136:139], a[96:99], a[16:19], v[196:199]\n\t"
    "v_exp_f32 v128, v128\n\t"
    "v_mfma_f32_16x16x32_bf16 v[136:139], a[100:103], a[20:23], v[136:139]\n\t"
    "ds_read_b128 a[80:83], v224 offset:20480\n\t"
    "v_mfma_f32_16x16x32_bf16 v[136:139], a[104:107], a[24:27], v[136:139]\n\t"
    "v_exp_f32 v129, v129\n\t"
    "v_mfma_f32_16x16x32_bf16 v[136:139], a[108:111], a[28:31], v[136:139]\n\t"
    "ds_read_b128 a[84:87], v225 offset:20480\n\t"
    "v_cvt_pk_bf16_f32 v160, v128, v129\n\t"
    "v_mfma_f32_16x16x32_bf16 v[140:143], a[124:127], a[28:31], v[196:199]\n\t"
    "v_exp_f32 v130, v130\n\t"
    "v_mfma_f32_16x16x32_bf16 v[140:143], a[120:123], a[24:27], v[140:143]\n\t"
    "ds_read_b128 a[88:91], v226 offset:20480\n\t"
    "v_mfma_f32_16x16x32_bf16 v[140:143], a[116:119], a[20:23], v[140:143]\n\t"
    "v_exp_f32 v131, v131\n\t"
    "v_mfma_f32_16x16x32_bf16 v[140:143], a[112:115], a[16:19], v[140:143]\n\t"
    "ds_read_b128 a[92:95], v227 offset:20480\n\t"
    "v_cvt_pk_bf16_f32 v161, v130, v131\n\t"
    "v_mfma_f32_16x16x32_bf16 v[220:223], a[192:195], v[172:175], v[220:223]\n\t"
    "v_exp_f32 v132, v132\n\t"
    "v_mfma_f32_16x16x32_bf16 v[96:99], a[160:163], v[172:175], v[96:99]\n\t"
    "buffer_load_dwordx4 a[220:223], v254, s[28:31], s43 offen offset:3072 sc1\n\t"
    "v_mfma_f32_16x16x32_bf16 v[96:99], a[128:131], v[156:159], v[96:99]\n\t"
    "v_exp_f32 v133, v133\n\t"
    "v_mfma_f32_16x16x32_bf16 v[100:103], a[164:167], v[172:175], v[100:103]\n\t"
    "v_cvt_pk_bf16_f32 v162, v132, v133\n\t"
    "v_mfma_f32_16x16x32_bf16 v[100:103], a[132:135], v[156:159], v[100:103]\n\t"
    "v_exp_f32 v134, v134\n\t"
    "v_mfma_f32_16x16x32_bf16 v[104:107], a[168:171], v[172:175], v[104:107]\n\t"
    "s_add_u32 s43, s61, 73728\n\t"
    "buffer_load_dwordx4 a[224:227], v254, s[28:31], s43 offen offset:0 sc1\n\t"
    "v_mfma_f32_16x16x32_bf16 v[104:107], a[136:139], v[156:159], v[104:107]\n\t"
    "v_exp_f32 v135, v135\n\t"
    "v_mfma_f32_16x16x32_bf16 v[108:111], a[172:175], v[172:175], v[108:111]\n\t"
    "v_cvt_pk_bf16_f32 v163, v134, v135\n\t"
    "v_mfma_f32_16x16x32_bf16 v[108:111], a[140:143], v[156:159], v[108:111]\n\t"
    "v_mfma_f32_16x16x32_bf16 v[128:131], a[96:99], a[32:35], v[200:203]\n\t"
    "v_exp_f32 v136, v136\n\t"
    "v_mfma_f32_16x16x32_bf16 v[128:131], a[100:103], a[36:39], v[128:131]\n\t"
    "v_mfma_f32_16x16x32_bf16 v[128:131], a[104:107], a[40:43], v[128:131]\n\t"
    "v_exp_f32 v137, v137\n\t"
    "v_mfma_f32_16x16x32_bf16 v[128:131], a[108:111], a[44:47], v[128:131]\n\t"
    "ds_read_b64_tr_b16 a[160:161], v228 offset:40960\n\t"
    "v_cvt_pk_bf16_f32 v164, v136, v137\n\t"
    "v_mfma_f32_16x16x32_bf16 v[132:135], a[124:127], a[44:47], v[200:203]\n\t"
    "s_add_u32 m0, s37, 0\n\t"
    "v_exp_f32 v138, v138\n\t"
    "v_mfma_f32_16x16x32_bf16 v[132:135], a[120:123], a[40:43], v[132:135]\n\t"
    "s_add_u32 s97, s61, 20480\n\t"
    "buffer_load_dwordx4 v254, s[28:31], s97 offen offset:0 sc1 lds\n\t"
    "v_mfma_f32_16x16x32_bf16 v[132:135], a[116:119], a[36:39], v[132:135]\n\t"
    "v_exp_f32 v139, v139\n\t"
    "v_mfma_f32_16x16x32_bf16 v[132:135], a[112:115], a[32:35], v[132:135]\n\t"
    "ds_read_b64_tr_b16 a[162:163], v228 offset:45056\n\t"
    "v_cvt_pk_bf16_f32 v165, v138, v139\n\t"
    "v_mfma_f32_16x16x32_bf16 v[216:219], a[192:195], v[152:155], v[216:219]\n\t"
    "v_exp_f32 v140, v140\n\t"
    "v_mfma_f32_16x16x32_bf16 v[80:83], a[176:179], v[168:171], v[80:83]\n\t"
    "s_waitcnt lgkmcnt(14)\n\t"
    "buffer_load_dwordx4 v254, s[28:31], s97 offen offset:1024 sc1 lds\n\t"
    "v_mfma_f32_16x16x32_bf16 v[80:83], a[144:147], v[152:155], v[80:83]\n\t"
    "v_exp_f32 v141, v141\n\t"
    "v_mfma_f32_16x16x32_bf16 v[84:87], a[180:183], v[168:171], v[84:87]\n\t"
    "ds_read_b64_tr_b16 a[164:165], v229 offset:40960\n\t"
    "v_cvt_pk_bf16_f32 v166, v140, v141\n\t"
    "v_mfma_f32_16x16x32_bf16 v[84:87], a[148:151], v[152:155], v[84:87]\n\t"
    "v_exp_f32 v142, v142\n\t"
    "v_mfma_f32_16x16x32_bf16 v[88:91], a[184:187], v[168:171], v[88:91]\n\t"
    "s_waitcnt lgkmcnt(11)\n\t"
    "buffer_load_dwordx4 v254, s[28:31], s97 offen offset:2048 sc1 lds\n\t"
    "v_mfma_f32_16x16x32_bf16 v[88:91], a[152:155], v[152:155], v[88:91]\n\t"
    "v_exp_f32 v143, v143\n\t"
    "v_mfma_f32_16x16x32_bf16 v[92:95], a[188:191], v[168:171], v[92:95]\n\t"
    "ds_read_b64_tr_b16 a[166:167], v229 offset:45056\n\t"
    "v_cvt_pk_bf16_f32 v167, v142, v143\n\t"
    "v_mfma_f32_16x16x32_bf16 v[92:95], a[156:159], v[152:155], v[92:95]\n\t"
    "buffer_load_dwordx4 v254, s[28:31], s97 offen offset:3072 sc1 lds\n\t"
    "v_mfma_f32_16x16x32_bf16 v[136:139], a[96:99], v[176:179], v[204:207]\n\t"
    "v_exp_f32 v128, v128\n\t"
    "v_mfma_f32_16x16x32_bf16 v[136:139], a[100:103], v[180:183], v[136:139]\n\t"
    "ds_read_b64_tr_b16 a[168:169], v230 offset:40960\n\t"
    "v_mfma_f32_16x16x32_bf16 v[136:139], a[104:107], v[184:187], v[136:139]\n\t"
    "v_exp_f32 v129, v129\n\t"
    "v_mfma_f32_16x16x32_bf16 v[136:139], a[108:111], v[188:191], v[136:139]\n\t"
    "ds_read_b64_tr_b16 a[170:171], v230 offset:45056\n\t"
    "v_cvt_pk_bf16_f32 v168, v128, v129\n\t"
    "v_mfma_f32_16x16x32_bf16 v[140:143], a[124:127], v[188:191], v[204:207]\n\t"
    "s_add_u32 m0, s37, 81920\n\t"
    "v_exp_f32 v130, v130\n\t"
    "v_mfma_f32_16x16x32_bf16 v[140:143], a[120:123], v[184:187], v[140:143]\n\t"
    "s_add_u32 s97, s61, 24576\n\t"
    "buffer_load_dwordx4 v254, s[28:31], s97 offen offset:0 sc1 lds\n\t"
    "v_mfma_f32_16x16x32_bf16 v[140:143], a[116:119], v[180:183], v[140:143]\n\t"
    "v_exp_f32 v131, v131\n\t"
    "v_mfma_f32_16x16x32_bf16 v[140:143], a[112:115], v[176:179], v[140:143]\n\t"
    "ds_read_b64_tr_b16 a[172:173], v231 offset:40960\n\t"
    "v_cvt_pk_bf16_f32 v169, v130, v131\n\t"
    "v_mfma_f32_16x16x32_bf16 v[220:223], a[192:195], v[156:159], v[220:223]\n\t"
    "v_exp_f32 v132, v132\n\t"
    "v_mfma_f32_16x16x32_bf16 v[112:115], a[176:179], v[172:175], v[112:115]\n\t"
    "buffer_load_dwordx4 v254, s[28:31], s97 offen offset:1024 sc1 lds\n\t"
    "v_mfma_f32_16x16x32_bf16 v[112:115], a[144:147], v[156:159], v[112:115]\n\t"
    "v_exp_f32 v133, v133\n\t"
    "v_mfma_f32_16x16x32_bf16 v[116:119], a[180:183], v[172:175], v[116:119]\n\t"
    "ds_read_b64_tr_b16 a[174:175], v231 offset:45056\n\t"
    "v_cvt_pk_bf16_f32 v170, v132, v133\n\t"
    "v_mfma_f32_16x16x32_bf16 v[116:119], a[148:151], v[156:159], v[116:119]\n\t"
    "v_exp_f32 v134, v134\n\t"
    "v_mfma_f32_16x16x32_bf16 v[120:123], a[184:187], v[172:175], v[120:123]\n\t"
    "buffer_load_dwordx4 v254, s[28:31], s97 offen offset:2048 sc1 lds\n\t"
    "v_mfma_f32_16x16x32_bf16 v[120:123], a[152:155], v[156:159], v[120:123]\n\t"
    "v_exp_f32 v135, v135\n\t"
    "v_mfma_f32_16x16x32_bf16 v[124:127], a[188:191], v[172:175], v[124:127]\n\t"
    "v_cvt_pk_bf16_f32 v171, v134, v135\n\t"
    "v_mfma_f32_16x16x32_bf16 v[124:127], a[156:159], v[156:159], v[124:127]\n\t"
    "s_waitcnt lgkmcnt(12)\n\t"
    "buffer_load_dwordx4 v254, s[28:31], s97 offen offset:3072 sc1 lds\n\t"
    "v_mfma_f32_16x16x32_bf16 v[128:131], a[64:67], a[32:35], v[200:203]\n\t"
    "v_exp_f32 v136, v136\n\t"
    "v_mfma_f32_16x16x32_bf16 v[128:131], a[68:71], a[36:39], v[128:131]\n\t"
    "ds_read_b64_tr_b16 a[176:177], v232 offset:40960\n\t"
    "ds_read_b64_tr_b16 a[178:179], v232 offset:45056\n\t"
    "v_mfma_f32_16x16x32_bf16 v[128:131], a[72:75], a[40:43], v[128:131]\n\t"
    "v_exp_f32 v137, v137\n\t"
    "v_mfma_f32_16x16x32_bf16 v[128:131], a[76:79], a[44:47], v[128:131]\n\t"
    "s_waitcnt lgkmcnt(10)\n\t"
    "ds_read_b64_tr_b16 a[180:181], v233 offset:40960\n\t"
    "v_cvt_pk_bf16_f32 v172, v136, v137\n\t"
    "v_mfma_f32_16x16x32_bf16 v[132:135], a[92:95], a[44:47], v[200:203]\n\t"
    "v_exp_f32 v138, v138\n\t"
    "v_mfma_f32_16x16x32_bf16 v[132:135], a[88:91], a[40:43], v[132:135]\n\t"
    "ds_read_b64_tr_b16 a[182:183], v233 offset:45056\n\t"
    "ds_read_b64_tr_b16 a[184:185], v234 offset:40960\n\t"
    "v_mfma_f32_16x16x32_bf16 v[132:135], a[84:87], a[36:39], v[132:135]\n\t"
    "v_exp_f32 v139, v139\n\t"
    "v_mfma_f32_16x16x32_bf16 v[132:135], a[80:83], a[32:35], v[132:135]\n\t"
    "ds_read_b64_tr_b16 a[186:187], v234 offset:45056\n\t"
    "v_cvt_pk_bf16_f32 v173, v138, v139\n\t"
    "v_mfma_f32_16x16x32_bf16 v[208:211], a[192:195], v[144:147], v[208:211]\n\t"
    "v_exp_f32 v140, v140\n\t"
    "v_mfma_f32_16x16x32_bf16 v[0:3], a[128:131], v[144:147], v[0:3]\n\t"
    "s_waitcnt lgkmcnt(10)\n\t"
    "ds_read_b64_tr_b16 a[188:189], v235 offset:40960\n\t"
    "ds_read_b64_tr_b16 a[190:191], v235 offset:45056\n\t"
    "v_mfma_f32_16x16x32_bf16 v[0:3], a[160:163], v[160:163], v[0:3]\n\t"
    "v_exp_f32 v141, v141\n\t"
    "v_mfma_f32_16x16x32_bf16 v[4:7], a[132:135], v[144:147], v[4:7]\n\t"
    "ds_read_b128 a[96:99], v224 offset:24576\n\t"
    "v_cvt_pk_bf16_f32 v174, v140, v141\n\t"
    "v_mfma_f32_16x16x32_bf16 v[4:7], a[164:167], v[160:163], v[4:7]\n\t"
    "v_exp_f32 v142, v142\n\t"
    "v_mfma_f32_16x16x32_bf16 v[8:11], a[136:139], v[144:147], v[8:11]\n\t"
    "s_waitcnt lgkmcnt(9)\n\t"
    "ds_read_b128 a[100:103], v225 offset:24576\n\t"
    "v_mfma_f32_16x16x32_bf16 v[8:11], a[168:171], v[160:163], v[8:11]\n\t"
    "v_exp_f32 v143, v143\n\t"
    "v_mfma_f32_16x16x32_bf16 v[12:15], a[140:143], v[144:147], v[12:15]\n\t"
    "ds_read_b128 a[104:107], v226 offset:24576\n\t"
    "v_cvt_pk_bf16_f32 v175, v142, v143\n\t"
    "v_mfma_f32_16x16x32_bf16 v[12:15], a[172:175], v[160:163], v[12:15]\n\t"
    "ds_read_b128 a[108:111], v227 offset:24576\n\t"
    "v_mfma_f32_16x16x32_bf16 v[136:139], a[64:67], v[176:179], v[204:207]\n\t"
    "v_exp_f32 v128, v128\n\t"
    "v_mfma_f32_16x16x32_bf16 v[136:139], a[68:71], v[180:183], v[136:139]\n\t"
    "ds_read_b128 a[112:115], v224 offset:28672\n\t"
    "v_mfma_f32_16x16x32_bf16 v[136:139], a[72:75], v[184:187], v[136:139]\n\t"
    "v_exp_f32 v129, v129\n\t"
    "v_mfma_f32_16x16x32_bf16 v[136:139], a[76:79], v[188:191], v[136:139]\n\t"
    "ds_read_b128 a[116:119], v225 offset:28672\n\t"
    "v_cvt_pk_bf16_f32 v152, v128, v129\n\t"
    "v_mfma_f32_16x16x32_bf16 v[140:143], a[92:95], v[188:191], v[204:207]\n\t"
    "v_exp_f32 v130, v130\n\t"
    "v_mfma_f32_16x16x32_bf16 v[140:143], a[88:91], v[184:187], v[140:143]\n\t"
    "ds_read_b128 a[120:123], v226 offset:28672\n\t"
    "v_mfma_f32_16x16x32_bf16 v[140:143], a[84:87], v[180:183], v[140:143]\n\t"
    "v_exp_f32 v131, v131\n\t"
    "v_mfma_f32_16x16x32_bf16 v[140:143], a[80:83], v[176:179], v[140:143]\n\t"
    "ds_read_b128 a[124:127], v227 offset:28672\n\t"
    "v_cvt_pk_bf16_f32 v153, v130, v131\n\t"
    "v_mfma_f32_16x16x32_bf16 v[212:215], a[192:195], v[148:151], v[212:215]\n\t"
    "v_exp_f32 v132, v132\n\t"
    "v_mfma_f32_16x16x32_bf16 v[32:35], a[128:131], v[148:151], v[32:35]\n\t"
    "buffer_load_dwordx4 a[228:231], v254, s[28:31], s43 offen offset:1024 sc1\n\t"
    "v_mfma_f32_16x16x32_bf16 v[32:35], a[160:163], v[164:167], v[32:35]\n\t"
    "v_exp_f32 v133, v133\n\t"
    "v_mfma_f32_16x16x32_bf16 v[36:39], a[132:135], v[148:151], v[36:39]\n\t"
    "v_cvt_pk_bf16_f32 v154, v132, v133\n\t"
    "v_mfma_f32_16x16x32_bf16 v[36:39], a[164:167], v[164:167], v[36:39]\n\t"
    "v_exp_f32 v134, v134\n\t"
    "v_mfma_f32_16x16x32_bf16 v[40:43], a[136:139], v[148:151], v[40:43]\n\t"
    "buffer_load_dwordx4 a[232:235], v254, s[28:31], s43 offen offset:2048 sc1\n\t"
    "v_mfma_f32_16x16x32_bf16 v[40:43], a[168:171], v[164:167], v[40:43]\n\t"
    "v_exp_f32 v135, v135\n\t"
    "v_mfma_f32_16x16x32_bf16 v[44:47], a[140:143], v[148:151], v[44:47]\n\t"
    "v_cvt_pk_bf16_f32 v155, v134, v135\n\t"
    "v_mfma_f32_16x16x32_bf16 v[44:47], a[172:175], v[164:167], v[44:47]\n\t"
    "v_mfma_f32_16x16x32_bf16 v[128:131], a[64:67], a[0:3], v[192:195]\n\t"
    "v_exp_f32 v136, v136\n\t"
    "v_mfma_f32_16x16x32_bf16 v[128:131], a[68:71], a[4:7], v[128:131]\n\t"
    "s_waitcnt lgkmcnt(0)\n\t"
    "v_mfma_f32_16x16x32_bf16 v[128:131], a[72:75], a[8:11], v[128:131]\n\t"
    "s_barrier\n\t"
    "v_exp_f32 v137, v137\n\t"
    "v_mfma_f32_16x16x32_bf16 v[128:131], a[76:79], a[12:15], v[128:131]\n\t"
    "ds_read_b64_tr_b16 a[128:129], v228 offset:49152\n\t"
    "v_cvt_pk_bf16_f32 v156, v136, v137\n\t"
    "v_mfma_f32_16x16x32_bf16 v[132:135], a[92:95], a[12:15], v[192:195]\n\t"
    "s_add_u32 m0, s37, 16384\n\t"
    "v_exp_f32 v138, v138\n\t"
    "v_mfma_f32_16x16x32_bf16 v[132:135], a[88:91], a[8:11], v[132:135]\n\t"
    "s_add_u32 s97, s61, 28672\n\t"
    "buffer_load_dwordx4 v254, s[28:31], s97 offen offset:0 sc1 lds\n\t"
    "v_mfma_f32_16x16x32_bf16 v[132:135], a[84:87], a[4:7], v[132:135]\n\t"
    "v_exp_f32 v139, v139\n\t"
    "v_mfma_f32_16x16x32_bf16 v[132:135], a[80:83], a[0:3], v[132:135]\n\t"
    "ds_read_b64_tr_b16 a[130:131], v228 offset:53248\n\t"
    "v_cvt_pk_bf16_f32 v157, v138, v139\n\t"
    "v_mfma_f32_16x16x32_bf16 v[208:211], a[192:195], v[160:163], v[208:211]\n\t"
    "v_exp_f32 v140, v140\n\t"
    "v_mfma_f32_16x16x32_bf16 v[16:19], a[144:147], v[144:147], v[16:19]\n\t"
    "buffer_load_dwordx4 v254, s[28:31], s97 offen offset:1024 sc1 lds\n\t"
    "v_mfma_f32_16x16x32_bf16 v[16:19], a[176:179], v[160:163], v[16:19]\n\t"
    "v_exp_f32 v141, v141\n\t"
    "v_mfma_f32_16x16x32_bf16 v[20:23], a[148:151], v[144:147], v[20:23]\n\t"
    "ds_read_b64_tr_b16 a[132:133], v229 offset:49152\n\t"
    "v_cvt_pk_bf16_f32 v158, v140, v141\n\t"
    "v_mfma_f32_16x16x32_bf16 v[20:23], a[180:183], v[160:163], v[20:23]\n\t"
    "v_exp_f32 v142, v142\n\t"
    "v_mfma_f32_16x16x32_bf16 v[24:27], a[152:155], v[144:147], v[24:27]\n\t"
    "buffer_load_dwordx4 v254, s[28:31], s97 offen offset:2048 sc1 lds\n\t"
    "v_mfma_f32_16x16x32_bf16 v[24:27], a[184:187], v[160:163], v[24:27]\n\t"
    "v_exp_f32 v143, v143\n\t"
    "v_mfma_f32_16x16x32_bf16 v[28:31], a[156:159], v[144:147], v[28:31]\n\t"
    "ds_read_b64_tr_b16 a[134:135], v229 offset:53248\n\t"
    "v_cvt_pk_bf16_f32 v159, v142, v143\n\t"
    "v_mfma_f32_16x16x32_bf16 v[28:31], a[188:191], v[160:163], v[28:31]\n\t"
    "buffer_load_dwordx4 v254, s[28:31], s97 offen offset:3072 sc1 lds\n\t"
    "v_mfma_f32_16x16x32_bf16 v[136:139], a[64:67], a[16:19], v[196:199]\n\t"
    "v_exp_f32 v128, v128\n\t"
    "v_mfma_f32_16x16x32_bf16 v[136:139], a[68:71], a[20:23], v[136:139]\n\t"
    "ds_read_b64_tr_b16 a[136:137], v230 offset:49152\n\t"
    "v_mfma_f32_16x16x32_bf16 v[136:139], a[72:75], a[24:27], v[136:139]\n\t"
    "v_exp_f32 v129, v129\n\t"
    "v_mfma_f32_16x16x32_bf16 v[136:139], a[76:79], a[28:31], v[136:139]\n\t"
    "ds_read_b64_tr_b16 a[138:139], v230 offset:53248\n\t"
    "v_cvt_pk_bf16_f32 v144, v128, v129\n\t"
    "v_mfma_f32_16x16x32_bf16 v[140:143], a[92:95], a[28:31], v[196:199]\n\t"
    "v_exp_f32 v130, v130\n\t"
    "v_mfma_f32_16x16x32_bf16 v[140:143], a[88:91], a[24:27], v[140:143]\n\t"
    "buffer_load_dwordx4 a[236:239], v254, s[28:31], s43 offen offset:3072 sc1\n\t"
    "v_mfma_f32_16x16x32_bf16 v[140:143], a[84:87], a[20:23], v[140:143]\n\t"
    "v_exp_f32 v131, v131\n\t"
    "v_mfma_f32_16x16x32_bf16 v[140:143], a[80:83], a[16:19], v[140:143]\n\t"
    "ds_read_b64_tr_b16 a[140:141], v231 offset:49152\n\t"
    "v_cvt_pk_bf16_f32 v145, v130, v131\n\t"
    "v_mfma_f32_16x16x32_bf16 v[212:215], a[192:195], v[164:167], v[212:215]\n\t"
    "v_exp_f32 v132, v132\n\t"
    "v_mfma_f32_16x16x32_bf16 v[48:51], a[144:147], v[148:151], v[48:51]\n\t"
    "s_add_u32 s43, s61, 77824\n\t"
    "buffer_load_dwordx4 a[240:243], v254, s[28:31], s43 offen offset:0 sc1\n\t"
    "v_mfma_f32_16x16x32_bf16 v[48:51], a[176:179], v[164:167], v[48:51]\n\t"
    "v_exp_f32 v133, v133\n\t"
    "v_mfma_f32_16x16x32_bf16 v[52:55], a[148:151], v[148:151], v[52:55]\n\t"
    "ds_read_b64_tr_b16 a[142:143], v231 offset:53248\n\t"
    "v_cvt_pk_bf16_f32 v146, v132, v133\n\t"
    "v_mfma_f32_16x16x32_bf16 v[52:55], a[180:183], v[164:167], v[52:55]\n\t"
    "v_exp_f32 v134, v134\n\t"
    "v_mfma_f32_16x16x32_bf16 v[56:59], a[152:155], v[148:151], v[56:59]\n\t"
    "buffer_load_dwordx4 a[244:247], v254, s[28:31], s43 offen offset:1024 sc1\n\t"
    "v_mfma_f32_16x16x32_bf16 v[56:59], a[184:187], v[164:167], v[56:59]\n\t"
    "v_exp_f32 v135, v135\n\t"
    "v_mfma_f32_16x16x32_bf16 v[60:63], a[156:159], v[148:151], v[60:63]\n\t"
    "v_cvt_pk_bf16_f32 v147, v134, v135\n\t"
    "v_mfma_f32_16x16x32_bf16 v[60:63], a[188:191], v[164:167], v[60:63]\n\t"
    "buffer_load_dwordx4 a[248:251], v254, s[28:31], s43 offen offset:2048 sc1\n\t"
    "v_mfma_f32_16x16x32_bf16 v[128:131], a[96:99], a[0:3], v[192:195]\n\t"
    "v_exp_f32 v136, v136\n\t"
    "v_mfma_f32_16x16x32_bf16 v[128:131], a[100:103], a[4:7], v[128:131]\n\t"
    "ds_read_b64_tr_b16 a[144:145], v232 offset:49152\n\t"
    "v_mfma_f32_16x16x32_bf16 v[128:131], a[104:107], a[8:11], v[128:131]\n\t"
    "s_waitcnt vmcnt(20)\n\t"
    "v_exp_f32 v137, v137\n\t"
    "v_mfma_f32_16x16x32_bf16 v[128:131], a[108:111], a[12:15], v[128:131]\n\t"
    "ds_read_b64_tr_b16 a[146:147], v232 offset:53248\n\t"
    "v_cvt_pk_bf16_f32 v148, v136, v137\n\t"
    "v_mfma_f32_16x16x32_bf16 v[132:135], a[124:127], a[12:15], v[192:195]\n\t"
    "s_add_u32 m0, s37, 98304\n\t"
    "v_exp_f32 v138, v138\n\t"
    "v_mfma_f32_16x16x32_bf16 v[132:135], a[120:123], a[8:11], v[132:135]\n\t"
    "s_add_u32 s97, s61, 32768\n\t"
    "buffer_load_dwordx4 v254, s[28:31], s97 offen offset:0 sc1 lds\n\t"
    "v_mfma_f32_16x16x32_bf16 v[132:135], a[116:119], a[4:7], v[132:135]\n\t"
    "v_exp_f32 v139, v139\n\t"
    "v_mfma_f32_16x16x32_bf16 v[132:135], a[112:115], a[0:3], v[132:135]\n\t"
    "ds_read_b64_tr_b16 a[148:149], v233 offset:49152\n\t"
    "v_cvt_pk_bf16_f32 v149, v138, v139\n\t"
    "v_mfma_f32_16x16x32_bf16 v[216:219], a[192:195], v[168:171], v[216:219]\n\t"
    "v_exp_f32 v140, v140\n\t"
    "v_mfma_f32_16x16x32_bf16 v[64:67], a[160:163], v[168:171], v[64:67]\n\t"
    "s_waitcnt lgkmcnt(7)\n\t"
    "buffer_load_dwordx4 v254, s[28:31], s97 offen offset:1024 sc1 lds\n\t"
    "v_mfma_f32_16x16x32_bf16 v[64:67], a[128:131], v[152:155], v[64:67]\n\t"
    "v_exp_f32 v141, v141\n\t"
    "v_mfma_f32_16x16x32_bf16 v[68:71], a[164:167], v[168:171], v[68:71]\n\t"
    "ds_read_b64_tr_b16 a[150:151], v233 offset:53248\n\t"
    "v_cvt_pk_bf16_f32 v150, v140, v141\n\t"
    "v_mfma_f32_16x16x32_bf16 v[68:71], a[132:135], v[152:155], v[68:71]\n\t"
    "v_exp_f32 v142, v142\n\t"
    "v_mfma_f32_16x16x32_bf16 v[72:75], a[168:171], v[168:171], v[72:75]\n\t"
    "s_waitcnt lgkmcnt(4)\n\t"
    "buffer_load_dwordx4 v254, s[28:31], s97 offen offset:2048 sc1 lds\n\t"
    "v_mfma_f32_16x16x32_bf16 v[72:75], a[136:139], v[152:155], v[72:75]\n\t"
    "v_exp_f32 v143, v143\n\t"
    "v_mfma_f32_16x16x32_bf16 v[76:79], a[172:175], v[168:171], v[76:79]\n\t"
    "ds_read_b64_tr_b16 a[152:153], v234 offset:49152\n\t"
    "v_cvt_pk_bf16_f32 v151, v142, v143\n\t"
    "v_mfma_f32_16x16x32_bf16 v[76:79], a[140:143], v[152:155], v[76:79]\n\t"
    "buffer_load_dwordx4 v254, s[28:31], s97 offen offset:3072 sc1 lds\n\t"
    "v_mfma_f32_16x16x32_bf16 v[136:139], a[96:99], a[16:19], v[196:199]\n\t"
    "v_exp_f32 v128, v128\n\t"
    "v_mfma_f32_16x16x32_bf16 v[136:139], a[100:103], a[20:23], v[136:139]\n\t"
    "buffer_load_dwordx4 a[252:255], v254, s[28:31], s43 offen offset:3072 sc1\n\t"
    "v_mfma_f32_16x16x32_bf16 v[136:139], a[104:107], a[24:27], v[136:139]\n\t"
    "v_exp_f32 v129, v129\n\t"
    "v_mfma_f32_16x16x32_bf16 v[136:139], a[108:111], a[28:31], v[136:139]\n\t"
    "ds_read_b64_tr_b16 a[154:155], v234 offset:53248\n\t"
    "v_cvt_pk_bf16_f32 v160, v128, v129\n\t"
    "v_mfma_f32_16x16x32_bf16 v[140:143], a[124:127], a[28:31], v[196:199]\n\t"
    "v_exp_f32 v130, v130\n\t"
    "v_mfma_f32_16x16x32_bf16 v[140:143], a[120:123], a[24:27], v[140:143]\n\t"
    "s_add_u32 s43, s61, 53248\n\t"
    "buffer_load_dwordx4 a[48:51], v254, s[28:31], s43 offen offset:1024 sc1\n\t"
    "v_mfma_f32_16x16x32_bf16 v[140:143], a[116:119], a[20:23], v[140:143]\n\t"
    "v_exp_f32 v131, v131\n\t"
    "v_mfma_f32_16x16x32_bf16 v[140:143], a[112:115], a[16:19], v[140:143]\n\t"
    "ds_read_b64_tr_b16 a[156:157], v235 offset:49152\n\t"
    "v_cvt_pk_bf16_f32 v161, v130, v131\n\t"
    "v_mfma_f32_16x16x32_bf16 v[220:223], a[192:195], v[172:175], v[220:223]\n\t"
    "v_exp_f32 v132, v132\n\t"
    "v_mfma_f32_16x16x32_bf16 v[96:99], a[160:163], v[172:175], v[96:99]\n\t"
    "buffer_load_dwordx4 a[52:55], v254, s[28:31], s43 offen offset:2048 sc1\n\t"
    "v_mfma_f32_16x16x32_bf16 v[96:99], a[128:131], v[156:159], v[96:99]\n\t"
    "v_exp_f32 v133, v133\n\t"
    "v_mfma_f32_16x16x32_bf16 v[100:103], a[164:167], v[172:175], v[100:103]\n\t"
    "ds_read_b64_tr_b16 a[158:159], v235 offset:53248\n\t"
    "v_cvt_pk_bf16_f32 v162, v132, v133\n\t"
    "v_mfma_f32_16x16x32_bf16 v[100:103], a[132:135], v[156:159], v[100:103]\n\t"
    "v_exp_f32 v134, v134\n\t"
    "v_mfma_f32_16x16x32_bf16 v[104:107], a[168:171], v[172:175], v[104:107]\n\t"
    "buffer_load_dwordx4 a[56:59], v254, s[28:31], s43 offen offset:3072 sc1\n\t"
    "v_mfma_f32_16x16x32_bf16 v[104:107], a[136:139], v[156:159], v[104:107]\n\t"
    "v_exp_f32 v135, v135\n\t"
    "v_mfma_f32_16x16x32_bf16 v[108:111], a[172:175], v[172:175], v[108:111]\n\t"
    "v_cvt_pk_bf16_f32 v163, v134, v135\n\t"
    "v_mfma_f32_16x16x32_bf16 v[108:111], a[140:143], v[156:159], v[108:111]\n\t"
    "s_add_u32 s43, s61, 57344\n\t"
    "buffer_load_dwordx4 a[60:63], v254, s[28:31], s43 offen offset:0 sc1\n\t"
    "v_mfma_f32_16x16x32_bf16 v[128:131], a[96:99], a[32:35], v[200:203]\n\t"
    "v_exp_f32 v136, v136\n\t"
    "v_mfma_f32_16x16x32_bf16 v[128:131], a[100:103], a[36:39], v[128:131]\n\t"
    "v_mfma_f32_16x16x32_bf16 v[128:131], a[104:107], a[40:43], v[128:131]\n\t"
    "v_exp_f32 v137, v137\n\t"
    "v_mfma_f32_16x16x32_bf16 v[128:131], a[108:111], a[44:47], v[128:131]\n\t"
    "ds_read_b64_tr_b16 a[160:161], v228 offset:57344\n\t"
    "v_cvt_pk_bf16_f32 v164, v136, v137\n\t"
    "v_mfma_f32_16x16x32_bf16 v[132:135], a[124:127], a[44:47], v[200:203]\n\t"
    "v_exp_f32 v138, v138\n\t"
    "v_mfma_f32_16x16x32_bf16 v[132:135], a[120:123], a[40:43], v[132:135]\n\t"
    "buffer_load_dwordx4 a[64:67], v254, s[28:31], s43 offen offset:1024 sc1\n\t"
    "v_mfma_f32_16x16x32_bf16 v[132:135], a[116:119], a[36:39], v[132:135]\n\t"
    "v_exp_f32 v139, v139\n\t"
    "v_mfma_f32_16x16x32_bf16 v[132:135], a[112:115], a[32:35], v[132:135]\n\t"
    "ds_read_b64_tr_b16 a[162:163], v228 offset:61440\n\t"
    "v_cvt_pk_bf16_f32 v165, v138, v139\n\t"
    "v_mfma_f32_16x16x32_bf16 v[216:219], a[192:195], v[152:155], v[216:219]\n\t"
    "v_exp_f32 v140, v140\n\t"
    "v_mfma_f32_16x16x32_bf16 v[80:83], a[176:179], v[168:171], v[80:83]\n\t"
    "s_waitcnt lgkmcnt(6)\n\t"
    "buffer_load_dwordx4 a[68:71], v254, s[28:31], s43 offen offset:2048 sc1\n\t"
    "v_mfma_f32_16x16x32_bf16 v[80:83], a[144:147], v[152:155], v[80:83]\n\t"
    "v_exp_f32 v141, v141\n\t"
    "v_mfma_f32_16x16x32_bf16 v[84:87], a[180:183], v[168:171], v[84:87]\n\t"
    "ds_read_b64_tr_b16 a[164:165], v229 offset:57344\n\t"
    "v_cvt_pk_bf16_f32 v166, v140, v141\n\t"
    "v_mfma_f32_16x16x32_bf16 v[84:87], a[148:151], v[152:155], v[84:87]\n\t"
    "v_exp_f32 v142, v142\n\t"
    "v_mfma_f32_16x16x32_bf16 v[88:91], a[184:187], v[168:171], v[88:91]\n\t"
    "s_waitcnt lgkmcnt(3)\n\t"
    "buffer_load_dwordx4 a[72:75], v254, s[28:31], s43 offen offset:3072 sc1\n\t"
    "v_mfma_f32_16x16x32_bf16 v[88:91], a[152:155], v[152:155], v[88:91]\n\t"
    "v_exp_f32 v143, v143\n\t"
    "v_mfma_f32_16x16x32_bf16 v[92:95], a[188:191], v[168:171], v[92:95]\n\t"
    "ds_read_b64_tr_b16 a[166:167], v229 offset:61440\n\t"
    "v_cvt_pk_bf16_f32 v167, v142, v143\n\t"
    "v_mfma_f32_16x16x32_bf16 v[92:95], a[156:159], v[152:155], v[92:95]\n\t"
    "s_add_u32 s43, s61, 61440\n\t"
    "buffer_load_dwordx4 a[76:79], v254, s[28:31], s43 offen offset:0 sc1\n\t"
    "v_mfma_f32_16x16x32_bf16 v[136:139], a[96:99], v[176:179], v[204:207]\n\t"
    "v_exp_f32 v128, v128\n\t"
    "v_mfma_f32_16x16x32_bf16 v[136:139], a[100:103], v[180:183], v[136:139]\n\t"
    "ds_read_b64_tr_b16 a[168:169], v230 offset:57344\n\t"
    "v_mfma_f32_16x16x32_bf16 v[136:139], a[104:107], v[184:187], v[136:139]\n\t"
    "v_exp_f32 v129, v129\n\t"
    "v_mfma_f32_16x16x32_bf16 v[136:139], a[108:111], v[188:191], v[136:139]\n\t"
    "ds_read_b64_tr_b16 a[170:171], v230 offset:61440\n\t"
    "v_cvt_pk_bf16_f32 v168, v128, v129\n\t"
    "v_mfma_f32_16x16x32_bf16 v[140:143], a[124:127], v[188:191], v[204:207]\n\t"
    "v_exp_f32 v130, v130\n\t"
    "v_mfma_f32_16x16x32_bf16 v[140:143], a[120:123], v[184:187], v[140:143]\n\t"
    "buffer_load_dwordx4 a[80:83], v254, s[28:31], s43 offen offset:1024 sc1\n\t"
    "v_mfma_f32_16x16x32_bf16 v[140:143], a[116:119], v[180:183], v[140:143]\n\t"
    "v_exp_f32 v131, v131\n\t"
    "v_mfma_f32_16x16x32_bf16 v[140:143], a[112:115], v[176:179], v[140:143]\n\t"
    "ds_read_b64_tr_b16 a[172:173], v231 offset:57344\n\t"
    "v_cvt_pk_bf16_f32 v169, v130, v131\n\t"
    "v_mfma_f32_16x16x32_bf16 v[220:223], a[192:195], v[156:159], v[220:223]\n\t"
    "v_exp_f32 v132, v132\n\t"
    "v_mfma_f32_16x16x32_bf16 v[112:115], a[176:179], v[172:175], v[112:115]\n\t"
    "buffer_load_dwordx4 a[84:87], v254, s[28:31], s43 offen offset:2048 sc1\n\t"
    "v_mfma_f32_16x16x32_bf16 v[112:115], a[144:147], v[156:159], v[112:115]\n\t"
    "v_exp_f32 v133, v133\n\t"
    "v_mfma_f32_16x16x32_bf16 v[116:119], a[180:183], v[172:175], v[116:119]\n\t"
    "ds_read_b64_tr_b16 a[174:175], v231 offset:61440\n\t"
    "v_cvt_pk_bf16_f32 v170, v132, v133\n\t"
    "v_mfma_f32_16x16x32_bf16 v[116:119], a[148:151], v[156:159], v[116:119]\n\t"
    "v_exp_f32 v134, v134\n\t"
    "v_mfma_f32_16x16x32_bf16 v[120:123], a[184:187], v[172:175], v[120:123]\n\t"
    "buffer_load_dwordx4 a[88:91], v254, s[28:31], s43 offen offset:3072 sc1\n\t"
    "v_mfma_f32_16x16x32_bf16 v[120:123], a[152:155], v[156:159], v[120:123]\n\t"
    "v_exp_f32 v135, v135\n\t"
    "v_mfma_f32_16x16x32_bf16 v[124:127], a[188:191], v[172:175], v[124:127]\n\t"
    "v_cvt_pk_bf16_f32 v171, v134, v135\n\t"
    "v_mfma_f32_16x16x32_bf16 v[124:127], a[156:159], v[156:159], v[124:127]\n\t"
    "s_add_u32 s43, s61, 65536\n\t"
    "buffer_load_dwordx4 a[92:95], v254, s[28:31], s43 offen offset:0 sc1\n\t"
    "v_mfma_f32_16x16x32_bf16 v[208:211], a[192:195], v[144:147], v[208:211]\n\t"
    "v_exp_f32 v136, v136\n\t"
    "v_mfma_f32_16x16x32_bf16 v[208:211], a[192:195], v[160:163], v[208:211]\n\t"
    "ds_read_b64_tr_b16 a[176:177], v232 offset:57344\n\t"
    "ds_read_b64_tr_b16 a[178:179], v232 offset:61440\n\t"
    "v_mfma_f32_16x16x32_bf16 v[0:3], a[128:131], v[144:147], v[0:3]\n\t"
    "s_waitcnt lgkmcnt(6)\n\t"
    "v_exp_f32 v137, v137\n\t"
    "v_mfma_f32_16x16x32_bf16 v[0:3], a[160:163], v[160:163], v[0:3]\n\t"
    "ds_read_b64_tr_b16 a[180:181], v233 offset:57344\n\t"
    "ds_read_b64_tr_b16 a[182:183], v233 offset:61440\n\t"
    "v_mfma_f32_16x16x32_bf16 v[4:7], a[132:135], v[144:147], v[4:7]\n\t"
    "v_exp_f32 v138, v138\n\t"
    "v_mfma_f32_16x16x32_bf16 v[4:7], a[164:167], v[160:163], v[4:7]\n\t"
    "ds_read_b64_tr_b16 a[184:185], v234 offset:57344\n\t"
    "ds_read_b64_tr_b16 a[186:187], v234 offset:61440\n\t"
    "v_mfma_f32_16x16x32_bf16 v[8:11], a[136:139], v[144:147], v[8:11]\n\t"
    "s_waitcnt lgkmcnt(6)\n\t"
    "v_exp_f32 v139, v139\n\t"
    "v_mfma_f32_16x16x32_bf16 v[8:11], a[168:171], v[160:163], v[8:11]\n\t"
    "ds_read_b64_tr_b16 a[188:189], v235 offset:57344\n\t"
    "ds_read_b64_tr_b16 a[190:191], v235 offset:61440\n\t"
    "v_mfma_f32_16x16x32_bf16 v[12:15], a[140:143], v[144:147], v[12:15]\n\t"
    "v_exp_f32 v140, v140\n\t"
    "v_mfma_f32_16x16x32_bf16 v[12:15], a[172:175], v[160:163], v[12:15]\n\t"
    "v_exp_f32 v141, v141\n\t"
    "v_mfma_f32_16x16x32_bf16 v[212:215], a[192:195], v[148:151], v[212:215]\n\t"
    "v_exp_f32 v142, v142\n\t"
    "v_mfma_f32_16x16x32_bf16 v[212:215], a[192:195], v[164:167], v[212:215]\n\t"
    "v_exp_f32 v143, v143\n\t"
    "v_mfma_f32_16x16x32_bf16 v[32:35], a[128:131], v[148:151], v[32:35]\n\t"
    "v_cvt_pk_bf16_f32 v172, v136, v137\n\t"
    "v_cvt_pk_bf16_f32 v173, v138, v139\n\t"
    "v_mfma_f32_16x16x32_bf16 v[32:35], a[160:163], v[164:167], v[32:35]\n\t"
    "v_cvt_pk_bf16_f32 v174, v140, v141\n\t"
    "v_cvt_pk_bf16_f32 v175, v142, v143\n\t"
    "v_mfma_f32_16x16x32_bf16 v[36:39], a[132:135], v[148:151], v[36:39]\n\t"
    "v_mov_b32 v242, 114688\n\t"
    "v_lshl_add_u32 v243, s77, 2, v242\n\t"
    "v_mfma_f32_16x16x32_bf16 v[36:39], a[164:167], v[164:167], v[36:39]\n\t"
    "s_waitcnt lgkmcnt(0)\n\t"
    "v_mov_b32 v249, 1568669696\n\t"
    "v_rcp_f32 v244, v208\n\t"
    "v_mfma_f32_16x16x32_bf16 v[40:43], a[136:139], v[148:151], v[40:43]\n\t"
    "s_barrier\n\t"
    "s_nop 0\n\t"
    "v_mul_f32 v0, v0, v244\n\t"
    "v_mfma_f32_16x16x32_bf16 v[40:43], a[168:171], v[164:167], v[40:43]\n\t"
    "v_mul_f32 v1, v1, v244\n\t"
    "v_mul_f32 v2, v2, v244\n\t"
    "v_mfma_f32_16x16x32_bf16 v[44:47], a[140:143], v[148:151], v[44:47]\n\t"
    "v_mul_f32 v3, v3, v244\n\t"
    "v_mul_f32 v4, v4, v244\n\t"
    "v_mfma_f32_16x16x32_bf16 v[44:47], a[172:175], v[164:167], v[44:47]\n\t"
    "v_mul_f32 v5, v5, v244\n\t"
    "v_mul_f32 v6, v6, v244\n\t"
    "v_mfma_f32_16x16x32_bf16 v[16:19], a[144:147], v[144:147], v[16:19]\n\t"
    "v_mul_f32 v7, v7, v244\n\t"
    "v_cvt_pk_bf16_f32 v0, v0, v1\n\t"
    "v_mfma_f32_16x16x32_bf16 v[16:19], a[176:179], v[160:163], v[16:19]\n\t"
    "v_cvt_pk_bf16_f32 v1, v2, v3\n\t"
    "v_cvt_pk_bf16_f32 v2, v4, v5\n\t"
    "v_mfma_f32_16x16x32_bf16 v[20:23], a[148:151], v[144:147], v[20:23]\n\t"
    "v_cvt_pk_bf16_f32 v3, v6, v7\n\t"
    "v_mfma_f32_16x16x32_bf16 v[20:23], a[180:183], v[160:163], v[20:23]\n\t"
    "s_add_u32 s40, s39, 0\n\t"
    "buffer_store_dwordx4 v[0:3], v254, s[28:31], s40 offen offset:0 sc1\n\t"
    "v_mfma_f32_16x16x32_bf16 v[24:27], a[152:155], v[144:147], v[24:27]\n\t"
    "v_mul_f32 v8, v8, v244\n\t"
    "v_mul_f32 v9, v9, v244\n\t"
    "v_mfma_f32_16x16x32_bf16 v[24:27], a[184:187], v[160:163], v[24:27]\n\t"
    "v_mul_f32 v10, v10, v244\n\t"
    "v_mul_f32 v11, v11, v244\n\t"
    "v_mfma_f32_16x16x32_bf16 v[28:31], a[156:159], v[144:147], v[28:31]\n\t"
    "v_mul_f32 v12, v12, v244\n\t"
    "v_mul_f32 v13, v13, v244\n\t"
    "v_mfma_f32_16x16x32_bf16 v[28:31], a[188:191], v[160:163], v[28:31]\n\t"
    "v_mul_f32 v14, v14, v244\n\t"
    "v_mul_f32 v15, v15, v244\n\t"
    "v_mfma_f32_16x16x32_bf16 v[48:51], a[144:147], v[148:151], v[48:51]\n\t"
    "v_cvt_pk_bf16_f32 v8, v8, v9\n\t"
    "v_cvt_pk_bf16_f32 v9, v10, v11\n\t"
    "v_mfma_f32_16x16x32_bf16 v[48:51], a[176:179], v[164:167], v[48:51]\n\t"
    "v_cvt_pk_bf16_f32 v10, v12, v13\n\t"
    "v_cvt_pk_bf16_f32 v11, v14, v15\n\t"
    "v_mfma_f32_16x16x32_bf16 v[52:55], a[148:151], v[148:151], v[52:55]\n\t"
    "s_add_u32 s40, s39, 0\n\t"
    "buffer_store_dwordx4 v[8:11], v254, s[28:31], s40 offen offset:1024 sc1\n\t"
    "v_mfma_f32_16x16x32_bf16 v[52:55], a[180:183], v[164:167], v[52:55]\n\t"
    "v_rcp_f32 v246, v212\n\t"
    "v_mfma_f32_16x16x32_bf16 v[56:59], a[152:155], v[148:151], v[56:59]\n\t"
    "s_nop 0\n\t"
    "v_mul_f32 v32, v32, v246\n\t"
    "v_mfma_f32_16x16x32_bf16 v[56:59], a[184:187], v[164:167], v[56:59]\n\t"
    "v_mul_f32 v33, v33, v246\n\t"
    "v_mul_f32 v34, v34, v246\n\t"
    "v_mfma_f32_16x16x32_bf16 v[60:63], a[156:159], v[148:151], v[60:63]\n\t"
    "v_mul_f32 v35, v35, v246\n\t"
    "v_mul_f32 v36, v36, v246\n\t"
    "v_mfma_f32_16x16x32_bf16 v[60:63], a[188:191], v[164:167], v[60:63]\n\t"
    "v_mul_f32 v37, v37, v246\n\t"
    "v_mul_f32 v38, v38, v246\n\t"
    "v_mfma_f32_16x16x32_bf16 v[216:219], a[192:195], v[168:171], v[216:219]\n\t"
    "v_mul_f32 v39, v39, v246\n\t"
    "v_cvt_pk_bf16_f32 v32, v32, v33\n\t"
    "v_mfma_f32_16x16x32_bf16 v[64:67], a[160:163], v[168:171], v[64:67]\n\t"
    "v_cvt_pk_bf16_f32 v33, v34, v35\n\t"
    "v_cvt_pk_bf16_f32 v34, v36, v37\n\t"
    "v_mfma_f32_16x16x32_bf16 v[68:71], a[164:167], v[168:171], v[68:71]\n\t"
    "v_cvt_pk_bf16_f32 v35, v38, v39\n\t"
    "v_mfma_f32_16x16x32_bf16 v[72:75], a[168:171], v[168:171], v[72:75]\n\t"
    "s_add_u32 s40, s39, 2048\n\t"
    "buffer_store_dwordx4 v[32:35], v254, s[28:31], s40 offen offset:0 sc1\n\t"
    "v_mfma_f32_16x16x32_bf16 v[76:79], a[172:175], v[168:171], v[76:79]\n\t"
    "v_mul_f32 v40, v40, v246\n\t"
    "v_mul_f32 v41, v41, v246\n\t"
    "v_mfma_f32_16x16x32_bf16 v[80:83], a[176:179], v[168:171], v[80:83]\n\t"
    "v_mul_f32 v42, v42, v246\n\t"
    "v_mul_f32 v43, v43, v246\n\t"
    "v_mfma_f32_16x16x32_bf16 v[84:87], a[180:183], v[168:171], v[84:87]\n\t"
    "v_mul_f32 v44, v44, v246\n\t"
    "v_mul_f32 v45, v45, v246\n\t"
    "v_mfma_f32_16x16x32_bf16 v[88:91], a[184:187], v[168:171], v[88:91]\n\t"
    "v_mul_f32 v46, v46, v246\n\t"
    "v_mul_f32 v47, v47, v246\n\t"
    "v_mfma_f32_16x16x32_bf16 v[92:95], a[188:191], v[168:171], v[92:95]\n\t"
    "v_cvt_pk_bf16_f32 v40, v40, v41\n\t"
    "v_cvt_pk_bf16_f32 v41, v42, v43\n\t"
    "v_mfma_f32_16x16x32_bf16 v[220:223], a[192:195], v[172:175], v[220:223]\n\t"
    "v_cvt_pk_bf16_f32 v42, v44, v45\n\t"
    "v_cvt_pk_bf16_f32 v43, v46, v47\n\t"
    "v_mfma_f32_16x16x32_bf16 v[96:99], a[160:163], v[172:175], v[96:99]\n\t"
    "s_add_u32 s40, s39, 2048\n\t"
    "buffer_store_dwordx4 v[40:43], v254, s[28:31], s40 offen offset:1024 sc1\n\t"
    "v_mfma_f32_16x16x32_bf16 v[100:103], a[164:167], v[172:175], v[100:103]\n\t"
    "v_mul_f32 v16, v16, v244\n\t"
    "v_mul_f32 v17, v17, v244\n\t"
    "v_mfma_f32_16x16x32_bf16 v[104:107], a[168:171], v[172:175], v[104:107]\n\t"
    "v_mul_f32 v18, v18, v244\n\t"
    "v_mul_f32 v19, v19, v244\n\t"
    "v_mfma_f32_16x16x32_bf16 v[108:111], a[172:175], v[172:175], v[108:111]\n\t"
    "v_mul_f32 v20, v20, v244\n\t"
    "v_mul_f32 v21, v21, v244\n\t"
    "v_mfma_f32_16x16x32_bf16 v[112:115], a[176:179], v[172:175], v[112:115]\n\t"
    "v_mul_f32 v22, v22, v244\n\t"
    "v_mul_f32 v23, v23, v244\n\t"
    "v_mfma_f32_16x16x32_bf16 v[116:119], a[180:183], v[172:175], v[116:119]\n\t"
    "v_cvt_pk_bf16_f32 v16, v16, v17\n\t"
    "v_cvt_pk_bf16_f32 v17, v18, v19\n\t"
    "v_mfma_f32_16x16x32_bf16 v[120:123], a[184:187], v[172:175], v[120:123]\n\t"
    "v_cvt_pk_bf16_f32 v18, v20, v21\n\t"
    "v_cvt_pk_bf16_f32 v19, v22, v23\n\t"
    "v_mfma_f32_16x16x32_bf16 v[124:127], a[188:191], v[172:175], v[124:127]\n\t"
    "s_add_u32 s40, s39, 8192\n\t"
    "buffer_store_dwordx4 v[16:19], v254, s[28:31], s40 offen offset:0 sc1\n\t"
    "v_max3_f32 v248, v208, v212, v216\n\t"
    "v_max_f32 v248, v248, v220\n\t"
    "v_cmp_nle_f32 vcc, v248, v249\n\t"
    "v_pk_mul_f32 v[24:25], v[24:25], v[244:245] op_sel_hi:[1,0]\n\t"
    "v_pk_mul_f32 v[26:27], v[26:27], v[244:245] op_sel_hi:[1,0]\n\t"
    "v_pk_mul_f32 v[28:29], v[28:29], v[244:245] op_sel_hi:[1,0]\n\t"
    "v_pk_mul_f32 v[30:31], v[30:31], v[244:245] op_sel_hi:[1,0]\n\t"
    "v_cvt_pk_bf16_f32 v24, v24, v25\n\t"
    "v_cvt_pk_bf16_f32 v25, v26, v27\n\t"
    "v_cvt_pk_bf16_f32 v26, v28, v29\n\t"
    "v_cvt_pk_bf16_f32 v27, v30, v31\n\t"
    "s_add_u32 s40, s39, 8192\n\t"
    "buffer_store_dwordx4 v[24:27], v254, s[28:31], s40 offen offset:1024 sc1\n\t"
    "s_cmp_lg_u64 vcc, 0\n\t"
    "s_cselect_b32 s99, 1, 0\n\t"
    "v_mov_b32 v248, s99\n\t"
    "ds_write_b32 v243, v248\n\t"
    "v_pk_mul_f32 v[48:49], v[48:49], v[246:247] op_sel_hi:[1,0]\n\t"
    "v_pk_mul_f32 v[50:51], v[50:51], v[246:247] op_sel_hi:[1,0]\n\t"
    "v_pk_mul_f32 v[52:53], v[52:53], v[246:247] op_sel_hi:[1,0]\n\t"
    "v_pk_mul_f32 v[54:55], v[54:55], v[246:247] op_sel_hi:[1,0]\n\t"
    "v_cvt_pk_bf16_f32 v48, v48, v49\n\t"
    "v_cvt_pk_bf16_f32 v49, v50, v51\n\t"
    "v_cvt_pk_bf16_f32 v50, v52, v53\n\t"
    "v_cvt_pk_bf16_f32 v51, v54, v55\n\t"
    "s_add_u32 s40, s39, 10240\n\t"
    "buffer_store_dwordx4 v[48:51], v254, s[28:31], s40 offen offset:0 sc1\n\t"
    "s_waitcnt lgkmcnt(0)\n\t"
    "s_barrier\n\t"
    "ds_read_b128 v[248:251], v242\n\t"
    "v_pk_mul_f32 v[56:57], v[56:57], v[246:247] op_sel_hi:[1,0]\n\t"
    "v_pk_mul_f32 v[58:59], v[58:59], v[246:247] op_sel_hi:[1,0]\n\t"
    "v_pk_mul_f32 v[60:61], v[60:61], v[246:247] op_sel_hi:[1,0]\n\t"
    "v_pk_mul_f32 v[62:63], v[62:63], v[246:247] op_sel_hi:[1,0]\n\t"
    "v_cvt_pk_bf16_f32 v56, v56, v57\n\t"
    "v_cvt_pk_bf16_f32 v57, v58, v59\n\t"
    "v_cvt_pk_bf16_f32 v58, v60, v61\n\t"
    "v_cvt_pk_bf16_f32 v59, v62, v63\n\t"
    "s_add_u32 s40, s39, 10240\n\t"
    "buffer_store_dwordx4 v[56:59], v254, s[28:31], s40 offen offset:1024 sc1\n\t"
    "s_waitcnt lgkmcnt(0)\n\t"
    "v_or3_b32 v248, v248, v249, v250\n\t"
    "v_or_b32 v248, v248, v251\n\t"
    "s_nop 0\n\t"
    "v_readfirstlane_b32 s99, v248\n\t"
    "v_rcp_f32 v244, v216\n\t"
    "s_nop 0\n\t"
    "v_pk_mul_f32 v[64:65], v[64:65], v[244:245] op_sel_hi:[1,0]\n\t"
    "v_pk_mul_f32 v[66:67], v[66:67], v[244:245] op_sel_hi:[1,0]\n\t"
    "v_pk_mul_f32 v[68:69], v[68:69], v[244:245] op_sel_hi:[1,0]\n\t"
    "v_pk_mul_f32 v[70:71], v[70:71], v[244:245] op_sel_hi:[1,0]\n\t"
    "v_cvt_pk_bf16_f32 v64, v64, v65\n\t"
    "v_cvt_pk_bf16_f32 v65, v66, v67\n\t"
    "v_cvt_pk_bf16_f32 v66, v68, v69\n\t"
    "v_cvt_pk_bf16_f32 v67, v70, v71\n\t"
    "s_add_u32 s40, s39, 4096\n\t"
    "buffer_store_dwordx4 v[64:67], v254, s[28:31], s40 offen offset:0 sc1\n\t"
    "s_andn2_b32 s99, s99, s41\n\t"
    "s_cbranch_scc1 L_fb%=\n\t"
    "L_epi%=:\n\t"
    "v_pk_mul_f32 v[72:73], v[72:73], v[244:245] op_sel_hi:[1,0]\n\t"
    "v_pk_mul_f32 v[74:75], v[74:75], v[244:245] op_sel_hi:[1,0]\n\t"
    "v_pk_mul_f32 v[76:77], v[76:77], v[244:245] op_sel_hi:[1,0]\n\t"
    "v_pk_mul_f32 v[78:79], v[78:79], v[244:245] op_sel_hi:[1,0]\n\t"
    "v_cvt_pk_bf16_f32 v72, v72, v73\n\t"
    "v_cvt_pk_bf16_f32 v73, v74, v75\n\t"
    "v_cvt_pk_bf16_f32 v74, v76, v77\n\t"
    "v_cvt_pk_bf16_f32 v75, v78, v79\n\t"
    "s_add_u32 s40, s39, 4096\n\t"
    "buffer_store_dwordx4 v[72:75], v254, s[28:31], s40 offen offset:1024 sc1\n\t"
    "v_pk_mul_f32 v[80:81], v[80:81], v[244:245] op_sel_hi:[1,0]\n\t"
    "v_pk_mul_f32 v[82:83], v[82:83], v[244:245] op_sel_hi:[1,0]\n\t"
    "v_pk_mul_f32 v[84:85], v[84:85], v[244:245] op_sel_hi:[1,0]\n\t"
    "v_pk_mul_f32 v[86:87], v[86:87], v[244:245] op_sel_hi:[1,0]\n\t"
    "v_cvt_pk_bf16_f32 v80, v80, v81\n\t"
    "v_cvt_pk_bf16_f32 v81, v82, v83\n\t"
    "v_cvt_pk_bf16_f32 v82, v84, v85\n\t"
    "v_cvt_pk_bf16_f32 v83, v86, v87\n\t"
    "s_add_u32 s40, s39, 12288\n\t"
    "buffer_store_dwordx4 v[80:83], v254, s[28:31], s40 offen offset:0 sc1\n\t"
    "v_pk_mul_f32 v[88:89], v[88:89], v[244:245] op_sel_hi:[1,0]\n\t"
    "v_pk_mul_f32 v[90:91], v[90:91], v[244:245] op_sel_hi:[1,0]\n\t"
    "v_pk_mul_f32 v[92:93], v[92:93], v[244:245] op_sel_hi:[1,0]\n\t"
    "v_pk_mul_f32 v[94:95], v[94:95], v[244:245] op_sel_hi:[1,0]\n\t"
    "v_cvt_pk_bf16_f32 v88, v88, v89\n\t"
    "v_cvt_pk_bf16_f32 v89, v90, v91\n\t"
    "v_cvt_pk_bf16_f32 v90, v92, v93\n\t"
    "v_cvt_pk_bf16_f32 v91, v94, v95\n\t"
    "s_add_u32 s40, s39, 12288\n\t"
    "buffer_store_dwordx4 v[88:91], v254, s[28:31], s40 offen offset:1024 sc1\n\t"
    "v_rcp_f32 v246, v220\n\t"
    "s_nop 0\n\t"
    "v_pk_mul_f32 v[96:97], v[96:97], v[246:247] op_sel_hi:[1,0]\n\t"
    "v_pk_mul_f32 v[98:99], v[98:99], v[246:247] op_sel_hi:[1,0]\n\t"
    "v_pk_mul_f32 v[100:101], v[100:101], v[246:247] op_sel_hi:[1,0]\n\t"
    "v_pk_mul_f32 v[102:103], v[102:103], v[246:247] op_sel_hi:[1,0]\n\t"
    "v_cvt_pk_bf16_f32 v96, v96, v97\n\t"
    "v_cvt_pk_bf16_f32 v97, v98, v99\n\t"
    "v_cvt_pk_bf16_f32 v98, v100, v101\n\t"
    "v_cvt_pk_bf16_f32 v99, v102, v103\n\t"
    "s_add_u32 s40, s39, 6144\n\t"
    "buffer_store_dwordx4 v[96:99], v254, s[28:31], s40 offen offset:0 sc1\n\t"
    "v_pk_mul_f32 v[104:105], v[104:105], v[246:247] op_sel_hi:[1,0]\n\t"
    "v_pk_mul_f32 v[106:107], v[106:107], v[246:247] op_sel_hi:[1,0]\n\t"
    "v_pk_mul_f32 v[108:109], v[108:109], v[246:247] op_sel_hi:[1,0]\n\t"
    "v_pk_mul_f32 v[110:111], v[110:111], v[246:247] op_sel_hi:[1,0]\n\t"
    "v_cvt_pk_bf16_f32 v104, v104, v105\n\t"
    "v_cvt_pk_bf16_f32 v105, v106, v107\n\t"
    "v_cvt_pk_bf16_f32 v106, v108, v109\n\t"
    "v_cvt_pk_bf16_f32 v107, v110, v111\n\t"
    "s_add_u32 s40, s39, 6144\n\t"
    "buffer_store_dwordx4 v[104:107], v254, s[28:31], s40 offen offset:1024 sc1\n\t"
    "v_pk_mul_f32 v[112:113], v[112:113], v[246:247] op_sel_hi:[1,0]\n\t"
    "v_pk_mul_f32 v[114:115], v[114:115], v[246:247] op_sel_hi:[1,0]\n\t"
    "v_pk_mul_f32 v[116:117], v[116:117], v[246:247] op_sel_hi:[1,0]\n\t"
    "v_pk_mul_f32 v[118:119], v[118:119], v[246:247] op_sel_hi:[1,0]\n\t"
    "v_cvt_pk_bf16_f32 v112, v112, v113\n\t"
    "v_cvt_pk_bf16_f32 v113, v114, v115\n\t"
    "v_cvt_pk_bf16_f32 v114, v116, v117\n\t"
    "v_cvt_pk_bf16_f32 v115, v118, v119\n\t"
    "s_add_u32 s40, s39, 14336\n\t"
    "buffer_store_dwordx4 v[112:115], v254, s[28:31], s40 offen offset:0 sc1\n\t"
    "v_pk_mul_f32 v[120:121], v[120:121], v[246:247] op_sel_hi:[1,0]\n\t"
    "v_pk_mul_f32 v[122:123], v[122:123], v[246:247] op_sel_hi:[1,0]\n\t"
    "v_pk_mul_f32 v[124:125], v[124:125], v[246:247] op_sel_hi:[1,0]\n\t"
    "v_pk_mul_f32 v[126:127], v[126:127], v[246:247] op_sel_hi:[1,0]\n\t"
    "v_cvt_pk_bf16_f32 v120, v120, v121\n\t"
    "v_cvt_pk_bf16_f32 v121, v122, v123\n\t"
    "v_cvt_pk_bf16_f32 v122, v124, v125\n\t"
    "v_cvt_pk_bf16_f32 v123, v126, v127\n\t"
    "s_add_u32 s40, s39, 14336\n\t"
    "buffer_store_dwordx4 v[120:123], v254, s[28:31], s40 offen offset:1024 sc1\n\t"
    "s_add_u32 s43, s61, 36864\n\t"
    "buffer_load_dwordx4 v[176:179], v254, s[28:31], s43 offen offset:0 sc1\n\t"
    "buffer_load_dwordx4 v[180:183], v254, s[28:31], s43 offen offset:1024 sc1\n\t"
    "buffer_load_dwordx4 v[184:187], v254, s[28:31], s43 offen offset:2048 sc1\n\t"
    "buffer_load_dwordx4 v[188:191], v254, s[28:31], s43 offen offset:3072 sc1\n\t"
    "s_add_u32 s43, s61, 40960\n\t"
    "buffer_load_dwordx4 a[144:147], v254, s[28:31], s43 offen offset:0 sc1\n\t"
    "buffer_load_dwordx4 a[148:151], v254, s[28:31], s43 offen offset:1024 sc1\n\t"
    "buffer_load_dwordx4 a[152:155], v254, s[28:31], s43 offen offset:2048 sc1\n\t"
    "buffer_load_dwordx4 a[156:159], v254, s[28:31], s43 offen offset:3072 sc1\n\t"
    "s_add_u32 s43, s61, 45056\n\t"
    "buffer_load_dwordx4 a[160:163], v254, s[28:31], s43 offen offset:0 sc1\n\t"
    "buffer_load_dwordx4 a[164:167], v254, s[28:31], s43 offen offset:1024 sc1\n\t"
    "v_add_u32 v4, s37, v254\n\t"
    "v_add_u32 v5, 65536, v4\n\t"
    "v_add_u32 v6, 131072, v4\n\t"
    "v_add_u32 v6, s37, v6\n\t"
    "v_lshlrev_b32 v78, 7, v255\n\t"
    "v_mov_b32 v79, 1\n\t"
    "v_add_u32 v20, 0, v255\n\t"
    "v_mul_u32_u24 v21, 205, v20\n\t"
    "v_lshrrev_b32 v21, 11, v21\n\t"
    "v_mul_u32_u24 v21, 1120, v21\n\t"
    "v_add_u32 v28, v21, v254\n\t"
    "v_add_u32 v20, 64, v255\n\t"
    "v_mul_u32_u24 v21, 205, v20\n\t"
    "v_lshrrev_b32 v21, 11, v21\n\t"
    "v_mul_u32_u24 v21, 1120, v21\n\t"
    "v_add_u32 v29, v21, v254\n\t"
    "v_add_u32 v20, 128, v255\n\t"
    "v_mul_u32_u24 v21, 205, v20\n\t"
    "v_lshrrev_b32 v21, 11, v21\n\t"
    "v_mul_u32_u24 v21, 1120, v21\n\t"
    "v_add_u32 v30, v21, v254\n\t"
    "v_cmp_le_u32 vcc, 20, v255\n\t"
    "v_mov_b32 v20, 4800\n\t"
    "s_nop 0\n\t"
    "v_cndmask_b32 v20, 0, v20, vcc\n\t"
    "v_add_u32 v31, v20, v254\n\t"
    "s_waitcnt vmcnt(39)\n\t"
    "ds_read_b128 a[0:3], v4 offset:32768\n\t"
    "ds_read_b128 a[4:7], v4 offset:33792\n\t"
    "ds_read_b128 a[8:11], v4 offset:34816\n\t"
    "ds_read_b128 a[12:15], v4 offset:35840\n\t"
    "ds_read_b128 a[16:19], v6 offset:0\n\t"
    "ds_read_b128 a[20:23], v6 offset:1024\n\t"
    "ds_read_b128 a[24:27], v6 offset:2048\n\t"
    "ds_read_b128 a[28:31], v6 offset:3072\n\t"
    "ds_read_b128 a[32:35], v6 offset:4096\n\t"
    "ds_read_b128 a[36:39], v6 offset:5120\n\t"
    "ds_read_b128 a[40:43], v6 offset:6144\n\t"
    "ds_read_b128 a[44:47], v6 offset:7168\n\t"
    "ds_read_b128 a[96:99], v4 offset:49152\n\t"
    "ds_read_b128 a[100:103], v4 offset:50176\n\t"
    "ds_read_b128 a[104:107], v4 offset:51200\n\t"
    "ds_read_b128 a[108:111], v4 offset:52224\n\t"
    "ds_read_b128 a[112:115], v5 offset:0\n\t"
    "ds_read_b128 a[116:119], v5 offset:1024\n\t"
    "ds_read_b128 a[120:123], v5 offset:2048\n\t"
    "ds_read_b128 a[124:127], v5 offset:3072\n\t"
    "ds_read_b128 a[128:131], v4 offset:0\n\t"
    "ds_read_b128 a[132:135], v4 offset:1024\n\t"
    "ds_read_b128 a[136:139], v4 offset:2048\n\t"
    "ds_read_b128 a[140:143], v4 offset:3072\n\t"
    "ds_read_b128 v[128:131], v5 offset:16384\n\t"
    "ds_read_b128 v[132:135], v5 offset:17408\n\t"
    "ds_read_b128 v[136:139], v5 offset:18432\n\t"
    "ds_read_b128 v[140:143], v5 offset:19456\n\t"
    "ds_read_b128 v[144:147], v4 offset:16384\n\t"
    "ds_read_b128 v[148:151], v4 offset:17408\n\t"
    "ds_read_b128 v[152:155], v4 offset:18432\n\t"
    "ds_read_b128 v[156:159], v4 offset:19456\n\t"
    "ds_read_b128 v[160:163], v5 offset:32768\n\t"
    "ds_read_b128 v[164:167], v5 offset:33792\n\t"
    "ds_read_b128 v[168:171], v5 offset:34816\n\t"
    "ds_read_b128 v[172:175], v5 offset:35840\n\t"
    "s_add_u32 s16, s18, s67\n\t"
    "s_addc_u32 s17, s19, 0\n\t"
    "s_waitcnt vmcnt(10) lgkmcnt(0)\n\t"
    "s_mov_b64 exec, 3\n\t"
    "global_atomic_add v78, v79, s[16:17]\n\t"
    "s_mov_b64 exec, -1\n\t"
    "s_barrier\n\t"
    "buffer_load_dwordx4 a[168:171], v254, s[28:31], s43 offen offset:2048 sc1\n\t"
    "buffer_load_dwordx4 a[172:175], v254, s[28:31], s43 offen offset:3072 sc1\n\t"
    "s_add_u32 s43, s61, 49152\n\t"
    "buffer_load_dwordx4 a[176:179], v254, s[28:31], s43 offen offset:0 sc1\n\t"
    "buffer_load_dwordx4 a[180:183], v254, s[28:31], s43 offen offset:1024 sc1\n\t"
    "buffer_load_dwordx4 a[184:187], v254, s[28:31], s43 offen offset:2048 sc1\n\t"
    "buffer_load_dwordx4 a[188:191], v254, s[28:31], s43 offen offset:3072 sc1\n\t"
    "s_add_u32 s43, s61, 53248\n\t"
    "buffer_load_dwordx4 a[192:195], v254, s[28:31], s43 offen offset:0 sc1\n\t"
    "s_add_u32 m0, s37, 81920\n\t"
    "s_add_u32 s43, s72, 66560\n\t"
    "buffer_load_dwordx4 v28, s[52:55], s71 offen offset:0 lds\n\t"
    "buffer_load_dwordx4 v29, s[52:55], s71 offen offset:1024 lds\n\t"
    "buffer_load_dwordx4 v30, s[52:55], s71 offen offset:2048 lds\n\t"
    "buffer_load_dwordx4 v31, s[28:31], s43 offen offset:3072 sc1 lds\n\t"
    "s_add_u32 s43, s77, 0\n\t"
    "s_and_b32 s43, s43, 3\n\t"
    "v_lshl_add_u32 v240, s43, 11, v254\n\t"
    "s_add_u32 s43, s77, 1\n\t"
    "s_and_b32 s43, s43, 3\n\t"
    "v_lshl_add_u32 v241, s43, 11, v254\n\t"
    "s_add_u32 s43, s77, 2\n\t"
    "s_and_b32 s43, s43, 3\n\t"
    "v_lshl_add_u32 v242, s43, 11, v254\n\t"
    "s_add_u32 s43, s77, 3\n\t"
    "s_and_b32 s43, s43, 3\n\t"
    "v_lshl_add_u32 v243, s43, 11, v254\n\t"
    "v_and_b32 v12, 1, v255\n\t"
    "v_lshlrev_b32 v12, 11, v12\n\t"
    "s_xor_b32 s1, s95, 0x40000\n\t"
    "buffer_load_dword v13, v12, s[28:31], s94 offen sc1\n\t"
    "s_add_u32 m0, s37, 0\n\t"
    "s_add_u32 s97, s63, 0\n\t"
    "buffer_load_dwordx4 v254, s[28:31], s97 offen offset:0 sc1 lds\n\t"
    "buffer_load_dwordx4 v254, s[28:31], s97 offen offset:1024 sc1 lds\n\t"
    "buffer_load_dwordx4 v254, s[28:31], s97 offen offset:2048 sc1 lds\n\t"
    "buffer_load_dwordx4 v254, s[28:31], s97 offen offset:3072 sc1 lds\n\t"
    "v_and_b32 v247, 15, v255\n\t"
    "v_lshlrev_b32 v249, 3, v247\n\t"
    "v_mul_u32_u24 v247, 160, v247\n\t"
    "v_and_b32 v248, 48, v255\n\t"
    "v_lshrrev_b32 v14, 1, v248\n\t"
    "s_add_u32 s43, s37, 81920\n\t"
    "v_add3_u32 v247, v247, v14, s43\n\t"
    "v_add_u32 v248, s43, v248\n\t"
    "v_add_u32 v250, s37, v254\n\t"
    "v_add_u32 v250, 98304, v250\n\t"
    "v_lshlrev_b32 v251, 1, v243\n\t"
    "v_sub_u32 v251, v251, v254\n\t"
    "v_add_u32 v251, 98304, v251\n\t"
    "v_lshlrev_b32 v252, 1, v242\n\t"
    "v_sub_u32 v252, v252, v254\n\t"
    "v_add_u32 v252, 98304, v252\n\t"
    "v_lshlrev_b32 v253, 1, v241\n\t"
    "v_sub_u32 v253, v253, v254\n\t"
    "v_add_u32 v253, 98304, v253\n\t"
    "v_bfe_u32 v14, v255, 4, 1\n\t"
    "v_lshrrev_b32 v15, 5, v255\n\t"
    "v_lshl_add_u32 v14, v14, 1, v15\n\t"
    "s_lshr_b32 s40, s72, 5\n\t"
    "v_add_u32 v14, s40, v14\n\t"
    "v_and_b32 v15, 0xf0, v254\n\t"
    "v_add_u32 v20, 0, v14\n\t"
    "v_lshrrev_b32 v21, 3, v20\n\t"
    "v_and_b32 v20, 7, v20\n\t"
    "v_lshl_add_u32 v20, v20, 8, v15\n\t"
    "v_lshl_add_u32 v244, v21, 13, v20\n\t"
    "v_add_u32 v20, 4, v14\n\t"
    "v_lshrrev_b32 v21, 3, v20\n\t"
    "v_and_b32 v20, 7, v20\n\t"
    "v_lshl_add_u32 v20, v20, 8, v15\n\t"
    "v_lshl_add_u32 v245, v21, 13, v20\n\t"
    "v_add_u32 v20, 8, v14\n\t"
    "v_lshrrev_b32 v21, 3, v20\n\t"
    "v_and_b32 v20, 7, v20\n\t"
    "v_lshl_add_u32 v20, v20, 8, v15\n\t"
    "v_lshl_add_u32 v246, v21, 13, v20\n\t"
    "s_mov_b32 s38, 0x20000\n\t"
    "s_waitcnt vmcnt(4)\n\t"
    "L_poll1%=:\n\t"
    "v_cmp_gt_u32 vcc, 4, v13\n\t"
    "s_sub_u32 s38, s38, 1\n\t"
    "s_cbranch_scc1 L_polld1%=\n\t"
    "s_nop 2\n\t"
    "s_cbranch_vccz L_polld1%=\n\t"
    "L_pollb1%=:\n\t"
    "s_sleep 2\n\t"
    "buffer_load_dword v13, v12, s[28:31], s94 offen sc1\n\t"
    "s_waitcnt vmcnt(0)\n\t"
    "s_branch L_poll1%=\n\t"
    "L_polld1%=:\n\t"
    "s_add_u32 s43, s93, 0\n\t"
    "buffer_load_dwordx4 v[80:83], v240, s[28:31], s43 offen offset:0 sc1\n\t"
    "buffer_load_dwordx4 v[84:87], v240, s[28:31], s43 offen offset:1024 sc1\n\t"
    "buffer_load_dwordx4 v[88:91], v241, s[28:31], s43 offen offset:0 sc1\n\t"
    "buffer_load_dwordx4 v[92:95], v241, s[28:31], s43 offen offset:1024 sc1\n\t"
    "buffer_load_dwordx4 v[96:99], v242, s[28:31], s43 offen offset:0 sc1\n\t"
    "buffer_load_dwordx4 v[100:103], v242, s[28:31], s43 offen offset:1024 sc1\n\t"
    "buffer_load_dwordx4 v[104:107], v243, s[28:31], s43 offen offset:0 sc1\n\t"
    "buffer_load_dwordx4 v[108:111], v243, s[28:31], s43 offen offset:1024 sc1\n\t"
    "s_add_u32 s43, s93, 8192\n\t"
    "buffer_load_dwordx4 v[112:115], v240, s[28:31], s43 offen offset:0 sc1\n\t"
    "buffer_load_dwordx4 v[116:119], v240, s[28:31], s43 offen offset:1024 sc1\n\t"
    "buffer_load_dwordx4 v[120:123], v241, s[28:31], s43 offen offset:0 sc1\n\t"
    "buffer_load_dwordx4 v[124:127], v241, s[28:31], s43 offen offset:1024 sc1\n\t"
    "s_waitcnt vmcnt(10)\n\t"
    "v_mfma_f32_16x16x32_bf16 v[220:223], a[0:3], v[80:83], 0\n\t"
    "v_mfma_f32_16x16x32_bf16 v[220:223], a[20:23], v[84:87], v[220:223]\n\t"
    "buffer_load_dwordx4 v[192:195], v242, s[28:31], s43 offen offset:0 sc1\n\t"
    "v_mfma_f32_16x16x32_bf16 v[224:227], a[4:7], v[80:83], 0\n\t"
    "v_mfma_f32_16x16x32_bf16 v[224:227], a[24:27], v[84:87], v[224:227]\n\t"
    "v_mfma_f32_16x16x32_bf16 v[228:231], a[8:11], v[80:83], 0\n\t"
    "v_mfma_f32_16x16x32_bf16 v[228:231], a[28:31], v[84:87], v[228:231]\n\t"
    "buffer_load_dwordx4 v[196:199], v242, s[28:31], s43 offen offset:1024 sc1\n\t"
    "v_mfma_f32_16x16x32_bf16 v[232:235], a[12:15], v[80:83], 0\n\t"
    "v_mfma_f32_16x16x32_bf16 v[232:235], a[32:35], v[84:87], v[232:235]\n\t"
    "v_mfma_f32_16x16x32_bf16 v[236:239], a[16:19], v[80:83], 0\n\t"
    "v_mfma_f32_16x16x32_bf16 v[236:239], a[36:39], v[84:87], v[236:239]\n\t"
    "s_waitcnt vmcnt(10)\n\t"
    "v_mfma_f32_16x16x32_bf16 v[20:23], a[0:3], v[88:91], 0\n\t"
    "buffer_load_dwordx4 v[200:203], v243, s[28:31], s43 offen offset:0 sc1\n\t"
    "v_mfma_f32_16x16x32_bf16 v[20:23], a[20:23], v[92:95], v[20:23]\n\t"
    "v_mfma_f32_16x16x32_bf16 v[24:27], a[4:7], v[88:91], 0\n\t"
    "v_mfma_f32_16x16x32_bf16 v[24:27], a[24:27], v[92:95], v[24:27]\n\t"
    "v_mfma_f32_16x16x32_bf16 v[28:31], a[8:11], v[88:91], 0\n\t"
    "buffer_load_dwordx4 v[204:207], v243, s[28:31], s43 offen offset:1024 sc1\n\t"
    "v_mfma_f32_16x16x32_bf16 v[28:31], a[28:31], v[92:95], v[28:31]\n\t"
    "v_mfma_f32_16x16x32_bf16 v[32:35], a[12:15], v[88:91], 0\n\t"
    "v_mfma_f32_16x16x32_bf16 v[32:35], a[32:35], v[92:95], v[32:35]\n\t"
    "v_mfma_f32_16x16x32_bf16 v[36:39], a[16:19], v[88:91], 0\n\t"
    "v_mfma_f32_16x16x32_bf16 v[36:39], a[36:39], v[92:95], v[36:39]\n\t"
    "s_waitcnt vmcnt(10)\n\t"
    "s_add_u32 s43, s93, 16384\n\t"
    "buffer_load_dwordx4 v[208:211], v240, s[28:31], s43 offen offset:0 sc1\n\t"
    "v_mfma_f32_16x16x32_bf16 v[40:43], a[0:3], v[96:99], 0\n\t"
    "v_mfma_f32_16x16x32_bf16 v[40:43], a[20:23], v[100:103], v[40:43]\n\t"
    "v_mfma_f32_16x16x32_bf16 v[44:47], a[4:7], v[96:99], 0\n\t"
    "v_mfma_f32_16x16x32_bf16 v[44:47], a[24:27], v[100:103], v[44:47]\n\t"
    "buffer_load_dwordx4 v[212:215], v240, s[28:31], s43 offen offset:1024 sc1\n\t"
    "v_mfma_f32_16x16x32_bf16 v[48:51], a[8:11], v[96:99], 0\n\t"
    "v_mfma_f32_16x16x32_bf16 v[48:51], a[28:31], v[100:103], v[48:51]\n\t"
    "v_mfma_f32_16x16x32_bf16 v[52:55], a[12:15], v[96:99], 0\n\t"
    "v_mfma_f32_16x16x32_bf16 v[52:55], a[32:35], v[100:103], v[52:55]\n\t"
    "v_mfma_f32_16x16x32_bf16 v[56:59], a[16:19], v[96:99], 0\n\t"
    "buffer_load_dwordx4 v[216:219], v241, s[28:31], s43 offen offset:0 sc1\n\t"
    "v_mfma_f32_16x16x32_bf16 v[56:59], a[36:39], v[100:103], v[56:59]\n\t"
    "s_waitcnt vmcnt(11)\n\t"
    "v_mfma_f32_16x16x32_bf16 v[60:63], a[0:3], v[104:107], 0\n\t"
    "v_mfma_f32_16x16x32_bf16 v[60:63], a[20:23], v[108:111], v[60:63]\n\t"
    "v_mfma_f32_16x16x32_bf16 v[64:67], a[4:7], v[104:107], 0\n\t"
    "buffer_load_dwordx4 v[0:3], v241, s[28:31], s43 offen offset:1024 sc1\n\t"
    "v_mfma_f32_16x16x32_bf16 v[64:67], a[24:27], v[108:111], v[64:67]\n\t"
    "v_mfma_f32_16x16x32_bf16 v[68:71], a[8:11], v[104:107], 0\n\t"
    "v_mfma_f32_16x16x32_bf16 v[68:71], a[28:31], v[108:111], v[68:71]\n\t"
    "v_mfma_f32_16x16x32_bf16 v[72:75], a[12:15], v[104:107], 0\n\t"
    "v_mfma_f32_16x16x32_bf16 v[72:75], a[32:35], v[108:111], v[72:75]\n\t"
    "buffer_load_dwordx4 v[4:7], v242, s[28:31], s43 offen offset:0 sc1\n\t"
    "v_mfma_f32_16x16x32_bf16 v[76:79], a[16:19], v[104:107], 0\n\t"
    "v_mfma_f32_16x16x32_bf16 v[76:79], a[36:39], v[108:111], v[76:79]\n\t"
    "s_waitcnt vmcnt(11)\n\t"
    "v_mfma_f32_16x16x32_bf16 v[220:223], a[40:43], v[112:115], v[220:223]\n\t"
    "v_mfma_f32_16x16x32_bf16 v[220:223], a[108:111], v[116:119], v[220:223]\n\t"
    "buffer_load_dwordx4 v[8:11], v242, s[28:31], s43 offen offset:1024 sc1\n\t"
    "v_mfma_f32_16x16x32_bf16 v[224:227], a[44:47], v[112:115], v[224:227]\n\t"
    "v_mfma_f32_16x16x32_bf16 v[224:227], a[112:115], v[116:119], v[224:227]\n\t"
    "v_mfma_f32_16x16x32_bf16 v[228:231], a[96:99], v[112:115], v[228:231]\n\t"
    "v_mfma_f32_16x16x32_bf16 v[228:231], a[116:119], v[116:119], v[228:231]\n\t"
    "v_mfma_f32_16x16x32_bf16 v[232:235], a[100:103], v[112:115], v[232:235]\n\t"
    "buffer_load_dwordx4 v[12:15], v243, s[28:31], s43 offen offset:0 sc1\n\t"
    "v_mfma_f32_16x16x32_bf16 v[232:235], a[120:123], v[116:119], v[232:235]\n\t"
    "v_mfma_f32_16x16x32_bf16 v[236:239], a[104:107], v[112:115], v[236:239]\n\t"
    "v_mfma_f32_16x16x32_bf16 v[236:239], a[124:127], v[116:119], v[236:239]\n\t"
    "s_waitcnt vmcnt(11)\n\t"
    "v_mfma_f32_16x16x32_bf16 v[20:23], a[40:43], v[120:123], v[20:23]\n\t"
    "buffer_load_dwordx4 v[16:19], v243, s[28:31], s43 offen offset:1024 sc1\n\t"
    "v_mfma_f32_16x16x32_bf16 v[20:23], a[108:111], v[124:127], v[20:23]\n\t"
    "v_mfma_f32_16x16x32_bf16 v[24:27], a[44:47], v[120:123], v[24:27]\n\t"
    "v_mfma_f32_16x16x32_bf16 v[24:27], a[112:115], v[124:127], v[24:27]\n\t"
    "v_mfma_f32_16x16x32_bf16 v[28:31], a[96:99], v[120:123], v[28:31]\n\t"
    "v_mfma_f32_16x16x32_bf16 v[28:31], a[116:119], v[124:127], v[28:31]\n\t"
    "s_add_u32 s43, s93, 24576\n\t"
    "buffer_load_dwordx4 v[80:83], v240, s[28:31], s43 offen offset:0 sc1\n\t"
    "v_mfma_f32_16x16x32_bf16 v[32:35], a[100:103], v[120:123], v[32:35]\n\t"
    "v_mfma_f32_16x16x32_bf16 v[32:35], a[120:123], v[124:127], v[32:35]\n\t"
    "v_mfma_f32_16x16x32_bf16 v[36:39], a[104:107], v[120:123], v[36:39]\n\t"
    "s_add_u32 m0, s37, 16384\n\t"
    "v_mfma_f32_16x16x32_bf16 v[36:39], a[124:127], v[124:127], v[36:39]\n\t"
    "s_waitcnt vmcnt(11)\n\t"
    "s_add_u32 s97, s63, 16384\n\t"
    "v_mfma_f32_16x16x32_bf16 v[40:43], a[40:43], v[192:195], v[40:43]\n\t"
    "buffer_load_dwordx4 v254, s[28:31], s97 offen offset:0 sc1 lds\n\t"
    "v_mfma_f32_16x16x32_bf16 v[40:43], a[108:111], v[196:199], v[40:43]\n\t"
    "buffer_load_dwordx4 v[84:87], v240, s[28:31], s43 offen offset:1024 sc1\n\t"
    "v_mfma_f32_16x16x32_bf16 v[44:47], a[44:47], v[192:195], v[44:47]\n\t"
    "v_mfma_f32_16x16x32_bf16 v[44:47], a[112:115], v[196:199], v[44:47]\n\t"
    "v_mfma_f32_16x16x32_bf16 v[48:51], a[96:99], v[192:195], v[48:51]\n\t"
    "v_mfma_f32_16x16x32_bf16 v[48:51], a[116:119], v[196:199], v[48:51]\n\t"
    "v_mfma_f32_16x16x32_bf16 v[52:55], a[100:103], v[192:195], v[52:55]\n\t"
    "buffer_load_dwordx4 v[88:91], v241, s[28:31], s43 offen offset:0 sc1\n\t"
    "v_mfma_f32_16x16x32_bf16 v[52:55], a[120:123], v[196:199], v[52:55]\n\t"
    "v_mfma_f32_16x16x32_bf16 v[56:59], a[104:107], v[192:195], v[56:59]\n\t"
    "v_mfma_f32_16x16x32_bf16 v[56:59], a[124:127], v[196:199], v[56:59]\n\t"
    "s_waitcnt vmcnt(12)\n\t"
    "v_mfma_f32_16x16x32_bf16 v[60:63], a[40:43], v[200:203], v[60:63]\n\t"
    "buffer_load_dwordx4 v[92:95], v241, s[28:31], s43 offen offset:1024 sc1\n\t"
    "v_mfma_f32_16x16x32_bf16 v[60:63], a[108:111], v[204:207], v[60:63]\n\t"
    "v_mfma_f32_16x16x32_bf16 v[64:67], a[44:47], v[200:203], v[64:67]\n\t"
    "v_mfma_f32_16x16x32_bf16 v[64:67], a[112:115], v[204:207], v[64:67]\n\t"
    "v_mfma_f32_16x16x32_bf16 v[68:71], a[96:99], v[200:203], v[68:71]\n\t"
    "buffer_load_dwordx4 v254, s[28:31], s97 offen offset:1024 sc1 lds\n\t"
    "v_mfma_f32_16x16x32_bf16 v[68:71], a[116:119], v[204:207], v[68:71]\n\t"
    "buffer_load_dwordx4 v[96:99], v242, s[28:31], s43 offen offset:0 sc1\n\t"
    "v_mfma_f32_16x16x32_bf16 v[72:75], a[100:103], v[200:203], v[72:75]\n\t"
    "v_mfma_f32_16x16x32_bf16 v[72:75], a[120:123], v[204:207], v[72:75]\n\t"
    "v_mfma_f32_16x16x32_bf16 v[76:79], a[104:107], v[200:203], v[76:79]\n\t"
    "v_mfma_f32_16x16x32_bf16 v[76:79], a[124:127], v[204:207], v[76:79]\n\t"
    "s_waitcnt vmcnt(13)\n\t"
    "buffer_load_dwordx4 v[100:103], v242, s[28:31], s43 offen offset:1024 sc1\n\t"
    "v_mfma_f32_16x16x32_bf16 v[220:223], a[128:131], v[208:211], v[220:223]\n\t"
    "v_mfma_f32_16x16x32_bf16 v[220:223], v[132:135], v[212:215], v[220:223]\n\t"
    "v_mfma_f32_16x16x32_bf16 v[224:227], a[132:135], v[208:211], v[224:227]\n\t"
    "v_mfma_f32_16x16x32_bf16 v[224:227], v[136:139], v[212:215], v[224:227]\n\t"
    "v_mfma_f32_16x16x32_bf16 v[228:231], a[136:139], v[208:211], v[228:231]\n\t"
    "buffer_load_dwordx4 v[104:107], v243, s[28:31], s43 offen offset:0 sc1\n\t"
    "v_mfma_f32_16x16x32_bf16 v[228:231], v[140:143], v[212:215], v[228:231]\n\t"
    "v_mfma_f32_16x16x32_bf16 v[232:235], a[140:143], v[208:211], v[232:235]\n\t"
    "v_mfma_f32_16x16x32_bf16 v[232:235], v[144:147], v[212:215], v[232:235]\n\t"
    "v_mfma_f32_16x16x32_bf16 v[236:239], v[128:131], v[208:211], v[236:239]\n\t"
    "buffer_load_dwordx4 v254, s[28:31], s97 offen offset:2048 sc1 lds\n\t"
    "v_mfma_f32_16x16x32_bf16 v[236:239], v[148:151], v[212:215], v[236:239]\n\t"
    "s_waitcnt vmcnt(14)\n\t"
    "buffer_load_dwordx4 v[108:111], v243, s[28:31], s43 offen offset:1024 sc1\n\t"
    "v_mfma_f32_16x16x32_bf16 v[20:23], a[128:131], v[216:219], v[20:23]\n\t"
    "v_mfma_f32_16x16x32_bf16 v[20:23], v[132:135], v[0:3], v[20:23]\n\t"
    "v_mfma_f32_16x16x32_bf16 v[24:27], a[132:135], v[216:219], v[24:27]\n\t"
    "v_mfma_f32_16x16x32_bf16 v[24:27], v[136:139], v[0:3], v[24:27]\n\t"
    "v_mfma_f32_16x16x32_bf16 v[28:31], a[136:139], v[216:219], v[28:31]\n\t"
    "s_add_u32 s43, s93, 32768\n\t"
    "buffer_load_dwordx4 v[112:115], v240, s[28:31], s43 offen offset:0 sc1\n\t"
    "v_mfma_f32_16x16x32_bf16 v[28:31], v[140:143], v[0:3], v[28:31]\n\t"
    "v_mfma_f32_16x16x32_bf16 v[32:35], a[140:143], v[216:219], v[32:35]\n\t"
    "v_mfma_f32_16x16x32_bf16 v[32:35], v[144:147], v[0:3], v[32:35]\n\t"
    "v_mfma_f32_16x16x32_bf16 v[36:39], v[128:131], v[216:219], v[36:39]\n\t"
    "buffer_load_dwordx4 v[116:119], v240, s[28:31], s43 offen offset:1024 sc1\n\t"
    "v_mfma_f32_16x16x32_bf16 v[36:39], v[148:151], v[0:3], v[36:39]\n\t"
    "s_waitcnt vmcnt(15)\n\t"
    "v_mfma_f32_16x16x32_bf16 v[40:43], a[128:131], v[4:7], v[40:43]\n\t"
    "v_mfma_f32_16x16x32_bf16 v[40:43], v[132:135], v[8:11], v[40:43]\n\t"
    "v_mfma_f32_16x16x32_bf16 v[44:47], a[132:135], v[4:7], v[44:47]\n\t"
    "buffer_load_dwordx4 v254, s[28:31], s97 offen offset:3072 sc1 lds\n\t"
    "v_mfma_f32_16x16x32_bf16 v[44:47], v[136:139], v[8:11], v[44:47]\n\t"
    "buffer_load_dwordx4 v[120:123], v241, s[28:31], s43 offen offset:0 sc1\n\t"
    "v_mfma_f32_16x16x32_bf16 v[48:51], a[136:139], v[4:7], v[48:51]\n\t"
    "v_mfma_f32_16x16x32_bf16 v[48:51], v[140:143], v[8:11], v[48:51]\n\t"
    "v_mfma_f32_16x16x32_bf16 v[52:55], a[140:143], v[4:7], v[52:55]\n\t"
    "v_mfma_f32_16x16x32_bf16 v[52:55], v[144:147], v[8:11], v[52:55]\n\t"
    "buffer_load_dwordx4 v[124:127], v241, s[28:31], s43 offen offset:1024 sc1\n\t"
    "v_mfma_f32_16x16x32_bf16 v[56:59], v[128:131], v[4:7], v[56:59]\n\t"
    "v_mfma_f32_16x16x32_bf16 v[56:59], v[148:151], v[8:11], v[56:59]\n\t"
    "s_waitcnt vmcnt(16)\n\t"
    "v_mfma_f32_16x16x32_bf16 v[60:63], a[128:131], v[12:15], v[60:63]\n\t"
    "v_mfma_f32_16x16x32_bf16 v[60:63], v[132:135], v[16:19], v[60:63]\n\t"
    "v_mfma_f32_16x16x32_bf16 v[64:67], a[132:135], v[12:15], v[64:67]\n\t"
    "buffer_load_dwordx4 v[192:195], v242, s[28:31], s43 offen offset:0 sc1\n\t"
    "v_mfma_f32_16x16x32_bf16 v[64:67], v[136:139], v[16:19], v[64:67]\n\t"
    "v_mfma_f32_16x16x32_bf16 v[68:71], a[136:139], v[12:15], v[68:71]\n\t"
    "s_add_u32 m0, s37, 32768\n\t"
    "v_mfma_f32_16x16x32_bf16 v[68:71], v[140:143], v[16:19], v[68:71]\n\t"
    "s_add_u32 s97, s63, 32768\n\t"
    "v_mfma_f32_16x16x32_bf16 v[72:75], a[140:143], v[12:15], v[72:75]\n\t"
    "buffer_load_dwordx4 v254, s[28:31], s97 offen offset:0 sc1 lds\n\t"
    "v_mfma_f32_16x16x32_bf16 v[72:75], v[144:147], v[16:19], v[72:75]\n\t"
    "buffer_load_dwordx4 v[196:199], v242, s[28:31], s43 offen offset:1024 sc1\n\t"
    "v_mfma_f32_16x16x32_bf16 v[76:79], v[128:131], v[12:15], v[76:79]\n\t"
    "v_mfma_f32_16x16x32_bf16 v[76:79], v[148:151], v[16:19], v[76:79]\n\t"
    "s_waitcnt vmcnt(16)\n\t"
    "v_mfma_f32_16x16x32_bf16 v[220:223], v[152:155], v[80:83], v[220:223]\n\t"
    "v_mfma_f32_16x16x32_bf16 v[220:223], v[172:175], v[84:87], v[220:223]\n\t"
    "v_mfma_f32_16x16x32_bf16 v[224:227], v[156:159], v[80:83], v[224:227]\n\t"
    "buffer_load_dwordx4 v[200:203], v243, s[28:31], s43 offen offset:0 sc1\n\t"
    "v_mfma_f32_16x16x32_bf16 v[224:227], v[176:179], v[84:87], v[224:227]\n\t"
    "v_mfma_f32_16x16x32_bf16 v[228:231], v[160:163], v[80:83], v[228:231]\n\t"
    "v_mfma_f32_16x16x32_bf16 v[228:231], v[180:183], v[84:87], v[228:231]\n\t"
    "v_mfma_f32_16x16x32_bf16 v[232:235], v[164:167], v[80:83], v[232:235]\n\t"
    "buffer_load_dwordx4 v[204:207], v243, s[28:31], s43 offen offset:1024 sc1\n\t"
    "v_mfma_f32_16x16x32_bf16 v[232:235], v[184:187], v[84:87], v[232:235]\n\t"
    "v_mfma_f32_16x16x32_bf16 v[236:239], v[168:171], v[80:83], v[236:239]\n\t"
    "v_mfma_f32_16x16x32_bf16 v[236:239], v[188:191], v[84:87], v[236:239]\n\t"
    "s_waitcnt vmcnt(16)\n\t"
    "v_mfma_f32_16x16x32_bf16 v[20:23], v[152:155], v[88:91], v[20:23]\n\t"
    "buffer_load_dwordx4 v254, s[28:31], s97 offen offset:1024 sc1 lds\n\t"
    "v_mfma_f32_16x16x32_bf16 v[20:23], v[172:175], v[92:95], v[20:23]\n\t"
    "s_add_u32 s43, s93, 40960\n\t"
    "buffer_load_dwordx4 v[208:211], v240, s[28:31], s43 offen offset:0 sc1\n\t"
    "v_mfma_f32_16x16x32_bf16 v[24:27], v[156:159], v[88:91], v[24:27]\n\t"
    "v_mfma_f32_16x16x32_bf16 v[24:27], v[176:179], v[92:95], v[24:27]\n\t"
    "v_mfma_f32_16x16x32_bf16 v[28:31], v[160:163], v[88:91], v[28:31]\n\t"
    "v_mfma_f32_16x16x32_bf16 v[28:31], v[180:183], v[92:95], v[28:31]\n\t"
    "buffer_load_dwordx4 v[212:215], v240, s[28:31], s43 offen offset:1024 sc1\n\t"
    "v_mfma_f32_16x16x32_bf16 v[32:35], v[164:167], v[88:91], v[32:35]\n\t"
    "v_mfma_f32_16x16x32_bf16 v[32:35], v[184:187], v[92:95], v[32:35]\n\t"
    "v_mfma_f32_16x16x32_bf16 v[36:39], v[168:171], v[88:91], v[36:39]\n\t"
    "v_mfma_f32_16x16x32_bf16 v[36:39], v[188:191], v[92:95], v[36:39]\n\t"
    "s_waitcnt vmcnt(16)\n\t"
    "v_mfma_f32_16x16x32_bf16 v[40:43], v[152:155], v[96:99], v[40:43]\n\t"
    "buffer_load_dwordx4 v[216:219], v241, s[28:31], s43 offen offset:0 sc1\n\t"
    "v_mfma_f32_16x16x32_bf16 v[40:43], v[172:175], v[100:103], v[40:43]\n\t"
    "v_mfma_f32_16x16x32_bf16 v[44:47], v[156:159], v[96:99], v[44:47]\n\t"
    "v_mfma_f32_16x16x32_bf16 v[44:47], v[176:179], v[100:103], v[44:47]\n\t"
    "v_mfma_f32_16x16x32_bf16 v[48:51], v[160:163], v[96:99], v[48:51]\n\t"
    "buffer_load_dwordx4 v254, s[28:31], s97 offen offset:2048 sc1 lds\n\t"
    "v_mfma_f32_16x16x32_bf16 v[48:51], v[180:183], v[100:103], v[48:51]\n\t"
    "buffer_load_dwordx4 v[0:3], v241, s[28:31], s43 offen offset:1024 sc1\n\t"
    "v_mfma_f32_16x16x32_bf16 v[52:55], v[164:167], v[96:99], v[52:55]\n\t"
    "v_mfma_f32_16x16x32_bf16 v[52:55], v[184:187], v[100:103], v[52:55]\n\t"
    "v_mfma_f32_16x16x32_bf16 v[56:59], v[168:171], v[96:99], v[56:59]\n\t"
    "v_mfma_f32_16x16x32_bf16 v[56:59], v[188:191], v[100:103], v[56:59]\n\t"
    "s_waitcnt vmcnt(16)\n\t"
    "v_mfma_f32_16x16x32_bf16 v[60:63], v[152:155], v[104:107], v[60:63]\n\t"
    "buffer_load_dwordx4 v[4:7], v242, s[28:31], s43 offen offset:0 sc1\n\t"
    "v_mfma_f32_16x16x32_bf16 v[60:63], v[172:175], v[108:111], v[60:63]\n\t"
    "v_mfma_f32_16x16x32_bf16 v[64:67], v[156:159], v[104:107], v[64:67]\n\t"
    "v_mfma_f32_16x16x32_bf16 v[64:67], v[176:179], v[108:111], v[64:67]\n\t"
    "v_mfma_f32_16x16x32_bf16 v[68:71], v[160:163], v[104:107], v[68:71]\n\t"
    "buffer_load_dwordx4 v[8:11], v242, s[28:31], s43 offen offset:1024 sc1\n\t"
    "v_mfma_f32_16x16x32_bf16 v[68:71], v[180:183], v[108:111], v[68:71]\n\t"
    "v_mfma_f32_16x16x32_bf16 v[72:75], v[164:167], v[104:107], v[72:75]\n\t"
    "v_mfma_f32_16x16x32_bf16 v[72:75], v[184:187], v[108:111], v[72:75]\n\t"
    "v_mfma_f32_16x16x32_bf16 v[76:79], v[168:171], v[104:107], v[76:79]\n\t"
    "buffer_load_dwordx4 v254, s[28:31], s97 offen offset:3072 sc1 lds\n\t"
    "v_mfma_f32_16x16x32_bf16 v[76:79], v[188:191], v[108:111], v[76:79]\n\t"
    "s_waitcnt vmcnt(17)\n\t"
    "buffer_load_dwordx4 v[12:15], v243, s[28:31], s43 offen offset:0 sc1\n\t"
    "v_mfma_f32_16x16x32_bf16 v[220:223], a[144:147], v[112:115], v[220:223]\n\t"
    "v_mfma_f32_16x16x32_bf16 v[220:223], a[164:167], v[116:119], v[220:223]\n\t"
    "v_mfma_f32_16x16x32_bf16 v[224:227], a[148:151], v[112:115], v[224:227]\n\t"
    "v_mfma_f32_16x16x32_bf16 v[224:227], a[168:171], v[116:119], v[224:227]\n\t"
    "buffer_load_dwordx4 v[16:19], v243, s[28:31], s43 offen offset:1024 sc1\n\t"
    "v_mfma_f32_16x16x32_bf16 v[228:231], a[152:155], v[112:115], v[228:231]\n\t"
    "v_mfma_f32_16x16x32_bf16 v[228:231], a[172:175], v[116:119], v[228:231]\n\t"
    "v_mfma_f32_16x16x32_bf16 v[232:235], a[156:159], v[112:115], v[232:235]\n\t"
    "v_mfma_f32_16x16x32_bf16 v[232:235], a[176:179], v[116:119], v[232:235]\n\t"
    "v_mfma_f32_16x16x32_bf16 v[236:239], a[160:163], v[112:115], v[236:239]\n\t"
    "s_add_u32 s43, s93, 49152\n\t"
    "buffer_load_dwordx4 v[80:83], v241, s[28:31], s43 offen offset:0 sc1\n\t"
    "v_mfma_f32_16x16x32_bf16 v[236:239], a[180:183], v[116:119], v[236:239]\n\t"
    "s_waitcnt vmcnt(17)\n\t"
    "v_mfma_f32_16x16x32_bf16 v[20:23], a[144:147], v[120:123], v[20:23]\n\t"
    "v_mfma_f32_16x16x32_bf16 v[20:23], a[164:167], v[124:127], v[20:23]\n\t"
    "v_mfma_f32_16x16x32_bf16 v[24:27], a[148:151], v[120:123], v[24:27]\n\t"
    "buffer_load_dwordx4 v[84:87], v241, s[28:31], s43 offen offset:1024 sc1\n\t"
    "v_mfma_f32_16x16x32_bf16 v[24:27], a[168:171], v[124:127], v[24:27]\n\t"
    "v_mfma_f32_16x16x32_bf16 v[28:31], a[152:155], v[120:123], v[28:31]\n\t"
    "v_mfma_f32_16x16x32_bf16 v[28:31], a[172:175], v[124:127], v[28:31]\n\t"
    "v_mfma_f32_16x16x32_bf16 v[32:35], a[156:159], v[120:123], v[32:35]\n\t"
    "v_mfma_f32_16x16x32_bf16 v[32:35], a[176:179], v[124:127], v[32:35]\n\t"
    "s_add_u32 s43, s93, 57344\n\t"
    "buffer_load_dwordx4 v[88:91], v241, s[28:31], s43 offen offset:0 sc1\n\t"
    "v_mfma_f32_16x16x32_bf16 v[36:39], a[160:163], v[120:123], v[36:39]\n\t"
    "v_mfma_f32_16x16x32_bf16 v[36:39], a[180:183], v[124:127], v[36:39]\n\t"
    "s_waitcnt vmcnt(16)\n\t"
    "v_mfma_f32_16x16x32_bf16 v[40:43], a[144:147], v[192:195], v[40:43]\n\t"
    "v_mfma_f32_16x16x32_bf16 v[40:43], a[164:167], v[196:199], v[40:43]\n\t"
    "buffer_load_dwordx4 v[92:95], v241, s[28:31], s43 offen offset:1024 sc1\n\t"
    "v_mfma_f32_16x16x32_bf16 v[44:47], a[148:151], v[192:195], v[44:47]\n\t"
    "v_mfma_f32_16x16x32_bf16 v[44:47], a[168:171], v[196:199], v[44:47]\n\t"
    "v_mfma_f32_16x16x32_bf16 v[48:51], a[152:155], v[192:195], v[48:51]\n\t"
    "v_mfma_f32_16x16x32_bf16 v[48:51], a[172:175], v[196:199], v[48:51]\n\t"
    "v_mfma_f32_16x16x32_bf16 v[52:55], a[156:159], v[192:195], v[52:55]\n\t"
    "s_add_u32 s43, s93, 49152\n\t"
    "buffer_load_dwordx4 v[96:99], v242, s[28:31], s43 offen offset:0 sc1\n\t"
    "v_mfma_f32_16x16x32_bf16 v[52:55], a[176:179], v[196:199], v[52:55]\n\t"
    "v_mfma_f32_16x16x32_bf16 v[56:59], a[160:163], v[192:195], v[56:59]\n\t"
    "v_mfma_f32_16x16x32_bf16 v[56:59], a[180:183], v[196:199], v[56:59]\n\t"
    "s_waitcnt vmcnt(16)\n\t"
    "v_mfma_f32_16x16x32_bf16 v[60:63], a[144:147], v[200:203], v[60:63]\n\t"
    "buffer_load_dwordx4 v[100:103], v242, s[28:31], s43 offen offset:1024 sc1\n\t"
    "v_mfma_f32_16x16x32_bf16 v[60:63], a[164:167], v[204:207], v[60:63]\n\t"
    "v_mfma_f32_16x16x32_bf16 v[64:67], a[148:151], v[200:203], v[64:67]\n\t"
    "v_mfma_f32_16x16x32_bf16 v[64:67], a[168:171], v[204:207], v[64:67]\n\t"
    "v_mfma_f32_16x16x32_bf16 v[68:71], a[152:155], v[200:203], v[68:71]\n\t"
    "v_mfma_f32_16x16x32_bf16 v[68:71], a[172:175], v[204:207], v[68:71]\n\t"
    "s_add_u32 s43, s93, 57344\n\t"
    "buffer_load_dwordx4 v[104:107], v242, s[28:31], s43 offen offset:0 sc1\n\t"
    "v_mfma_f32_16x16x32_bf16 v[72:75], a[156:159], v[200:203], v[72:75]\n\t"
    "v_mfma_f32_16x16x32_bf16 v[72:75], a[176:179], v[204:207], v[72:75]\n\t"
    "v_mfma_f32_16x16x32_bf16 v[76:79], a[160:163], v[200:203], v[76:79]\n\t"
    "v_mfma_f32_16x16x32_bf16 v[76:79], a[180:183], v[204:207], v[76:79]\n\t"
    "s_waitcnt vmcnt(15)\n\t"
    "buffer_load_dwordx4 v[108:111], v242, s[28:31], s43 offen offset:1024 sc1\n\t"
    "v_mfma_f32_16x16x32_bf16 v[220:223], a[184:187], v[208:211], v[220:223]\n\t"
    "v_mfma_f32_16x16x32_bf16 v[220:223], a[56:59], v[212:215], v[220:223]\n\t"
    "v_mfma_f32_16x16x32_bf16 v[224:227], a[188:191], v[208:211], v[224:227]\n\t"
    "v_mfma_f32_16x16x32_bf16 v[224:227], a[60:63], v[212:215], v[224:227]\n\t"
    "v_mfma_f32_16x16x32_bf16 v[228:231], a[192:195], v[208:211], v[228:231]\n\t"
    "s_add_u32 s43, s93, 49152\n\t"
    "buffer_load_dwordx4 v[112:115], v243, s[28:31], s43 offen offset:0 sc1\n\t"
    "v_mfma_f32_16x16x32_bf16 v[228:231], a[64:67], v[212:215], v[228:231]\n\t"
    "v_mfma_f32_16x16x32_bf16 v[232:235], a[48:51], v[208:211], v[232:235]\n\t"
    "v_mfma_f32_16x16x32_bf16 v[232:235], a[68:71], v[212:215], v[232:235]\n\t"
    "v_mfma_f32_16x16x32_bf16 v[236:239], a[52:55], v[208:211], v[236:239]\n\t"
    "buffer_load_dwordx4 v[116:119], v243, s[28:31], s43 offen offset:1024 sc1\n\t"
    "v_mfma_f32_16x16x32_bf16 v[236:239], a[72:75], v[212:215], v[236:239]\n\t"
    "s_waitcnt vmcnt(15)\n\t"
    "v_mfma_f32_16x16x32_bf16 v[20:23], a[184:187], v[216:219], v[20:23]\n\t"
    "v_mfma_f32_16x16x32_bf16 v[20:23], a[56:59], v[0:3], v[20:23]\n\t"
    "v_mfma_f32_16x16x32_bf16 v[24:27], a[188:191], v[216:219], v[24:27]\n\t"
    "v_mfma_f32_16x16x32_bf16 v[24:27], a[60:63], v[0:3], v[24:27]\n\t"
    "s_add_u32 s43, s93, 57344\n\t"
    "buffer_load_dwordx4 v[120:123], v243, s[28:31], s43 offen offset:0 sc1\n\t"
    "v_mfma_f32_16x16x32_bf16 v[28:31], a[192:195], v[216:219], v[28:31]\n\t"
    "v_mfma_f32_16x16x32_bf16 v[28:31], a[64:67], v[0:3], v[28:31]\n\t"
    "v_mfma_f32_16x16x32_bf16 v[32:35], a[48:51], v[216:219], v[32:35]\n\t"
    "v_mfma_f32_16x16x32_bf16 v[32:35], a[68:71], v[0:3], v[32:35]\n\t"
    "buffer_load_dwordx4 v[124:127], v243, s[28:31], s43 offen offset:1024 sc1\n\t"
    "v_mfma_f32_16x16x32_bf16 v[36:39], a[52:55], v[216:219], v[36:39]\n\t"
    "v_mfma_f32_16x16x32_bf16 v[36:39], a[72:75], v[0:3], v[36:39]\n\t"
    "s_waitcnt vmcnt(15)\n\t"
    "v_mfma_f32_16x16x32_bf16 v[40:43], a[184:187], v[4:7], v[40:43]\n\t"
    "v_mfma_f32_16x16x32_bf16 v[40:43], a[56:59], v[8:11], v[40:43]\n\t"
    "v_mfma_f32_16x16x32_bf16 v[44:47], a[188:191], v[4:7], v[44:47]\n\t"
    "s_add_u32 s43, s93, 49152\n\t"
    "buffer_load_dwordx4 v[192:195], v240, s[28:31], s43 offen offset:0 sc1\n\t"
    "v_mfma_f32_16x16x32_bf16 v[44:47], a[60:63], v[8:11], v[44:47]\n\t"
    "v_mfma_f32_16x16x32_bf16 v[48:51], a[192:195], v[4:7], v[48:51]\n\t"
    "v_mfma_f32_16x16x32_bf16 v[48:51], a[64:67], v[8:11], v[48:51]\n\t"
    "v_mfma_f32_16x16x32_bf16 v[52:55], a[48:51], v[4:7], v[52:55]\n\t"
    "buffer_load_dwordx4 v[196:199], v240, s[28:31], s43 offen offset:1024 sc1\n\t"
    "v_mfma_f32_16x16x32_bf16 v[52:55], a[68:71], v[8:11], v[52:55]\n\t"
    "v_mfma_f32_16x16x32_bf16 v[56:59], a[52:55], v[4:7], v[56:59]\n\t"
    "v_mfma_f32_16x16x32_bf16 v[56:59], a[72:75], v[8:11], v[56:59]\n\t"
    "s_waitcnt vmcnt(14)\n\t"
    "v_mfma_f32_16x16x32_bf16 v[60:63], a[184:187], v[12:15], v[60:63]\n\t"
    "v_mfma_f32_16x16x32_bf16 v[60:63], a[56:59], v[16:19], v[60:63]\n\t"
    "s_add_u32 s43, s93, 57344\n\t"
    "buffer_load_dwordx4 v[200:203], v240, s[28:31], s43 offen offset:0 sc1\n\t"
    "v_mfma_f32_16x16x32_bf16 v[64:67], a[188:191], v[12:15], v[64:67]\n\t"
    "v_mfma_f32_16x16x32_bf16 v[64:67], a[60:63], v[16:19], v[64:67]\n\t"
    "v_mfma_f32_16x16x32_bf16 v[68:71], a[192:195], v[12:15], v[68:71]\n\t"
    "v_mfma_f32_16x16x32_bf16 v[68:71], a[64:67], v[16:19], v[68:71]\n\t"
    "buffer_load_dwordx4 v[204:207], v240, s[28:31], s43 offen offset:1024 sc1\n\t"
    "v_mfma_f32_16x16x32_bf16 v[72:75], a[48:51], v[12:15], v[72:75]\n\t"
    "v_mfma_f32_16x16x32_bf16 v[72:75], a[68:71], v[16:19], v[72:75]\n\t"
    "v_mfma_f32_16x16x32_bf16 v[76:79], a[52:55], v[12:15], v[76:79]\n\t"
    "v_mfma_f32_16x16x32_bf16 v[76:79], a[72:75], v[16:19], v[76:79]\n\t"
    "s_waitcnt vmcnt(12)\n\t"
    "v_mfma_f32_16x16x32_bf16 v[20:23], a[76:79], v[80:83], v[20:23]\n\t"
    "v_mfma_f32_16x16x32_bf16 v[20:23], a[196:199], v[84:87], v[20:23]\n\t"
    "v_mfma_f32_16x16x32_bf16 v[20:23], a[216:219], v[88:91], v[20:23]\n\t"
    "v_mfma_f32_16x16x32_bf16 v[20:23], a[236:239], v[92:95], v[20:23]\n\t"
    "v_mfma_f32_16x16x32_bf16 v[24:27], a[80:83], v[80:83], v[24:27]\n\t"
    "v_mfma_f32_16x16x32_bf16 v[24:27], a[200:203], v[84:87], v[24:27]\n\t"
    "v_mfma_f32_16x16x32_bf16 v[24:27], a[220:223], v[88:91], v[24:27]\n\t"
    "v_mfma_f32_16x16x32_bf16 v[24:27], a[240:243], v[92:95], v[24:27]\n\t"
    "v_mfma_f32_16x16x32_bf16 v[28:31], a[84:87], v[80:83], v[28:31]\n\t"
    "v_mfma_f32_16x16x32_bf16 v[28:31], a[204:207], v[84:87], v[28:31]\n\t"
    "v_mfma_f32_16x16x32_bf16 v[28:31], a[224:227], v[88:91], v[28:31]\n\t"
    "v_mfma_f32_16x16x32_bf16 v[28:31], a[244:247], v[92:95], v[28:31]\n\t"
    "v_mfma_f32_16x16x32_bf16 v[32:35], a[88:91], v[80:83], v[32:35]\n\t"
    "ds_write_b128 v250, v[20:23] offset:0\n\t"
    "v_mfma_f32_16x16x32_bf16 v[32:35], a[208:211], v[84:87], v[32:35]\n\t"
    "v_mfma_f32_16x16x32_bf16 v[32:35], a[228:231], v[88:91], v[32:35]\n\t"
    "v_mfma_f32_16x16x32_bf16 v[32:35], a[248:251], v[92:95], v[32:35]\n\t"
    "v_mfma_f32_16x16x32_bf16 v[36:39], a[92:95], v[80:83], v[36:39]\n\t"
    "ds_write_b128 v250, v[24:27] offset:1024\n\t"
    "v_mfma_f32_16x16x32_bf16 v[36:39], a[212:215], v[84:87], v[36:39]\n\t"
    "s_add_u32 s43, s60, 0\n\t"
    "buffer_load_dwordx4 v[80:83], v240, s[28:31], s43 offen offset:0 sc1\n\t"
    "v_mfma_f32_16x16x32_bf16 v[36:39], a[232:235], v[88:91], v[36:39]\n\t"
    "v_mfma_f32_16x16x32_bf16 v[36:39], a[252:255], v[92:95], v[36:39]\n\t"
    "s_waitcnt vmcnt(9)\n\t"
    "v_mfma_f32_16x16x32_bf16 v[40:43], a[76:79], v[96:99], v[40:43]\n\t"
    "ds_write_b128 v250, v[28:31] offset:2048\n\t"
    "v_mfma_f32_16x16x32_bf16 v[40:43], a[196:199], v[100:103], v[40:43]\n\t"
    "buffer_load_dwordx4 v[84:87], v240, s[28:31], s43 offen offset:1024 sc1\n\t"
    "v_mfma_f32_16x16x32_bf16 v[40:43], a[216:219], v[104:107], v[40:43]\n\t"
    "v_mfma_f32_16x16x32_bf16 v[40:43], a[236:239], v[108:111], v[40:43]\n\t"
    "v_mfma_f32_16x16x32_bf16 v[44:47], a[80:83], v[96:99], v[44:47]\n\t"
    "ds_write_b128 v250, v[32:35] offset:3072\n\t"
    "v_mfma_f32_16x16x32_bf16 v[44:47], a[200:203], v[100:103], v[44:47]\n\t"
    "v_mfma_f32_16x16x32_bf16 v[44:47], a[220:223], v[104:107], v[44:47]\n\t"
    "buffer_load_dwordx4 v[88:91], v241, s[28:31], s43 offen offset:0 sc1\n\t"
    "v_mfma_f32_16x16x32_bf16 v[44:47], a[240:243], v[108:111], v[44:47]\n\t"
    "v_mfma_f32_16x16x32_bf16 v[48:51], a[84:87], v[96:99], v[48:51]\n\t"
    "ds_write_b128 v250, v[36:39] offset:16384\n\t"
    "v_mfma_f32_16x16x32_bf16 v[48:51], a[204:207], v[100:103], v[48:51]\n\t"
    "v_mfma_f32_16x16x32_bf16 v[48:51], a[224:227], v[104:107], v[48:51]\n\t"
    "buffer_load_dwordx4 v[92:95], v241, s[28:31], s43 offen offset:1024 sc1\n\t"
    "v_mfma_f32_16x16x32_bf16 v[48:51], a[244:247], v[108:111], v[48:51]\n\t"
    "v_mfma_f32_16x16x32_bf16 v[52:55], a[88:91], v[96:99], v[52:55]\n\t"
    "ds_write_b128 v250, v[40:43] offset:17408\n\t"
    "v_mfma_f32_16x16x32_bf16 v[52:55], a[208:211], v[100:103], v[52:55]\n\t"
    "v_mfma_f32_16x16x32_bf16 v[52:55], a[228:231], v[104:107], v[52:55]\n\t"
    "v_mfma_f32_16x16x32_bf16 v[52:55], a[248:251], v[108:111], v[52:55]\n\t"
    "v_mfma_f32_16x16x32_bf16 v[56:59], a[92:95], v[96:99], v[56:59]\n\t"
    "ds_write_b128 v250, v[44:47] offset:18432\n\t"
    "v_mfma_f32_16x16x32_bf16 v[56:59], a[212:215], v[100:103], v[56:59]\n\t"
    "buffer_load_dwordx4 v[96:99], v242, s[28:31], s43 offen offset:0 sc1\n\t"
    "v_mfma_f32_16x16x32_bf16 v[56:59], a[232:235], v[104:107], v[56:59]\n\t"
    "v_mfma_f32_16x16x32_bf16 v[56:59], a[252:255], v[108:111], v[56:59]\n\t"
    "s_waitcnt vmcnt(9)\n\t"
    "v_mfma_f32_16x16x32_bf16 v[60:63], a[76:79], v[112:115], v[60:63]\n\t"
    "ds_write_b128 v250, v[48:51] offset:19456\n\t"
    "v_mfma_f32_16x16x32_bf16 v[60:63], a[196:199], v[116:119], v[60:63]\n\t"
    "buffer_load_dwordx4 v[100:103], v242, s[28:31], s43 offen offset:1024 sc1\n\t"
    "v_mfma_f32_16x16x32_bf16 v[60:63], a[216:219], v[120:123], v[60:63]\n\t"
    "v_mfma_f32_16x16x32_bf16 v[60:63], a[236:239], v[124:127], v[60:63]\n\t"
    "v_mfma_f32_16x16x32_bf16 v[64:67], a[80:83], v[112:115], v[64:67]\n\t"
    "ds_write_b128 v250, v[52:55] offset:32768\n\t"
    "v_mfma_f32_16x16x32_bf16 v[64:67], a[200:203], v[116:119], v[64:67]\n\t"
    "v_mfma_f32_16x16x32_bf16 v[64:67], a[220:223], v[120:123], v[64:67]\n\t"
    "buffer_load_dwordx4 v[104:107], v243, s[28:31], s43 offen offset:0 sc1\n\t"
    "v_mfma_f32_16x16x32_bf16 v[64:67], a[240:243], v[124:127], v[64:67]\n\t"
    "v_mfma_f32_16x16x32_bf16 v[68:71], a[84:87], v[112:115], v[68:71]\n\t"
    "ds_write_b128 v250, v[56:59] offset:33792\n\t"
    "v_mfma_f32_16x16x32_bf16 v[68:71], a[204:207], v[116:119], v[68:71]\n\t"
    "v_mfma_f32_16x16x32_bf16 v[68:71], a[224:227], v[120:123], v[68:71]\n\t"
    "buffer_load_dwordx4 v[108:111], v243, s[28:31], s43 offen offset:1024 sc1\n\t"
    "v_mfma_f32_16x16x32_bf16 v[68:71], a[244:247], v[124:127], v[68:71]\n\t"
    "v_mfma_f32_16x16x32_bf16 v[72:75], a[88:91], v[112:115], v[72:75]\n\t"
    "ds_write_b128 v250, v[60:63] offset:34816\n\t"
    "v_mfma_f32_16x16x32_bf16 v[72:75], a[208:211], v[116:119], v[72:75]\n\t"
    "v_mfma_f32_16x16x32_bf16 v[72:75], a[228:231], v[120:123], v[72:75]\n\t"
    "v_mfma_f32_16x16x32_bf16 v[72:75], a[248:251], v[124:127], v[72:75]\n\t"
    "v_mfma_f32_16x16x32_bf16 v[76:79], a[92:95], v[112:115], v[76:79]\n\t"
    "ds_write_b128 v250, v[64:67] offset:35840\n\t"
    "v_mfma_f32_16x16x32_bf16 v[76:79], a[212:215], v[116:119], v[76:79]\n\t"
    "s_add_u32 s43, s60, 8192\n\t"
    "buffer_load_dwordx4 v[112:115], v240, s[28:31], s43 offen offset:0 sc1\n\t"
    "v_mfma_f32_16x16x32_bf16 v[76:79], a[232:235], v[120:123], v[76:79]\n\t"
    "v_mfma_f32_16x16x32_bf16 v[76:79], a[252:255], v[124:127], v[76:79]\n\t"
    "s_waitcnt vmcnt(9)\n\t"
    "v_mfma_f32_16x16x32_bf16 v[220:223], a[76:79], v[192:195], v[220:223]\n\t"
    "ds_write_b128 v250, v[68:71] offset:49152\n\t"
    "v_mfma_f32_16x16x32_bf16 v[220:223], a[196:199], v[196:199], v[220:223]\n\t"
    "buffer_load_dwordx4 v[116:119], v240, s[28:31], s43 offen offset:1024 sc1\n\t"
    "v_mfma_f32_16x16x32_bf16 v[220:223], a[216:219], v[200:203], v[220:223]\n\t"
    "v_mfma_f32_16x16x32_bf16 v[220:223], a[236:239], v[204:207], v[220:223]\n\t"
    "v_mfma_f32_16x16x32_bf16 v[224:227], a[80:83], v[192:195], v[224:227]\n\t"
    "ds_write_b128 v250, v[72:75] offset:50176\n\t"
    "v_mfma_f32_16x16x32_bf16 v[224:227], a[200:203], v[196:199], v[224:227]\n\t"
    "v_mfma_f32_16x16x32_bf16 v[224:227], a[220:223], v[200:203], v[224:227]\n\t"
    "buffer_load_dwordx4 v[120:123], v241, s[28:31], s43 offen offset:0 sc1\n\t"
    "v_mfma_f32_16x16x32_bf16 v[224:227], a[240:243], v[204:207], v[224:227]\n\t"
    "v_mfma_f32_16x16x32_bf16 v[228:231], a[84:87], v[192:195], v[228:231]\n\t"
    "ds_write_b128 v250, v[76:79] offset:51200\n\t"
    "v_mfma_f32_16x16x32_bf16 v[228:231], a[204:207], v[196:199], v[228:231]\n\t"
    "v_mfma_f32_16x16x32_bf16 v[228:231], a[224:227], v[200:203], v[228:231]\n\t"
    "buffer_load_dwordx4 v[124:127], v241, s[28:31], s43 offen offset:1024 sc1\n\t"
    "v_mfma_f32_16x16x32_bf16 v[228:231], a[244:247], v[204:207], v[228:231]\n\t"
    "v_mfma_f32_16x16x32_bf16 v[232:235], a[88:91], v[192:195], v[232:235]\n\t"
    "v_mfma_f32_16x16x32_bf16 v[232:235], a[208:211], v[196:199], v[232:235]\n\t"
    "v_mfma_f32_16x16x32_bf16 v[232:235], a[228:231], v[200:203], v[232:235]\n\t"
    "v_mfma_f32_16x16x32_bf16 v[232:235], a[248:251], v[204:207], v[232:235]\n\t"
    "v_mfma_f32_16x16x32_bf16 v[236:239], a[92:95], v[192:195], v[236:239]\n\t"
    "v_mfma_f32_16x16x32_bf16 v[236:239], a[212:215], v[196:199], v[236:239]\n\t"
    "buffer_load_dwordx4 v[192:195], v242, s[28:31], s43 offen offset:0 sc1\n\t"
    "v_mfma_f32_16x16x32_bf16 v[236:239], a[232:235], v[200:203], v[236:239]\n\t"
    "v_mfma_f32_16x16x32_bf16 v[236:239], a[252:255], v[204:207], v[236:239]\n\t"
    "s_waitcnt vmcnt(12)\n\t"
    "v_mfma_f32_16x16x32_bf16 v[0:3], a[0:3], v[80:83], 0\n\t"
    "s_waitcnt vmcnt(11) lgkmcnt(0)\n\t"
    "v_mfma_f32_16x16x32_bf16 v[0:3], a[20:23], v[84:87], v[0:3]\n\t"
    "s_barrier\n\t"
    "v_mfma_f32_16x16x32_bf16 v[4:7], a[4:7], v[80:83], 0\n\t"
    "ds_read_b128 v[208:211], v251 offset:0\n\t"
    "v_mfma_f32_16x16x32_bf16 v[4:7], a[24:27], v[84:87], v[4:7]\n\t"
    "ds_read_b128 v[212:215], v252 offset:17408\n\t"
    "v_mfma_f32_16x16x32_bf16 v[8:11], a[8:11], v[80:83], 0\n\t"
    "ds_read_b128 v[216:219], v253 offset:34816\n\t"
    "v_mfma_f32_16x16x32_bf16 v[8:11], a[28:31], v[84:87], v[8:11]\n\t"
    "buffer_load_dwordx4 v[196:199], v242, s[28:31], s43 offen offset:1024 sc1\n\t"
    "v_mfma_f32_16x16x32_bf16 v[12:15], a[12:15], v[80:83], 0\n\t"
    "s_waitcnt lgkmcnt(0)\n\t"
    "v_add_f32 v220, v220, v208\n\t"
    "v_add_f32 v221, v221, v209\n\t"
    "v_add_f32 v222, v222, v210\n\t"
    "v_mfma_f32_16x16x32_bf16 v[12:15], a[32:35], v[84:87], v[12:15]\n\t"
    "v_add_f32 v223, v223, v211\n\t"
    "v_add_f32 v220, v220, v212\n\t"
    "v_add_f32 v221, v221, v213\n\t"
    "v_mfma_f32_16x16x32_bf16 v[16:19], a[16:19], v[80:83], 0\n\t"
    "v_add_f32 v222, v222, v214\n\t"
    "v_add_f32 v223, v223, v215\n\t"
    "v_add_f32 v220, v220, v216\n\t"
    "v_mfma_f32_16x16x32_bf16 v[16:19], a[36:39], v[84:87], v[16:19]\n\t"
    "s_waitcnt vmcnt(10)\n\t"
    "v_add_f32 v221, v221, v217\n\t"
    "v_add_f32 v222, v222, v218\n\t"
    "v_add_f32 v223, v223, v219\n\t"
    "v_mfma_f32_16x16x32_bf16 v[20:23], a[0:3], v[88:91], 0\n\t"
    "buffer_load_dwordx4 v[200:203], v243, s[28:31], s43 offen offset:0 sc1\n\t"
    "v_cvt_pk_bf16_f32 v220, v220, v221\n\t"
    "v_mfma_f32_16x16x32_bf16 v[20:23], a[20:23], v[92:95], v[20:23]\n\t"
    "v_cvt_pk_bf16_f32 v221, v222, v223\n\t"
    "v_mfma_f32_16x16x32_bf16 v[24:27], a[4:7], v[88:91], 0\n\t"
    "v_mfma_f32_16x16x32_bf16 v[24:27], a[24:27], v[92:95], v[24:27]\n\t"
    "v_mfma_f32_16x16x32_bf16 v[28:31], a[8:11], v[88:91], 0\n\t"
    "buffer_load_dwordx4 v[204:207], v243, s[28:31], s43 offen offset:1024 sc1\n\t"
    "v_mfma_f32_16x16x32_bf16 v[28:31], a[28:31], v[92:95], v[28:31]\n\t"
    "v_mfma_f32_16x16x32_bf16 v[32:35], a[12:15], v[88:91], 0\n\t"
    "ds_read_b128 v[208:211], v251 offset:1024\n\t"
    "v_mfma_f32_16x16x32_bf16 v[32:35], a[32:35], v[92:95], v[32:35]\n\t"
    "ds_read_b128 v[212:215], v252 offset:18432\n\t"
    "v_mfma_f32_16x16x32_bf16 v[36:39], a[16:19], v[88:91], 0\n\t"
    "ds_read_b128 v[216:219], v253 offset:35840\n\t"
    "v_mfma_f32_16x16x32_bf16 v[36:39], a[36:39], v[92:95], v[36:39]\n\t"
    "s_waitcnt vmcnt(11)\n\t"
    "s_add_u32 s43, s60, 16384\n\t"
    "buffer_load_dwordx4 v[80:83], v240, s[28:31], s43 offen offset:0 sc1\n\t"
    "v_mfma_f32_16x16x32_bf16 v[40:43], a[0:3], v[96:99], 0\n\t"
    "s_waitcnt vmcnt(11) lgkmcnt(0)\n\t"
    "v_add_f32 v224, v224, v208\n\t"
    "v_add_f32 v225, v225, v209\n\t"
    "v_add_f32 v226, v226, v210\n\t"
    "v_mfma_f32_16x16x32_bf16 v[40:43], a[20:23], v[100:103], v[40:43]\n\t"
    "v_add_f32 v227, v227, v211\n\t"
    "v_add_f32 v224, v224, v212\n\t"
    "v_add_f32 v225, v225, v213\n\t"
    "v_mfma_f32_16x16x32_bf16 v[44:47], a[4:7], v[96:99], 0\n\t"
    "v_add_f32 v226, v226, v214\n\t"
    "v_add_f32 v227, v227, v215\n\t"
    "v_add_f32 v224, v224, v216\n\t"
    "v_mfma_f32_16x16x32_bf16 v[44:47], a[24:27], v[100:103], v[44:47]\n\t"
    "buffer_load_dwordx4 v[84:87], v240, s[28:31], s43 offen offset:1024 sc1\n\t"
    "v_add_f32 v225, v225, v217\n\t"
    "v_mfma_f32_16x16x32_bf16 v[48:51], a[8:11], v[96:99], 0\n\t"
    "v_add_f32 v226, v226, v218\n\t"
    "v_add_f32 v227, v227, v219\n\t"
    "v_cvt_pk_bf16_f32 v222, v224, v225\n\t"
    "v_mfma_f32_16x16x32_bf16 v[48:51], a[28:31], v[100:103], v[48:51]\n\t"
    "v_cvt_pk_bf16_f32 v223, v226, v227\n\t"
    "v_mfma_f32_16x16x32_bf16 v[52:55], a[12:15], v[96:99], 0\n\t"
    "v_mfma_f32_16x16x32_bf16 v[52:55], a[32:35], v[100:103], v[52:55]\n\t"
    "v_mfma_f32_16x16x32_bf16 v[56:59], a[16:19], v[96:99], 0\n\t"
    "buffer_load_dwordx4 v[88:91], v241, s[28:31], s43 offen offset:0 sc1\n\t"
    "v_mfma_f32_16x16x32_bf16 v[56:59], a[36:39], v[100:103], v[56:59]\n\t"
    "s_waitcnt vmcnt(11)\n\t"
    "v_mfma_f32_16x16x32_bf16 v[60:63], a[0:3], v[104:107], 0\n\t"
    "buffer_store_dwordx4 v[220:223], v254, s[28:31], s1 offen offset:0 sc1\n\t"
    "ds_read_b128 v[208:211], v251 offset:2048\n\t"
    "v_mfma_f32_16x16x32_bf16 v[60:63], a[20:23], v[108:111], v[60:63]\n\t"
    "ds_read_b128 v[212:215], v252 offset:19456\n\t"
    "v_mfma_f32_16x16x32_bf16 v[64:67], a[4:7], v[104:107], 0\n\t"
    "ds_read_b128 v[216:219], v253 offset:49152\n\t"
    "v_mfma_f32_16x16x32_bf16 v[64:67], a[24:27], v[108:111], v[64:67]\n\t"
    "buffer_load_dwordx4 v[92:95], v241, s[28:31], s43 offen offset:1024 sc1\n\t"
    "v_mfma_f32_16x16x32_bf16 v[68:71], a[8:11], v[104:107], 0\n\t"
    "s_waitcnt lgkmcnt(0)\n\t"
    "v_add_f32 v228, v228, v208\n\t"
    "v_add_f32 v229, v229, v209\n\t"
    "v_add_f32 v230, v230, v210\n\t"
    "v_mfma_f32_16x16x32_bf16 v[68:71], a[28:31], v[108:111], v[68:71]\n\t"
    "v_add_f32 v231, v231, v211\n\t"
    "v_add_f32 v228, v228, v212\n\t"
    "v_add_f32 v229, v229, v213\n\t"
    "v_mfma_f32_16x16x32_bf16 v[72:75], a[12:15], v[104:107], 0\n\t"
    "v_add_f32 v230, v230, v214\n\t"
    "v_add_f32 v231, v231, v215\n\t"
    "v_add_f32 v228, v228, v216\n\t"
    "v_mfma_f32_16x16x32_bf16 v[72:75], a[32:35], v[108:111], v[72:75]\n\t"
    "v_add_f32 v229, v229, v217\n\t"
    "v_add_f32 v230, v230, v218\n\t"
    "v_add_f32 v231, v231, v219\n\t"
    "v_mfma_f32_16x16x32_bf16 v[76:79], a[16:19], v[104:107], 0\n\t"
    "buffer_load_dwordx4 v[96:99], v242, s[28:31], s43 offen offset:0 sc1\n\t"
    "v_cvt_pk_bf16_f32 v224, v228, v229\n\t"
    "v_mfma_f32_16x16x32_bf16 v[76:79], a[36:39], v[108:111], v[76:79]\n\t"
    "s_waitcnt vmcnt(12)\n\t"
    "v_cvt_pk_bf16_f32 v225, v230, v231\n\t"
    "v_mfma_f32_16x16x32_bf16 v[0:3], a[40:43], v[112:115], v[0:3]\n\t"
    "v_mfma_f32_16x16x32_bf16 v[0:3], a[108:111], v[116:119], v[0:3]\n\t"
    "v_mfma_f32_16x16x32_bf16 v[4:7], a[44:47], v[112:115], v[4:7]\n\t"
    "buffer_load_dwordx4 v[100:103], v242, s[28:31], s43 offen offset:1024 sc1\n\t"
    "v_mfma_f32_16x16x32_bf16 v[4:7], a[112:115], v[116:119], v[4:7]\n\t"
    "v_mfma_f32_16x16x32_bf16 v[8:11], a[96:99], v[112:115], v[8:11]\n\t"
    "ds_read_b128 v[208:211], v251 offset:3072\n\t"
    "v_mfma_f32_16x16x32_bf16 v[8:11], a[116:119], v[116:119], v[8:11]\n\t"
    "ds_read_b128 v[212:215], v252 offset:32768\n\t"
    "v_mfma_f32_16x16x32_bf16 v[12:15], a[100:103], v[112:115], v[12:15]\n\t"
    "ds_read_b128 v[216:219], v253 offset:50176\n\t"
    "v_mfma_f32_16x16x32_bf16 v[12:15], a[120:123], v[116:119], v[12:15]\n\t"
    "buffer_load_dwordx4 a[0:3], v243, s[28:31], s43 offen offset:0 sc1\n\t"
    "v_mfma_f32_16x16x32_bf16 v[16:19], a[104:107], v[112:115], v[16:19]\n\t"
    "s_waitcnt lgkmcnt(2)\n\t"
    "v_add_f32 v232, v232, v208\n\t"
    "v_add_f32 v233, v233, v209\n\t"
    "v_add_f32 v234, v234, v210\n\t"
    "v_mfma_f32_16x16x32_bf16 v[16:19], a[124:127], v[116:119], v[16:19]\n\t"
    "s_waitcnt vmcnt(12) lgkmcnt(0)\n\t"
    "v_add_f32 v235, v235, v211\n\t"
    "v_add_f32 v232, v232, v212\n\t"
    "v_add_f32 v233, v233, v213\n\t"
    "v_mfma_f32_16x16x32_bf16 v[20:23], a[40:43], v[120:123], v[20:23]\n\t"
    "v_add_f32 v234, v234, v214\n\t"
    "v_add_f32 v235, v235, v215\n\t"
    "v_add_f32 v232, v232, v216\n\t"
    "v_mfma_f32_16x16x32_bf16 v[20:23], a[108:111], v[124:127], v[20:23]\n\t"
    "buffer_load_dwordx4 a[20:23], v243, s[28:31], s43 offen offset:1024 sc1\n\t"
    "v_add_f32 v233, v233, v217\n\t"
    "v_mfma_f32_16x16x32_bf16 v[24:27], a[44:47], v[120:123], v[24:27]\n\t"
    "v_add_f32 v234, v234, v218\n\t"
    "v_add_f32 v235, v235, v219\n\t"
    "v_cvt_pk_bf16_f32 v226, v232, v233\n\t"
    "v_mfma_f32_16x16x32_bf16 v[24:27], a[112:115], v[124:127], v[24:27]\n\t"
    "v_cvt_pk_bf16_f32 v227, v234, v235\n\t"
    "v_mfma_f32_16x16x32_bf16 v[28:31], a[96:99], v[120:123], v[28:31]\n\t"
    "v_mfma_f32_16x16x32_bf16 v[28:31], a[116:119], v[124:127], v[28:31]\n\t"
    "v_mfma_f32_16x16x32_bf16 v[32:35], a[100:103], v[120:123], v[32:35]\n\t"
    "s_add_u32 s43, s60, 24576\n\t"
    "buffer_load_dwordx4 a[4:7], v240, s[28:31], s43 offen offset:0 sc1\n\t"
    "v_mfma_f32_16x16x32_bf16 v[32:35], a[120:123], v[124:127], v[32:35]\n\t"
    "v_mfma_f32_16x16x32_bf16 v[36:39], a[104:107], v[120:123], v[36:39]\n\t"
    "buffer_store_dwordx4 v[224:227], v254, s[28:31], s1 offen offset:1024 sc1\n\t"
    "ds_read_b128 v[208:211], v251 offset:16384\n\t"
    "v_mfma_f32_16x16x32_bf16 v[36:39], a[124:127], v[124:127], v[36:39]\n\t"
    "s_waitcnt vmcnt(13)\n\t"
    "ds_read_b128 v[212:215], v252 offset:33792\n\t"
    "v_mfma_f32_16x16x32_bf16 v[40:43], a[40:43], v[192:195], v[40:43]\n\t"
    "ds_read_b128 v[216:219], v253 offset:51200\n\t"
    "v_mfma_f32_16x16x32_bf16 v[40:43], a[108:111], v[196:199], v[40:43]\n\t"
    "buffer_load_dwordx4 a[24:27], v240, s[28:31], s43 offen offset:1024 sc1\n\t"
    "v_mfma_f32_16x16x32_bf16 v[44:47], a[44:47], v[192:195], v[44:47]\n\t"
    "s_waitcnt lgkmcnt(0)\n\t"
    "v_add_f32 v236, v236, v208\n\t"
    "v_add_f32 v237, v237, v209\n\t"
    "v_add_f32 v238, v238, v210\n\t"
    "v_mfma_f32_16x16x32_bf16 v[44:47], a[112:115], v[196:199], v[44:47]\n\t"
    "v_add_f32 v239, v239, v211\n\t"
    "v_add_f32 v236, v236, v212\n\t"
    "v_add_f32 v237, v237, v213\n\t"
    "v_mfma_f32_16x16x32_bf16 v[48:51], a[96:99], v[192:195], v[48:51]\n\t"
    "v_add_f32 v238, v238, v214\n\t"
    "v_add_f32 v239, v239, v215\n\t"
    "v_add_f32 v236, v236, v216\n\t"
    "v_mfma_f32_16x16x32_bf16 v[48:51], a[116:119], v[196:199], v[48:51]\n\t"
    "v_add_f32 v237, v237, v217\n\t"
    "v_add_f32 v238, v238, v218\n\t"
    "v_add_f32 v239, v239, v219\n\t"
    "v_mfma_f32_16x16x32_bf16 v[52:55], a[100:103], v[192:195], v[52:55]\n\t"
    "buffer_load_dwordx4 a[8:11], v241, s[28:31], s43 offen offset:0 sc1\n\t"
    "v_cvt_pk_bf16_f32 v228, v236, v237\n\t"
    "v_mfma_f32_16x16x32_bf16 v[52:55], a[120:123], v[196:199], v[52:55]\n\t"
    "v_cvt_pk_bf16_f32 v229, v238, v239\n\t"
    "v_mfma_f32_16x16x32_bf16 v[56:59], a[104:107], v[192:195], v[56:59]\n\t"
    "v_mfma_f32_16x16x32_bf16 v[56:59], a[124:127], v[196:199], v[56:59]\n\t"
    "s_waitcnt vmcnt(13)\n\t"
    "v_mfma_f32_16x16x32_bf16 v[60:63], a[40:43], v[200:203], v[60:63]\n\t"
    "buffer_load_dwordx4 a[28:31], v241, s[28:31], s43 offen offset:1024 sc1\n\t"
    "v_mfma_f32_16x16x32_bf16 v[60:63], a[108:111], v[204:207], v[60:63]\n\t"
    "v_mfma_f32_16x16x32_bf16 v[64:67], a[44:47], v[200:203], v[64:67]\n\t"
    "buffer_store_dwordx4 v[228:231], v254, s[28:31], s1 offen offset:2048 sc1\n\t"
    "v_mfma_f32_16x16x32_bf16 v[64:67], a[112:115], v[204:207], v[64:67]\n\t"
    "v_mfma_f32_16x16x32_bf16 v[68:71], a[96:99], v[200:203], v[68:71]\n\t"
    "v_mfma_f32_16x16x32_bf16 v[68:71], a[116:119], v[204:207], v[68:71]\n\t"
    "buffer_load_dwordx4 a[12:15], v242, s[28:31], s43 offen offset:0 sc1\n\t"
    "v_mfma_f32_16x16x32_bf16 v[72:75], a[100:103], v[200:203], v[72:75]\n\t"
    "v_mfma_f32_16x16x32_bf16 v[72:75], a[120:123], v[204:207], v[72:75]\n\t"
    "v_mfma_f32_16x16x32_bf16 v[76:79], a[104:107], v[200:203], v[76:79]\n\t"
    "v_mfma_f32_16x16x32_bf16 v[76:79], a[124:127], v[204:207], v[76:79]\n\t"
    "s_waitcnt vmcnt(14)\n\t"
    "buffer_load_dwordx4 v[104:107], v242, s[28:31], s43 offen offset:1024 sc1\n\t"
    "v_mfma_f32_16x16x32_bf16 v[0:3], a[128:131], v[80:83], v[0:3]\n\t"
    "v_mfma_f32_16x16x32_bf16 v[0:3], v[132:135], v[84:87], v[0:3]\n\t"
    "v_mfma_f32_16x16x32_bf16 v[4:7], a[132:135], v[80:83], v[4:7]\n\t"
    "v_mfma_f32_16x16x32_bf16 v[4:7], v[136:139], v[84:87], v[4:7]\n\t"
    "v_mfma_f32_16x16x32_bf16 v[8:11], a[136:139], v[80:83], v[8:11]\n\t"
    "buffer_load_dwordx4 a[32:35], v243, s[28:31], s43 offen offset:0 sc1\n\t"
    "v_mfma_f32_16x16x32_bf16 v[8:11], v[140:143], v[84:87], v[8:11]\n\t"
    "v_mfma_f32_16x16x32_bf16 v[12:15], a[140:143], v[80:83], v[12:15]\n\t"
    "v_mfma_f32_16x16x32_bf16 v[12:15], v[144:147], v[84:87], v[12:15]\n\t"
    "v_mfma_f32_16x16x32_bf16 v[16:19], v[128:131], v[80:83], v[16:19]\n\t"
    "buffer_load_dwordx4 v[108:111], v243, s[28:31], s43 offen offset:1024 sc1\n\t"
    "v_mfma_f32_16x16x32_bf16 v[16:19], v[148:151], v[84:87], v[16:19]\n\t"
    "s_waitcnt vmcnt(14)\n\t"
    "v_mfma_f32_16x16x32_bf16 v[20:23], a[128:131], v[88:91], v[20:23]\n\t"
    "v_mfma_f32_16x16x32_bf16 v[20:23], v[132:135], v[92:95], v[20:23]\n\t"
    "v_mfma_f32_16x16x32_bf16 v[24:27], a[132:135], v[88:91], v[24:27]\n\t"
    "v_mfma_f32_16x16x32_bf16 v[24:27], v[136:139], v[92:95], v[24:27]\n\t"
    "s_add_u32 s43, s60, 32768\n\t"
    "buffer_load_dwordx4 a[16:19], v240, s[28:31], s43 offen offset:0 sc1\n\t"
    "v_mfma_f32_16x16x32_bf16 v[28:31], a[136:139], v[88:91], v[28:31]\n\t"
    "v_mfma_f32_16x16x32_bf16 v[28:31], v[140:143], v[92:95], v[28:31]\n\t"
    "v_mfma_f32_16x16x32_bf16 v[32:35], a[140:143], v[88:91], v[32:35]\n\t"
    "v_mfma_f32_16x16x32_bf16 v[32:35], v[144:147], v[92:95], v[32:35]\n\t"
    "buffer_load_dwordx4 a[36:39], v240, s[28:31], s43 offen offset:1024 sc1\n\t"
    "v_mfma_f32_16x16x32_bf16 v[36:39], v[128:131], v[88:91], v[36:39]\n\t"
    "v_mfma_f32_16x16x32_bf16 v[36:39], v[148:151], v[92:95], v[36:39]\n\t"
    "s_waitcnt vmcnt(14)\n\t"
    "v_mfma_f32_16x16x32_bf16 v[40:43], a[128:131], v[96:99], v[40:43]\n\t"
    "v_mfma_f32_16x16x32_bf16 v[40:43], v[132:135], v[100:103], v[40:43]\n\t"
    "v_mfma_f32_16x16x32_bf16 v[44:47], a[132:135], v[96:99], v[44:47]\n\t"
    "buffer_load_dwordx4 v[112:115], v241, s[28:31], s43 offen offset:0 sc1\n\t"
    "v_mfma_f32_16x16x32_bf16 v[44:47], v[136:139], v[100:103], v[44:47]\n\t"
    "v_mfma_f32_16x16x32_bf16 v[48:51], a[136:139], v[96:99], v[48:51]\n\t"
    "v_mfma_f32_16x16x32_bf16 v[48:51], v[140:143], v[100:103], v[48:51]\n\t"
    "v_mfma_f32_16x16x32_bf16 v[52:55], a[140:143], v[96:99], v[52:55]\n\t"
    "buffer_load_dwordx4 v[116:119], v241, s[28:31], s43 offen offset:1024 sc1\n\t"
    "v_mfma_f32_16x16x32_bf16 v[52:55], v[144:147], v[100:103], v[52:55]\n\t"
    "v_mfma_f32_16x16x32_bf16 v[56:59], v[128:131], v[96:99], v[56:59]\n\t"
    "v_mfma_f32_16x16x32_bf16 v[56:59], v[148:151], v[100:103], v[56:59]\n\t"
    "s_waitcnt vmcnt(14)\n\t"
    "v_mfma_f32_16x16x32_bf16 v[60:63], a[128:131], a[0:3], v[60:63]\n\t"
    "v_mfma_f32_16x16x32_bf16 v[60:63], v[132:135], a[20:23], v[60:63]\n\t"
    "buffer_load_dwordx4 v[120:123], v242, s[28:31], s43 offen offset:0 sc1\n\t"
    "v_mfma_f32_16x16x32_bf16 v[64:67], a[132:135], a[0:3], v[64:67]\n\t"
    "v_mfma_f32_16x16x32_bf16 v[64:67], v[136:139], a[20:23], v[64:67]\n\t"
    "v_mfma_f32_16x16x32_bf16 v[68:71], a[136:139], a[0:3], v[68:71]\n\t"
    "v_mfma_f32_16x16x32_bf16 v[68:71], v[140:143], a[20:23], v[68:71]\n\t"
    "buffer_load_dwordx4 v[124:127], v242, s[28:31], s43 offen offset:1024 sc1\n\t"
    "v_mfma_f32_16x16x32_bf16 v[72:75], a[140:143], a[0:3], v[72:75]\n\t"
    "v_mfma_f32_16x16x32_bf16 v[72:75], v[144:147], a[20:23], v[72:75]\n\t"
    "v_mfma_f32_16x16x32_bf16 v[76:79], v[128:131], a[0:3], v[76:79]\n\t"
    "v_mfma_f32_16x16x32_bf16 v[76:79], v[148:151], a[20:23], v[76:79]\n\t"
    "s_waitcnt vmcnt(13)\n\t"
    "v_mfma_f32_16x16x32_bf16 v[0:3], v[152:155], a[4:7], v[0:3]\n\t"
    "buffer_load_dwordx4 v[192:195], v243, s[28:31], s43 offen offset:0 sc1\n\t"
    "v_mfma_f32_16x16x32_bf16 v[0:3], v[172:175], a[24:27], v[0:3]\n\t"
    "v_mfma_f32_16x16x32_bf16 v[4:7], v[156:159], a[4:7], v[4:7]\n\t"
    "v_mfma_f32_16x16x32_bf16 v[4:7], v[176:179], a[24:27], v[4:7]\n\t"
    "v_mfma_f32_16x16x32_bf16 v[8:11], v[160:163], a[4:7], v[8:11]\n\t"
    "buffer_load_dwordx4 v[196:199], v243, s[28:31], s43 offen offset:1024 sc1\n\t"
    "v_mfma_f32_16x16x32_bf16 v[8:11], v[180:183], a[24:27], v[8:11]\n\t"
    "v_mfma_f32_16x16x32_bf16 v[12:15], v[164:167], a[4:7], v[12:15]\n\t"
    "v_mfma_f32_16x16x32_bf16 v[12:15], v[184:187], a[24:27], v[12:15]\n\t"
    "v_mfma_f32_16x16x32_bf16 v[16:19], v[168:171], a[4:7], v[16:19]\n\t"
    "v_mfma_f32_16x16x32_bf16 v[16:19], v[188:191], a[24:27], v[16:19]\n\t"
    "s_waitcnt vmcnt(12)\n\t"
    "s_add_u32 s43, s60, 40960\n\t"
    "buffer_load_dwordx4 v[208:211], v240, s[28:31], s43 offen offset:0 sc1\n\t"
    "v_mfma_f32_16x16x32_bf16 v[20:23], v[152:155], a[8:11], v[20:23]\n\t"
    "v_mov_b32 v220, s96\n\t"
    "v_lshlrev_b32 v221, 26, v255\n\t"
    "v_mfma_f32_16x16x32_bf16 v[20:23], v[172:175], a[28:31], v[20:23]\n\t"
    "s_add_u32 s1, s1, 4096\n\t"
    "v_mfma_f32_16x16x32_bf16 v[24:27], v[156:159], a[8:11], v[24:27]\n\t"
    "buffer_store_dword v220, v221, s[28:31], s1 offen offset:1024 sc1\n\t"
    "v_mfma_f32_16x16x32_bf16 v[24:27], v[176:179], a[28:31], v[24:27]\n\t"
    "buffer_load_dwordx4 v[212:215], v240, s[28:31], s43 offen offset:1024 sc1\n\t"
    "v_mfma_f32_16x16x32_bf16 v[28:31], v[160:163], a[8:11], v[28:31]\n\t"
    "s_barrier\n\t"
    "v_mfma_f32_16x16x32_bf16 v[28:31], v[180:183], a[28:31], v[28:31]\n\t"
    "v_mfma_f32_16x16x32_bf16 v[32:35], v[164:167], a[8:11], v[32:35]\n\t"
    "v_mfma_f32_16x16x32_bf16 v[32:35], v[184:187], a[28:31], v[32:35]\n\t"
    "v_mfma_f32_16x16x32_bf16 v[36:39], v[168:171], a[8:11], v[36:39]\n\t"
    "buffer_load_dwordx4 v[216:219], v241, s[28:31], s43 offen offset:0 sc1\n\t"
    "v_mfma_f32_16x16x32_bf16 v[36:39], v[188:191], a[28:31], v[36:39]\n\t"
    "s_waitcnt vmcnt(14)\n\t"
    "v_mfma_f32_16x16x32_bf16 v[40:43], v[152:155], a[12:15], v[40:43]\n\t"
    "v_mfma_f32_16x16x32_bf16 v[40:43], v[172:175], v[104:107], v[40:43]\n\t"
    "v_mfma_f32_16x16x32_bf16 v[44:47], v[156:159], a[12:15], v[44:47]\n\t"
    "buffer_load_dwordx4 a[40:43], v241, s[28:31], s43 offen offset:1024 sc1\n\t"
    "v_mfma_f32_16x16x32_bf16 v[44:47], v[176:179], v[104:107], v[44:47]\n\t"
    "v_mfma_f32_16x16x32_bf16 v[48:51], v[160:163], a[12:15], v[48:51]\n\t"
    "v_mfma_f32_16x16x32_bf16 v[48:51], v[180:183], v[104:107], v[48:51]\n\t"
    "v_mfma_f32_16x16x32_bf16 v[52:55], v[164:167], a[12:15], v[52:55]\n\t"
    "v_mfma_f32_16x16x32_bf16 v[52:55], v[184:187], v[104:107], v[52:55]\n\t"
    "buffer_load_dwordx4 a[108:111], v242, s[28:31], s43 offen offset:0 sc1\n\t"
    "v_mfma_f32_16x16x32_bf16 v[56:59], v[168:171], a[12:15], v[56:59]\n\t"
    "v_mfma_f32_16x16x32_bf16 v[56:59], v[188:191], v[104:107], v[56:59]\n\t"
    "s_waitcnt vmcnt(14)\n\t"
    "v_mfma_f32_16x16x32_bf16 v[60:63], v[152:155], a[32:35], v[60:63]\n\t"
    "v_mfma_f32_16x16x32_bf16 v[60:63], v[172:175], v[108:111], v[60:63]\n\t"
    "buffer_load_dwordx4 a[44:47], v242, s[28:31], s43 offen offset:1024 sc1\n\t"
    "v_mfma_f32_16x16x32_bf16 v[64:67], v[156:159], a[32:35], v[64:67]\n\t"
    "v_mfma_f32_16x16x32_bf16 v[64:67], v[176:179], v[108:111], v[64:67]\n\t"
    "v_mfma_f32_16x16x32_bf16 v[68:71], v[160:163], a[32:35], v[68:71]\n\t"
    "v_mfma_f32_16x16x32_bf16 v[68:71], v[180:183], v[108:111], v[68:71]\n\t"
    "v_mfma_f32_16x16x32_bf16 v[72:75], v[164:167], a[32:35], v[72:75]\n\t"
    "buffer_load_dwordx4 a[112:115], v243, s[28:31], s43 offen offset:0 sc1\n\t"
    "v_mfma_f32_16x16x32_bf16 v[72:75], v[184:187], v[108:111], v[72:75]\n\t"
    "v_mfma_f32_16x16x32_bf16 v[76:79], v[168:171], a[32:35], v[76:79]\n\t"
    "v_mfma_f32_16x16x32_bf16 v[76:79], v[188:191], v[108:111], v[76:79]\n\t"
    "s_waitcnt vmcnt(14)\n\t"
    "v_mfma_f32_16x16x32_bf16 v[0:3], a[144:147], a[16:19], v[0:3]\n\t"
    "buffer_load_dwordx4 a[96:99], v243, s[28:31], s43 offen offset:1024 sc1\n\t"
    "v_mfma_f32_16x16x32_bf16 v[0:3], a[164:167], a[36:39], v[0:3]\n\t"
    "v_mfma_f32_16x16x32_bf16 v[4:7], a[148:151], a[16:19], v[4:7]\n\t"
    "ds_read_b64 v[178:179], v247 offset:0\n\t"
    "v_mfma_f32_16x16x32_bf16 v[4:7], a[168:171], a[36:39], v[4:7]\n\t"
    "v_mfma_f32_16x16x32_bf16 v[8:11], a[152:155], a[16:19], v[8:11]\n\t"
    "ds_read_b64 v[180:181], v247 offset:32\n\t"
    "v_mfma_f32_16x16x32_bf16 v[8:11], a[172:175], a[36:39], v[8:11]\n\t"
    "s_add_u32 s43, s60, 49152\n\t"
    "buffer_load_dwordx4 a[116:119], v241, s[28:31], s43 offen offset:0 sc1\n\t"
    "v_mfma_f32_16x16x32_bf16 v[12:15], a[156:159], a[16:19], v[12:15]\n\t"
    "v_mfma_f32_16x16x32_bf16 v[12:15], a[176:179], a[36:39], v[12:15]\n\t"
    "ds_read_b64 v[182:183], v247 offset:64\n\t"
    "v_mfma_f32_16x16x32_bf16 v[16:19], a[160:163], a[16:19], v[16:19]\n\t"
    "v_mfma_f32_16x16x32_bf16 v[16:19], a[180:183], a[36:39], v[16:19]\n\t"
    "s_waitcnt vmcnt(14)\n\t"
    "buffer_load_dwordx4 a[100:103], v241, s[28:31], s43 offen offset:1024 sc1\n\t"
    "v_mfma_f32_16x16x32_bf16 v[20:23], a[144:147], v[112:115], v[20:23]\n\t"
    "ds_read_b64 v[184:185], v247 offset:96\n\t"
    "v_mfma_f32_16x16x32_bf16 v[20:23], a[164:167], v[116:119], v[20:23]\n\t"
    "v_mfma_f32_16x16x32_bf16 v[24:27], a[148:151], v[112:115], v[24:27]\n\t"
    "v_mfma_f32_16x16x32_bf16 v[24:27], a[168:171], v[116:119], v[24:27]\n\t"
    "ds_read_b64 v[186:187], v247 offset:128\n\t"
    "v_mfma_f32_16x16x32_bf16 v[28:31], a[152:155], v[112:115], v[28:31]\n\t"
    "s_add_u32 s43, s60, 57344\n\t"
    "buffer_load_dwordx4 v[200:203], v241, s[28:31], s43 offen offset:0 sc1\n\t"
    "v_mfma_f32_16x16x32_bf16 v[28:31], a[172:175], v[116:119], v[28:31]\n\t"
    "v_mfma_f32_16x16x32_bf16 v[32:35], a[156:159], v[112:115], v[32:35]\n\t"
    "ds_read_b128 v[128:131], v248 offset:3072\n\t"
    "v_mfma_f32_16x16x32_bf16 v[32:35], a[176:179], v[116:119], v[32:35]\n\t"
    "v_mfma_f32_16x16x32_bf16 v[36:39], a[160:163], v[112:115], v[36:39]\n\t"
    "buffer_load_dwordx4 a[120:123], v241, s[28:31], s43 offen offset:1024 sc1\n\t"
    "v_mfma_f32_16x16x32_bf16 v[36:39], a[180:183], v[116:119], v[36:39]\n\t"
    "s_waitcnt vmcnt(15)\n\t"
    "ds_read_b128 v[132:135], v248 offset:3136\n\t"
    "v_mfma_f32_16x16x32_bf16 v[40:43], a[144:147], v[120:123], v[40:43]\n\t"
    "v_mfma_f32_16x16x32_bf16 v[40:43], a[164:167], v[124:127], v[40:43]\n\t"
    "ds_read_b128 v[136:139], v248 offset:3200\n\t"
    "v_mfma_f32_16x16x32_bf16 v[44:47], a[148:151], v[120:123], v[44:47]\n\t"
    "v_mfma_f32_16x16x32_bf16 v[44:47], a[168:171], v[124:127], v[44:47]\n\t"
    "s_add_u32 s43, s60, 49152\n\t"
    "buffer_load_dwordx4 v[204:207], v242, s[28:31], s43 offen offset:0 sc1\n\t"
    "v_mfma_f32_16x16x32_bf16 v[48:51], a[152:155], v[120:123], v[48:51]\n\t"
    "v_mfma_f32_16x16x32_bf16 v[48:51], a[172:175], v[124:127], v[48:51]\n\t"
    "ds_read_b128 v[140:143], v248 offset:3264\n\t"
    "v_mfma_f32_16x16x32_bf16 v[52:55], a[156:159], v[120:123], v[52:55]\n\t"
    "v_mfma_f32_16x16x32_bf16 v[52:55], a[176:179], v[124:127], v[52:55]\n\t"
    "buffer_load_dwordx4 a[104:107], v242, s[28:31], s43 offen offset:1024 sc1\n\t"
    "v_mfma_f32_16x16x32_bf16 v[56:59], a[160:163], v[120:123], v[56:59]\n\t"
    "ds_read_b128 v[144:147], v248 offset:3328\n\t"
    "v_mfma_f32_16x16x32_bf16 v[56:59], a[180:183], v[124:127], v[56:59]\n\t"
    "s_waitcnt vmcnt(15)\n\t"
    "v_mfma_f32_16x16x32_bf16 v[60:63], a[144:147], v[192:195], v[60:63]\n\t"
    "ds_read_b128 v[148:151], v248 offset:3392\n\t"
    "v_mfma_f32_16x16x32_bf16 v[60:63], a[164:167], v[196:199], v[60:63]\n\t"
    "v_mfma_f32_16x16x32_bf16 v[64:67], a[148:151], v[192:195], v[64:67]\n\t"
    "s_add_u32 s43, s60, 57344\n\t"
    "buffer_load_dwordx4 a[124:127], v242, s[28:31], s43 offen offset:0 sc1\n\t"
    "v_mfma_f32_16x16x32_bf16 v[64:67], a[168:171], v[196:199], v[64:67]\n\t"
    "ds_read_b128 v[152:155], v248 offset:3456\n\t"
    "v_mfma_f32_16x16x32_bf16 v[68:71], a[152:155], v[192:195], v[68:71]\n\t"
    "v_mfma_f32_16x16x32_bf16 v[68:71], a[172:175], v[196:199], v[68:71]\n\t"
    "v_mfma_f32_16x16x32_bf16 v[72:75], a[156:159], v[192:195], v[72:75]\n\t"
    "buffer_load_dwordx4 v[80:83], v242, s[28:31], s43 offen offset:1024 sc1\n\t"
    "v_mfma_f32_16x16x32_bf16 v[72:75], a[176:179], v[196:199], v[72:75]\n\t"
    "ds_read_b128 v[156:159], v248 offset:3520\n\t"
    "v_mfma_f32_16x16x32_bf16 v[76:79], a[160:163], v[192:195], v[76:79]\n\t"
    "v_mfma_f32_16x16x32_bf16 v[76:79], a[180:183], v[196:199], v[76:79]\n\t"
    "s_waitcnt vmcnt(14)\n\t"
    "ds_read_b128 v[160:163], v248 offset:3584\n\t"
    "v_mfma_f32_16x16x32_bf16 v[0:3], a[184:187], v[208:211], v[0:3]\n\t"
    "v_mfma_f32_16x16x32_bf16 v[0:3], a[56:59], v[212:215], v[0:3]\n\t"
    "s_add_u32 s43, s60, 49152\n\t"
    "buffer_load_dwordx4 v[84:87], v243, s[28:31], s43 offen offset:0 sc1\n\t"
    "v_mfma_f32_16x16x32_bf16 v[4:7], a[188:191], v[208:211], v[4:7]\n\t"
    "ds_read_b128 v[164:167], v248 offset:3648\n\t"
    "v_mfma_f32_16x16x32_bf16 v[4:7], a[60:63], v[212:215], v[4:7]\n\t"
    "v_mfma_f32_16x16x32_bf16 v[8:11], a[192:195], v[208:211], v[8:11]\n\t"
    "v_mfma_f32_16x16x32_bf16 v[8:11], a[64:67], v[212:215], v[8:11]\n\t"
    "buffer_load_dwordx4 v[88:91], v243, s[28:31], s43 offen offset:1024 sc1\n\t"
    "v_mfma_f32_16x16x32_bf16 v[12:15], a[48:51], v[208:211], v[12:15]\n\t"
    "v_mfma_f32_16x16x32_bf16 v[12:15], a[68:71], v[212:215], v[12:15]\n\t"
    "v_mfma_f32_16x16x32_bf16 v[16:19], a[52:55], v[208:211], v[16:19]\n\t"
    "v_mfma_f32_16x16x32_bf16 v[16:19], a[72:75], v[212:215], v[16:19]\n\t"
    "s_waitcnt vmcnt(14)\n\t"
    "v_mfma_f32_16x16x32_bf16 v[20:23], a[184:187], v[216:219], v[20:23]\n\t"
    "s_add_u32 s1, s95, 5120\n\t"
    "buffer_load_dword v224, off, s[28:31], s1 sc1\n\t"
    "v_mfma_f32_16x16x32_bf16 v[20:23], a[56:59], a[40:43], v[20:23]\n\t"
    "s_add_u32 s43, s60, 57344\n\t"
    "buffer_load_dwordx4 v[92:95], v243, s[28:31], s43 offen offset:0 sc1\n\t"
    "v_mfma_f32_16x16x32_bf16 v[24:27], a[188:191], v[216:219], v[24:27]\n\t"
    "v_mfma_f32_16x16x32_bf16 v[24:27], a[60:63], a[40:43], v[24:27]\n\t"
    "v_mfma_f32_16x16x32_bf16 v[28:31], a[192:195], v[216:219], v[28:31]\n\t"
    "v_mfma_f32_16x16x32_bf16 v[28:31], a[64:67], a[40:43], v[28:31]\n\t"
    "buffer_load_dwordx4 v[96:99], v243, s[28:31], s43 offen offset:1024 sc1\n\t"
    "v_mfma_f32_16x16x32_bf16 v[32:35], a[48:51], v[216:219], v[32:35]\n\t"
    "s_waitcnt lgkmcnt(13)\n\t"
    "v_lshlrev_b32 v168, 16, v178\n\t"
    "v_and_b32 v169, 0xffff0000, v178\n\t"
    "v_mfma_f32_16x16x32_bf16 v[32:35], a[68:71], a[40:43], v[32:35]\n\t"
    "v_lshlrev_b32 v170, 16, v179\n\t"
    "v_and_b32 v171, 0xffff0000, v179\n\t"
    "v_mfma_f32_16x16x32_bf16 v[36:39], a[52:55], v[216:219], v[36:39]\n\t"
    "v_lshlrev_b32 v172, 16, v180\n\t"
    "v_and_b32 v173, 0xffff0000, v180\n\t"
    "v_mfma_f32_16x16x32_bf16 v[36:39], a[72:75], a[40:43], v[36:39]\n\t"
    "s_waitcnt vmcnt(15)\n\t"
    "v_lshlrev_b32 v174, 16, v181\n\t"
    "v_and_b32 v175, 0xffff0000, v181\n\t"
    "v_mfma_f32_16x16x32_bf16 v[40:43], a[184:187], a[108:111], v[40:43]\n\t"
    "s_add_u32 s43, s60, 49152\n\t"
    "buffer_load_dwordx4 v[100:103], v240, s[28:31], s43 offen offset:0 sc1\n\t"
    "v_mfma_f32_16x16x32_bf16 v[40:43], a[56:59], a[44:47], v[40:43]\n\t"
    "s_waitcnt lgkmcnt(11)\n\t"
    "v_lshlrev_b32 v176, 16, v182\n\t"
    "v_and_b32 v177, 0xffff0000, v182\n\t"
    "v_mfma_f32_16x16x32_bf16 v[44:47], a[188:191], a[108:111], v[44:47]\n\t"
    "v_lshlrev_b32 v178, 16, v183\n\t"
    "v_and_b32 v179, 0xffff0000, v183\n\t"
    "v_mfma_f32_16x16x32_bf16 v[44:47], a[60:63], a[44:47], v[44:47]\n\t"
    "v_lshlrev_b32 v180, 16, v184\n\t"
    "v_and_b32 v181, 0xffff0000, v184\n\t"
    "v_mfma_f32_16x16x32_bf16 v[48:51], a[192:195], a[108:111], v[48:51]\n\t"
    "buffer_load_dwordx4 a[0:3], v240, s[28:31], s43 offen offset:1024 sc1\n\t"
    "v_mfma_f32_16x16x32_bf16 v[48:51], a[64:67], a[44:47], v[48:51]\n\t"
    "v_lshlrev_b32 v182, 16, v185\n\t"
    "v_and_b32 v183, 0xffff0000, v185\n\t"
    "v_mfma_f32_16x16x32_bf16 v[52:55], a[48:51], a[108:111], v[52:55]\n\t"
    "s_waitcnt lgkmcnt(10)\n\t"
    "v_lshlrev_b32 v184, 16, v186\n\t"
    "v_and_b32 v185, 0xffff0000, v186\n\t"
    "v_mfma_f32_16x16x32_bf16 v[52:55], a[68:71], a[44:47], v[52:55]\n\t"
    "v_lshlrev_b32 v186, 16, v187\n\t"
    "v_and_b32 v187, 0xffff0000, v187\n\t"
    "v_mfma_f32_16x16x32_bf16 v[56:59], a[52:55], a[108:111], v[56:59]\n\t"
    "v_mfma_f32_16x16x32_bf16 v[56:59], a[72:75], a[44:47], v[56:59]\n\t"
    "s_waitcnt vmcnt(15)\n\t"
    "s_add_u32 s43, s60, 57344\n\t"
    "buffer_load_dwordx4 a[20:23], v240, s[28:31], s43 offen offset:0 sc1\n\t"
    "v_mfma_f32_16x16x32_bf16 v[60:63], a[184:187], a[112:115], v[60:63]\n\t"
    "v_mfma_f32_16x16x32_bf16 v[60:63], a[56:59], a[96:99], v[60:63]\n\t"
    "v_mfma_f32_16x16x32_bf16 v[64:67], a[188:191], a[112:115], v[64:67]\n\t"
    "v_mfma_f32_16x16x32_bf16 v[64:67], a[60:63], a[96:99], v[64:67]\n\t"
    "buffer_load_dwordx4 a[4:7], v240, s[28:31], s43 offen offset:1024 sc1\n\t"
    "v_mfma_f32_16x16x32_bf16 v[68:71], a[192:195], a[112:115], v[68:71]\n\t"
    "v_mfma_f32_16x16x32_bf16 v[68:71], a[64:67], a[96:99], v[68:71]\n\t"
    "v_mfma_f32_16x16x32_bf16 v[72:75], a[48:51], a[112:115], v[72:75]\n\t"
    "v_mfma_f32_16x16x32_bf16 v[72:75], a[68:71], a[96:99], v[72:75]\n\t"
    "v_mfma_f32_16x16x32_bf16 v[76:79], a[52:55], a[112:115], v[76:79]\n\t"
    "v_mfma_f32_16x16x32_bf16 v[76:79], a[72:75], a[96:99], v[76:79]\n\t"
    "s_waitcnt vmcnt(13)\n\t"
    "v_mfma_f32_16x16x32_bf16 v[20:23], a[76:79], a[116:119], v[20:23]\n\t"
    "v_mfma_f32_16x16x32_bf16 v[20:23], a[196:199], a[100:103], v[20:23]\n\t"
    "v_mfma_f32_16x16x32_bf16 v[20:23], a[216:219], v[200:203], v[20:23]\n\t"
    "v_mfma_f32_16x16x32_bf16 v[20:23], a[236:239], a[120:123], v[20:23]\n\t"
    "v_mfma_f32_16x16x32_bf16 v[24:27], a[80:83], a[116:119], v[24:27]\n\t"
    "v_mfma_f32_16x16x32_bf16 v[24:27], a[200:203], a[100:103], v[24:27]\n\t"
    "v_mfma_f32_16x16x32_bf16 v[24:27], a[220:223], v[200:203], v[24:27]\n\t"
    "v_mfma_f32_16x16x32_bf16 v[24:27], a[240:243], a[120:123], v[24:27]\n\t"
    "v_mfma_f32_16x16x32_bf16 v[28:31], a[84:87], a[116:119], v[28:31]\n\t"
    "v_mfma_f32_16x16x32_bf16 v[28:31], a[204:207], a[100:103], v[28:31]\n\t"
    "v_mfma_f32_16x16x32_bf16 v[28:31], a[224:227], v[200:203], v[28:31]\n\t"
    "v_mfma_f32_16x16x32_bf16 v[28:31], a[244:247], a[120:123], v[28:31]\n\t"
    "v_mfma_f32_16x16x32_bf16 v[32:35], a[88:91], a[116:119], v[32:35]\n\t"
    "ds_write_b128 v250, v[20:23] offset:0\n\t"
    "v_mfma_f32_16x16x32_bf16 v[32:35], a[208:211], a[100:103], v[32:35]\n\t"
    "v_mfma_f32_16x16x32_bf16 v[32:35], a[228:231], v[200:203], v[32:35]\n\t"
    "v_mfma_f32_16x16x32_bf16 v[32:35], a[248:251], a[120:123], v[32:35]\n\t"
    "v_mfma_f32_16x16x32_bf16 v[36:39], a[92:95], a[116:119], v[36:39]\n\t"
    "ds_write_b128 v250, v[24:27] offset:1024\n\t"
    "v_mfma_f32_16x16x32_bf16 v[36:39], a[212:215], a[100:103], v[36:39]\n\t"
    "v_mfma_f32_16x16x32_bf16 v[36:39], a[232:235], v[200:203], v[36:39]\n\t"
    "v_mfma_f32_16x16x32_bf16 v[36:39], a[252:255], a[120:123], v[36:39]\n\t"
    "s_waitcnt vmcnt(6)\n\t"
    "v_mfma_f32_16x16x32_bf16 v[40:43], a[76:79], v[204:207], v[40:43]\n\t"
    "ds_write_b128 v250, v[28:31] offset:2048\n\t"
    "v_readfirstlane_b32 s99, v224\n\t"
    "v_mfma_f32_16x16x32_bf16 v[40:43], a[196:199], a[104:107], v[40:43]\n\t"
    "s_cmp_lg_u32 s99, s96\n\t"
    "s_nop 0\n\t"
    "s_cbranch_scc1 L_pxw%=\n\t"
    "L_pxok%=:\n\t"
    "v_mfma_f32_16x16x32_bf16 v[40:43], a[216:219], a[124:127], v[40:43]\n\t"
    "s_add_u32 s43, s95, 0\n\t"
    "buffer_load_dwordx4 v[220:223], v254, s[28:31], s43 offen offset:0 sc1\n\t"
    "v_mfma_f32_16x16x32_bf16 v[40:43], a[236:239], v[80:83], v[40:43]\n\t"
    "v_mfma_f32_16x16x32_bf16 v[44:47], a[80:83], v[204:207], v[44:47]\n\t"
    "ds_write_b128 v250, v[32:35] offset:3072\n\t"
    "v_mfma_f32_16x16x32_bf16 v[44:47], a[200:203], a[104:107], v[44:47]\n\t"
    "buffer_load_dwordx4 v[224:227], v254, s[28:31], s43 offen offset:1024 sc1\n\t"
    "v_mfma_f32_16x16x32_bf16 v[44:47], a[220:223], a[124:127], v[44:47]\n\t"
    "buffer_load_dwordx4 v[228:231], v254, s[28:31], s43 offen offset:2048 sc1\n\t"
    "v_mfma_f32_16x16x32_bf16 v[44:47], a[240:243], v[80:83], v[44:47]\n\t"
    "v_mfma_f32_16x16x32_bf16 v[48:51], a[84:87], v[204:207], v[48:51]\n\t"
    "ds_write_b128 v250, v[36:39] offset:16384\n\t"
    "v_mfma_f32_16x16x32_bf16 v[48:51], a[204:207], a[104:107], v[48:51]\n\t"
    "v_mfma_f32_16x16x32_bf16 v[48:51], a[224:227], a[124:127], v[48:51]\n\t"
    "v_mfma_f32_16x16x32_bf16 v[48:51], a[244:247], v[80:83], v[48:51]\n\t"
    "v_mfma_f32_16x16x32_bf16 v[52:55], a[88:91], v[204:207], v[52:55]\n\t"
    "ds_write_b128 v250, v[40:43] offset:17408\n\t"
    "v_mfma_f32_16x16x32_bf16 v[52:55], a[208:211], a[104:107], v[52:55]\n\t"
    "v_mfma_f32_16x16x32_bf16 v[52:55], a[228:231], a[124:127], v[52:55]\n\t"
    "v_mfma_f32_16x16x32_bf16 v[52:55], a[248:251], v[80:83], v[52:55]\n\t"
    "v_mfma_f32_16x16x32_bf16 v[56:59], a[92:95], v[204:207], v[56:59]\n\t"
    "s_add_u32 m0, s37, 49152\n\t"
    "ds_write_b128 v250, v[44:47] offset:18432\n\t"
    "v_mfma_f32_16x16x32_bf16 v[56:59], a[212:215], a[104:107], v[56:59]\n\t"
    "s_add_u32 s97, s63, 49152\n\t"
    "v_mfma_f32_16x16x32_bf16 v[56:59], a[232:235], a[124:127], v[56:59]\n\t"
    "buffer_load_dwordx4 v254, s[28:31], s97 offen offset:0 sc1 lds\n\t"
    "v_mfma_f32_16x16x32_bf16 v[56:59], a[252:255], v[80:83], v[56:59]\n\t"
    "v_mfma_f32_16x16x32_bf16 v[60:63], a[76:79], v[84:87], v[60:63]\n\t"
    "ds_write_b128 v250, v[48:51] offset:19456\n\t"
    "v_mfma_f32_16x16x32_bf16 v[60:63], a[196:199], v[88:91], v[60:63]\n\t"
    "s_waitcnt vmcnt(8)\n\t"
    "v_mfma_f32_16x16x32_bf16 v[60:63], a[216:219], v[92:95], v[60:63]\n\t"
    "buffer_load_dwordx4 v254, s[28:31], s97 offen offset:1024 sc1 lds\n\t"
    "v_mfma_f32_16x16x32_bf16 v[60:63], a[236:239], v[96:99], v[60:63]\n\t"
    "v_mfma_f32_16x16x32_bf16 v[64:67], a[80:83], v[84:87], v[64:67]\n\t"
    "ds_write_b128 v250, v[52:55] offset:32768\n\t"
    "v_mfma_f32_16x16x32_bf16 v[64:67], a[200:203], v[88:91], v[64:67]\n\t"
    "v_mfma_f32_16x16x32_bf16 v[64:67], a[220:223], v[92:95], v[64:67]\n\t"
    "buffer_load_dwordx4 v254, s[28:31], s97 offen offset:2048 sc1 lds\n\t"
    "v_mfma_f32_16x16x32_bf16 v[64:67], a[240:243], v[96:99], v[64:67]\n\t"
    "v_mfma_f32_16x16x32_bf16 v[68:71], a[84:87], v[84:87], v[68:71]\n\t"
    "ds_write_b128 v250, v[56:59] offset:33792\n\t"
    "v_mfma_f32_16x16x32_bf16 v[68:71], a[204:207], v[88:91], v[68:71]\n\t"
    "v_mfma_f32_16x16x32_bf16 v[68:71], a[224:227], v[92:95], v[68:71]\n\t"
    "buffer_load_dwordx4 v254, s[28:31], s97 offen offset:3072 sc1 lds\n\t"
    "v_mfma_f32_16x16x32_bf16 v[68:71], a[244:247], v[96:99], v[68:71]\n\t"
    "v_mfma_f32_16x16x32_bf16 v[72:75], a[88:91], v[84:87], v[72:75]\n\t"
    "s_add_u32 m0, s37, 65536\n\t"
    "ds_write_b128 v250, v[60:63] offset:34816\n\t"
    "v_mfma_f32_16x16x32_bf16 v[72:75], a[208:211], v[88:91], v[72:75]\n\t"
    "s_add_u32 s97, s63, 65536\n\t"
    "v_mfma_f32_16x16x32_bf16 v[72:75], a[228:231], v[92:95], v[72:75]\n\t"
    "buffer_load_dwordx4 v254, s[28:31], s97 offen offset:0 sc1 lds\n\t"
    "v_mfma_f32_16x16x32_bf16 v[72:75], a[248:251], v[96:99], v[72:75]\n\t"
    "v_mfma_f32_16x16x32_bf16 v[76:79], a[92:95], v[84:87], v[76:79]\n\t"
    "ds_write_b128 v250, v[64:67] offset:35840\n\t"
    "v_mfma_f32_16x16x32_bf16 v[76:79], a[212:215], v[88:91], v[76:79]\n\t"
    "v_mfma_f32_16x16x32_bf16 v[76:79], a[232:235], v[92:95], v[76:79]\n\t"
    "buffer_load_dwordx4 v254, s[28:31], s97 offen offset:1024 sc1 lds\n\t"
    "v_mfma_f32_16x16x32_bf16 v[76:79], a[252:255], v[96:99], v[76:79]\n\t"
    "s_waitcnt vmcnt(9)\n\t"
    "v_mfma_f32_16x16x32_bf16 v[0:3], a[76:79], v[100:103], v[0:3]\n\t"
    "ds_write_b128 v250, v[68:71] offset:49152\n\t"
    "v_mfma_f32_16x16x32_bf16 v[0:3], a[196:199], a[0:3], v[0:3]\n\t"
    "v_mfma_f32_16x16x32_bf16 v[0:3], a[216:219], a[20:23], v[0:3]\n\t"
    "buffer_load_dwordx4 v254, s[28:31], s97 offen offset:2048 sc1 lds\n\t"
    "v_mfma_f32_16x16x32_bf16 v[0:3], a[236:239], a[4:7], v[0:3]\n\t"
    "v_mfma_f32_16x16x32_bf16 v[4:7], a[80:83], v[100:103], v[4:7]\n\t"
    "ds_write_b128 v250, v[72:75] offset:50176\n\t"
    "v_mfma_f32_16x16x32_bf16 v[4:7], a[200:203], a[0:3], v[4:7]\n\t"
    "v_mfma_f32_16x16x32_bf16 v[4:7], a[220:223], a[20:23], v[4:7]\n\t"
    "buffer_load_dwordx4 v254, s[28:31], s97 offen offset:3072 sc1 lds\n\t"
    "v_mfma_f32_16x16x32_bf16 v[4:7], a[240:243], a[4:7], v[4:7]\n\t"
    "v_mfma_f32_16x16x32_bf16 v[8:11], a[84:87], v[100:103], v[8:11]\n\t"
    "ds_write_b128 v250, v[76:79] offset:51200\n\t"
    "v_mfma_f32_16x16x32_bf16 v[8:11], a[204:207], a[0:3], v[8:11]\n\t"
    "v_mfma_f32_16x16x32_bf16 v[8:11], a[224:227], a[20:23], v[8:11]\n\t"
    "v_mfma_f32_16x16x32_bf16 v[8:11], a[244:247], a[4:7], v[8:11]\n\t"
    "v_mfma_f32_16x16x32_bf16 v[12:15], a[88:91], v[100:103], v[12:15]\n\t"
    "v_mfma_f32_16x16x32_bf16 v[12:15], a[208:211], a[0:3], v[12:15]\n\t"
    "v_mfma_f32_16x16x32_bf16 v[12:15], a[228:231], a[20:23], v[12:15]\n\t"
    "v_mfma_f32_16x16x32_bf16 v[12:15], a[248:251], a[4:7], v[12:15]\n\t"
    "v_mfma_f32_16x16x32_bf16 v[16:19], a[92:95], v[100:103], v[16:19]\n\t"
    "v_mfma_f32_16x16x32_bf16 v[16:19], a[212:215], a[0:3], v[16:19]\n\t"
    "v_mfma_f32_16x16x32_bf16 v[16:19], a[232:235], a[20:23], v[16:19]\n\t"
    "v_mfma_f32_16x16x32_bf16 v[16:19], a[252:255], a[4:7], v[16:19]\n\t"
    "L_k2e%=:\n\t"
    "s_waitcnt lgkmcnt(0)\n\t"
    "s_barrier\n\t"
    "ds_read_b128 v[20:23], v251 offset:0\n\t"
    "ds_read_b128 v[40:43], v252 offset:17408\n\t"
    "ds_read_b128 v[60:63], v253 offset:34816\n\t"
    "ds_read_b128 v[24:27], v251 offset:1024\n\t"
    "ds_read_b128 v[44:47], v252 offset:18432\n\t"
    "ds_read_b128 v[64:67], v253 offset:35840\n\t"
    "ds_read_b128 v[28:31], v251 offset:2048\n\t"
    "ds_read_b128 v[48:51], v252 offset:19456\n\t"
    "ds_read_b128 v[68:71], v253 offset:49152\n\t"
    "ds_read_b128 v[32:35], v251 offset:3072\n\t"
    "ds_read_b128 v[52:55], v252 offset:32768\n\t"
    "ds_read_b128 v[72:75], v253 offset:50176\n\t"
    "ds_read_b128 v[36:39], v251 offset:16384\n\t"
    "ds_read_b128 v[56:59], v252 offset:33792\n\t"
    "ds_read_b128 v[76:79], v253 offset:51200\n\t"
    "s_add_u32 m0, s37, 81920\n\t"
    "s_add_u32 s97, s63, 81920\n\t"
    "buffer_load_dwordx4 v254, s[28:31], s97 offen offset:0 sc1 lds\n\t"
    "s_waitcnt lgkmcnt(14)\n\t"
    "v_pk_add_f32 v[0:1], v[0:1], v[20:21]\n\t"
    "s_waitcnt lgkmcnt(12)\n\t"
    "v_pk_add_f32 v[40:41], v[40:41], v[60:61]\n\t"
    "v_pk_add_f32 v[2:3], v[2:3], v[22:23]\n\t"
    "v_pk_add_f32 v[42:43], v[42:43], v[62:63]\n\t"
    "v_pk_add_f32 v[0:1], v[0:1], v[40:41]\n\t"
    "v_pk_add_f32 v[2:3], v[2:3], v[42:43]\n\t"
    "s_waitcnt lgkmcnt(11)\n\t"
    "v_pk_add_f32 v[4:5], v[4:5], v[24:25]\n\t"
    "s_waitcnt lgkmcnt(9)\n\t"
    "v_pk_add_f32 v[44:45], v[44:45], v[64:65]\n\t"
    "v_pk_add_f32 v[6:7], v[6:7], v[26:27]\n\t"
    "v_pk_add_f32 v[46:47], v[46:47], v[66:67]\n\t"
    "v_pk_add_f32 v[4:5], v[4:5], v[44:45]\n\t"
    "v_pk_add_f32 v[6:7], v[6:7], v[46:47]\n\t"
    "s_waitcnt lgkmcnt(8)\n\t"
    "v_pk_add_f32 v[8:9], v[8:9], v[28:29]\n\t"
    "s_waitcnt lgkmcnt(6)\n\t"
    "v_pk_add_f32 v[48:49], v[48:49], v[68:69]\n\t"
    "v_pk_add_f32 v[10:11], v[10:11], v[30:31]\n\t"
    "v_pk_add_f32 v[50:51], v[50:51], v[70:71]\n\t"
    "v_pk_add_f32 v[8:9], v[8:9], v[48:49]\n\t"
    "v_pk_add_f32 v[10:11], v[10:11], v[50:51]\n\t"
    "s_waitcnt lgkmcnt(5)\n\t"
    "v_pk_add_f32 v[12:13], v[12:13], v[32:33]\n\t"
    "s_waitcnt lgkmcnt(3)\n\t"
    "v_pk_add_f32 v[52:53], v[52:53], v[72:73]\n\t"
    "v_pk_add_f32 v[14:15], v[14:15], v[34:35]\n\t"
    "v_pk_add_f32 v[54:55], v[54:55], v[74:75]\n\t"
    "v_pk_add_f32 v[12:13], v[12:13], v[52:53]\n\t"
    "buffer_load_dwordx4 v254, s[28:31], s97 offen offset:1024 sc1 lds\n\t"
    "v_pk_add_f32 v[14:15], v[14:15], v[54:55]\n\t"
    "s_waitcnt lgkmcnt(2)\n\t"
    "v_pk_add_f32 v[16:17], v[16:17], v[36:37]\n\t"
    "s_waitcnt lgkmcnt(0)\n\t"
    "v_pk_add_f32 v[56:57], v[56:57], v[76:77]\n\t"
    "v_pk_add_f32 v[18:19], v[18:19], v[38:39]\n\t"
    "v_pk_add_f32 v[58:59], v[58:59], v[78:79]\n\t"
    "v_pk_add_f32 v[16:17], v[16:17], v[56:57]\n\t"
    "v_pk_add_f32 v[18:19], v[18:19], v[58:59]\n\t"
    "s_barrier\n\t"
    "s_waitcnt vmcnt(12)\n\t"
    "v_lshlrev_b32 v80, 16, v220\n\t"
    "v_and_b32 v81, 0xffff0000, v220\n\t"
    "v_lshlrev_b32 v82, 16, v221\n\t"
    "v_and_b32 v83, 0xffff0000, v221\n\t"
    "v_lshlrev_b32 v84, 16, v222\n\t"
    "v_and_b32 v85, 0xffff0000, v222\n\t"
    "v_lshlrev_b32 v86, 16, v223\n\t"
    "v_and_b32 v87, 0xffff0000, v223\n\t"
    "s_waitcnt vmcnt(11)\n\t"
    "v_lshlrev_b32 v88, 16, v224\n\t"
    "v_and_b32 v89, 0xffff0000, v224\n\t"
    "v_lshlrev_b32 v90, 16, v225\n\t"
    "v_and_b32 v91, 0xffff0000, v225\n\t"
    "v_lshlrev_b32 v92, 16, v226\n\t"
    "v_and_b32 v93, 0xffff0000, v226\n\t"
    "v_lshlrev_b32 v94, 16, v227\n\t"
    "buffer_load_dwordx4 v254, s[28:31], s97 offen offset:2048 sc1 lds\n\t"
    "v_and_b32 v95, 0xffff0000, v227\n\t"
    "s_waitcnt vmcnt(11)\n\t"
    "v_lshlrev_b32 v96, 16, v228\n\t"
    "v_and_b32 v97, 0xffff0000, v228\n\t"
    "v_lshlrev_b32 v98, 16, v229\n\t"
    "v_and_b32 v99, 0xffff0000, v229\n\t"
    "v_pk_add_f32 v[0:1], v[0:1], v[80:81]\n\t"
    "v_pk_add_f32 v[2:3], v[2:3], v[82:83]\n\t"
    "v_pk_add_f32 v[4:5], v[4:5], v[84:85]\n\t"
    "v_pk_add_f32 v[6:7], v[6:7], v[86:87]\n\t"
    "v_pk_add_f32 v[8:9], v[8:9], v[88:89]\n\t"
    "v_pk_add_f32 v[10:11], v[10:11], v[90:91]\n\t"
    "v_pk_add_f32 v[12:13], v[12:13], v[92:93]\n\t"
    "v_pk_add_f32 v[14:15], v[14:15], v[94:95]\n\t"
    "v_pk_add_f32 v[16:17], v[16:17], v[96:97]\n\t"
    "v_pk_add_f32 v[18:19], v[18:19], v[98:99]\n\t"
    "v_pk_fma_f32 v[0:1], v[0:1], v[128:129], v[168:169]\n\t"
    "v_pk_fma_f32 v[2:3], v[2:3], v[130:131], v[170:171]\n\t"
    "v_pk_fma_f32 v[4:5], v[4:5], v[132:133], v[172:173]\n\t"
    "v_pk_fma_f32 v[6:7], v[6:7], v[134:135], v[174:175]\n\t"
    "v_pk_fma_f32 v[8:9], v[8:9], v[136:137], v[176:177]\n\t"
    "v_pk_fma_f32 v[10:11], v[10:11], v[138:139], v[178:179]\n\t"
    "v_pk_fma_f32 v[12:13], v[12:13], v[140:141], v[180:181]\n\t"
    "v_pk_fma_f32 v[14:15], v[14:15], v[142:143], v[182:183]\n\t"
    "buffer_load_dwordx4 v254, s[28:31], s97 offen offset:3072 sc1 lds\n\t"
    "v_pk_fma_f32 v[16:17], v[16:17], v[144:145], v[184:185]\n\t"
    "v_pk_fma_f32 v[18:19], v[18:19], v[146:147], v[186:187]\n\t"
    "v_pk_mul_f32 v[148:149], v[148:149], v[0:1]\n\t"
    "v_pk_mul_f32 v[150:151], v[150:151], v[2:3]\n\t"
    "v_pk_mul_f32 v[152:153], v[152:153], v[4:5]\n\t"
    "v_pk_mul_f32 v[154:155], v[154:155], v[6:7]\n\t"
    "v_pk_mul_f32 v[156:157], v[156:157], v[8:9]\n\t"
    "v_pk_mul_f32 v[158:159], v[158:159], v[10:11]\n\t"
    "v_pk_mul_f32 v[160:161], v[160:161], v[12:13]\n\t"
    "v_pk_mul_f32 v[162:163], v[162:163], v[14:15]\n\t"
    "v_pk_mul_f32 v[164:165], v[164:165], v[16:17]\n\t"
    "v_pk_mul_f32 v[166:167], v[166:167], v[18:19]\n\t"
    "v_cvt_pk_bf16_f32 v20, v148, v149\n\t"
    "v_cvt_pk_bf16_f32 v21, v150, v151\n\t"
    "v_cvt_pk_bf16_f32 v22, v152, v153\n\t"
    "v_cvt_pk_bf16_f32 v23, v154, v155\n\t"
    "v_cvt_pk_bf16_f32 v24, v156, v157\n\t"
    "v_cvt_pk_bf16_f32 v25, v158, v159\n\t"
    "v_cvt_pk_bf16_f32 v26, v160, v161\n\t"
    "v_cvt_pk_bf16_f32 v27, v162, v163\n\t"
    "v_cvt_pk_bf16_f32 v28, v164, v165\n\t"
    "v_cvt_pk_bf16_f32 v29, v166, v167\n\t"
    "v_mov_b32 v30, v28\n\t"
    "v_mov_b32 v31, v29\n\t"
    "s_nop 1\n\t"
    "v_permlane16_swap_b32 v20, v22\n\t"
    "v_permlane16_swap_b32 v21, v23\n\t"
    "v_permlane16_swap_b32 v24, v26\n\t"
    "v_permlane16_swap_b32 v25, v27\n\t"
    "v_permlane16_swap_b32 v28, v30\n\t"
    "v_permlane16_swap_b32 v29, v31\n\t"
    "buffer_store_dwordx4 v[20:23], v244, s[28:31], s14 offen sc1\n\t"
    "buffer_store_dwordx4 v[24:27], v245, s[28:31], s14 offen sc1\n\t"
    "s_mov_b32 exec_lo, 0xffff\n\t"
    "s_mov_b32 exec_hi, 0xffff\n\t"
    "buffer_store_dwordx4 v[28:31], v246, s[28:31], s14 offen sc1\n\t"
    "s_mov_b64 exec, -1\n\t"
    "v_pk_add_f32 v[42:43], v[0:1], v[2:3]\n\t"
    "v_pk_mul_f32 v[44:45], v[0:1], v[0:1]\n\t"
    "v_pk_fma_f32 v[44:45], v[2:3], v[2:3], v[44:45]\n\t"
    "v_pk_add_f32 v[42:43], v[42:43], v[4:5]\n\t"
    "v_pk_fma_f32 v[44:45], v[4:5], v[4:5], v[44:45]\n\t"
    "v_pk_add_f32 v[42:43], v[42:43], v[6:7]\n\t"
    "v_pk_fma_f32 v[44:45], v[6:7], v[6:7], v[44:45]\n\t"
    "v_pk_add_f32 v[42:43], v[42:43], v[8:9]\n\t"
    "v_pk_fma_f32 v[44:45], v[8:9], v[8:9], v[44:45]\n\t"
    "v_pk_add_f32 v[42:43], v[42:43], v[10:11]\n\t"
    "v_pk_fma_f32 v[44:45], v[10:11], v[10:11], v[44:45]\n\t"
    "v_pk_add_f32 v[42:43], v[42:43], v[12:13]\n\t"
    "v_pk_fma_f32 v[44:45], v[12:13], v[12:13], v[44:45]\n\t"
    "v_pk_add_f32 v[42:43], v[42:43], v[14:15]\n\t"
    "v_pk_fma_f32 v[44:45], v[14:15], v[14:15], v[44:45]\n\t"
    "v_pk_add_f32 v[42:43], v[42:43], v[16:17]\n\t"
    "v_pk_fma_f32 v[44:45], v[16:17], v[16:17], v[44:45]\n\t"
    "v_pk_add_f32 v[42:43], v[42:43], v[18:19]\n\t"
    "v_pk_fma_f32 v[44:45], v[18:19], v[18:19], v[44:45]\n\t"
    "v_add_f32 v40, v42, v43\n\t"
    "v_add_f32 v41, v44, v45\n\t"
    "v_mov_b32 v42, v40\n\t"
    "v_mov_b32 v43, v41\n\t"
    "s_nop 0\n\t"
    "v_permlane16_swap_b32 v40, v42\n\t"
    "v_permlane16_swap_b32 v41, v43\n\t"
    "v_add_f32 v40, v40, v42\n\t"
    "v_add_f32 v41, v41, v43\n\t"
    "v_mov_b32 v42, v40\n\t"
    "v_mov_b32 v43, v41\n\t"
    "s_nop 0\n\t"
    "v_permlane32_swap_b32 v40, v42\n\t"
    "v_permlane32_swap_b32 v41, v43\n\t"
    "v_add_f32 v40, v40, v42\n\t"
    "v_add_f32 v41, v41, v43\n\t"
    "s_bfe_u32 s43, s95, 0x3000f\n\t"
    "s_lshl_b32 s43, s43, 7\n\t"
    "s_add_u32 s43, s43, s15\n\t"
    "v_mov_b32_dpp v42, v40 quad_perm:[1,1,3,3] row_mask:0xf bank_mask:0xf\n\t"
    "v_mov_b32_dpp v43, v41 quad_perm:[1,1,3,3] row_mask:0xf bank_mask:0xf\n\t"
    "s_mov_b64 exec, 0x5555\n\t"
    "buffer_store_dwordx4 v[40:43], v249, s[28:31], s43 offen sc1\n\t"
    "s_mov_b64 exec, -1\n\t"
    "v_cvt_pk_bf16_f32 v244, v0, v1\n\t"
    "v_cvt_pk_bf16_f32 v245, v2, v3\n\t"
    "v_cvt_pk_bf16_f32 v246, v4, v5\n\t"
    "v_cvt_pk_bf16_f32 v247, v6, v7\n\t"
    "v_cvt_pk_bf16_f32 v248, v8, v9\n\t"
    "v_cvt_pk_bf16_f32 v249, v10, v11\n\t"
    "v_cvt_pk_bf16_f32 v250, v12, v13\n\t"
    "v_cvt_pk_bf16_f32 v251, v14, v15\n\t"
    "v_cvt_pk_bf16_f32 v252, v16, v17\n\t"
    "v_cvt_pk_bf16_f32 v253, v18, v19\n\t"
    "v_lshlrev_b32 v78, 7, v255\n\t"
    "v_mov_b32 v79, 1\n\t"
    "s_add_u32 m0, s37, 98304\n\t"
    "s_add_u32 s97, s63, 98304\n\t"
    "buffer_load_dwordx4 v254, s[28:31], s97 offen offset:0 sc1 lds\n\t"
    "buffer_load_dwordx4 v254, s[28:31], s97 offen offset:1024 sc1 lds\n\t"
    "buffer_load_dwordx4 v254, s[28:31], s97 offen offset:2048 sc1 lds\n\t"
    "buffer_load_dwordx4 v254, s[28:31], s97 offen offset:3072 sc1 lds\n\t"
    "s_bfe_u32 s40, s68, 0x5000a\n\t"
    "s_mul_i32 s40, s40, 512\n\t"
    "s_add_u32 s40, s40, 622592\n\t"
    "v_mov_b32 v240, v254\n\t"
    "v_add_u32 v241, 0x10000, v254\n\t"
    "v_add_u32 v242, 0x20000, v254\n\t"
    "v_and_b32 v243, 0x300, v254\n\t"
    "v_and_b32 v80, 1, v255\n\t"
    "v_lshl_add_u32 v243, v80, 7, v243\n\t"
    "v_and_b32 v80, 14, v255\n\t"
    "v_lshl_add_u32 v243, v80, 3, v243\n\t"
    "s_add_u32 s16, s18, s40\n\t"
    "s_addc_u32 s17, s19, 0\n\t"
    "s_waitcnt vmcnt(4) lgkmcnt(0)\n\t"
    "s_barrier\n\t"
    "s_cmp_lg_u32 s77, 0\n\t"
    "s_cbranch_scc1 L_sig2%=\n\t"
    "L_sigb2%=:\n\t"
    "s_mov_b64 exec, 15\n\t"
    "global_atomic_add v78, v79, s[16:17]\n\t"
    "s_mov_b64 exec, -1\n\t"
    "L_sig2%=:\n\t"
    "s_cmp_eq_u32 s77, 3\n\t"
    "s_cselect_b32 s11, 67108864, s66\n\t"
    "s_cselect_b32 s12, 67108864, s65\n\t"
    "s_cmp_ge_u32 s74, 27\n\t"
    "s_cbranch_scc1 L_k3skip%=\n\t"
    "L_k3%=:\n\t"
    "s_add_u32 m0, s37, 114688\n\t"
    "s_add_u32 s97, s63, 114688\n\t"
    "buffer_load_dwordx4 v254, s[28:31], s97 offen offset:0 sc1 lds\n\t"
    "buffer_load_dwordx4 v254, s[28:31], s97 offen offset:1024 sc1 lds\n\t"
    "buffer_load_dwordx4 v254, s[28:31], s97 offen offset:2048 sc1 lds\n\t"
    "buffer_load_dwordx4 v254, s[28:31], s97 offen offset:3072 sc1 lds\n\t"
    "buffer_load_dword v128, off, s[28:31], s70 sc1\n\t"
    "s_add_u32 m0, s37, 131072\n\t"
    "s_add_u32 s97, s63, 131072\n\t"
    "buffer_load_dwordx4 v254, s[28:31], s97 offen offset:0 sc1 lds\n\t"
    "buffer_load_dwordx4 v254, s[28:31], s97 offen offset:1024 sc1 lds\n\t"
    "buffer_load_dwordx4 v254, s[28:31], s97 offen offset:2048 sc1 lds\n\t"
    "buffer_load_dwordx4 v254, s[28:31], s97 offen offset:3072 sc1 lds\n\t"
    "s_add_u32 m0, s37, 147456\n\t"
    "s_add_u32 s97, s63, 147456\n\t"
    "buffer_load_dwordx4 v254, s[28:31], s97 offen offset:0 sc1 lds\n\t"
    "buffer_load_dwordx4 v254, s[28:31], s97 offen offset:1024 sc1 lds\n\t"
    "buffer_load_dwordx4 v254, s[28:31], s97 offen offset:2048 sc1 lds\n\t"
    "buffer_load_dwordx4 v254, s[28:31], s97 offen offset:3072 sc1 lds\n\t"
    "ds_read_b128 a[0:3], v240 offset:0\n\t"
    "ds_read_b128 a[4:7], v240 offset:1024\n\t"
    "ds_read_b128 a[8:11], v240 offset:2048\n\t"
    "ds_read_b128 a[12:15], v240 offset:3072\n\t"
    "ds_read_b128 a[16:19], v240 offset:4096\n\t"
    "ds_read_b128 a[20:23], v240 offset:5120\n\t"
    "ds_read_b128 a[24:27], v240 offset:6144\n\t"
    "ds_read_b128 a[28:31], v240 offset:7168\n\t"
    "ds_read_b128 a[32:35], v240 offset:8192\n\t"
    "ds_read_b128 a[36:39], v240 offset:9216\n\t"
    "ds_read_b128 a[40:43], v240 offset:10240\n\t"
    "ds_read_b128 a[44:47], v240 offset:11264\n\t"
    "ds_read_b128 a[48:51], v240 offset:12288\n\t"
    "ds_read_b128 a[52:55], v240 offset:13312\n\t"
    "ds_read_b128 a[56:59], v240 offset:14336\n\t"
    "ds_read_b128 a[60:63], v240 offset:15360\n\t"
    "s_waitcnt vmcnt(8)\n\t"
    "v_readfirstlane_b32 s43, v128\n\t"
    "s_nop 0\n\t"
    "s_cmp_ge_u32 s43, 8\n\t"
    "s_cbranch_scc1 L_polld2%=\n\t"
    "L_pollb2%=:\n\t"
    "s_mov_b32 s38, 0x20000\n\t"
    "L_poll2%=:\n\t"
    "buffer_load_dword v128, off, s[28:31], s70 sc1\n\t"
    "s_waitcnt vmcnt(0)\n\t"
    "v_readfirstlane_b32 s43, v128\n\t"
    "s_nop 0\n\t"
    "s_cmp_ge_u32 s43, 8\n\t"
    "s_cbranch_scc1 L_polld2%=\n\t"
    "s_sleep 2\n\t"
    "s_sub_u32 s38, s38, 1\n\t"
    "s_cbranch_scc0 L_poll2%=\n\t"
    "L_polld2%=:\n\t"
    "s_add_u32 s43, s62, 0\n\t"
    "buffer_load_dwordx4 a[128:131], v254, s[28:31], s43 offen offset:0 sc1\n\t"
    "buffer_load_dwordx4 a[132:135], v254, s[28:31], s43 offen offset:1024 sc1\n\t"
    "buffer_load_dwordx4 a[136:139], v254, s[28:31], s43 offen offset:2048 sc1\n\t"
    "buffer_load_dwordx4 a[140:143], v254, s[28:31], s43 offen offset:3072 sc1\n\t"
    "s_add_u32 s43, s62, 4096\n\t"
    "buffer_load_dwordx4 a[144:147], v254, s[28:31], s43 offen offset:0 sc1\n\t"
    "buffer_load_dwordx4 a[148:151], v254, s[28:31], s43 offen offset:1024 sc1\n\t"
    "buffer_load_dwordx4 a[152:155], v254, s[28:31], s43 offen offset:2048 sc1\n\t"
    "buffer_load_dwordx4 a[156:159], v254, s[28:31], s43 offen offset:3072 sc1\n\t"
    "s_add_u32 s43, s62, 8192\n\t"
    "buffer_load_dwordx4 a[160:163], v254, s[28:31], s43 offen offset:0 sc1\n\t"
    "buffer_load_dwordx4 a[164:167], v254, s[28:31], s43 offen offset:1024 sc1\n\t"
    "buffer_load_dwordx4 a[168:171], v254, s[28:31], s43 offen offset:2048 sc1\n\t"
    "buffer_load_dwordx4 a[172:175], v254, s[28:31], s43 offen offset:3072 sc1\n\t"
    "L_k3m%=:\n\t"
    "s_waitcnt vmcnt(10) lgkmcnt(6)\n\t"
    "v_mfma_f32_16x16x32_bf16 v[0:3], a[0:3], a[128:131], 0\n\t"
    "v_mfma_f32_16x16x32_bf16 v[0:3], a[32:35], a[132:135], v[0:3]\n\t"
    "v_mfma_f32_16x16x32_bf16 v[4:7], a[4:7], a[128:131], 0\n\t"
    "ds_read_b128 a[64:67], v240 offset:16384\n\t"
    "v_mfma_f32_16x16x32_bf16 v[4:7], a[36:39], a[132:135], v[4:7]\n\t"
    "s_add_u32 s43, s62, 12288\n\t"
    "buffer_load_dwordx4 a[176:179], v254, s[28:31], s43 offen offset:0 sc1\n\t"
    "v_mfma_f32_16x16x32_bf16 v[8:11], a[8:11], a[128:131], 0\n\t"
    "s_waitcnt lgkmcnt(5)\n\t"
    "v_mfma_f32_16x16x32_bf16 v[8:11], a[40:43], a[132:135], v[8:11]\n\t"
    "ds_read_b128 a[96:99], v240 offset:24576\n\t"
    "v_mfma_f32_16x16x32_bf16 v[12:15], a[12:15], a[128:131], 0\n\t"
    "v_mfma_f32_16x16x32_bf16 v[12:15], a[44:47], a[132:135], v[12:15]\n\t"
    "v_mfma_f32_16x16x32_bf16 v[16:19], a[16:19], a[128:131], 0\n\t"
    "s_waitcnt lgkmcnt(4)\n\t"
    "buffer_load_dwordx4 a[180:183], v254, s[28:31], s43 offen offset:1024 sc1\n\t"
    "v_mfma_f32_16x16x32_bf16 v[16:19], a[48:51], a[132:135], v[16:19]\n\t"
    "ds_read_b128 a[68:71], v240 offset:17408\n\t"
    "v_mfma_f32_16x16x32_bf16 v[20:23], a[20:23], a[128:131], 0\n\t"
    "v_mfma_f32_16x16x32_bf16 v[20:23], a[52:55], a[132:135], v[20:23]\n\t"
    "v_mfma_f32_16x16x32_bf16 v[24:27], a[24:27], a[128:131], 0\n\t"
    "s_waitcnt lgkmcnt(3)\n\t"
    "ds_read_b128 a[100:103], v240 offset:25600\n\t"
    "v_mfma_f32_16x16x32_bf16 v[24:27], a[56:59], a[132:135], v[24:27]\n\t"
    "v_mfma_f32_16x16x32_bf16 v[28:31], a[28:31], a[128:131], 0\n\t"
    "buffer_load_dwordx4 a[184:187], v254, s[28:31], s43 offen offset:2048 sc1\n\t"
    "v_mfma_f32_16x16x32_bf16 v[28:31], a[60:63], a[132:135], v[28:31]\n\t"
    "s_waitcnt vmcnt(11)\n\t"
    "v_mfma_f32_16x16x32_bf16 v[32:35], a[0:3], a[136:139], 0\n\t"
    "ds_read_b128 a[72:75], v240 offset:18432\n\t"
    "v_mfma_f32_16x16x32_bf16 v[32:35], a[32:35], a[140:143], v[32:35]\n\t"
    "v_mfma_f32_16x16x32_bf16 v[36:39], a[4:7], a[136:139], 0\n\t"
    "ds_read_b128 a[104:107], v240 offset:26624\n\t"
    "v_mfma_f32_16x16x32_bf16 v[36:39], a[36:39], a[140:143], v[36:39]\n\t"
    "buffer_load_dwordx4 a[188:191], v254, s[28:31], s43 offen offset:3072 sc1\n\t"
    "v_mfma_f32_16x16x32_bf16 v[40:43], a[8:11], a[136:139], 0\n\t"
    "v_mfma_f32_16x16x32_bf16 v[40:43], a[40:43], a[140:143], v[40:43]\n\t"
    "v_mfma_f32_16x16x32_bf16 v[44:47], a[12:15], a[136:139], 0\n\t"
    "ds_read_b128 a[76:79], v240 offset:19456\n\t"
    "v_mfma_f32_16x16x32_bf16 v[44:47], a[44:47], a[140:143], v[44:47]\n\t"
    "v_mfma_f32_16x16x32_bf16 v[48:51], a[16:19], a[136:139], 0\n\t"
    "v_mfma_f32_16x16x32_bf16 v[48:51], a[48:51], a[140:143], v[48:51]\n\t"
    "s_add_u32 s43, s62, 16384\n\t"
    "buffer_load_dwordx4 a[192:195], v254, s[28:31], s43 offen offset:0 sc1\n\t"
    "v_mfma_f32_16x16x32_bf16 v[52:55], a[20:23], a[136:139], 0\n\t"
    "ds_read_b128 a[108:111], v240 offset:27648\n\t"
    "v_mfma_f32_16x16x32_bf16 v[52:55], a[52:55], a[140:143], v[52:55]\n\t"
    "v_mfma_f32_16x16x32_bf16 v[56:59], a[24:27], a[136:139], 0\n\t"
    "v_mfma_f32_16x16x32_bf16 v[56:59], a[56:59], a[140:143], v[56:59]\n\t"
    "ds_read_b128 a[80:83], v240 offset:20480\n\t"
    "v_mfma_f32_16x16x32_bf16 v[60:63], a[28:31], a[136:139], 0\n\t"
    "buffer_load_dwordx4 a[196:199], v254, s[28:31], s43 offen offset:1024 sc1\n\t"
    "v_mfma_f32_16x16x32_bf16 v[60:63], a[60:63], a[140:143], v[60:63]\n\t"
    "s_waitcnt vmcnt(12)\n\t"
    "v_mfma_f32_16x16x32_bf16 v[64:67], a[0:3], a[144:147], 0\n\t"
    "ds_read_b128 a[112:115], v240 offset:28672\n\t"
    "v_mfma_f32_16x16x32_bf16 v[64:67], a[32:35], a[148:151], v[64:67]\n\t"
    "v_mfma_f32_16x16x32_bf16 v[68:71], a[4:7], a[144:147], 0\n\t"
    "v_mfma_f32_16x16x32_bf16 v[68:71], a[36:39], a[148:151], v[68:71]\n\t"
    "ds_read_b128 a[84:87], v240 offset:21504\n\t"
    "v_mfma_f32_16x16x32_bf16 v[72:75], a[8:11], a[144:147], 0\n\t"
    "buffer_load_dwordx4 a[200:203], v254, s[28:31], s43 offen offset:2048 sc1\n\t"
    "v_mfma_f32_16x16x32_bf16 v[72:75], a[40:43], a[148:151], v[72:75]\n\t"
    "v_mfma_f32_16x16x32_bf16 v[76:79], a[12:15], a[144:147], 0\n\t"
    "v_mfma_f32_16x16x32_bf16 v[76:79], a[44:47], a[148:151], v[76:79]\n\t"
    "ds_read_b128 a[116:119], v240 offset:29696\n\t"
    "v_mfma_f32_16x16x32_bf16 v[80:83], a[16:19], a[144:147], 0\n\t"
    "v_mfma_f32_16x16x32_bf16 v[80:83], a[48:51], a[148:151], v[80:83]\n\t"
    "buffer_load_dwordx4 a[204:207], v254, s[28:31], s43 offen offset:3072 sc1\n\t"
    "v_mfma_f32_16x16x32_bf16 v[84:87], a[20:23], a[144:147], 0\n\t"
    "v_mfma_f32_16x16x32_bf16 v[84:87], a[52:55], a[148:151], v[84:87]\n\t"
    "ds_read_b128 a[88:91], v240 offset:22528\n\t"
    "v_mfma_f32_16x16x32_bf16 v[88:91], a[24:27], a[144:147], 0\n\t"
    "v_mfma_f32_16x16x32_bf16 v[88:91], a[56:59], a[148:151], v[88:91]\n\t"
    "ds_read_b128 a[120:123], v240 offset:30720\n\t"
    "v_mfma_f32_16x16x32_bf16 v[92:95], a[28:31], a[144:147], 0\n\t"
    "v_mfma_f32_16x16x32_bf16 v[92:95], a[60:63], a[148:151], v[92:95]\n\t"
    "s_waitcnt vmcnt(12)\n\t"
    "s_add_u32 s43, s62, 20480\n\t"
    "buffer_load_dwordx4 a[208:211], v254, s[28:31], s43 offen offset:0 sc1\n\t"
    "v_mfma_f32_16x16x32_bf16 v[96:99], a[0:3], a[152:155], 0\n\t"
    "v_mfma_f32_16x16x32_bf16 v[96:99], a[32:35], a[156:159], v[96:99]\n\t"
    "ds_read_b128 a[92:95], v240 offset:23552\n\t"
    "v_mfma_f32_16x16x32_bf16 v[100:103], a[4:7], a[152:155], 0\n\t"
    "v_mfma_f32_16x16x32_bf16 v[100:103], a[36:39], a[156:159], v[100:103]\n\t"
    "v_mfma_f32_16x16x32_bf16 v[104:107], a[8:11], a[152:155], 0\n\t"
    "buffer_load_dwordx4 a[212:215], v254, s[28:31], s43 offen offset:1024 sc1\n\t"
    "v_mfma_f32_16x16x32_bf16 v[104:107], a[40:43], a[156:159], v[104:107]\n\t"
    "ds_read_b128 a[124:127], v240 offset:31744\n\t"
    "v_mfma_f32_16x16x32_bf16 v[108:111], a[12:15], a[152:155], 0\n\t"
    "v_mfma_f32_16x16x32_bf16 v[108:111], a[44:47], a[156:159], v[108:111]\n\t"
    "v_mfma_f32_16x16x32_bf16 v[112:115], a[16:19], a[152:155], 0\n\t"
    "v_mfma_f32_16x16x32_bf16 v[112:115], a[48:51], a[156:159], v[112:115]\n\t"
    "v_mfma_f32_16x16x32_bf16 v[116:119], a[20:23], a[152:155], 0\n\t"
    "buffer_load_dwordx4 a[216:219], v254, s[28:31], s43 offen offset:2048 sc1\n\t"
    "v_mfma_f32_16x16x32_bf16 v[116:119], a[52:55], a[156:159], v[116:119]\n\t"
    "v_mfma_f32_16x16x32_bf16 v[120:123], a[24:27], a[152:155], 0\n\t"
    "v_mfma_f32_16x16x32_bf16 v[120:123], a[56:59], a[156:159], v[120:123]\n\t"
    "v_mfma_f32_16x16x32_bf16 v[124:127], a[28:31], a[152:155], 0\n\t"
    "v_mfma_f32_16x16x32_bf16 v[124:127], a[60:63], a[156:159], v[124:127]\n\t"
    "s_waitcnt vmcnt(13) lgkmcnt(12)\n\t"
    "buffer_load_dwordx4 a[220:223], v254, s[28:31], s43 offen offset:3072 sc1\n\t"
    "v_mfma_f32_16x16x32_bf16 v[0:3], a[64:67], a[160:163], v[0:3]\n\t"
    "v_mfma_f32_16x16x32_bf16 v[0:3], a[96:99], a[164:167], v[0:3]\n\t"
    "v_mfma_f32_16x16x32_bf16 v[4:7], a[68:71], a[160:163], v[4:7]\n\t"
    "ds_read_b128 a[0:3], v240 offset:32768\n\t"
    "v_mfma_f32_16x16x32_bf16 v[4:7], a[100:103], a[164:167], v[4:7]\n\t"
    "s_waitcnt lgkmcnt(9)\n\t"
    "v_mfma_f32_16x16x32_bf16 v[8:11], a[72:75], a[160:163], v[8:11]\n\t"
    "ds_read_b128 a[32:35], v240 offset:40960\n\t"
    "v_mfma_f32_16x16x32_bf16 v[8:11], a[104:107], a[164:167], v[8:11]\n\t"
    "s_add_u32 s43, s62, 24576\n\t"
    "buffer_load_dwordx4 a[224:227], v254, s[28:31], s43 offen offset:0 sc1\n\t"
    "v_mfma_f32_16x16x32_bf16 v[12:15], a[76:79], a[160:163], v[12:15]\n\t"
    "v_mfma_f32_16x16x32_bf16 v[12:15], a[108:111], a[164:167], v[12:15]\n\t"
    "s_waitcnt lgkmcnt(6)\n\t"
    "v_mfma_f32_16x16x32_bf16 v[16:19], a[80:83], a[160:163], v[16:19]\n\t"
    "ds_read_b128 a[4:7], v240 offset:33792\n\t"
    "v_mfma_f32_16x16x32_bf16 v[16:19], a[112:115], a[164:167], v[16:19]\n\t"
    "v_mfma_f32_16x16x32_bf16 v[20:23], a[84:87], a[160:163], v[20:23]\n\t"
    "buffer_load_dwordx4 a[228:231], v254, s[28:31], s43 offen offset:1024 sc1\n\t"
    "v_mfma_f32_16x16x32_bf16 v[20:23], a[116:119], a[164:167], v[20:23]\n\t"
    "s_waitcnt lgkmcnt(3)\n\t"
    "v_mfma_f32_16x16x32_bf16 v[24:27], a[88:91], a[160:163], v[24:27]\n\t"
    "ds_read_b128 a[36:39], v240 offset:41984\n\t"
    "v_mfma_f32_16x16x32_bf16 v[24:27], a[120:123], a[164:167], v[24:27]\n\t"
    "v_mfma_f32_16x16x32_bf16 v[28:31], a[92:95], a[160:163], v[28:31]\n\t"
    "v_mfma_f32_16x16x32_bf16 v[28:31], a[124:127], a[164:167], v[28:31]\n\t"
    "s_waitcnt vmcnt(14)\n\t"
    "ds_read_b128 a[8:11], v240 offset:34816\n\t"
    "v_mfma_f32_16x16x32_bf16 v[32:35], a[64:67], a[168:171], v[32:35]\n\t"
    "buffer_load_dwordx4 a[232:235], v254, s[28:31], s43 offen offset:2048 sc1\n\t"
    "v_mfma_f32_16x16x32_bf16 v[32:35], a[96:99], a[172:175], v[32:35]\n\t"
    "v_mfma_f32_16x16x32_bf16 v[36:39], a[68:71], a[168:171], v[36:39]\n\t"
    "ds_read_b128 a[40:43], v240 offset:43008\n\t"
    "v_mfma_f32_16x16x32_bf16 v[36:39], a[100:103], a[172:175], v[36:39]\n\t"
    "v_mfma_f32_16x16x32_bf16 v[40:43], a[72:75], a[168:171], v[40:43]\n\t"
    "v_mfma_f32_16x16x32_bf16 v[40:43], a[104:107], a[172:175], v[40:43]\n\t"
    "buffer_load_dwordx4 a[236:239], v254, s[28:31], s43 offen offset:3072 sc1\n\t"
    "v_mfma_f32_16x16x32_bf16 v[44:47], a[76:79], a[168:171], v[44:47]\n\t"
    "ds_read_b128 a[12:15], v240 offset:35840\n\t"
    "v_mfma_f32_16x16x32_bf16 v[44:47], a[108:111], a[172:175], v[44:47]\n\t"
    "v_mfma_f32_16x16x32_bf16 v[48:51], a[80:83], a[168:171], v[48:51]\n\t"
    "v_mfma_f32_16x16x32_bf16 v[48:51], a[112:115], a[172:175], v[48:51]\n\t"
    "ds_read_b128 a[44:47], v240 offset:44032\n\t"
    "v_mfma_f32_16x16x32_bf16 v[52:55], a[84:87], a[168:171], v[52:55]\n\t"
    "v_mfma_f32_16x16x32_bf16 v[52:55], a[116:119], a[172:175], v[52:55]\n\t"
    "s_add_u32 s43, s62, 28672\n\t"
    "buffer_load_dwordx4 a[240:243], v254, s[28:31], s43 offen offset:0 sc1\n\t"
    "v_mfma_f32_16x16x32_bf16 v[56:59], a[88:91], a[168:171], v[56:59]\n\t"
    "v_mfma_f32_16x16x32_bf16 v[56:59], a[120:123], a[172:175], v[56:59]\n\t"
    "ds_read_b128 a[16:19], v240 offset:36864\n\t"
    "v_mfma_f32_16x16x32_bf16 v[60:63], a[92:95], a[168:171], v[60:63]\n\t"
    "v_mfma_f32_16x16x32_bf16 v[60:63], a[124:127], a[172:175], v[60:63]\n\t"
    "s_waitcnt vmcnt(15)\n\t"
    "ds_read_b128 a[48:51], v240 offset:45056\n\t"
    "v_mfma_f32_16x16x32_bf16 v[64:67], a[64:67], a[176:179], v[64:67]\n\t"
    "buffer_load_dwordx4 a[244:247], v254, s[28:31], s43 offen offset:1024 sc1\n\t"
    "v_mfma_f32_16x16x32_bf16 v[64:67], a[96:99], a[180:183], v[64:67]\n\t"
    "v_mfma_f32_16x16x32_bf16 v[68:71], a[68:71], a[176:179], v[68:71]\n\t"
    "v_mfma_f32_16x16x32_bf16 v[68:71], a[100:103], a[180:183], v[68:71]\n\t"
    "ds_read_b128 a[20:23], v240 offset:37888\n\t"
    "v_mfma_f32_16x16x32_bf16 v[72:75], a[72:75], a[176:179], v[72:75]\n\t"
    "v_mfma_f32_16x16x32_bf16 v[72:75], a[104:107], a[180:183], v[72:75]\n\t"
    "v_mfma_f32_16x16x32_bf16 v[76:79], a[76:79], a[176:179], v[76:79]\n\t"
    "buffer_load_dwordx4 a[248:251], v254, s[28:31], s43 offen offset:2048 sc1\n\t"
    "v_mfma_f32_16x16x32_bf16 v[76:79], a[108:111], a[180:183], v[76:79]\n\t"
    "ds_read_b128 a[52:55], v240 offset:46080\n\t"
    "v_mfma_f32_16x16x32_bf16 v[80:83], a[80:83], a[176:179], v[80:83]\n\t"
    "v_mfma_f32_16x16x32_bf16 v[80:83], a[112:115], a[180:183], v[80:83]\n\t"
    "v_mfma_f32_16x16x32_bf16 v[84:87], a[84:87], a[176:179], v[84:87]\n\t"
    "ds_read_b128 a[24:27], v240 offset:38912\n\t"
    "v_mfma_f32_16x16x32_bf16 v[84:87], a[116:119], a[180:183], v[84:87]\n\t"
    "buffer_load_dwordx4 a[252:255], v254, s[28:31], s43 offen offset:3072 sc1\n\t"
    "v_mfma_f32_16x16x32_bf16 v[88:91], a[88:91], a[176:179], v[88:91]\n\t"
    "v_mfma_f32_16x16x32_bf16 v[88:91], a[120:123], a[180:183], v[88:91]\n\t"
    "ds_read_b128 a[56:59], v240 offset:47104\n\t"
    "v_mfma_f32_16x16x32_bf16 v[92:95], a[92:95], a[176:179], v[92:95]\n\t"
    "v_mfma_f32_16x16x32_bf16 v[92:95], a[124:127], a[180:183], v[92:95]\n\t"
    "s_waitcnt vmcnt(16)\n\t"
    "v_mfma_f32_16x16x32_bf16 v[96:99], a[64:67], a[184:187], v[96:99]\n\t"
    "ds_read_b128 a[28:31], v240 offset:39936\n\t"
    "v_mfma_f32_16x16x32_bf16 v[96:99], a[96:99], a[188:191], v[96:99]\n\t"
    "s_add_u32 s43, s62, 32768\n\t"
    "buffer_load_dwordx4 a[128:131], v254, s[28:31], s43 offen offset:0 sc1\n\t"
    "v_mfma_f32_16x16x32_bf16 v[100:103], a[68:71], a[184:187], v[100:103]\n\t"
    "v_mfma_f32_16x16x32_bf16 v[100:103], a[100:103], a[188:191], v[100:103]\n\t"
    "v_mfma_f32_16x16x32_bf16 v[104:107], a[72:75], a[184:187], v[104:107]\n\t"
    "ds_read_b128 a[60:63], v240 offset:48128\n\t"
    "v_mfma_f32_16x16x32_bf16 v[104:107], a[104:107], a[188:191], v[104:107]\n\t"
    "v_mfma_f32_16x16x32_bf16 v[108:111], a[76:79], a[184:187], v[108:111]\n\t"
    "s_add_u32 s43, s73, 0\n\t"
    "buffer_load_dwordx4 v[224:227], v243, s[28:31], s43 offen offset:0 sc1\n\t"
    "v_mfma_f32_16x16x32_bf16 v[108:111], a[108:111], a[188:191], v[108:111]\n\t"
    "v_mfma_f32_16x16x32_bf16 v[112:115], a[80:83], a[184:187], v[112:115]\n\t"
    "v_mfma_f32_16x16x32_bf16 v[112:115], a[112:115], a[188:191], v[112:115]\n\t"
    "v_mfma_f32_16x16x32_bf16 v[116:119], a[84:87], a[184:187], v[116:119]\n\t"
    "v_mfma_f32_16x16x32_bf16 v[116:119], a[116:119], a[188:191], v[116:119]\n\t"
    "v_mfma_f32_16x16x32_bf16 v[120:123], a[88:91], a[184:187], v[120:123]\n\t"
    "s_add_u32 s43, s62, 32768\n\t"
    "buffer_load_dwordx4 a[132:135], v254, s[28:31], s43 offen offset:1024 sc1\n\t"
    "v_mfma_f32_16x16x32_bf16 v[120:123], a[120:123], a[188:191], v[120:123]\n\t"
    "v_mfma_f32_16x16x32_bf16 v[124:127], a[92:95], a[184:187], v[124:127]\n\t"
    "v_mfma_f32_16x16x32_bf16 v[124:127], a[124:127], a[188:191], v[124:127]\n\t"
    "s_waitcnt vmcnt(17) lgkmcnt(12)\n\t"
    "v_mfma_f32_16x16x32_bf16 v[0:3], a[0:3], a[192:195], v[0:3]\n\t"
    "v_mfma_f32_16x16x32_bf16 v[0:3], a[32:35], a[196:199], v[0:3]\n\t"
    "s_add_u32 s43, s73, 0\n\t"
    "buffer_load_dwordx4 v[228:231], v243, s[28:31], s43 offen offset:1024 sc1\n\t"
    "v_mfma_f32_16x16x32_bf16 v[4:7], a[4:7], a[192:195], v[4:7]\n\t"
    "ds_read_b128 a[64:67], v240 offset:49152\n\t"
    "v_mfma_f32_16x16x32_bf16 v[4:7], a[36:39], a[196:199], v[4:7]\n\t"
    "s_waitcnt lgkmcnt(9)\n\t"
    "v_mfma_f32_16x16x32_bf16 v[8:11], a[8:11], a[192:195], v[8:11]\n\t"
    "ds_read_b128 a[96:99], v240 offset:57344\n\t"
    "v_mfma_f32_16x16x32_bf16 v[8:11], a[40:43], a[196:199], v[8:11]\n\t"
    "v_mfma_f32_16x16x32_bf16 v[12:15], a[12:15], a[192:195], v[12:15]\n\t"
    "v_mfma_f32_16x16x32_bf16 v[12:15], a[44:47], a[196:199], v[12:15]\n\t"
    "s_waitcnt lgkmcnt(6)\n\t"
    "buffer_load_dwordx4 v[232:235], v243, s[28:31], s43 offen offset:2048 sc1\n\t"
    "v_mfma_f32_16x16x32_bf16 v[16:19], a[16:19], a[192:195], v[16:19]\n\t"
    "ds_read_b128 a[68:71], v240 offset:50176\n\t"
    "v_mfma_f32_16x16x32_bf16 v[16:19], a[48:51], a[196:199], v[16:19]\n\t"
    "v_mfma_f32_16x16x32_bf16 v[20:23], a[20:23], a[192:195], v[20:23]\n\t"
    "v_mfma_f32_16x16x32_bf16 v[20:23], a[52:55], a[196:199], v[20:23]\n\t"
    "s_waitcnt lgkmcnt(3)\n\t"
    "ds_read_b128 a[100:103], v240 offset:58368\n\t"
    "v_mfma_f32_16x16x32_bf16 v[24:27], a[24:27], a[192:195], v[24:27]\n\t"
    "buffer_load_dwordx4 v[236:239], v243, s[28:31], s43 offen offset:3072 sc1\n\t"
    "v_mfma_f32_16x16x32_bf16 v[24:27], a[56:59], a[196:199], v[24:27]\n\t"
    "v_mfma_f32_16x16x32_bf16 v[28:31], a[28:31], a[192:195], v[28:31]\n\t"
    "v_mfma_f32_16x16x32_bf16 v[28:31], a[60:63], a[196:199], v[28:31]\n\t"
    "s_waitcnt vmcnt(18)\n\t"
    "ds_read_b128 a[72:75], v240 offset:51200\n\t"
    "v_mfma_f32_16x16x32_bf16 v[32:35], a[0:3], a[200:203], v[32:35]\n\t"
    "v_mfma_f32_16x16x32_bf16 v[32:35], a[32:35], a[204:207], v[32:35]\n\t"
    "v_mfma_f32_16x16x32_bf16 v[36:39], a[4:7], a[200:203], v[36:39]\n\t"
    "s_add_u32 s43, s62, 32768\n\t"
    "buffer_load_dwordx4 a[136:139], v254, s[28:31], s43 offen offset:2048 sc1\n\t"
    "v_mfma_f32_16x16x32_bf16 v[36:39], a[36:39], a[204:207], v[36:39]\n\t"
    "ds_read_b128 a[104:107], v240 offset:59392\n\t"
    "v_mfma_f32_16x16x32_bf16 v[40:43], a[8:11], a[200:203], v[40:43]\n\t"
    "v_mfma_f32_16x16x32_bf16 v[40:43], a[40:43], a[204:207], v[40:43]\n\t"
    "ds_read_b128 a[76:79], v240 offset:52224\n\t"
    "v_mfma_f32_16x16x32_bf16 v[44:47], a[12:15], a[200:203], v[44:47]\n\t"
    "v_mfma_f32_16x16x32_bf16 v[44:47], a[44:47], a[204:207], v[44:47]\n\t"
    "buffer_load_dwordx4 a[140:143], v254, s[28:31], s43 offen offset:3072 sc1\n\t"
    "v_mfma_f32_16x16x32_bf16 v[48:51], a[16:19], a[200:203], v[48:51]\n\t"
    "v_mfma_f32_16x16x32_bf16 v[48:51], a[48:51], a[204:207], v[48:51]\n\t"
    "ds_read_b128 a[108:111], v240 offset:60416\n\t"
    "v_mfma_f32_16x16x32_bf16 v[52:55], a[20:23], a[200:203], v[52:55]\n\t"
    "v_mfma_f32_16x16x32_bf16 v[52:55], a[52:55], a[204:207], v[52:55]\n\t"
    "v_mfma_f32_16x16x32_bf16 v[56:59], a[24:27], a[200:203], v[56:59]\n\t"
    "ds_read_b128 a[80:83], v240 offset:53248\n\t"
    "v_mfma_f32_16x16x32_bf16 v[56:59], a[56:59], a[204:207], v[56:59]\n\t"
    "s_add_u32 s43, s62, 36864\n\t"
    "buffer_load_dwordx4 a[144:147], v254, s[28:31], s43 offen offset:0 sc1\n\t"
    "v_mfma_f32_16x16x32_bf16 v[60:63], a[28:31], a[200:203], v[60:63]\n\t"
    "v_mfma_f32_16x16x32_bf16 v[60:63], a[60:63], a[204:207], v[60:63]\n\t"
    "s_waitcnt vmcnt(19)\n\t"
    "v_mfma_f32_16x16x32_bf16 v[64:67], a[0:3], a[208:211], v[64:67]\n\t"
    "ds_read_b128 a[112:115], v240 offset:61440\n\t"
    "v_mfma_f32_16x16x32_bf16 v[64:67], a[32:35], a[212:215], v[64:67]\n\t"
    "v_mfma_f32_16x16x32_bf16 v[68:71], a[4:7], a[208:211], v[68:71]\n\t"
    "buffer_load_dwordx4 a[148:151], v254, s[28:31], s43 offen offset:1024 sc1\n\t"
    "v_mfma_f32_16x16x32_bf16 v[68:71], a[36:39], a[212:215], v[68:71]\n\t"
    "v_mfma_f32_16x16x32_bf16 v[72:75], a[8:11], a[208:211], v[72:75]\n\t"
    "ds_read_b128 a[84:87], v240 offset:54272\n\t"
    "v_mfma_f32_16x16x32_bf16 v[72:75], a[40:43], a[212:215], v[72:75]\n\t"
    "v_mfma_f32_16x16x32_bf16 v[76:79], a[12:15], a[208:211], v[76:79]\n\t"
    "ds_read_b128 a[116:119], v240 offset:62464\n\t"
    "v_mfma_f32_16x16x32_bf16 v[76:79], a[44:47], a[212:215], v[76:79]\n\t"
    "v_mfma_f32_16x16x32_bf16 v[80:83], a[16:19], a[208:211], v[80:83]\n\t"
    "buffer_load_dwordx4 a[152:155], v254, s[28:31], s43 offen offset:2048 sc1\n\t"
    "v_mfma_f32_16x16x32_bf16 v[80:83], a[48:51], a[212:215], v[80:83]\n\t"
    "v_mfma_f32_16x16x32_bf16 v[84:87], a[20:23], a[208:211], v[84:87]\n\t"
    "ds_read_b128 a[88:91], v240 offset:55296\n\t"
    "v_mfma_f32_16x16x32_bf16 v[84:87], a[52:55], a[212:215], v[84:87]\n\t"
    "v_mfma_f32_16x16x32_bf16 v[88:91], a[24:27], a[208:211], v[88:91]\n\t"
    "v_mfma_f32_16x16x32_bf16 v[88:91], a[56:59], a[212:215], v[88:91]\n\t"
    "buffer_load_dwordx4 a[156:159], v254, s[28:31], s43 offen offset:3072 sc1\n\t"
    "v_mfma_f32_16x16x32_bf16 v[92:95], a[28:31], a[208:211], v[92:95]\n\t"
    "ds_read_b128 a[120:123], v240 offset:63488\n\t"
    "v_mfma_f32_16x16x32_bf16 v[92:95], a[60:63], a[212:215], v[92:95]\n\t"
    "s_waitcnt vmcnt(20)\n\t"
    "v_mfma_f32_16x16x32_bf16 v[96:99], a[0:3], a[216:219], v[96:99]\n\t"
    "v_mfma_f32_16x16x32_bf16 v[96:99], a[32:35], a[220:223], v[96:99]\n\t"
    "ds_read_b128 a[92:95], v240 offset:56320\n\t"
    "v_mfma_f32_16x16x32_bf16 v[100:103], a[4:7], a[216:219], v[100:103]\n\t"
    "v_mfma_f32_16x16x32_bf16 v[100:103], a[36:39], a[220:223], v[100:103]\n\t"
    "s_add_u32 s43, s62, 40960\n\t"
    "buffer_load_dwordx4 a[160:163], v254, s[28:31], s43 offen offset:0 sc1\n\t"
    "v_mfma_f32_16x16x32_bf16 v[104:107], a[8:11], a[216:219], v[104:107]\n\t"
    "v_mfma_f32_16x16x32_bf16 v[104:107], a[40:43], a[220:223], v[104:107]\n\t"
    "ds_read_b128 a[124:127], v240 offset:64512\n\t"
    "v_mfma_f32_16x16x32_bf16 v[108:111], a[12:15], a[216:219], v[108:111]\n\t"
    "v_mfma_f32_16x16x32_bf16 v[108:111], a[44:47], a[220:223], v[108:111]\n\t"
    "v_mfma_f32_16x16x32_bf16 v[112:115], a[16:19], a[216:219], v[112:115]\n\t"
    "buffer_load_dwordx4 a[164:167], v254, s[28:31], s43 offen offset:1024 sc1\n\t"
    "v_mfma_f32_16x16x32_bf16 v[112:115], a[48:51], a[220:223], v[112:115]\n\t"
    "v_mfma_f32_16x16x32_bf16 v[116:119], a[20:23], a[216:219], v[116:119]\n\t"
    "v_mfma_f32_16x16x32_bf16 v[116:119], a[52:55], a[220:223], v[116:119]\n\t"
    "v_mfma_f32_16x16x32_bf16 v[120:123], a[24:27], a[216:219], v[120:123]\n\t"
    "v_mfma_f32_16x16x32_bf16 v[120:123], a[56:59], a[220:223], v[120:123]\n\t"
    "v_mfma_f32_16x16x32_bf16 v[124:127], a[28:31], a[216:219], v[124:127]\n\t"
    "buffer_load_dwordx4 a[168:171], v254, s[28:31], s43 offen offset:2048 sc1\n\t"
    "v_mfma_f32_16x16x32_bf16 v[124:127], a[60:63], a[220:223], v[124:127]\n\t"
    "s_waitcnt vmcnt(21) lgkmcnt(12)\n\t"
    "v_mfma_f32_16x16x32_bf16 v[0:3], a[64:67], a[224:227], v[0:3]\n\t"
    "v_mfma_f32_16x16x32_bf16 v[0:3], a[96:99], a[228:231], v[0:3]\n\t"
    "v_mfma_f32_16x16x32_bf16 v[4:7], a[68:71], a[224:227], v[4:7]\n\t"
    "ds_read_b128 a[0:3], v241 offset:0\n\t"
    "v_mfma_f32_16x16x32_bf16 v[4:7], a[100:103], a[228:231], v[4:7]\n\t"
    "s_waitcnt lgkmcnt(9)\n\t"
    "buffer_load_dwordx4 a[172:175], v254, s[28:31], s43 offen offset:3072 sc1\n\t"
    "v_mfma_f32_16x16x32_bf16 v[8:11], a[72:75], a[224:227], v[8:11]\n\t"
    "v_mfma_f32_16x16x32_bf16 v[8:11], a[104:107], a[228:231], v[8:11]\n\t"
    "ds_read_b128 a[32:35], v241 offset:8192\n\t"
    "v_mfma_f32_16x16x32_bf16 v[12:15], a[76:79], a[224:227], v[12:15]\n\t"
    "v_mfma_f32_16x16x32_bf16 v[12:15], a[108:111], a[228:231], v[12:15]\n\t"
    "s_waitcnt lgkmcnt(6)\n\t"
    "v_mfma_f32_16x16x32_bf16 v[16:19], a[80:83], a[224:227], v[16:19]\n\t"
    "ds_read_b128 a[4:7], v241 offset:1024\n\t"
    "v_mfma_f32_16x16x32_bf16 v[16:19], a[112:115], a[228:231], v[16:19]\n\t"
    "s_add_u32 s43, s62, 45056\n\t"
    "buffer_load_dwordx4 a[176:179], v254, s[28:31], s43 offen offset:0 sc1\n\t"
    "v_mfma_f32_16x16x32_bf16 v[20:23], a[84:87], a[224:227], v[20:23]\n\t"
    "v_mfma_f32_16x16x32_bf16 v[20:23], a[116:119], a[228:231], v[20:23]\n\t"
    "s_waitcnt lgkmcnt(3)\n\t"
    "v_mfma_f32_16x16x32_bf16 v[24:27], a[88:91], a[224:227], v[24:27]\n\t"
    "ds_read_b128 a[36:39], v241 offset:9216\n\t"
    "v_mfma_f32_16x16x32_bf16 v[24:27], a[120:123], a[228:231], v[24:27]\n\t"
    "v_mfma_f32_16x16x32_bf16 v[28:31], a[92:95], a[224:227], v[28:31]\n\t"
    "buffer_load_dwordx4 a[180:183], v254, s[28:31], s43 offen offset:1024 sc1\n\t"
    "v_mfma_f32_16x16x32_bf16 v[28:31], a[124:127], a[228:231], v[28:31]\n\t"
    "s_waitcnt vmcnt(22)\n\t"
    "v_mfma_f32_16x16x32_bf16 v[32:35], a[64:67], a[232:235], v[32:35]\n\t"
    "ds_read_b128 a[8:11], v241 offset:2048\n\t"
    "v_mfma_f32_16x16x32_bf16 v[32:35], a[96:99], a[236:239], v[32:35]\n\t"
    "v_mfma_f32_16x16x32_bf16 v[36:39], a[68:71], a[232:235], v[36:39]\n\t"
    "ds_read_b128 a[40:43], v241 offset:10240\n\t"
    "v_mfma_f32_16x16x32_bf16 v[36:39], a[100:103], a[236:239], v[36:39]\n\t"
    "v_mfma_f32_16x16x32_bf16 v[40:43], a[72:75], a[232:235], v[40:43]\n\t"
    "buffer_load_dwordx4 a[184:187], v254, s[28:31], s43 offen offset:2048 sc1\n\t"
    "v_mfma_f32_16x16x32_bf16 v[40:43], a[104:107], a[236:239], v[40:43]\n\t"
    "v_mfma_f32_16x16x32_bf16 v[44:47], a[76:79], a[232:235], v[44:47]\n\t"
    "ds_read_b128 a[12:15], v241 offset:3072\n\t"
    "v_mfma_f32_16x16x32_bf16 v[44:47], a[108:111], a[236:239], v[44:47]\n\t"
    "v_mfma_f32_16x16x32_bf16 v[48:51], a[80:83], a[232:235], v[48:51]\n\t"
    "v_mfma_f32_16x16x32_bf16 v[48:51], a[112:115], a[236:239], v[48:51]\n\t"
    "buffer_load_dwordx4 a[188:191], v254, s[28:31], s43 offen offset:3072 sc1\n\t"
    "v_mfma_f32_16x16x32_bf16 v[52:55], a[84:87], a[232:235], v[52:55]\n\t"
    "ds_read_b128 a[44:47], v241 offset:11264\n\t"
    "v_mfma_f32_16x16x32_bf16 v[52:55], a[116:119], a[236:239], v[52:55]\n\t"
    "v_mfma_f32_16x16x32_bf16 v[56:59], a[88:91], a[232:235], v[56:59]\n\t"
    "v_mfma_f32_16x16x32_bf16 v[56:59], a[120:123], a[236:239], v[56:59]\n\t"
    "ds_read_b128 a[16:19], v241 offset:4096\n\t"
    "v_mfma_f32_16x16x32_bf16 v[60:63], a[92:95], a[232:235], v[60:63]\n\t"
    "v_mfma_f32_16x16x32_bf16 v[60:63], a[124:127], a[236:239], v[60:63]\n\t"
    "s_waitcnt vmcnt(22)\n\t"
    "s_add_u32 s43, s62, 49152\n\t"
    "buffer_load_dwordx4 a[192:195], v254, s[28:31], s43 offen offset:0 sc1\n\t"
    "v_mfma_f32_16x16x32_bf16 v[64:67], a[64:67], a[240:243], v[64:67]\n\t"
    "ds_read_b128 a[48:51], v241 offset:12288\n\t"
    "v_mfma_f32_16x16x32_bf16 v[64:67], a[96:99], a[244:247], v[64:67]\n\t"
    "v_mfma_f32_16x16x32_bf16 v[68:71], a[68:71], a[240:243], v[68:71]\n\t"
    "v_mfma_f32_16x16x32_bf16 v[68:71], a[100:103], a[244:247], v[68:71]\n\t"
    "ds_read_b128 a[20:23], v241 offset:5120\n\t"
    "v_mfma_f32_16x16x32_bf16 v[72:75], a[72:75], a[240:243], v[72:75]\n\t"
    "buffer_load_dwordx4 a[196:199], v254, s[28:31], s43 offen offset:1024 sc1\n\t"
    "v_mfma_f32_16x16x32_bf16 v[72:75], a[104:107], a[244:247], v[72:75]\n\t"
    "v_mfma_f32_16x16x32_bf16 v[76:79], a[76:79], a[240:243], v[76:79]\n\t"
    "v_mfma_f32_16x16x32_bf16 v[76:79], a[108:111], a[244:247], v[76:79]\n\t"
    "ds_read_b128 a[52:55], v241 offset:13312\n\t"
    "v_mfma_f32_16x16x32_bf16 v[80:83], a[80:83], a[240:243], v[80:83]\n\t"
    "v_mfma_f32_16x16x32_bf16 v[80:83], a[112:115], a[244:247], v[80:83]\n\t"
    "v_mfma_f32_16x16x32_bf16 v[84:87], a[84:87], a[240:243], v[84:87]\n\t"
    "buffer_load_dwordx4 a[200:203], v254, s[28:31], s43 offen offset:2048 sc1\n\t"
    "v_mfma_f32_16x16x32_bf16 v[84:87], a[116:119], a[244:247], v[84:87]\n\t"
    "ds_read_b128 a[24:27], v241 offset:6144\n\t"
    "v_mfma_f32_16x16x32_bf16 v[88:91], a[88:91], a[240:243], v[88:91]\n\t"
    "v_mfma_f32_16x16x32_bf16 v[88:91], a[120:123], a[244:247], v[88:91]\n\t"
    "ds_read_b128 a[56:59], v241 offset:14336\n\t"
    "v_mfma_f32_16x16x32_bf16 v[92:95], a[92:95], a[240:243], v[92:95]\n\t"
    "v_mfma_f32_16x16x32_bf16 v[92:95], a[124:127], a[244:247], v[92:95]\n\t"
    "s_waitcnt vmcnt(23)\n\t"
    "buffer_load_dwordx4 a[204:207], v254, s[28:31], s43 offen offset:3072 sc1\n\t"
    "v_mfma_f32_16x16x32_bf16 v[96:99], a[64:67], a[248:251], v[96:99]\n\t"
    "v_mfma_f32_16x16x32_bf16 v[96:99], a[96:99], a[252:255], v[96:99]\n\t"
    "ds_read_b128 a[28:31], v241 offset:7168\n\t"
    "v_mfma_f32_16x16x32_bf16 v[100:103], a[68:71], a[248:251], v[100:103]\n\t"
    "v_mfma_f32_16x16x32_bf16 v[100:103], a[100:103], a[252:255], v[100:103]\n\t"
    "v_mfma_f32_16x16x32_bf16 v[104:107], a[72:75], a[248:251], v[104:107]\n\t"
    "ds_read_b128 a[60:63], v241 offset:15360\n\t"
    "v_mfma_f32_16x16x32_bf16 v[104:107], a[104:107], a[252:255], v[104:107]\n\t"
    "s_add_u32 s43, s62, 53248\n\t"
    "buffer_load_dwordx4 a[208:211], v254, s[28:31], s43 offen offset:0 sc1\n\t"
    "v_mfma_f32_16x16x32_bf16 v[108:111], a[76:79], a[248:251], v[108:111]\n\t"
    "v_mfma_f32_16x16x32_bf16 v[108:111], a[108:111], a[252:255], v[108:111]\n\t"
    "v_mfma_f32_16x16x32_bf16 v[112:115], a[80:83], a[248:251], v[112:115]\n\t"
    "v_mfma_f32_16x16x32_bf16 v[112:115], a[112:115], a[252:255], v[112:115]\n\t"
    "v_mfma_f32_16x16x32_bf16 v[116:119], a[84:87], a[248:251], v[116:119]\n\t"
    "buffer_load_dwordx4 a[212:215], v254, s[28:31], s43 offen offset:1024 sc1\n\t"
    "v_mfma_f32_16x16x32_bf16 v[116:119], a[116:119], a[252:255], v[116:119]\n\t"
    "v_mfma_f32_16x16x32_bf16 v[120:123], a[88:91], a[248:251], v[120:123]\n\t"
    "v_mfma_f32_16x16x32_bf16 v[120:123], a[120:123], a[252:255], v[120:123]\n\t"
    "v_mfma_f32_16x16x32_bf16 v[124:127], a[92:95], a[248:251], v[124:127]\n\t"
    "v_mfma_f32_16x16x32_bf16 v[124:127], a[124:127], a[252:255], v[124:127]\n\t"
    "s_waitcnt vmcnt(23) lgkmcnt(12)\n\t"
    "v_mfma_f32_16x16x32_bf16 v[0:3], a[0:3], a[128:131], v[0:3]\n\t"
    "buffer_load_dwordx4 a[216:219], v254, s[28:31], s43 offen offset:2048 sc1\n\t"
    "v_mfma_f32_16x16x32_bf16 v[0:3], a[32:35], a[132:135], v[0:3]\n\t"
    "v_mfma_f32_16x16x32_bf16 v[4:7], a[4:7], a[128:131], v[4:7]\n\t"
    "ds_read_b128 a[64:67], v241 offset:16384\n\t"
    "v_mfma_f32_16x16x32_bf16 v[4:7], a[36:39], a[132:135], v[4:7]\n\t"
    "s_waitcnt lgkmcnt(9)\n\t"
    "s_lshl_b32 s98, s10, 7\n\t"
    "v_mfma_f32_16x16x32_bf16 v[8:11], a[8:11], a[128:131], v[8:11]\n\t"
    "s_add_u32 s98, s98, 643072\n\t"
    "buffer_load_dword v192, off, s[28:31], s98 sc1\n\t"
    "v_mfma_f32_16x16x32_bf16 v[8:11], a[40:43], a[132:135], v[8:11]\n\t"
    "buffer_load_dwordx4 a[220:223], v254, s[28:31], s43 offen offset:3072 sc1\n\t"
    "v_mfma_f32_16x16x32_bf16 v[12:15], a[12:15], a[128:131], v[12:15]\n\t"
    "ds_read_b128 a[96:99], v241 offset:24576\n\t"
    "v_mfma_f32_16x16x32_bf16 v[12:15], a[44:47], a[132:135], v[12:15]\n\t"
    "s_waitcnt lgkmcnt(6)\n\t"
    "v_mfma_f32_16x16x32_bf16 v[16:19], a[16:19], a[128:131], v[16:19]\n\t"
    "v_mfma_f32_16x16x32_bf16 v[16:19], a[48:51], a[132:135], v[16:19]\n\t"
    "ds_read_b128 a[68:71], v241 offset:17408\n\t"
    "v_mfma_f32_16x16x32_bf16 v[20:23], a[20:23], a[128:131], v[20:23]\n\t"
    "v_mfma_f32_16x16x32_bf16 v[20:23], a[52:55], a[132:135], v[20:23]\n\t"
    "s_waitcnt lgkmcnt(3)\n\t"
    "s_add_u32 s43, s62, 57344\n\t"
    "buffer_load_dwordx4 a[224:227], v254, s[28:31], s43 offen offset:0 sc1\n\t"
    "v_mfma_f32_16x16x32_bf16 v[24:27], a[24:27], a[128:131], v[24:27]\n\t"
    "v_mfma_f32_16x16x32_bf16 v[24:27], a[56:59], a[132:135], v[24:27]\n\t"
    "ds_read_b128 a[100:103], v241 offset:25600\n\t"
    "v_mfma_f32_16x16x32_bf16 v[28:31], a[28:31], a[128:131], v[28:31]\n\t"
    "v_mfma_f32_16x16x32_bf16 v[28:31], a[60:63], a[132:135], v[28:31]\n\t"
    "s_waitcnt vmcnt(22)\n\t"
    "ds_read_b128 a[72:75], v241 offset:18432\n\t"
    "v_mfma_f32_16x16x32_bf16 v[32:35], a[0:3], a[136:139], v[32:35]\n\t"
    "buffer_load_dwordx4 a[228:231], v254, s[28:31], s43 offen offset:1024 sc1\n\t"
    "v_mfma_f32_16x16x32_bf16 v[32:35], a[32:35], a[140:143], v[32:35]\n\t"
    "v_mfma_f32_16x16x32_bf16 v[36:39], a[4:7], a[136:139], v[36:39]\n\t"
    "v_mfma_f32_16x16x32_bf16 v[36:39], a[36:39], a[140:143], v[36:39]\n\t"
    "ds_read_b128 a[104:107], v241 offset:26624\n\t"
    "v_mfma_f32_16x16x32_bf16 v[40:43], a[8:11], a[136:139], v[40:43]\n\t"
    "v_mfma_f32_16x16x32_bf16 v[40:43], a[40:43], a[140:143], v[40:43]\n\t"
    "v_mfma_f32_16x16x32_bf16 v[44:47], a[12:15], a[136:139], v[44:47]\n\t"
    "buffer_load_dwordx4 a[232:235], v254, s[28:31], s43 offen offset:2048 sc1\n\t"
    "v_mfma_f32_16x16x32_bf16 v[44:47], a[44:47], a[140:143], v[44:47]\n\t"
    "ds_read_b128 a[76:79], v241 offset:19456\n\t"
    "v_mfma_f32_16x16x32_bf16 v[48:51], a[16:19], a[136:139], v[48:51]\n\t"
    "v_mfma_f32_16x16x32_bf16 v[48:51], a[48:51], a[140:143], v[48:51]\n\t"
    "ds_read_b128 a[108:111], v241 offset:27648\n\t"
    "v_mfma_f32_16x16x32_bf16 v[52:55], a[20:23], a[136:139], v[52:55]\n\t"
    "v_mfma_f32_16x16x32_bf16 v[52:55], a[52:55], a[140:143], v[52:55]\n\t"
    "buffer_load_dwordx4 a[236:239], v254, s[28:31], s43 offen offset:3072 sc1\n\t"
    "v_mfma_f32_16x16x32_bf16 v[56:59], a[24:27], a[136:139], v[56:59]\n\t"
    "v_mfma_f32_16x16x32_bf16 v[56:59], a[56:59], a[140:143], v[56:59]\n\t"
    "ds_read_b128 a[80:83], v241 offset:20480\n\t"
    "v_mfma_f32_16x16x32_bf16 v[60:63], a[28:31], a[136:139], v[60:63]\n\t"
    "v_mfma_f32_16x16x32_bf16 v[60:63], a[60:63], a[140:143], v[60:63]\n\t"
    "s_waitcnt vmcnt(23)\n\t"
    "v_mfma_f32_16x16x32_bf16 v[64:67], a[0:3], a[144:147], v[64:67]\n\t"
    "ds_read_b128 a[112:115], v241 offset:28672\n\t"
    "v_mfma_f32_16x16x32_bf16 v[64:67], a[32:35], a[148:151], v[64:67]\n\t"
    "s_add_u32 s43, s62, 61440\n\t"
    "buffer_load_dwordx4 a[240:243], v254, s[28:31], s43 offen offset:0 sc1\n\t"
    "v_mfma_f32_16x16x32_bf16 v[68:71], a[4:7], a[144:147], v[68:71]\n\t"
    "v_mfma_f32_16x16x32_bf16 v[68:71], a[36:39], a[148:151], v[68:71]\n\t"
    "ds_read_b128 a[84:87], v241 offset:21504\n\t"
    "v_mfma_f32_16x16x32_bf16 v[72:75], a[8:11], a[144:147], v[72:75]\n\t"
    "v_mfma_f32_16x16x32_bf16 v[72:75], a[40:43], a[148:151], v[72:75]\n\t"
    "v_mfma_f32_16x16x32_bf16 v[76:79], a[12:15], a[144:147], v[76:79]\n\t"
    "buffer_load_dwordx4 a[244:247], v254, s[28:31], s43 offen offset:1024 sc1\n\t"
    "v_mfma_f32_16x16x32_bf16 v[76:79], a[44:47], a[148:151], v[76:79]\n\t"
    "ds_read_b128 a[116:119], v241 offset:29696\n\t"
    "v_mfma_f32_16x16x32_bf16 v[80:83], a[16:19], a[144:147], v[80:83]\n\t"
    "v_mfma_f32_16x16x32_bf16 v[80:83], a[48:51], a[148:151], v[80:83]\n\t"
    "v_mfma_f32_16x16x32_bf16 v[84:87], a[20:23], a[144:147], v[84:87]\n\t"
    "ds_read_b128 a[88:91], v241 offset:22528\n\t"
    "v_mfma_f32_16x16x32_bf16 v[84:87], a[52:55], a[148:151], v[84:87]\n\t"
    "v_mfma_f32_16x16x32_bf16 v[88:91], a[24:27], a[144:147], v[88:91]\n\t"
    "buffer_load_dwordx4 a[248:251], v254, s[28:31], s43 offen offset:2048 sc1\n\t"
    "v_mfma_f32_16x16x32_bf16 v[88:91], a[56:59], a[148:151], v[88:91]\n\t"
    "ds_read_b128 a[120:123], v241 offset:30720\n\t"
    "v_mfma_f32_16x16x32_bf16 v[92:95], a[28:31], a[144:147], v[92:95]\n\t"
    "v_mfma_f32_16x16x32_bf16 v[92:95], a[60:63], a[148:151], v[92:95]\n\t"
    "s_waitcnt vmcnt(24)\n\t"
    "v_mfma_f32_16x16x32_bf16 v[96:99], a[0:3], a[152:155], v[96:99]\n\t"
    "ds_read_b128 a[92:95], v241 offset:23552\n\t"
    "v_mfma_f32_16x16x32_bf16 v[96:99], a[32:35], a[156:159], v[96:99]\n\t"
    "buffer_load_dwordx4 a[252:255], v254, s[28:31], s43 offen offset:3072 sc1\n\t"
    "v_mfma_f32_16x16x32_bf16 v[100:103], a[4:7], a[152:155], v[100:103]\n\t"
    "v_mfma_f32_16x16x32_bf16 v[100:103], a[36:39], a[156:159], v[100:103]\n\t"
    "v_mfma_f32_16x16x32_bf16 v[104:107], a[8:11], a[152:155], v[104:107]\n\t"
    "ds_read_b128 a[124:127], v241 offset:31744\n\t"
    "v_mfma_f32_16x16x32_bf16 v[104:107], a[40:43], a[156:159], v[104:107]\n\t"
    "v_mfma_f32_16x16x32_bf16 v[108:111], a[12:15], a[152:155], v[108:111]\n\t"
    "v_mfma_f32_16x16x32_bf16 v[108:111], a[44:47], a[156:159], v[108:111]\n\t"
    "s_add_u32 s43, s62, 65536\n\t"
    "buffer_load_dwordx4 a[128:131], v254, s[28:31], s43 offen offset:0 sc1\n\t"
    "v_mfma_f32_16x16x32_bf16 v[112:115], a[16:19], a[152:155], v[112:115]\n\t"
    "v_mfma_f32_16x16x32_bf16 v[112:115], a[48:51], a[156:159], v[112:115]\n\t"
    "v_mfma_f32_16x16x32_bf16 v[116:119], a[20:23], a[152:155], v[116:119]\n\t"
    "v_mfma_f32_16x16x32_bf16 v[116:119], a[52:55], a[156:159], v[116:119]\n\t"
    "v_mfma_f32_16x16x32_bf16 v[120:123], a[24:27], a[152:155], v[120:123]\n\t"
    "buffer_load_dwordx4 a[132:135], v254, s[28:31], s43 offen offset:1024 sc1\n\t"
    "v_mfma_f32_16x16x32_bf16 v[120:123], a[56:59], a[156:159], v[120:123]\n\t"
    "v_mfma_f32_16x16x32_bf16 v[124:127], a[28:31], a[152:155], v[124:127]\n\t"
    "v_mfma_f32_16x16x32_bf16 v[124:127], a[60:63], a[156:159], v[124:127]\n\t"
    "s_waitcnt vmcnt(26) lgkmcnt(15)\n\t"
    "v_mfma_f32_16x16x32_bf16 v[0:3], a[64:67], a[160:163], v[0:3]\n\t"
    "s_waitcnt vmcnt(25) lgkmcnt(14)\n\t"
    "v_mfma_f32_16x16x32_bf16 v[0:3], a[96:99], a[164:167], v[0:3]\n\t"
    "s_barrier\n\t"
    "s_waitcnt lgkmcnt(10)\n\t"
    "v_mfma_f32_16x16x32_bf16 v[4:7], a[68:71], a[160:163], v[4:7]\n\t"
    "buffer_load_dwordx4 a[136:139], v254, s[28:31], s43 offen offset:2048 sc1\n\t"
    "v_mfma_f32_16x16x32_bf16 v[4:7], a[100:103], a[164:167], v[4:7]\n\t"
    "ds_read_b128 a[0:3], v241 offset:32768\n\t"
    "v_mfma_f32_16x16x32_bf16 v[8:11], a[72:75], a[160:163], v[8:11]\n\t"
    "v_mfma_f32_16x16x32_bf16 v[8:11], a[104:107], a[164:167], v[8:11]\n\t"
    "s_waitcnt lgkmcnt(7)\n\t"
    "ds_read_b128 a[32:35], v241 offset:40960\n\t"
    "v_mfma_f32_16x16x32_bf16 v[12:15], a[76:79], a[160:163], v[12:15]\n\t"
    "v_mfma_f32_16x16x32_bf16 v[12:15], a[108:111], a[164:167], v[12:15]\n\t"
    "buffer_load_dwordx4 a[140:143], v254, s[28:31], s43 offen offset:3072 sc1\n\t"
    "v_mfma_f32_16x16x32_bf16 v[16:19], a[80:83], a[160:163], v[16:19]\n\t"
    "v_mfma_f32_16x16x32_bf16 v[16:19], a[112:115], a[164:167], v[16:19]\n\t"
    "s_waitcnt lgkmcnt(4)\n\t"
    "ds_read_b128 a[4:7], v241 offset:33792\n\t"
    "v_mfma_f32_16x16x32_bf16 v[20:23], a[84:87], a[160:163], v[20:23]\n\t"
    "v_mfma_f32_16x16x32_bf16 v[20:23], a[116:119], a[164:167], v[20:23]\n\t"
    "v_mfma_f32_16x16x32_bf16 v[24:27], a[88:91], a[160:163], v[24:27]\n\t"
    "ds_read_b128 a[36:39], v241 offset:41984\n\t"
    "v_mfma_f32_16x16x32_bf16 v[24:27], a[120:123], a[164:167], v[24:27]\n\t"
    "s_waitcnt lgkmcnt(4)\n\t"
    "s_add_u32 s43, s62, 69632\n\t"
    "buffer_load_dwordx4 a[144:147], v254, s[28:31], s43 offen offset:0 sc1\n\t"
    "v_mfma_f32_16x16x32_bf16 v[28:31], a[92:95], a[160:163], v[28:31]\n\t"
    "v_mfma_f32_16x16x32_bf16 v[28:31], a[124:127], a[164:167], v[28:31]\n\t"
    "s_waitcnt vmcnt(26)\n\t"
    "ds_read_b128 a[8:11], v241 offset:34816\n\t"
    "v_mfma_f32_16x16x32_bf16 v[32:35], a[64:67], a[168:171], v[32:35]\n\t"
    "v_mfma_f32_16x16x32_bf16 v[32:35], a[96:99], a[172:175], v[32:35]\n\t"
    "v_mfma_f32_16x16x32_bf16 v[36:39], a[68:71], a[168:171], v[36:39]\n\t"
    "s_add_u32 s43, s62, 73728\n\t"
    "buffer_load_dwordx4 a[160:163], v254, s[28:31], s43 offen offset:0 sc1\n\t"
    "v_mfma_f32_16x16x32_bf16 v[36:39], a[100:103], a[172:175], v[36:39]\n\t"
    "ds_read_b128 a[40:43], v241 offset:43008\n\t"
    "v_mfma_f32_16x16x32_bf16 v[40:43], a[72:75], a[168:171], v[40:43]\n\t"
    "v_mfma_f32_16x16x32_bf16 v[40:43], a[104:107], a[172:175], v[40:43]\n\t"
    "v_mfma_f32_16x16x32_bf16 v[44:47], a[76:79], a[168:171], v[44:47]\n\t"
    "ds_read_b128 a[12:15], v241 offset:35840\n\t"
    "v_mfma_f32_16x16x32_bf16 v[44:47], a[108:111], a[172:175], v[44:47]\n\t"
    "v_mfma_f32_16x16x32_bf16 v[48:51], a[80:83], a[168:171], v[48:51]\n\t"
    "buffer_load_dwordx4 a[164:167], v254, s[28:31], s43 offen offset:1024 sc1\n\t"
    "v_mfma_f32_16x16x32_bf16 v[48:51], a[112:115], a[172:175], v[48:51]\n\t"
    "ds_read_b128 a[44:47], v241 offset:44032\n\t"
    "v_mfma_f32_16x16x32_bf16 v[52:55], a[84:87], a[168:171], v[52:55]\n\t"
    "v_mfma_f32_16x16x32_bf16 v[52:55], a[116:119], a[172:175], v[52:55]\n\t"
    "v_mfma_f32_16x16x32_bf16 v[56:59], a[88:91], a[168:171], v[56:59]\n\t"
    "ds_read_b128 a[16:19], v241 offset:36864\n\t"
    "v_mfma_f32_16x16x32_bf16 v[56:59], a[120:123], a[172:175], v[56:59]\n\t"
    "s_add_u32 s43, s62, 69632\n\t"
    "buffer_load_dwordx4 a[148:151], v254, s[28:31], s43 offen offset:1024 sc1\n\t"
    "v_mfma_f32_16x16x32_bf16 v[60:63], a[92:95], a[168:171], v[60:63]\n\t"
    "v_mfma_f32_16x16x32_bf16 v[60:63], a[124:127], a[172:175], v[60:63]\n\t"
    "s_waitcnt vmcnt(17)\n\t"
    "v_mfma_f32_16x16x32_bf16 v[64:67], a[64:67], a[176:179], v[64:67]\n\t"
    "ds_read_b128 a[48:51], v241 offset:45056\n\t"
    "v_readfirstlane_b32 s99, v192\n\t"
    "v_mfma_f32_16x16x32_bf16 v[64:67], a[96:99], a[180:183], v[64:67]\n\t"
    "s_cmp_eq_u32 s99, 0\n\t"
    "s_nop 0\n\t"
    "s_cbranch_scc1 L_ubw%=\n\t"
    "L_ubok%=:\n\t"
    "v_mfma_f32_16x16x32_bf16 v[68:71], a[68:71], a[176:179], v[68:71]\n\t"
    "s_lshl_b32 s98, s10, 11\n\t"
    "v_mfma_f32_16x16x32_bf16 v[68:71], a[100:103], a[180:183], v[68:71]\n\t"
    "s_add_u32 s43, s98, 262144\n\t"
    "buffer_load_dwordx4 v[192:195], v254, s[28:31], s43 offen offset:0 sc1\n\t"
    "v_mfma_f32_16x16x32_bf16 v[72:75], a[72:75], a[176:179], v[72:75]\n\t"
    "ds_read_b128 a[20:23], v241 offset:37888\n\t"
    "s_mov_b32 vcc_lo, 0xaaaaaaaa\n\t"
    "v_mfma_f32_16x16x32_bf16 v[72:75], a[104:107], a[180:183], v[72:75]\n\t"
    "s_mov_b32 vcc_hi, 0xaaaaaaaa\n\t"
    "v_add_f32_dpp v224, v224, v224 quad_perm:[1,0,3,2] row_mask:0xf bank_mask:0xf\n\t"
    "v_mfma_f32_16x16x32_bf16 v[76:79], a[76:79], a[176:179], v[76:79]\n\t"
    "v_add_f32_dpp v225, v225, v225 quad_perm:[1,0,3,2] row_mask:0xf bank_mask:0xf\n\t"
    "v_add_f32_dpp v226, v226, v226 quad_perm:[1,0,3,2] row_mask:0xf bank_mask:0xf\n\t"
    "v_mfma_f32_16x16x32_bf16 v[76:79], a[108:111], a[180:183], v[76:79]\n\t"
    "ds_read_b128 a[52:55], v241 offset:46080\n\t"
    "v_add_f32_dpp v227, v227, v227 quad_perm:[1,0,3,2] row_mask:0xf bank_mask:0xf\n\t"
    "v_mfma_f32_16x16x32_bf16 v[80:83], a[80:83], a[176:179], v[80:83]\n\t"
    "buffer_load_dwordx4 v[208:211], v254, s[28:31], s43 offen offset:1024 sc1\n\t"
    "v_mfma_f32_16x16x32_bf16 v[80:83], a[112:115], a[180:183], v[80:83]\n\t"
    "v_cndmask_b32 v224, v224, v226, vcc\n\t"
    "v_cndmask_b32 v225, v225, v227, vcc\n\t"
    "v_mfma_f32_16x16x32_bf16 v[84:87], a[84:87], a[176:179], v[84:87]\n\t"
    "v_add_f32_dpp v228, v228, v228 quad_perm:[1,0,3,2] row_mask:0xf bank_mask:0xf\n\t"
    "v_add_f32_dpp v229, v229, v229 quad_perm:[1,0,3,2] row_mask:0xf bank_mask:0xf\n\t"
    "v_mfma_f32_16x16x32_bf16 v[84:87], a[116:119], a[180:183], v[84:87]\n\t"
    "ds_read_b128 a[24:27], v241 offset:38912\n\t"
    "v_add_f32_dpp v230, v230, v230 quad_perm:[1,0,3,2] row_mask:0xf bank_mask:0xf\n\t"
    "v_mfma_f32_16x16x32_bf16 v[88:91], a[88:91], a[176:179], v[88:91]\n\t"
    "v_add_f32_dpp v231, v231, v231 quad_perm:[1,0,3,2] row_mask:0xf bank_mask:0xf\n\t"
    "v_cndmask_b32 v228, v228, v230, vcc\n\t"
    "v_mfma_f32_16x16x32_bf16 v[88:91], a[120:123], a[180:183], v[88:91]\n\t"
    "ds_read_b128 a[56:59], v241 offset:47104\n\t"
    "v_cndmask_b32 v229, v229, v231, vcc\n\t"
    "v_mfma_f32_16x16x32_bf16 v[92:95], a[92:95], a[176:179], v[92:95]\n\t"
    "s_add_u32 s43, s62, 73728\n\t"
    "buffer_load_dwordx4 a[168:171], v254, s[28:31], s43 offen offset:2048 sc1\n\t"
    "v_mfma_f32_16x16x32_bf16 v[92:95], a[124:127], a[180:183], v[92:95]\n\t"
    "v_add_f32_dpp v232, v232, v232 quad_perm:[1,0,3,2] row_mask:0xf bank_mask:0xf\n\t"
    "v_add_f32_dpp v233, v233, v233 quad_perm:[1,0,3,2] row_mask:0xf bank_mask:0xf\n\t"
    "v_mfma_f32_16x16x32_bf16 v[96:99], a[64:67], a[184:187], v[96:99]\n\t"
    "v_add_f32_dpp v234, v234, v234 quad_perm:[1,0,3,2] row_mask:0xf bank_mask:0xf\n\t"
    "v_add_f32_dpp v235, v235, v235 quad_perm:[1,0,3,2] row_mask:0xf bank_mask:0xf\n\t"
    "v_mfma_f32_16x16x32_bf16 v[96:99], a[96:99], a[188:191], v[96:99]\n\t"
    "ds_read_b128 a[28:31], v241 offset:39936\n\t"
    "v_cndmask_b32 v232, v232, v234, vcc\n\t"
    "v_mfma_f32_16x16x32_bf16 v[100:103], a[68:71], a[184:187], v[100:103]\n\t"
    "v_cndmask_b32 v233, v233, v235, vcc\n\t"
    "v_add_f32_dpp v236, v236, v236 quad_perm:[1,0,3,2] row_mask:0xf bank_mask:0xf\n\t"
    "v_mfma_f32_16x16x32_bf16 v[100:103], a[100:103], a[188:191], v[100:103]\n\t"
    "buffer_load_dwordx4 a[172:175], v254, s[28:31], s43 offen offset:3072 sc1\n\t"
    "v_mfma_f32_16x16x32_bf16 v[104:107], a[72:75], a[184:187], v[104:107]\n\t"
    "v_add_f32_dpp v237, v237, v237 quad_perm:[1,0,3,2] row_mask:0xf bank_mask:0xf\n\t"
    "v_add_f32_dpp v238, v238, v238 quad_perm:[1,0,3,2] row_mask:0xf bank_mask:0xf\n\t"
    "v_mfma_f32_16x16x32_bf16 v[104:107], a[104:107], a[188:191], v[104:107]\n\t"
    "ds_read_b128 a[60:63], v241 offset:48128\n\t"
    "v_add_f32_dpp v239, v239, v239 quad_perm:[1,0,3,2] row_mask:0xf bank_mask:0xf\n\t"
    "v_mfma_f32_16x16x32_bf16 v[108:111], a[76:79], a[184:187], v[108:111]\n\t"
    "v_cndmask_b32 v236, v236, v238, vcc\n\t"
    "v_cndmask_b32 v237, v237, v239, vcc\n\t"
    "v_mfma_f32_16x16x32_bf16 v[108:111], a[108:111], a[188:191], v[108:111]\n\t"
    "v_mov_b32 v226, v224\n\t"
    "v_mov_b32 v227, v225\n\t"
    "v_mfma_f32_16x16x32_bf16 v[112:115], a[80:83], a[184:187], v[112:115]\n\t"
    "v_mov_b32 v230, v228\n\t"
    "v_mov_b32 v231, v229\n\t"
    "v_mfma_f32_16x16x32_bf16 v[112:115], a[112:115], a[188:191], v[112:115]\n\t"
    "s_add_u32 s43, s62, 77824\n\t"
    "buffer_load_dwordx4 a[176:179], v254, s[28:31], s43 offen offset:0 sc1\n\t"
    "v_mfma_f32_16x16x32_bf16 v[116:119], a[84:87], a[184:187], v[116:119]\n\t"
    "v_mov_b32 v234, v232\n\t"
    "v_mov_b32 v235, v233\n\t"
    "v_mfma_f32_16x16x32_bf16 v[116:119], a[116:119], a[188:191], v[116:119]\n\t"
    "v_mov_b32 v238, v236\n\t"
    "v_mov_b32 v239, v237\n\t"
    "v_mfma_f32_16x16x32_bf16 v[120:123], a[88:91], a[184:187], v[120:123]\n\t"
    "v_permlane16_swap_b32 v224, v226\n\t"
    "v_permlane16_swap_b32 v225, v227\n\t"
    "v_mfma_f32_16x16x32_bf16 v[120:123], a[120:123], a[188:191], v[120:123]\n\t"
    "v_permlane16_swap_b32 v228, v230\n\t"
    "v_permlane16_swap_b32 v229, v231\n\t"
    "v_mfma_f32_16x16x32_bf16 v[124:127], a[92:95], a[184:187], v[124:127]\n\t"
    "buffer_load_dwordx4 a[180:183], v254, s[28:31], s43 offen offset:1024 sc1\n\t"
    "v_mfma_f32_16x16x32_bf16 v[124:127], a[124:127], a[188:191], v[124:127]\n\t"
    "s_waitcnt lgkmcnt(12)\n\t"
    "v_permlane16_swap_b32 v232, v234\n\t"
    "v_permlane16_swap_b32 v233, v235\n\t"
    "v_mfma_f32_16x16x32_bf16 v[0:3], a[0:3], a[192:195], v[0:3]\n\t"
    "v_permlane16_swap_b32 v236, v238\n\t"
    "v_permlane16_swap_b32 v237, v239\n\t"
    "v_mfma_f32_16x16x32_bf16 v[0:3], a[32:35], a[196:199], v[0:3]\n\t"
    "v_add_f32 v224, v224, v226\n\t"
    "v_add_f32 v225, v225, v227\n\t"
    "v_mfma_f32_16x16x32_bf16 v[4:7], a[4:7], a[192:195], v[4:7]\n\t"
    "ds_read_b128 a[64:67], v241 offset:49152\n\t"
    "v_add_f32 v228, v228, v230\n\t"
    "v_mfma_f32_16x16x32_bf16 v[4:7], a[36:39], a[196:199], v[4:7]\n\t"
    "s_waitcnt lgkmcnt(9)\n\t"
    "v_add_f32 v229, v229, v231\n\t"
    "v_add_f32 v232, v232, v234\n\t"
    "v_mfma_f32_16x16x32_bf16 v[8:11], a[8:11], a[192:195], v[8:11]\n\t"
    "s_add_u32 s43, s62, 69632\n\t"
    "buffer_load_dwordx4 a[152:155], v254, s[28:31], s43 offen offset:2048 sc1\n\t"
    "v_mfma_f32_16x16x32_bf16 v[8:11], a[40:43], a[196:199], v[8:11]\n\t"
    "ds_read_b128 a[96:99], v241 offset:57344\n\t"
    "v_add_f32 v233, v233, v235\n\t"
    "v_mfma_f32_16x16x32_bf16 v[12:15], a[12:15], a[192:195], v[12:15]\n\t"
    "v_add_f32 v236, v236, v238\n\t"
    "v_add_f32 v237, v237, v239\n\t"
    "v_mfma_f32_16x16x32_bf16 v[12:15], a[44:47], a[196:199], v[12:15]\n\t"
    "s_waitcnt lgkmcnt(6)\n\t"
    "v_mov_b32 v226, v224\n\t"
    "v_mov_b32 v227, v225\n\t"
    "v_mfma_f32_16x16x32_bf16 v[16:19], a[16:19], a[192:195], v[16:19]\n\t"
    "ds_read_b128 a[68:71], v241 offset:50176\n\t"
    "v_mov_b32 v230, v228\n\t"
    "v_mfma_f32_16x16x32_bf16 v[16:19], a[48:51], a[196:199], v[16:19]\n\t"
    "buffer_load_dwordx4 a[156:159], v254, s[28:31], s43 offen offset:3072 sc1\n\t"
    "v_mfma_f32_16x16x32_bf16 v[20:23], a[20:23], a[192:195], v[20:23]\n\t"
    "v_mov_b32 v231, v229\n\t"
    "v_mov_b32 v234, v232\n\t"
    "v_mfma_f32_16x16x32_bf16 v[20:23], a[52:55], a[196:199], v[20:23]\n\t"
    "s_waitcnt lgkmcnt(3)\n\t"
    "v_mov_b32 v235, v233\n\t"
    "v_mov_b32 v238, v236\n\t"
    "v_mfma_f32_16x16x32_bf16 v[24:27], a[24:27], a[192:195], v[24:27]\n\t"
    "ds_read_b128 a[100:103], v241 offset:58368\n\t"
    "v_mov_b32 v239, v237\n\t"
    "v_mfma_f32_16x16x32_bf16 v[24:27], a[56:59], a[196:199], v[24:27]\n\t"
    "v_permlane32_swap_b32 v224, v226\n\t"
    "v_permlane32_swap_b32 v225, v227\n\t"
    "v_mfma_f32_16x16x32_bf16 v[28:31], a[28:31], a[192:195], v[28:31]\n\t"
    "v_permlane32_swap_b32 v228, v230\n\t"
    "v_permlane32_swap_b32 v229, v231\n\t"
    "v_mfma_f32_16x16x32_bf16 v[28:31], a[60:63], a[196:199], v[28:31]\n\t"
    "s_add_u32 s43, s62, 77824\n\t"
    "buffer_load_dwordx4 a[184:187], v254, s[28:31], s43 offen offset:2048 sc1\n\t"
    "v_mfma_f32_16x16x32_bf16 v[32:35], a[0:3], a[200:203], v[32:35]\n\t"
    "ds_read_b128 a[72:75], v241 offset:51200\n\t"
    "v_permlane32_swap_b32 v232, v234\n\t"
    "v_mfma_f32_16x16x32_bf16 v[32:35], a[32:35], a[204:207], v[32:35]\n\t"
    "v_permlane32_swap_b32 v233, v235\n\t"
    "v_permlane32_swap_b32 v236, v238\n\t"
    "v_mfma_f32_16x16x32_bf16 v[36:39], a[4:7], a[200:203], v[36:39]\n\t"
    "ds_read_b128 a[104:107], v241 offset:59392\n\t"
    "v_permlane32_swap_b32 v237, v239\n\t"
    "v_mfma_f32_16x16x32_bf16 v[36:39], a[36:39], a[204:207], v[36:39]\n\t"
    "v_add_f32 v224, v224, v226\n\t"
    "v_add_f32 v225, v225, v227\n\t"
    "v_mfma_f32_16x16x32_bf16 v[40:43], a[8:11], a[200:203], v[40:43]\n\t"
    "buffer_load_dwordx4 a[188:191], v254, s[28:31], s43 offen offset:3072 sc1\n\t"
    "v_mfma_f32_16x16x32_bf16 v[40:43], a[40:43], a[204:207], v[40:43]\n\t"
    "v_add_f32 v228, v228, v230\n\t"
    "v_add_f32 v229, v229, v231\n\t"
    "v_mfma_f32_16x16x32_bf16 v[44:47], a[12:15], a[200:203], v[44:47]\n\t"
    "ds_read_b128 a[76:79], v241 offset:52224\n\t"
    "v_add_f32 v232, v232, v234\n\t"
    "v_mfma_f32_16x16x32_bf16 v[44:47], a[44:47], a[204:207], v[44:47]\n\t"
    "v_add_f32 v233, v233, v235\n\t"
    "v_add_f32 v236, v236, v238\n\t"
    "v_mfma_f32_16x16x32_bf16 v[48:51], a[16:19], a[200:203], v[48:51]\n\t"
    "v_add_f32 v237, v237, v239\n\t"
    "v_mul_f32 v224, 0xbacccccd, v224\n\t"
    "v_mfma_f32_16x16x32_bf16 v[48:51], a[48:51], a[204:207], v[48:51]\n\t"
    "ds_read_b128 a[108:111], v241 offset:60416\n\t"
    "v_mul_f32 v226, v224, v224\n\t"
    "v_mfma_f32_16x16x32_bf16 v[52:55], a[20:23], a[200:203], v[52:55]\n\t"
    "s_add_u32 s43, s66, 32768\n\t"
    "buffer_load_dwordx4 v[128:131], v254, s[28:31], s43 offen offset:0 sc1\n\t"
    "v_mfma_f32_16x16x32_bf16 v[52:55], a[52:55], a[204:207], v[52:55]\n\t"
    "v_mul_f32 v225, 0x3acccccd, v225\n\t"
    "v_sub_f32 v225, v225, v226\n\t"
    "v_mfma_f32_16x16x32_bf16 v[56:59], a[24:27], a[200:203], v[56:59]\n\t"
    "v_max_f32 v225, 0, v225\n\t"
    "v_add_f32 v225, 0x3727c5ac, v225\n\t"
    "v_mfma_f32_16x16x32_bf16 v[56:59], a[56:59], a[204:207], v[56:59]\n\t"
    "ds_read_b128 a[80:83], v241 offset:53248\n\t"
    "v_sqrt_f32 v225, v225\n\t"
    "v_mfma_f32_16x16x32_bf16 v[60:63], a[28:31], a[200:203], v[60:63]\n\t"
    "v_cvt_pk_bf16_f32 v226, v224, v224\n\t"
    "v_and_b32 v226, 0xffff0000, v226\n\t"
    "v_mfma_f32_16x16x32_bf16 v[60:63], a[60:63], a[204:207], v[60:63]\n\t"
    "buffer_load_dwordx4 v[132:135], v254, s[28:31], s43 offen offset:1024 sc1\n\t"
    "v_mfma_f32_16x16x32_bf16 v[64:67], a[0:3], a[208:211], v[64:67]\n\t"
    "ds_read_b128 a[112:115], v241 offset:61440\n\t"
    "v_sub_f32 v226, v224, v226\n\t"
    "v_mfma_f32_16x16x32_bf16 v[64:67], a[32:35], a[212:215], v[64:67]\n\t"
    "v_cvt_pk_bf16_f32 v224, v224, v226\n\t"
    "v_cvt_pk_bf16_f32 v226, v225, v225\n\t"
    "v_mfma_f32_16x16x32_bf16 v[68:71], a[4:7], a[208:211], v[68:71]\n\t"
    "v_and_b32 v226, 0xffff0000, v226\n\t"
    "v_sub_f32 v226, v225, v226\n\t"
    "v_mfma_f32_16x16x32_bf16 v[68:71], a[36:39], a[212:215], v[68:71]\n\t"
    "ds_read_b128 a[84:87], v241 offset:54272\n\t"
    "v_cvt_pk_bf16_f32 v226, v225, v226\n\t"
    "v_mfma_f32_16x16x32_bf16 v[72:75], a[8:11], a[208:211], v[72:75]\n\t"
    "v_mov_b32 v225, v224\n\t"
    "v_mov_b32 v227, v226\n\t"
    "v_mfma_f32_16x16x32_bf16 v[72:75], a[40:43], a[212:215], v[72:75]\n\t"
    "buffer_load_dwordx4 v[136:139], v254, s[28:31], s43 offen offset:2048 sc1\n\t"
    "v_mfma_f32_16x16x32_bf16 v[76:79], a[12:15], a[208:211], v[76:79]\n\t"
    "v_mul_f32 v228, 0xbacccccd, v228\n\t"
    "v_mul_f32 v230, v228, v228\n\t"
    "v_mfma_f32_16x16x32_bf16 v[76:79], a[44:47], a[212:215], v[76:79]\n\t"
    "ds_read_b128 a[116:119], v241 offset:62464\n\t"
    "v_mul_f32 v229, 0x3acccccd, v229\n\t"
    "v_mfma_f32_16x16x32_bf16 v[80:83], a[16:19], a[208:211], v[80:83]\n\t"
    "v_sub_f32 v229, v229, v230\n\t"
    "v_max_f32 v229, 0, v229\n\t"
    "v_mfma_f32_16x16x32_bf16 v[80:83], a[48:51], a[212:215], v[80:83]\n\t"
    "v_add_f32 v229, 0x3727c5ac, v229\n\t"
    "v_sqrt_f32 v229, v229\n\t"
    "v_mfma_f32_16x16x32_bf16 v[84:87], a[20:23], a[208:211], v[84:87]\n\t"
    "buffer_load_dwordx4 v[140:143], v254, s[28:31], s43 offen offset:3072 sc1\n\t"
    "v_mfma_f32_16x16x32_bf16 v[84:87], a[52:55], a[212:215], v[84:87]\n\t"
    "ds_read_b128 a[88:91], v241 offset:55296\n\t"
    "v_cvt_pk_bf16_f32 v230, v228, v228\n\t"
    "v_mfma_f32_16x16x32_bf16 v[88:91], a[24:27], a[208:211], v[88:91]\n\t"
    "v_and_b32 v230, 0xffff0000, v230\n\t"
    "v_sub_f32 v230, v228, v230\n\t"
    "v_mfma_f32_16x16x32_bf16 v[88:91], a[56:59], a[212:215], v[88:91]\n\t"
    "ds_read_b128 a[120:123], v241 offset:63488\n\t"
    "v_cvt_pk_bf16_f32 v228, v228, v230\n\t"
    "v_mfma_f32_16x16x32_bf16 v[92:95], a[28:31], a[208:211], v[92:95]\n\t"
    "v_cvt_pk_bf16_f32 v230, v229, v229\n\t"
    "v_and_b32 v230, 0xffff0000, v230\n\t"
    "v_mfma_f32_16x16x32_bf16 v[92:95], a[60:63], a[212:215], v[92:95]\n\t"
    "v_sub_f32 v230, v229, v230\n\t"
    "v_cvt_pk_bf16_f32 v230, v229, v230\n\t"
    "v_mfma_f32_16x16x32_bf16 v[96:99], a[0:3], a[216:219], v[96:99]\n\t"
    "s_waitcnt vmcnt(30)\n\t"
    "s_add_u32 s43, s66, 36864\n\t"
    "buffer_load_dwordx4 v[144:147], v254, s[28:31], s43 offen offset:0 sc1\n\t"
    "v_mfma_f32_16x16x32_bf16 v[96:99], a[32:35], a[220:223], v[96:99]\n\t"
    "ds_read_b128 a[92:95], v241 offset:56320\n\t"
    "v_mov_b32 v229, v228\n\t"
    "v_mfma_f32_16x16x32_bf16 v[100:103], a[4:7], a[216:219], v[100:103]\n\t"
    "v_mov_b32 v231, v230\n\t"
    "v_mul_f32 v232, 0xbacccccd, v232\n\t"
    "v_mfma_f32_16x16x32_bf16 v[100:103], a[36:39], a[220:223], v[100:103]\n\t"
    "v_mul_f32 v234, v232, v232\n\t"
    "v_mul_f32 v233, 0x3acccccd, v233\n\t"
    "v_mfma_f32_16x16x32_bf16 v[104:107], a[8:11], a[216:219], v[104:107]\n\t"
    "ds_read_b128 a[124:127], v241 offset:64512\n\t"
    "v_sub_f32 v233, v233, v234\n\t"
    "v_mfma_f32_16x16x32_bf16 v[104:107], a[40:43], a[220:223], v[104:107]\n\t"
    "buffer_load_dwordx4 v[148:151], v254, s[28:31], s43 offen offset:1024 sc1\n\t"
    "v_mfma_f32_16x16x32_bf16 v[108:111], a[12:15], a[216:219], v[108:111]\n\t"
    "v_max_f32 v233, 0, v233\n\t"
    "v_add_f32 v233, 0x3727c5ac, v233\n\t"
    "v_mfma_f32_16x16x32_bf16 v[108:111], a[44:47], a[220:223], v[108:111]\n\t"
    "v_sqrt_f32 v233, v233\n\t"
    "v_mfma_f32_16x16x32_bf16 v[112:115], a[16:19], a[216:219], v[112:115]\n\t"
    "v_cvt_pk_bf16_f32 v234, v232, v232\n\t"
    "v_and_b32 v234, 0xffff0000, v234\n\t"
    "v_mfma_f32_16x16x32_bf16 v[112:115], a[48:51], a[220:223], v[112:115]\n\t"
    "v_sub_f32 v234, v232, v234\n\t"
    "v_cvt_pk_bf16_f32 v232, v232, v234\n\t"
    "v_mfma_f32_16x16x32_bf16 v[116:119], a[20:23], a[216:219], v[116:119]\n\t"
    "v_cvt_pk_bf16_f32 v234, v233, v233\n\t"
    "v_and_b32 v234, 0xffff0000, v234\n\t"
    "v_mfma_f32_16x16x32_bf16 v[116:119], a[52:55], a[220:223], v[116:119]\n\t"
    "buffer_load_dwordx4 v[152:155], v254, s[28:31], s43 offen offset:2048 sc1\n\t"
    "v_mfma_f32_16x16x32_bf16 v[120:123], a[24:27], a[216:219], v[120:123]\n\t"
    "v_sub_f32 v234, v233, v234\n\t"
    "v_cvt_pk_bf16_f32 v234, v233, v234\n\t"
    "v_mfma_f32_16x16x32_bf16 v[120:123], a[56:59], a[220:223], v[120:123]\n\t"
    "v_mov_b32 v233, v232\n\t"
    "v_mov_b32 v235, v234\n\t"
    "v_mfma_f32_16x16x32_bf16 v[124:127], a[28:31], a[216:219], v[124:127]\n\t"
    "v_mul_f32 v236, 0xbacccccd, v236\n\t"
    "v_mul_f32 v238, v236, v236\n\t"
    "v_mfma_f32_16x16x32_bf16 v[124:127], a[60:63], a[220:223], v[124:127]\n\t"
    "s_waitcnt vmcnt(31) lgkmcnt(12)\n\t"
    "v_mul_f32 v237, 0x3acccccd, v237\n\t"
    "v_sub_f32 v237, v237, v238\n\t"
    "v_mfma_f32_16x16x32_bf16 v[0:3], a[64:67], a[224:227], v[0:3]\n\t"
    "buffer_load_dwordx4 v[156:159], v254, s[28:31], s43 offen offset:3072 sc1\n\t"
    "v_mfma_f32_16x16x32_bf16 v[0:3], a[96:99], a[228:231], v[0:3]\n\t"
    "v_max_f32 v237, 0, v237\n\t"
    "v_add_f32 v237, 0x3727c5ac, v237\n\t"
    "v_mfma_f32_16x16x32_bf16 v[4:7], a[68:71], a[224:227], v[4:7]\n\t"
    "ds_read_b128 a[0:3], v242 offset:0\n\t"
    "v_sqrt_f32 v237, v237\n\t"
    "v_mfma_f32_16x16x32_bf16 v[4:7], a[100:103], a[228:231], v[4:7]\n\t"
    "s_waitcnt lgkmcnt(9)\n\t"
    "v_cvt_pk_bf16_f32 v238, v236, v236\n\t"
    "v_and_b32 v238, 0xffff0000, v238\n\t"
    "v_mfma_f32_16x16x32_bf16 v[8:11], a[72:75], a[224:227], v[8:11]\n\t"
    "ds_read_b128 a[32:35], v242 offset:8192\n\t"
    "v_sub_f32 v238, v236, v238\n\t"
    "v_mfma_f32_16x16x32_bf16 v[8:11], a[104:107], a[228:231], v[8:11]\n\t"
    "v_cvt_pk_bf16_f32 v236, v236, v238\n\t"
    "v_cvt_pk_bf16_f32 v238, v237, v237\n\t"
    "v_mfma_f32_16x16x32_bf16 v[12:15], a[76:79], a[224:227], v[12:15]\n\t"
    "s_add_u32 s43, s66, 40960\n\t"
    "buffer_load_dwordx4 v[160:163], v254, s[28:31], s43 offen offset:0 sc1\n\t"
    "v_mfma_f32_16x16x32_bf16 v[12:15], a[108:111], a[228:231], v[12:15]\n\t"
    "s_waitcnt lgkmcnt(6)\n\t"
    "ds_read_b128 a[4:7], v242 offset:1024\n\t"
    "v_and_b32 v238, 0xffff0000, v238\n\t"
    "v_mfma_f32_16x16x32_bf16 v[16:19], a[80:83], a[224:227], v[16:19]\n\t"
    "v_sub_f32 v238, v237, v238\n\t"
    "v_cvt_pk_bf16_f32 v238, v237, v238\n\t"
    "v_mfma_f32_16x16x32_bf16 v[16:19], a[112:115], a[228:231], v[16:19]\n\t"
    "v_mov_b32 v237, v236\n\t"
    "v_mov_b32 v239, v238\n\t"
    "v_mfma_f32_16x16x32_bf16 v[20:23], a[84:87], a[224:227], v[20:23]\n\t"
    "ds_read_b128 a[36:39], v242 offset:9216\n\t"
    "s_mov_b64 s[0:1], 0xffff\n\t"
    "v_mfma_f32_16x16x32_bf16 v[20:23], a[116:119], a[228:231], v[20:23]\n\t"
    "s_waitcnt lgkmcnt(5)\n\t"
    "buffer_load_dwordx4 v[164:167], v254, s[28:31], s43 offen offset:1024 sc1\n\t"
    "v_mfma_f32_16x16x32_bf16 v[24:27], a[88:91], a[224:227], v[24:27]\n\t"
    "s_lshl_b64 s[2:3], s[0:1], 16\n\t"
    "s_lshl_b64 s[4:5], s[0:1], 32\n\t"
    "v_mfma_f32_16x16x32_bf16 v[24:27], a[120:123], a[228:231], v[24:27]\n\t"
    "ds_read_b128 a[8:11], v242 offset:2048\n\t"
    "s_lshl_b64 s[6:7], s[0:1], 48\n\t"
    "v_mfma_f32_16x16x32_bf16 v[28:31], a[92:95], a[224:227], v[28:31]\n\t"
    "s_waitcnt vmcnt(19) lgkmcnt(5)\n\t"
    "v_cndmask_b32 v204, 0, v192, s[6:7]\n\t"
    "v_cndmask_b32 v205, 0, v193, s[6:7]\n\t"
    "v_mfma_f32_16x16x32_bf16 v[28:31], a[124:127], a[228:231], v[28:31]\n\t"
    "ds_read_b128 a[40:43], v242 offset:10240\n\t"
    "v_cndmask_b32 v206, 0, v194, s[6:7]\n\t"
    "v_mfma_f32_16x16x32_bf16 v[32:35], a[64:67], a[232:235], v[32:35]\n\t"
    "v_cndmask_b32 v207, 0, v195, s[6:7]\n\t"
    "v_cndmask_b32 v200, 0, v192, s[4:5]\n\t"
    "v_mfma_f32_16x16x32_bf16 v[32:35], a[96:99], a[236:239], v[32:35]\n\t"
    "buffer_load_dwordx4 v[168:171], v254, s[28:31], s43 offen offset:2048 sc1\n\t"
    "v_mfma_f32_16x16x32_bf16 v[36:39], a[68:71], a[232:235], v[36:39]\n\t"
    "v_cndmask_b32 v201, 0, v193, s[4:5]\n\t"
    "v_cndmask_b32 v202, 0, v194, s[4:5]\n\t"
    "v_mfma_f32_16x16x32_bf16 v[36:39], a[100:103], a[236:239], v[36:39]\n\t"
    "ds_read_b128 a[12:15], v242 offset:3072\n\t"
    "v_cndmask_b32 v203, 0, v195, s[4:5]\n\t"
    "v_mfma_f32_16x16x32_bf16 v[40:43], a[72:75], a[232:235], v[40:43]\n\t"
    "v_cndmask_b32 v196, 0, v192, s[2:3]\n\t"
    "v_cndmask_b32 v197, 0, v193, s[2:3]\n\t"
    "v_mfma_f32_16x16x32_bf16 v[40:43], a[104:107], a[236:239], v[40:43]\n\t"
    "ds_read_b128 a[44:47], v242 offset:11264\n\t"
    "v_cndmask_b32 v198, 0, v194, s[2:3]\n\t"
    "v_mfma_f32_16x16x32_bf16 v[44:47], a[76:79], a[232:235], v[44:47]\n\t"
    "buffer_load_dwordx4 v[172:175], v254, s[28:31], s43 offen offset:3072 sc1\n\t"
    "v_mfma_f32_16x16x32_bf16 v[44:47], a[108:111], a[236:239], v[44:47]\n\t"
    "v_cndmask_b32 v199, 0, v195, s[2:3]\n\t"
    "v_cndmask_b32 v192, 0, v192, s[0:1]\n\t"
    "v_mfma_f32_16x16x32_bf16 v[48:51], a[80:83], a[232:235], v[48:51]\n\t"
    "v_cndmask_b32 v193, 0, v193, s[0:1]\n\t"
    "v_cndmask_b32 v194, 0, v194, s[0:1]\n\t"
    "v_mfma_f32_16x16x32_bf16 v[48:51], a[112:115], a[236:239], v[48:51]\n\t"
    "ds_read_b128 a[16:19], v242 offset:4096\n\t"
    "v_cndmask_b32 v195, 0, v195, s[0:1]\n\t"
    "v_mfma_f32_16x16x32_bf16 v[52:55], a[84:87], a[232:235], v[52:55]\n\t"
    "s_waitcnt vmcnt(20)\n\t"
    "v_cndmask_b32 v220, 0, v208, s[6:7]\n\t"
    "v_cndmask_b32 v221, 0, v209, s[6:7]\n\t"
    "v_mfma_f32_16x16x32_bf16 v[52:55], a[116:119], a[236:239], v[52:55]\n\t"
    "ds_read_b128 a[48:51], v242 offset:12288\n\t"
    "v_cndmask_b32 v222, 0, v210, s[6:7]\n\t"
    "v_mfma_f32_16x16x32_bf16 v[56:59], a[88:91], a[232:235], v[56:59]\n\t"
    "s_add_u32 s43, s66, 45056\n\t"
    "buffer_load_dwordx4 v[176:179], v254, s[28:31], s43 offen offset:0 sc1\n\t"
    "v_mfma_f32_16x16x32_bf16 v[56:59], a[120:123], a[236:239], v[56:59]\n\t"
    "v_cndmask_b32 v223, 0, v211, s[6:7]\n\t"
    "v_cndmask_b32 v216, 0, v208, s[4:5]\n\t"
    "v_mfma_f32_16x16x32_bf16 v[60:63], a[92:95], a[232:235], v[60:63]\n\t"
    "ds_read_b128 a[20:23], v242 offset:5120\n\t"
    "v_cndmask_b32 v217, 0, v209, s[4:5]\n\t"
    "v_mfma_f32_16x16x32_bf16 v[60:63], a[124:127], a[236:239], v[60:63]\n\t"
    "v_cndmask_b32 v218, 0, v210, s[4:5]\n\t"
    "v_cndmask_b32 v219, 0, v211, s[4:5]\n\t"
    "v_mfma_f32_16x16x32_bf16 v[64:67], a[64:67], a[240:243], v[64:67]\n\t"
    "v_cndmask_b32 v212, 0, v208, s[2:3]\n\t"
    "v_cndmask_b32 v213, 0, v209, s[2:3]\n\t"
    "v_mfma_f32_16x16x32_bf16 v[64:67], a[96:99], a[244:247], v[64:67]\n\t"
    "buffer_load_dwordx4 v[180:183], v254, s[28:31], s43 offen offset:1024 sc1\n\t"
    "v_mfma_f32_16x16x32_bf16 v[68:71], a[68:71], a[240:243], v[68:71]\n\t"
    "ds_read_b128 a[52:55], v242 offset:13312\n\t"
    "v_cndmask_b32 v214, 0, v210, s[2:3]\n\t"
    "v_mfma_f32_16x16x32_bf16 v[68:71], a[100:103], a[244:247], v[68:71]\n\t"
    "v_cndmask_b32 v215, 0, v211, s[2:3]\n\t"
    "v_cndmask_b32 v208, 0, v208, s[0:1]\n\t"
    "v_mfma_f32_16x16x32_bf16 v[72:75], a[72:75], a[240:243], v[72:75]\n\t"
    "ds_read_b128 a[24:27], v242 offset:6144\n\t"
    "v_cndmask_b32 v209, 0, v209, s[0:1]\n\t"
    "v_mfma_f32_16x16x32_bf16 v[72:75], a[104:107], a[244:247], v[72:75]\n\t"
    "v_cndmask_b32 v210, 0, v210, s[0:1]\n\t"
    "v_cndmask_b32 v211, 0, v211, s[0:1]\n\t"
    "v_mfma_f32_16x16x32_bf16 v[76:79], a[76:79], a[240:243], v[76:79]\n\t"
    "ds_read_b128 a[56:59], v242 offset:14336\n\t"
    "v_mfma_f32_16x16x32_bf16 v[76:79], a[108:111], a[244:247], v[76:79]\n\t"
    "buffer_load_dwordx4 v[184:187], v254, s[28:31], s43 offen offset:2048 sc1\n\t"
    "v_mfma_f32_16x16x32_bf16 v[80:83], a[80:83], a[240:243], v[80:83]\n\t"
    "v_mfma_f32_16x16x32_bf16 v[80:83], a[112:115], a[244:247], v[80:83]\n\t"
    "v_mfma_f32_16x16x32_bf16 v[84:87], a[84:87], a[240:243], v[84:87]\n\t"
    "ds_read_b128 a[28:31], v242 offset:7168\n\t"
    "v_mfma_f32_16x16x32_bf16 v[84:87], a[116:119], a[244:247], v[84:87]\n\t"
    "v_mfma_f32_16x16x32_bf16 v[88:91], a[88:91], a[240:243], v[88:91]\n\t"
    "buffer_load_dwordx4 v[188:191], v254, s[28:31], s43 offen offset:3072 sc1\n\t"
    "v_mfma_f32_16x16x32_bf16 v[88:91], a[120:123], a[244:247], v[88:91]\n\t"
    "ds_read_b128 a[60:63], v242 offset:15360\n\t"
    "v_mfma_f32_16x16x32_bf16 v[92:95], a[92:95], a[240:243], v[92:95]\n\t"
    "v_mfma_f32_16x16x32_bf16 v[92:95], a[124:127], a[244:247], v[92:95]\n\t"
    "v_mfma_f32_16x16x32_bf16 v[96:99], a[64:67], a[248:251], v[96:99]\n\t"
    "v_mfma_f32_16x16x32_bf16 v[96:99], a[96:99], a[252:255], v[96:99]\n\t"
    "v_mfma_f32_16x16x32_bf16 v[100:103], a[68:71], a[248:251], v[100:103]\n\t"
    "s_add_u32 s43, s66, 16384\n\t"
    "buffer_load_dwordx4 a[192:195], v254, s[28:31], s43 offen offset:0 sc1\n\t"
    "v_mfma_f32_16x16x32_bf16 v[100:103], a[100:103], a[252:255], v[100:103]\n\t"
    "ds_read_b128 a[64:67], v242 offset:16384\n\t"
    "v_mfma_f32_16x16x32_bf16 v[104:107], a[72:75], a[248:251], v[104:107]\n\t"
    "ds_read_b128 a[96:99], v242 offset:24576\n\t"
    "v_mfma_f32_16x16x32_bf16 v[104:107], a[104:107], a[252:255], v[104:107]\n\t"
    "ds_read_b128 a[68:71], v242 offset:17408\n\t"
    "v_mfma_f32_16x16x32_bf16 v[108:111], a[76:79], a[248:251], v[108:111]\n\t"
    "ds_read_b128 a[100:103], v242 offset:25600\n\t"
    "v_mfma_f32_16x16x32_bf16 v[108:111], a[108:111], a[252:255], v[108:111]\n\t"
    "buffer_load_dwordx4 a[196:199], v254, s[28:31], s43 offen offset:1024 sc1\n\t"
    "v_mfma_f32_16x16x32_bf16 v[112:115], a[80:83], a[248:251], v[112:115]\n\t"
    "ds_read_b128 a[72:75], v242 offset:18432\n\t"
    "v_mfma_f32_16x16x32_bf16 v[112:115], a[112:115], a[252:255], v[112:115]\n\t"
    "ds_read_b128 a[104:107], v242 offset:26624\n\t"
    "v_mfma_f32_16x16x32_bf16 v[116:119], a[84:87], a[248:251], v[116:119]\n\t"
    "ds_read_b128 a[76:79], v242 offset:19456\n\t"
    "v_mfma_f32_16x16x32_bf16 v[116:119], a[116:119], a[252:255], v[116:119]\n\t"
    "ds_read_b128 a[108:111], v242 offset:27648\n\t"
    "v_mfma_f32_16x16x32_bf16 v[120:123], a[88:91], a[248:251], v[120:123]\n\t"
    "ds_read_b128 a[80:83], v242 offset:20480\n\t"
    "v_mfma_f32_16x16x32_bf16 v[120:123], a[120:123], a[252:255], v[120:123]\n\t"
    "buffer_load_dwordx4 a[200:203], v254, s[28:31], s43 offen offset:2048 sc1\n\t"
    "v_mfma_f32_16x16x32_bf16 v[124:127], a[92:95], a[248:251], v[124:127]\n\t"
    "ds_read_b128 a[112:115], v242 offset:28672\n\t"
    "v_mfma_f32_16x16x32_bf16 v[124:127], a[124:127], a[252:255], v[124:127]\n\t"
    "s_waitcnt lgkmcnt(15)\n\t"
    "ds_read_b128 a[84:87], v242 offset:21504\n\t"
    "v_mfma_f32_16x16x32_bf16 v[0:3], a[0:3], a[128:131], v[0:3]\n\t"
    "ds_read_b128 a[116:119], v242 offset:29696\n\t"
    "v_mfma_f32_16x16x32_bf16 v[0:3], a[32:35], a[132:135], v[0:3]\n\t"
    "ds_read_b128 a[88:91], v242 offset:22528\n\t"
    "v_mfma_f32_16x16x32_bf16 v[4:7], a[4:7], a[128:131], v[4:7]\n\t"
    "buffer_load_dwordx4 a[204:207], v254, s[28:31], s43 offen offset:3072 sc1\n\t"
    "v_mfma_f32_16x16x32_bf16 v[4:7], a[36:39], a[132:135], v[4:7]\n\t"
    "ds_read_b128 a[120:123], v242 offset:30720\n\t"
    "v_mfma_f32_16x16x32_bf16 v[8:11], a[8:11], a[128:131], v[8:11]\n\t"
    "ds_read_b128 a[92:95], v242 offset:23552\n\t"
    "v_mfma_f32_16x16x32_bf16 v[8:11], a[40:43], a[132:135], v[8:11]\n\t"
    "ds_read_b128 a[124:127], v242 offset:31744\n\t"
    "v_mfma_f32_16x16x32_bf16 v[12:15], a[12:15], a[128:131], v[12:15]\n\t"
    "v_mfma_f32_16x16x32_bf16 v[12:15], a[44:47], a[132:135], v[12:15]\n\t"
    "v_mfma_f32_16x16x32_bf16 v[16:19], a[16:19], a[128:131], v[16:19]\n\t"
    "s_add_u32 s43, s66, 20480\n\t"
    "buffer_load_dwordx4 a[208:211], v254, s[28:31], s43 offen offset:0 sc1\n\t"
    "v_mfma_f32_16x16x32_bf16 v[16:19], a[48:51], a[132:135], v[16:19]\n\t"
    "v_mfma_f32_16x16x32_bf16 v[20:23], a[20:23], a[128:131], v[20:23]\n\t"
    "s_waitcnt lgkmcnt(15)\n\t"
    "v_mfma_f32_16x16x32_bf16 v[20:23], a[52:55], a[132:135], v[20:23]\n\t"
    "v_mfma_f32_16x16x32_bf16 v[24:27], a[24:27], a[128:131], v[24:27]\n\t"
    "v_mfma_f32_16x16x32_bf16 v[24:27], a[56:59], a[132:135], v[24:27]\n\t"
    "buffer_load_dwordx4 a[212:215], v254, s[28:31], s43 offen offset:1024 sc1\n\t"
    "v_mfma_f32_16x16x32_bf16 v[28:31], a[28:31], a[128:131], v[28:31]\n\t"
    "v_mfma_f32_16x16x32_bf16 v[28:31], a[60:63], a[132:135], v[28:31]\n\t"
    "v_mfma_f32_16x16x32_bf16 v[0:3], a[64:67], a[160:163], v[0:3]\n\t"
    "s_waitcnt lgkmcnt(12)\n\t"
    "v_mfma_f32_16x16x32_bf16 v[0:3], a[96:99], a[164:167], v[0:3]\n\t"
    "v_mfma_f32_16x16x32_bf16 v[0:3], v[192:195], v[224:227], v[0:3]\n\t"
    "v_mfma_f32_16x16x32_bf16 v[4:7], a[68:71], a[160:163], v[4:7]\n\t"
    "buffer_load_dwordx4 a[216:219], v254, s[28:31], s43 offen offset:2048 sc1\n\t"
    "v_mfma_f32_16x16x32_bf16 v[4:7], a[100:103], a[164:167], v[4:7]\n\t"
    "v_mfma_f32_16x16x32_bf16 v[4:7], v[196:199], v[224:227], v[4:7]\n\t"
    "s_waitcnt lgkmcnt(9)\n\t"
    "v_mfma_f32_16x16x32_bf16 v[8:11], a[72:75], a[160:163], v[8:11]\n\t"
    "v_mfma_f32_16x16x32_bf16 v[8:11], a[104:107], a[164:167], v[8:11]\n\t"
    "v_mfma_f32_16x16x32_bf16 v[8:11], v[200:203], v[224:227], v[8:11]\n\t"
    "buffer_load_dwordx4 a[220:223], v254, s[28:31], s43 offen offset:3072 sc1\n\t"
    "v_mfma_f32_16x16x32_bf16 v[12:15], a[76:79], a[160:163], v[12:15]\n\t"
    "s_waitcnt lgkmcnt(6)\n\t"
    "v_mfma_f32_16x16x32_bf16 v[12:15], a[108:111], a[164:167], v[12:15]\n\t"
    "v_mfma_f32_16x16x32_bf16 v[12:15], v[204:207], v[224:227], v[12:15]\n\t"
    "v_mfma_f32_16x16x32_bf16 v[16:19], a[80:83], a[160:163], v[16:19]\n\t"
    "v_mfma_f32_16x16x32_bf16 v[16:19], a[112:115], a[164:167], v[16:19]\n\t"
    "v_mfma_f32_16x16x32_bf16 v[16:19], v[208:211], v[224:227], v[16:19]\n\t"
    "s_waitcnt lgkmcnt(3)\n\t"
    "s_add_u32 s43, s66, 24576\n\t"
    "buffer_load_dwordx4 a[224:227], v254, s[28:31], s43 offen offset:0 sc1\n\t"
    "v_mfma_f32_16x16x32_bf16 v[20:23], a[84:87], a[160:163], v[20:23]\n\t"
    "v_mfma_f32_16x16x32_bf16 v[20:23], a[116:119], a[164:167], v[20:23]\n\t"
    "v_mfma_f32_16x16x32_bf16 v[20:23], v[212:215], v[224:227], v[20:23]\n\t"
    "v_mfma_f32_16x16x32_bf16 v[24:27], a[88:91], a[160:163], v[24:27]\n\t"
    "s_waitcnt lgkmcnt(0)\n\t"
    "v_mfma_f32_16x16x32_bf16 v[24:27], a[120:123], a[164:167], v[24:27]\n\t"
    "buffer_load_dwordx4 a[228:231], v254, s[28:31], s43 offen offset:1024 sc1\n\t"
    "v_mfma_f32_16x16x32_bf16 v[24:27], v[216:219], v[224:227], v[24:27]\n\t"
    "v_mfma_f32_16x16x32_bf16 v[28:31], a[92:95], a[160:163], v[28:31]\n\t"
    "v_mfma_f32_16x16x32_bf16 v[28:31], a[124:127], a[164:167], v[28:31]\n\t"
    "v_mfma_f32_16x16x32_bf16 v[28:31], v[220:223], v[224:227], v[28:31]\n\t"
    "v_mfma_f32_16x16x32_bf16 v[32:35], a[0:3], a[136:139], v[32:35]\n\t"
    "v_mfma_f32_16x16x32_bf16 v[32:35], a[32:35], a[140:143], v[32:35]\n\t"
    "s_waitcnt vmcnt(32)\n\t"
    "buffer_load_dwordx4 a[232:235], v254, s[28:31], s43 offen offset:2048 sc1\n\t"
    "v_mfma_f32_16x16x32_bf16 v[32:35], a[64:67], a[168:171], v[32:35]\n\t"
    "v_max_f32 v0, 0, v0\n\t"
    "v_max_f32 v1, 0, v1\n\t"
    "v_mfma_f32_16x16x32_bf16 v[32:35], a[96:99], a[172:175], v[32:35]\n\t"
    "v_max_f32 v2, 0, v2\n\t"
    "v_max_f32 v3, 0, v3\n\t"
    "v_mfma_f32_16x16x32_bf16 v[32:35], v[192:195], v[228:231], v[32:35]\n\t"
    "v_max_f32 v4, 0, v4\n\t"
    "v_max_f32 v5, 0, v5\n\t"
    "v_mfma_f32_16x16x32_bf16 v[36:39], a[4:7], a[136:139], v[36:39]\n\t"
    "v_max_f32 v6, 0, v6\n\t"
    "v_max_f32 v7, 0, v7\n\t"
    "v_mfma_f32_16x16x32_bf16 v[36:39], a[36:39], a[140:143], v[36:39]\n\t"
    "buffer_load_dwordx4 a[236:239], v254, s[28:31], s43 offen offset:3072 sc1\n\t"
    "v_mfma_f32_16x16x32_bf16 v[36:39], a[68:71], a[168:171], v[36:39]\n\t"
    "v_mul_f32 v0, v0, v16\n\t"
    "v_mul_f32 v1, v1, v17\n\t"
    "v_mfma_f32_16x16x32_bf16 v[36:39], a[100:103], a[172:175], v[36:39]\n\t"
    "v_mul_f32 v2, v2, v18\n\t"
    "v_mul_f32 v3, v3, v19\n\t"
    "v_mfma_f32_16x16x32_bf16 v[36:39], v[196:199], v[228:231], v[36:39]\n\t"
    "v_mul_f32 v4, v4, v20\n\t"
    "v_mul_f32 v5, v5, v21\n\t"
    "v_mfma_f32_16x16x32_bf16 v[40:43], a[8:11], a[136:139], v[40:43]\n\t"
    "v_mul_f32 v6, v6, v22\n\t"
    "v_mul_f32 v7, v7, v23\n\t"
    "v_mfma_f32_16x16x32_bf16 v[40:43], a[40:43], a[140:143], v[40:43]\n\t"
    "v_cvt_pk_bf16_f32 v0, v0, v1\n\t"
    "v_cvt_pk_bf16_f32 v1, v2, v3\n\t"
    "v_mfma_f32_16x16x32_bf16 v[40:43], a[72:75], a[168:171], v[40:43]\n\t"
    "s_add_u32 s43, s66, 28672\n\t"
    "buffer_load_dwordx4 a[240:243], v254, s[28:31], s43 offen offset:0 sc1\n\t"
    "v_mfma_f32_16x16x32_bf16 v[40:43], a[104:107], a[172:175], v[40:43]\n\t"
    "v_cvt_pk_bf16_f32 v2, v4, v5\n\t"
    "v_cvt_pk_bf16_f32 v3, v6, v7\n\t"
    "v_mfma_f32_16x16x32_bf16 v[40:43], v[200:203], v[228:231], v[40:43]\n\t"
    "v_mfma_f32_16x16x32_bf16 v[44:47], a[12:15], a[136:139], v[44:47]\n\t"
    "v_mfma_f32_16x16x32_bf16 v[44:47], a[44:47], a[140:143], v[44:47]\n\t"
    "s_add_u32 s40, s64, 0\n\t"
    "buffer_store_dwordx4 v[0:3], v254, s[28:31], s40 offen offset:0 sc1\n\t"
    "v_mfma_f32_16x16x32_bf16 v[44:47], a[76:79], a[168:171], v[44:47]\n\t"
    "buffer_load_dwordx4 a[244:247], v254, s[28:31], s43 offen offset:1024 sc1\n\t"
    "v_mfma_f32_16x16x32_bf16 v[44:47], a[108:111], a[172:175], v[44:47]\n\t"
    "v_max_f32 v8, 0, v8\n\t"
    "v_max_f32 v9, 0, v9\n\t"
    "v_mfma_f32_16x16x32_bf16 v[44:47], v[204:207], v[228:231], v[44:47]\n\t"
    "v_max_f32 v10, 0, v10\n\t"
    "v_max_f32 v11, 0, v11\n\t"
    "v_mfma_f32_16x16x32_bf16 v[48:51], a[16:19], a[136:139], v[48:51]\n\t"
    "v_max_f32 v12, 0, v12\n\t"
    "v_max_f32 v13, 0, v13\n\t"
    "v_mfma_f32_16x16x32_bf16 v[48:51], a[48:51], a[140:143], v[48:51]\n\t"
    "v_max_f32 v14, 0, v14\n\t"
    "v_max_f32 v15, 0, v15\n\t"
    "v_mfma_f32_16x16x32_bf16 v[48:51], a[80:83], a[168:171], v[48:51]\n\t"
    "v_mul_f32 v8, v8, v24\n\t"
    "v_mul_f32 v9, v9, v25\n\t"
    "v_mfma_f32_16x16x32_bf16 v[48:51], a[112:115], a[172:175], v[48:51]\n\t"
    "buffer_load_dwordx4 a[248:251], v254, s[28:31], s43 offen offset:2048 sc1\n\t"
    "v_mfma_f32_16x16x32_bf16 v[48:51], v[208:211], v[228:231], v[48:51]\n\t"
    "v_mul_f32 v10, v10, v26\n\t"
    "v_mul_f32 v11, v11, v27\n\t"
    "v_mfma_f32_16x16x32_bf16 v[52:55], a[20:23], a[136:139], v[52:55]\n\t"
    "v_mul_f32 v12, v12, v28\n\t"
    "v_mul_f32 v13, v13, v29\n\t"
    "v_mfma_f32_16x16x32_bf16 v[52:55], a[52:55], a[140:143], v[52:55]\n\t"
    "v_mul_f32 v14, v14, v30\n\t"
    "v_mul_f32 v15, v15, v31\n\t"
    "v_mfma_f32_16x16x32_bf16 v[52:55], a[84:87], a[168:171], v[52:55]\n\t"
    "v_cvt_pk_bf16_f32 v8, v8, v9\n\t"
    "v_cvt_pk_bf16_f32 v9, v10, v11\n\t"
    "v_mfma_f32_16x16x32_bf16 v[52:55], a[116:119], a[172:175], v[52:55]\n\t"
    "buffer_load_dwordx4 a[252:255], v254, s[28:31], s43 offen offset:3072 sc1\n\t"
    "v_mfma_f32_16x16x32_bf16 v[52:55], v[212:215], v[228:231], v[52:55]\n\t"
    "v_cvt_pk_bf16_f32 v10, v12, v13\n\t"
    "v_cvt_pk_bf16_f32 v11, v14, v15\n\t"
    "v_mfma_f32_16x16x32_bf16 v[56:59], a[24:27], a[136:139], v[56:59]\n\t"
    "v_mfma_f32_16x16x32_bf16 v[56:59], a[56:59], a[140:143], v[56:59]\n\t"
    "v_mfma_f32_16x16x32_bf16 v[56:59], a[88:91], a[168:171], v[56:59]\n\t"
    "s_add_u32 s40, s64, 0\n\t"
    "buffer_store_dwordx4 v[8:11], v254, s[28:31], s40 offen offset:1024 sc1\n\t"
    "v_mfma_f32_16x16x32_bf16 v[56:59], a[120:123], a[172:175], v[56:59]\n\t"
    "v_mfma_f32_16x16x32_bf16 v[56:59], v[216:219], v[228:231], v[56:59]\n\t"
    "s_add_u32 s43, s66, 0\n\t"
    "buffer_load_dwordx4 a[128:131], v254, s[28:31], s43 offen offset:0 sc1\n\t"
    "v_mfma_f32_16x16x32_bf16 v[60:63], a[28:31], a[136:139], v[60:63]\n\t"
    "v_mfma_f32_16x16x32_bf16 v[60:63], a[60:63], a[140:143], v[60:63]\n\t"
    "v_mfma_f32_16x16x32_bf16 v[60:63], a[92:95], a[168:171], v[60:63]\n\t"
    "v_mfma_f32_16x16x32_bf16 v[60:63], a[124:127], a[172:175], v[60:63]\n\t"
    "v_mfma_f32_16x16x32_bf16 v[60:63], v[220:223], v[228:231], v[60:63]\n\t"
    "buffer_load_dwordx4 a[132:135], v254, s[28:31], s43 offen offset:1024 sc1\n\t"
    "v_mfma_f32_16x16x32_bf16 v[64:67], a[0:3], a[144:147], v[64:67]\n\t"
    "v_mfma_f32_16x16x32_bf16 v[64:67], a[32:35], a[148:151], v[64:67]\n\t"
    "s_waitcnt vmcnt(40)\n\t"
    "v_mfma_f32_16x16x32_bf16 v[64:67], a[64:67], a[176:179], v[64:67]\n\t"
    "v_max_f32 v32, 0, v32\n\t"
    "v_max_f32 v33, 0, v33\n\t"
    "v_mfma_f32_16x16x32_bf16 v[64:67], a[96:99], a[180:183], v[64:67]\n\t"
    "v_max_f32 v34, 0, v34\n\t"
    "v_max_f32 v35, 0, v35\n\t"
    "v_mfma_f32_16x16x32_bf16 v[64:67], v[192:195], v[232:235], v[64:67]\n\t"
    "v_max_f32 v36, 0, v36\n\t"
    "v_max_f32 v37, 0, v37\n\t"
    "v_mfma_f32_16x16x32_bf16 v[68:71], a[4:7], a[144:147], v[68:71]\n\t"
    "s_add_u32 s43, s66, 8192\n\t"
    "buffer_load_dwordx4 a[160:163], v254, s[28:31], s43 offen offset:0 sc1\n\t"
    "v_mfma_f32_16x16x32_bf16 v[68:71], a[36:39], a[148:151], v[68:71]\n\t"
    "v_max_f32 v38, 0, v38\n\t"
    "v_max_f32 v39, 0, v39\n\t"
    "v_mfma_f32_16x16x32_bf16 v[68:71], a[68:71], a[176:179], v[68:71]\n\t"
    "v_mul_f32 v32, v32, v48\n\t"
    "v_mul_f32 v33, v33, v49\n\t"
    "v_mfma_f32_16x16x32_bf16 v[68:71], a[100:103], a[180:183], v[68:71]\n\t"
    "v_mul_f32 v34, v34, v50\n\t"
    "v_mul_f32 v35, v35, v51\n\t"
    "v_mfma_f32_16x16x32_bf16 v[68:71], v[196:199], v[232:235], v[68:71]\n\t"
    "v_mul_f32 v36, v36, v52\n\t"
    "v_mul_f32 v37, v37, v53\n\t"
    "v_mfma_f32_16x16x32_bf16 v[72:75], a[8:11], a[144:147], v[72:75]\n\t"
    "buffer_load_dwordx4 a[164:167], v254, s[28:31], s43 offen offset:1024 sc1\n\t"
    "v_mfma_f32_16x16x32_bf16 v[72:75], a[40:43], a[148:151], v[72:75]\n\t"
    "v_mul_f32 v38, v38, v54\n\t"
    "v_mul_f32 v39, v39, v55\n\t"
    "v_mfma_f32_16x16x32_bf16 v[72:75], a[72:75], a[176:179], v[72:75]\n\t"
    "v_cvt_pk_bf16_f32 v32, v32, v33\n\t"
    "v_cvt_pk_bf16_f32 v33, v34, v35\n\t"
    "v_mfma_f32_16x16x32_bf16 v[72:75], a[104:107], a[180:183], v[72:75]\n\t"
    "v_cvt_pk_bf16_f32 v34, v36, v37\n\t"
    "v_cvt_pk_bf16_f32 v35, v38, v39\n\t"
    "v_mfma_f32_16x16x32_bf16 v[72:75], v[200:203], v[232:235], v[72:75]\n\t"
    "v_mfma_f32_16x16x32_bf16 v[76:79], a[12:15], a[144:147], v[76:79]\n\t"
    "v_mfma_f32_16x16x32_bf16 v[76:79], a[44:47], a[148:151], v[76:79]\n\t"
    "s_add_u32 s43, s66, 0\n\t"
    "buffer_load_dwordx4 a[136:139], v254, s[28:31], s43 offen offset:2048 sc1\n\t"
    "v_mfma_f32_16x16x32_bf16 v[76:79], a[76:79], a[176:179], v[76:79]\n\t"
    "s_add_u32 s40, s64, 2048\n\t"
    "buffer_store_dwordx4 v[32:35], v254, s[28:31], s40 offen offset:0 sc1\n\t"
    "v_mfma_f32_16x16x32_bf16 v[76:79], a[108:111], a[180:183], v[76:79]\n\t"
    "v_max_f32 v40, 0, v40\n\t"
    "v_max_f32 v41, 0, v41\n\t"
    "v_mfma_f32_16x16x32_bf16 v[76:79], v[204:207], v[232:235], v[76:79]\n\t"
    "v_max_f32 v42, 0, v42\n\t"
    "v_max_f32 v43, 0, v43\n\t"
    "v_mfma_f32_16x16x32_bf16 v[80:83], a[16:19], a[144:147], v[80:83]\n\t"
    "v_max_f32 v44, 0, v44\n\t"
    "v_max_f32 v45, 0, v45\n\t"
    "v_mfma_f32_16x16x32_bf16 v[80:83], a[48:51], a[148:151], v[80:83]\n\t"
    "buffer_load_dwordx4 a[140:143], v254, s[28:31], s43 offen offset:3072 sc1\n\t"
    "v_mfma_f32_16x16x32_bf16 v[80:83], a[80:83], a[176:179], v[80:83]\n\t"
    "v_max_f32 v46, 0, v46\n\t"
    "v_max_f32 v47, 0, v47\n\t"
    "v_mfma_f32_16x16x32_bf16 v[80:83], a[112:115], a[180:183], v[80:83]\n\t"
    "v_mul_f32 v40, v40, v56\n\t"
    "v_mul_f32 v41, v41, v57\n\t"
    "v_mfma_f32_16x16x32_bf16 v[80:83], v[208:211], v[232:235], v[80:83]\n\t"
    "v_mul_f32 v42, v42, v58\n\t"
    "v_mul_f32 v43, v43, v59\n\t"
    "v_mfma_f32_16x16x32_bf16 v[84:87], a[20:23], a[144:147], v[84:87]\n\t"
    "v_mul_f32 v44, v44, v60\n\t"
    "v_mul_f32 v45, v45, v61\n\t"
    "v_mfma_f32_16x16x32_bf16 v[84:87], a[52:55], a[148:151], v[84:87]\n\t"
    "v_mul_f32 v46, v46, v62\n\t"
    "v_mul_f32 v47, v47, v63\n\t"
    "v_mfma_f32_16x16x32_bf16 v[84:87], a[84:87], a[176:179], v[84:87]\n\t"
    "s_add_u32 s43, s66, 8192\n\t"
    "buffer_load_dwordx4 a[168:171], v254, s[28:31], s43 offen offset:2048 sc1\n\t"
    "v_mfma_f32_16x16x32_bf16 v[84:87], a[116:119], a[180:183], v[84:87]\n\t"
    "v_cvt_pk_bf16_f32 v40, v40, v41\n\t"
    "v_cvt_pk_bf16_f32 v41, v42, v43\n\t"
    "v_mfma_f32_16x16x32_bf16 v[84:87], v[212:215], v[232:235], v[84:87]\n\t"
    "v_cvt_pk_bf16_f32 v42, v44, v45\n\t"
    "v_cvt_pk_bf16_f32 v43, v46, v47\n\t"
    "v_mfma_f32_16x16x32_bf16 v[88:91], a[24:27], a[144:147], v[88:91]\n\t"
    "v_mfma_f32_16x16x32_bf16 v[88:91], a[56:59], a[148:151], v[88:91]\n\t"
    "v_mfma_f32_16x16x32_bf16 v[88:91], a[88:91], a[176:179], v[88:91]\n\t"
    "buffer_load_dwordx4 a[172:175], v254, s[28:31], s43 offen offset:3072 sc1\n\t"
    "v_mfma_f32_16x16x32_bf16 v[88:91], a[120:123], a[180:183], v[88:91]\n\t"
    "s_add_u32 s40, s64, 2048\n\t"
    "buffer_store_dwordx4 v[40:43], v254, s[28:31], s40 offen offset:1024 sc1\n\t"
    "v_mfma_f32_16x16x32_bf16 v[88:91], v[216:219], v[232:235], v[88:91]\n\t"
    "v_mfma_f32_16x16x32_bf16 v[92:95], a[28:31], a[144:147], v[92:95]\n\t"
    "v_mfma_f32_16x16x32_bf16 v[92:95], a[60:63], a[148:151], v[92:95]\n\t"
    "v_mfma_f32_16x16x32_bf16 v[92:95], a[92:95], a[176:179], v[92:95]\n\t"
    "v_mfma_f32_16x16x32_bf16 v[92:95], a[124:127], a[180:183], v[92:95]\n\t"
    "v_mfma_f32_16x16x32_bf16 v[92:95], v[220:223], v[232:235], v[92:95]\n\t"
    "s_waitcnt vmcnt(44)\n\t"
    "v_mfma_f32_16x16x32_bf16 v[96:99], a[0:3], a[152:155], v[96:99]\n\t"
    "v_mfma_f32_16x16x32_bf16 v[96:99], a[32:35], a[156:159], v[96:99]\n\t"
    "v_mfma_f32_16x16x32_bf16 v[96:99], a[64:67], a[184:187], v[96:99]\n\t"
    "v_max_f32 v64, 0, v64\n\t"
    "v_max_f32 v65, 0, v65\n\t"
    "v_mfma_f32_16x16x32_bf16 v[96:99], a[96:99], a[188:191], v[96:99]\n\t"
    "v_max_f32 v66, 0, v66\n\t"
    "v_max_f32 v67, 0, v67\n\t"
    "v_mfma_f32_16x16x32_bf16 v[96:99], v[192:195], v[236:239], v[96:99]\n\t"
    "v_max_f32 v68, 0, v68\n\t"
    "v_max_f32 v69, 0, v69\n\t"
    "v_mfma_f32_16x16x32_bf16 v[100:103], a[4:7], a[152:155], v[100:103]\n\t"
    "v_max_f32 v70, 0, v70\n\t"
    "v_max_f32 v71, 0, v71\n\t"
    "v_mfma_f32_16x16x32_bf16 v[100:103], a[36:39], a[156:159], v[100:103]\n\t"
    "v_mul_f32 v64, v64, v80\n\t"
    "v_mul_f32 v65, v65, v81\n\t"
    "v_mfma_f32_16x16x32_bf16 v[100:103], a[68:71], a[184:187], v[100:103]\n\t"
    "v_mul_f32 v66, v66, v82\n\t"
    "v_mul_f32 v67, v67, v83\n\t"
    "v_mfma_f32_16x16x32_bf16 v[100:103], a[100:103], a[188:191], v[100:103]\n\t"
    "v_mul_f32 v68, v68, v84\n\t"
    "v_mul_f32 v69, v69, v85\n\t"
    "v_mfma_f32_16x16x32_bf16 v[100:103], v[196:199], v[236:239], v[100:103]\n\t"
    "v_mul_f32 v70, v70, v86\n\t"
    "v_mul_f32 v71, v71, v87\n\t"
    "v_mfma_f32_16x16x32_bf16 v[104:107], a[8:11], a[152:155], v[104:107]\n\t"
    "v_cvt_pk_bf16_f32 v64, v64, v65\n\t"
    "v_cvt_pk_bf16_f32 v65, v66, v67\n\t"
    "v_mfma_f32_16x16x32_bf16 v[104:107], a[40:43], a[156:159], v[104:107]\n\t"
    "v_cvt_pk_bf16_f32 v66, v68, v69\n\t"
    "v_cvt_pk_bf16_f32 v67, v70, v71\n\t"
    "v_mfma_f32_16x16x32_bf16 v[104:107], a[72:75], a[184:187], v[104:107]\n\t"
    "v_mfma_f32_16x16x32_bf16 v[104:107], a[104:107], a[188:191], v[104:107]\n\t"
    "v_mfma_f32_16x16x32_bf16 v[104:107], v[200:203], v[236:239], v[104:107]\n\t"
    "v_mfma_f32_16x16x32_bf16 v[108:111], a[12:15], a[152:155], v[108:111]\n\t"
    "v_mfma_f32_16x16x32_bf16 v[108:111], a[44:47], a[156:159], v[108:111]\n\t"
    "s_add_u32 s40, s64, 4096\n\t"
    "buffer_store_dwordx4 v[64:67], v254, s[28:31], s40 offen offset:0 sc1\n\t"
    "v_mfma_f32_16x16x32_bf16 v[108:111], a[76:79], a[184:187], v[108:111]\n\t"
    "v_mfma_f32_16x16x32_bf16 v[108:111], a[108:111], a[188:191], v[108:111]\n\t"
    "v_max_f32 v72, 0, v72\n\t"
    "v_max_f32 v73, 0, v73\n\t"
    "v_mfma_f32_16x16x32_bf16 v[108:111], v[204:207], v[236:239], v[108:111]\n\t"
    "v_max_f32 v74, 0, v74\n\t"
    "v_max_f32 v75, 0, v75\n\t"
    "v_mfma_f32_16x16x32_bf16 v[112:115], a[16:19], a[152:155], v[112:115]\n\t"
    "v_max_f32 v76, 0, v76\n\t"
    "v_max_f32 v77, 0, v77\n\t"
    "v_mfma_f32_16x16x32_bf16 v[112:115], a[48:51], a[156:159], v[112:115]\n\t"
    "v_max_f32 v78, 0, v78\n\t"
    "v_max_f32 v79, 0, v79\n\t"
    "v_mfma_f32_16x16x32_bf16 v[112:115], a[80:83], a[184:187], v[112:115]\n\t"
    "v_mul_f32 v72, v72, v88\n\t"
    "v_mul_f32 v73, v73, v89\n\t"
    "v_mfma_f32_16x16x32_bf16 v[112:115], a[112:115], a[188:191], v[112:115]\n\t"
    "v_mul_f32 v74, v74, v90\n\t"
    "v_mul_f32 v75, v75, v91\n\t"
    "v_mfma_f32_16x16x32_bf16 v[112:115], v[208:211], v[236:239], v[112:115]\n\t"
    "v_mul_f32 v76, v76, v92\n\t"
    "v_mul_f32 v77, v77, v93\n\t"
    "v_mfma_f32_16x16x32_bf16 v[116:119], a[20:23], a[152:155], v[116:119]\n\t"
    "v_mul_f32 v78, v78, v94\n\t"
    "v_mul_f32 v79, v79, v95\n\t"
    "v_mfma_f32_16x16x32_bf16 v[116:119], a[52:55], a[156:159], v[116:119]\n\t"
    "v_cvt_pk_bf16_f32 v72, v72, v73\n\t"
    "v_cvt_pk_bf16_f32 v73, v74, v75\n\t"
    "v_mfma_f32_16x16x32_bf16 v[116:119], a[84:87], a[184:187], v[116:119]\n\t"
    "v_cvt_pk_bf16_f32 v74, v76, v77\n\t"
    "v_cvt_pk_bf16_f32 v75, v78, v79\n\t"
    "v_mfma_f32_16x16x32_bf16 v[116:119], a[116:119], a[188:191], v[116:119]\n\t"
    "v_mfma_f32_16x16x32_bf16 v[116:119], v[212:215], v[236:239], v[116:119]\n\t"
    "v_mfma_f32_16x16x32_bf16 v[120:123], a[24:27], a[152:155], v[120:123]\n\t"
    "v_mfma_f32_16x16x32_bf16 v[120:123], a[56:59], a[156:159], v[120:123]\n\t"
    "v_mfma_f32_16x16x32_bf16 v[120:123], a[88:91], a[184:187], v[120:123]\n\t"
    "s_add_u32 s40, s64, 4096\n\t"
    "buffer_store_dwordx4 v[72:75], v254, s[28:31], s40 offen offset:1024 sc1\n\t"
    "v_mfma_f32_16x16x32_bf16 v[120:123], a[120:123], a[188:191], v[120:123]\n\t"
    "v_mfma_f32_16x16x32_bf16 v[120:123], v[216:219], v[236:239], v[120:123]\n\t"
    "v_mfma_f32_16x16x32_bf16 v[124:127], a[28:31], a[152:155], v[124:127]\n\t"
    "v_mfma_f32_16x16x32_bf16 v[124:127], a[60:63], a[156:159], v[124:127]\n\t"
    "v_mfma_f32_16x16x32_bf16 v[124:127], a[92:95], a[184:187], v[124:127]\n\t"
    "v_mfma_f32_16x16x32_bf16 v[124:127], a[124:127], a[188:191], v[124:127]\n\t"
    "v_mfma_f32_16x16x32_bf16 v[124:127], v[220:223], v[236:239], v[124:127]\n\t"
    "L_k3e%=:\n\t"
    "v_max_f32 v96, 0, v96\n\t"
    "v_max_f32 v97, 0, v97\n\t"
    "v_max_f32 v98, 0, v98\n\t"
    "v_max_f32 v99, 0, v99\n\t"
    "v_max_f32 v100, 0, v100\n\t"
    "v_max_f32 v101, 0, v101\n\t"
    "v_max_f32 v102, 0, v102\n\t"
    "v_max_f32 v103, 0, v103\n\t"
    "v_pk_mul_f32 v[96:97], v[96:97], v[112:113]\n\t"
    "v_pk_mul_f32 v[98:99], v[98:99], v[114:115]\n\t"
    "v_pk_mul_f32 v[100:101], v[100:101], v[116:117]\n\t"
    "v_pk_mul_f32 v[102:103], v[102:103], v[118:119]\n\t"
    "v_cvt_pk_bf16_f32 v96, v96, v97\n\t"
    "v_cvt_pk_bf16_f32 v97, v98, v99\n\t"
    "v_cvt_pk_bf16_f32 v98, v100, v101\n\t"
    "v_cvt_pk_bf16_f32 v99, v102, v103\n\t"
    "s_add_u32 s40, s64, 6144\n\t"
    "buffer_store_dwordx4 v[96:99], v254, s[28:31], s40 offen offset:0 sc1\n\t"
    "v_max_f32 v104, 0, v104\n\t"
    "v_max_f32 v105, 0, v105\n\t"
    "v_max_f32 v106, 0, v106\n\t"
    "v_max_f32 v107, 0, v107\n\t"
    "v_max_f32 v108, 0, v108\n\t"
    "v_max_f32 v109, 0, v109\n\t"
    "v_max_f32 v110, 0, v110\n\t"
    "v_max_f32 v111, 0, v111\n\t"
    "v_pk_mul_f32 v[104:105], v[104:105], v[120:121]\n\t"
    "v_pk_mul_f32 v[106:107], v[106:107], v[122:123]\n\t"
    "v_pk_mul_f32 v[108:109], v[108:109], v[124:125]\n\t"
    "v_pk_mul_f32 v[110:111], v[110:111], v[126:127]\n\t"
    "v_cvt_pk_bf16_f32 v104, v104, v105\n\t"
    "v_cvt_pk_bf16_f32 v105, v106, v107\n\t"
    "v_cvt_pk_bf16_f32 v106, v108, v109\n\t"
    "v_cvt_pk_bf16_f32 v107, v110, v111\n\t"
    "s_add_u32 s40, s64, 6144\n\t"
    "buffer_store_dwordx4 v[104:107], v254, s[28:31], s40 offen offset:1024 sc1\n\t"
    "s_add_u32 s43, s66, 4096\n\t"
    "buffer_load_dwordx4 a[144:147], v254, s[28:31], s43 offen offset:0 sc1\n\t"
    "buffer_load_dwordx4 a[148:151], v254, s[28:31], s43 offen offset:1024 sc1\n\t"
    "buffer_load_dwordx4 a[152:155], v254, s[28:31], s43 offen offset:2048 sc1\n\t"
    "buffer_load_dwordx4 a[156:159], v254, s[28:31], s43 offen offset:3072 sc1\n\t"
    "s_add_u32 s43, s66, 12288\n\t"
    "buffer_load_dwordx4 a[176:179], v254, s[28:31], s43 offen offset:0 sc1\n\t"
    "buffer_load_dwordx4 a[180:183], v254, s[28:31], s43 offen offset:1024 sc1\n\t"
    "v_lshlrev_b32 v78, 7, v255\n\t"
    "v_mov_b32 v79, 1\n\t"
    "s_add_u32 s16, s18, s69\n\t"
    "s_addc_u32 s17, s19, 0\n\t"
    "s_waitcnt vmcnt(6) lgkmcnt(0)\n\t"
    "s_mov_b64 exec, 3\n\t"
    "global_atomic_add v78, v79, s[16:17]\n\t"
    "s_mov_b64 exec, -1\n\t"
    "s_barrier\n\t"
    "buffer_load_dwordx4 a[184:187], v254, s[28:31], s43 offen offset:2048 sc1\n\t"
    "buffer_load_dwordx4 a[188:191], v254, s[28:31], s43 offen offset:3072 sc1\n\t"
    "s_add_u32 s43, s66, 49152\n\t"
    "buffer_load_dwordx4 v[192:195], v254, s[28:31], s43 offen offset:0 sc1\n\t"
    "buffer_load_dwordx4 v[196:199], v254, s[28:31], s43 offen offset:1024 sc1\n\t"
    "buffer_load_dwordx4 v[200:203], v254, s[28:31], s43 offen offset:2048 sc1\n\t"
    "buffer_load_dwordx4 v[204:207], v254, s[28:31], s43 offen offset:3072 sc1\n\t"
    "s_add_u32 s43, s66, 53248\n\t"
    "buffer_load_dwordx4 v[208:211], v254, s[28:31], s43 offen offset:0 sc1\n\t"
    "buffer_load_dwordx4 v[212:215], v254, s[28:31], s43 offen offset:1024 sc1\n\t"
    "buffer_load_dwordx4 v[216:219], v254, s[28:31], s43 offen offset:2048 sc1\n\t"
    "buffer_load_dwordx4 v[220:223], v254, s[28:31], s43 offen offset:3072 sc1\n\t"
    "s_add_u32 s43, s66, 57344\n\t"
    "buffer_load_dwordx4 v[224:227], v254, s[28:31], s43 offen offset:0 sc1\n\t"
    "buffer_load_dwordx4 v[228:231], v254, s[28:31], s43 offen offset:1024 sc1\n\t"
    "s_branch L_k4%=\n\t"
    "L_ubw%=:\n\t"
    "s_mov_b32 s38, 0x20000\n\t"
    "L_poll4%=:\n\t"
    "buffer_load_dword v192, off, s[28:31], s98 sc1\n\t"
    "s_waitcnt vmcnt(0)\n\t"
    "v_readfirstlane_b32 s43, v192\n\t"
    "s_nop 0\n\t"
    "s_cmp_ge_u32 s43, 1\n\t"
    "s_cbranch_scc1 L_polld4%=\n\t"
    "s_sleep 2\n\t"
    "s_sub_u32 s38, s38, 1\n\t"
    "s_cbranch_scc0 L_poll4%=\n\t"
    "L_polld4%=:\n\t"
    "s_branch L_ubok%=\n\t"
    "L_k3skip%=:\n\t"
    "s_cmp_ge_u32 s10, 27\n\t"
    "s_cbranch_scc1 L_k3none%=\n\t"
    "L_k3ub%=:\n\t"
    "s_add_u32 m0, s37, 114688\n\t"
    "s_add_u32 s97, s63, 114688\n\t"
    "buffer_load_dwordx4 v254, s[28:31], s97 offen offset:0 sc1 lds\n\t"
    "buffer_load_dwordx4 v254, s[28:31], s97 offen offset:1024 sc1 lds\n\t"
    "buffer_load_dwordx4 v254, s[28:31], s97 offen offset:2048 sc1 lds\n\t"
    "buffer_load_dwordx4 v254, s[28:31], s97 offen offset:3072 sc1 lds\n\t"
    "s_add_u32 m0, s37, 131072\n\t"
    "s_add_u32 s97, s63, 131072\n\t"
    "buffer_load_dwordx4 v254, s[28:31], s97 offen offset:0 sc1 lds\n\t"
    "buffer_load_dwordx4 v254, s[28:31], s97 offen offset:1024 sc1 lds\n\t"
    "buffer_load_dwordx4 v254, s[28:31], s97 offen offset:2048 sc1 lds\n\t"
    "buffer_load_dwordx4 v254, s[28:31], s97 offen offset:3072 sc1 lds\n\t"
    "s_add_u32 m0, s37, 147456\n\t"
    "s_add_u32 s97, s63, 147456\n\t"
    "buffer_load_dwordx4 v254, s[28:31], s97 offen offset:0 sc1 lds\n\t"
    "buffer_load_dwordx4 v254, s[28:31], s97 offen offset:1024 sc1 lds\n\t"
    "buffer_load_dwordx4 v254, s[28:31], s97 offen offset:2048 sc1 lds\n\t"
    "buffer_load_dwordx4 v254, s[28:31], s97 offen offset:3072 sc1 lds\n\t"
    "v_and_b32 v80, 15, v255\n\t"
    "v_mul_u32_u24 v81, 1280, v80\n\t"
    "v_and_b32 v82, 48, v255\n\t"
    "v_add_u32 v81, v81, v82\n\t"
    "s_mov_b32 s43, 83200\n\t"
    "s_mov_b32 exec_lo, 0x00030003\n\t"
    "s_mov_b32 exec_hi, 0x00030003\n\t"
    "buffer_load_dwordx4 v[0:3], v81, s[28:31], s43 offen offset:0 sc1\n\t"
    "buffer_load_dwordx4 v[4:7], v81, s[28:31], s43 offen offset:64 sc1\n\t"
    "buffer_load_dwordx4 v[8:11], v81, s[28:31], s43 offen offset:128 sc1\n\t"
    "buffer_load_dwordx4 v[12:15], v81, s[28:31], s43 offen offset:192 sc1\n\t"
    "buffer_load_dwordx4 v[16:19], v81, s[28:31], s43 offen offset:256 sc1\n\t"
    "buffer_load_dwordx4 v[20:23], v81, s[28:31], s43 offen offset:320 sc1\n\t"
    "buffer_load_dwordx4 v[24:27], v81, s[28:31], s43 offen offset:384 sc1\n\t"
    "buffer_load_dwordx4 v[28:31], v81, s[28:31], s43 offen offset:448 sc1\n\t"
    "buffer_load_dwordx4 v[32:35], v81, s[28:31], s43 offen offset:512 sc1\n\t"
    "buffer_load_dwordx4 v[36:39], v81, s[28:31], s43 offen offset:576 sc1\n\t"
    "buffer_load_dwordx4 v[40:43], v81, s[28:31], s43 offen offset:640 sc1\n\t"
    "buffer_load_dwordx4 v[44:47], v81, s[28:31], s43 offen offset:704 sc1\n\t"
    "buffer_load_dwordx4 v[48:51], v81, s[28:31], s43 offen offset:768 sc1\n\t"
    "buffer_load_dwordx4 v[52:55], v81, s[28:31], s43 offen offset:832 sc1\n\t"
    "buffer_load_dwordx4 v[56:59], v81, s[28:31], s43 offen offset:896 sc1\n\t"
    "buffer_load_dwordx4 v[60:63], v81, s[28:31], s43 offen offset:960 sc1\n\t"
    "buffer_load_dwordx4 v[64:67], v81, s[28:31], s43 offen offset:1024 sc1\n\t"
    "buffer_load_dwordx4 v[68:71], v81, s[28:31], s43 offen offset:1088 sc1\n\t"
    "buffer_load_dwordx4 v[72:75], v81, s[28:31], s43 offen offset:1152 sc1\n\t"
    "buffer_load_dwordx4 v[76:79], v81, s[28:31], s43 offen offset:1216 sc1\n\t"
    "s_mov_b64 exec, -1\n\t"
    "s_lshr_b32 s43, s37, 1\n\t"
    "v_add_u32 v84, s43, v240\n\t"
    "v_add_u32 v85, s43, v241\n\t"
    "v_add_u32 v86, s43, v242\n\t"
    "s_waitcnt vmcnt(0)\n\t"
    "s_barrier\n\t"
    "ds_read_b128 a[64:67], v84 offset:0\n\t"
    "ds_read_b128 a[68:71], v84 offset:1024\n\t"
    "ds_read_b128 a[72:75], v84 offset:8192\n\t"
    "ds_read_b128 a[76:79], v84 offset:9216\n\t"
    "ds_read_b128 a[80:83], v84 offset:16384\n\t"
    "ds_read_b128 a[84:87], v84 offset:17408\n\t"
    "ds_read_b128 a[88:91], v84 offset:24576\n\t"
    "ds_read_b128 a[92:95], v84 offset:25600\n\t"
    "s_waitcnt lgkmcnt(4)\n\t"
    "v_mfma_f32_16x16x32_bf16 v[88:91], v[0:3], a[64:67], 0\n\t"
    "ds_read_b128 a[96:99], v84 offset:32768\n\t"
    "v_mfma_f32_16x16x32_bf16 v[92:95], v[0:3], a[68:71], 0\n\t"
    "ds_read_b128 a[100:103], v84 offset:33792\n\t"
    "v_mfma_f32_16x16x32_bf16 v[88:91], v[4:7], a[72:75], v[88:91]\n\t"
    "ds_read_b128 a[104:107], v84 offset:40960\n\t"
    "v_mfma_f32_16x16x32_bf16 v[92:95], v[4:7], a[76:79], v[92:95]\n\t"
    "s_waitcnt lgkmcnt(3)\n\t"
    "ds_read_b128 a[108:111], v84 offset:41984\n\t"
    "v_mfma_f32_16x16x32_bf16 v[88:91], v[8:11], a[80:83], v[88:91]\n\t"
    "ds_read_b128 a[112:115], v84 offset:49152\n\t"
    "v_mfma_f32_16x16x32_bf16 v[92:95], v[8:11], a[84:87], v[92:95]\n\t"
    "ds_read_b128 a[116:119], v84 offset:50176\n\t"
    "v_mfma_f32_16x16x32_bf16 v[88:91], v[12:15], a[88:91], v[88:91]\n\t"
    "ds_read_b128 a[120:123], v84 offset:57344\n\t"
    "v_mfma_f32_16x16x32_bf16 v[92:95], v[12:15], a[92:95], v[92:95]\n\t"
    "s_waitcnt lgkmcnt(3)\n\t"
    "ds_read_b128 a[124:127], v84 offset:58368\n\t"
    "v_mfma_f32_16x16x32_bf16 v[88:91], v[16:19], a[96:99], v[88:91]\n\t"
    "ds_read_b128 a[128:131], v85 offset:0\n\t"
    "v_mfma_f32_16x16x32_bf16 v[92:95], v[16:19], a[100:103], v[92:95]\n\t"
    "ds_read_b128 a[132:135], v85 offset:1024\n\t"
    "v_mfma_f32_16x16x32_bf16 v[88:91], v[20:23], a[104:107], v[88:91]\n\t"
    "ds_read_b128 a[136:139], v85 offset:8192\n\t"
    "v_mfma_f32_16x16x32_bf16 v[92:95], v[20:23], a[108:111], v[92:95]\n\t"
    "s_waitcnt lgkmcnt(3)\n\t"
    "ds_read_b128 a[140:143], v85 offset:9216\n\t"
    "v_mfma_f32_16x16x32_bf16 v[88:91], v[24:27], a[112:115], v[88:91]\n\t"
    "ds_read_b128 a[144:147], v85 offset:16384\n\t"
    "v_mfma_f32_16x16x32_bf16 v[92:95], v[24:27], a[116:119], v[92:95]\n\t"
    "ds_read_b128 a[148:151], v85 offset:17408\n\t"
    "v_mfma_f32_16x16x32_bf16 v[88:91], v[28:31], a[120:123], v[88:91]\n\t"
    "ds_read_b128 a[152:155], v85 offset:24576\n\t"
    "v_mfma_f32_16x16x32_bf16 v[92:95], v[28:31], a[124:127], v[92:95]\n\t"
    "s_waitcnt lgkmcnt(3)\n\t"
    "ds_read_b128 a[156:159], v85 offset:25600\n\t"
    "v_mfma_f32_16x16x32_bf16 v[88:91], v[32:35], a[128:131], v[88:91]\n\t"
    "ds_read_b128 a[160:163], v85 offset:32768\n\t"
    "v_mfma_f32_16x16x32_bf16 v[92:95], v[32:35], a[132:135], v[92:95]\n\t"
    "ds_read_b128 a[164:167], v85 offset:33792\n\t"
    "v_mfma_f32_16x16x32_bf16 v[88:91], v[36:39], a[136:139], v[88:91]\n\t"
    "ds_read_b128 a[168:171], v85 offset:40960\n\t"
    "v_mfma_f32_16x16x32_bf16 v[92:95], v[36:39], a[140:143], v[92:95]\n\t"
    "s_waitcnt lgkmcnt(3)\n\t"
    "ds_read_b128 a[172:175], v85 offset:41984\n\t"
    "v_mfma_f32_16x16x32_bf16 v[88:91], v[40:43], a[144:147], v[88:91]\n\t"
    "ds_read_b128 a[176:179], v85 offset:49152\n\t"
    "v_mfma_f32_16x16x32_bf16 v[92:95], v[40:43], a[148:151], v[92:95]\n\t"
    "ds_read_b128 a[180:183], v85 offset:50176\n\t"
    "v_mfma_f32_16x16x32_bf16 v[88:91], v[44:47], a[152:155], v[88:91]\n\t"
    "ds_read_b128 a[184:187], v85 offset:57344\n\t"
    "v_mfma_f32_16x16x32_bf16 v[92:95], v[44:47], a[156:159], v[92:95]\n\t"
    "s_waitcnt lgkmcnt(3)\n\t"
    "ds_read_b128 a[188:191], v85 offset:58368\n\t"
    "v_mfma_f32_16x16x32_bf16 v[88:91], v[48:51], a[160:163], v[88:91]\n\t"
    "ds_read_b128 a[192:195], v86 offset:0\n\t"
    "v_mfma_f32_16x16x32_bf16 v[92:95], v[48:51], a[164:167], v[92:95]\n\t"
    "ds_read_b128 a[196:199], v86 offset:1024\n\t"
    "v_mfma_f32_16x16x32_bf16 v[88:91], v[52:55], a[168:171], v[88:91]\n\t"
    "ds_read_b128 a[200:203], v86 offset:8192\n\t"
    "v_mfma_f32_16x16x32_bf16 v[92:95], v[52:55], a[172:175], v[92:95]\n\t"
    "s_waitcnt lgkmcnt(3)\n\t"
    "ds_read_b128 a[204:207], v86 offset:9216\n\t"
    "v_mfma_f32_16x16x32_bf16 v[88:91], v[56:59], a[176:179], v[88:91]\n\t"
    "ds_read_b128 a[208:211], v86 offset:16384\n\t"
    "v_mfma_f32_16x16x32_bf16 v[92:95], v[56:59], a[180:183], v[92:95]\n\t"
    "ds_read_b128 a[212:215], v86 offset:17408\n\t"
    "v_mfma_f32_16x16x32_bf16 v[88:91], v[60:63], a[184:187], v[88:91]\n\t"
    "ds_read_b128 a[216:219], v86 offset:24576\n\t"
    "v_mfma_f32_16x16x32_bf16 v[92:95], v[60:63], a[188:191], v[92:95]\n\t"
    "s_waitcnt lgkmcnt(3)\n\t"
    "ds_read_b128 a[220:223], v86 offset:25600\n\t"
    "v_mfma_f32_16x16x32_bf16 v[88:91], v[64:67], a[192:195], v[88:91]\n\t"
    "v_mfma_f32_16x16x32_bf16 v[92:95], v[64:67], a[196:199], v[92:95]\n\t"
    "v_mfma_f32_16x16x32_bf16 v[88:91], v[68:71], a[200:203], v[88:91]\n\t"
    "v_mfma_f32_16x16x32_bf16 v[92:95], v[68:71], a[204:207], v[92:95]\n\t"
    "s_waitcnt lgkmcnt(0)\n\t"
    "v_mfma_f32_16x16x32_bf16 v[88:91], v[72:75], a[208:211], v[88:91]\n\t"
    "v_mfma_f32_16x16x32_bf16 v[92:95], v[72:75], a[212:215], v[92:95]\n\t"
    "v_mfma_f32_16x16x32_bf16 v[88:91], v[76:79], a[216:219], v[88:91]\n\t"
    "v_mfma_f32_16x16x32_bf16 v[92:95], v[76:79], a[220:223], v[92:95]\n\t"
    "s_lshl2_add_u32 s98, s10, s77\n\t"
    "s_mul_i32 s98, s98, 512\n\t"
    "s_add_u32 s98, s98, 262144\n\t"
    "s_mul_i32 s99, s10, 128\n\t"
    "s_add_u32 s99, s99, 643072\n\t"
    "v_lshlrev_b32 v78, 7, v255\n\t"
    "v_mov_b32 v79, 1\n\t"
    "s_mov_b64 exec, 0xffff\n\t"
    "v_mov_b32 v91, v88\n\t"
    "v_cvt_pk_bf16_f32 v88, v89, v89\n\t"
    "v_and_b32 v80, 0xffff0000, v88\n\t"
    "v_sub_f32 v80, v89, v80\n\t"
    "v_cvt_pk_bf16_f32 v89, v80, v80\n\t"
    "v_cvt_pk_bf16_f32 v90, v91, v91\n\t"
    "v_and_b32 v80, 0xffff0000, v90\n\t"
    "v_sub_f32 v80, v91, v80\n\t"
    "v_cvt_pk_bf16_f32 v91, v80, v80\n\t"
    "buffer_store_dwordx4 v[88:91], v254, s[28:31], s98 offen offset:0 sc1\n\t"
    "v_mov_b32 v95, v92\n\t"
    "v_cvt_pk_bf16_f32 v92, v93, v93\n\t"
    "v_and_b32 v80, 0xffff0000, v92\n\t"
    "v_sub_f32 v80, v93, v80\n\t"
    "v_cvt_pk_bf16_f32 v93, v80, v80\n\t"
    "v_cvt_pk_bf16_f32 v94, v95, v95\n\t"
    "v_and_b32 v80, 0xffff0000, v94\n\t"
    "v_sub_f32 v80, v95, v80\n\t"
    "v_cvt_pk_bf16_f32 v95, v80, v80\n\t"
    "buffer_store_dwordx4 v[92:95], v254, s[28:31], s98 offen offset:256 sc1\n\t"
    "s_mov_b64 exec, -1\n\t"
    "s_add_u32 s16, s18, s99\n\t"
    "s_addc_u32 s17, s19, 0\n\t"
    "s_waitcnt vmcnt(0) lgkmcnt(0)\n\t"
    "s_barrier\n\t"
    "s_cmp_lg_u32 s77, 0\n\t"
    "s_cbranch_scc1 L_sig4%=\n\t"
    "L_sigb4%=:\n\t"
    "s_mov_b64 exec, 1\n\t"
    "global_atomic_add v78, v79, s[16:17]\n\t"
    "s_mov_b64 exec, -1\n\t"
    "L_sig4%=:\n\t"
    "L_k3none%=:\n\t"
    "s_add_u32 s43, s66, 0\n\t"
    "buffer_load_dwordx4 a[128:131], v254, s[28:31], s43 offen offset:0 sc1\n\t"
    "buffer_load_dwordx4 a[132:135], v254, s[28:31], s43 offen offset:1024 sc1\n\t"
    "buffer_load_dwordx4 a[136:139], v254, s[28:31], s43 offen offset:2048 sc1\n\t"
    "buffer_load_dwordx4 a[140:143], v254, s[28:31], s43 offen offset:3072 sc1\n\t"
    "s_add_u32 s43, s66, 4096\n\t"
    "buffer_load_dwordx4 a[144:147], v254, s[28:31], s43 offen offset:0 sc1\n\t"
    "buffer_load_dwordx4 a[148:151], v254, s[28:31], s43 offen offset:1024 sc1\n\t"
    "buffer_load_dwordx4 a[152:155], v254, s[28:31], s43 offen offset:2048 sc1\n\t"
    "buffer_load_dwordx4 a[156:159], v254, s[28:31], s43 offen offset:3072 sc1\n\t"
    "s_add_u32 s43, s66, 8192\n\t"
    "buffer_load_dwordx4 a[160:163], v254, s[28:31], s43 offen offset:0 sc1\n\t"
    "buffer_load_dwordx4 a[164:167], v254, s[28:31], s43 offen offset:1024 sc1\n\t"
    "buffer_load_dwordx4 a[168:171], v254, s[28:31], s43 offen offset:2048 sc1\n\t"
    "buffer_load_dwordx4 a[172:175], v254, s[28:31], s43 offen offset:3072 sc1\n\t"
    "s_add_u32 s43, s66, 12288\n\t"
    "buffer_load_dwordx4 a[176:179], v254, s[28:31], s43 offen offset:0 sc1\n\t"
    "buffer_load_dwordx4 a[180:183], v254, s[28:31], s43 offen offset:1024 sc1\n\t"
    "buffer_load_dwordx4 a[184:187], v254, s[28:31], s43 offen offset:2048 sc1\n\t"
    "buffer_load_dwordx4 a[188:191], v254, s[28:31], s43 offen offset:3072 sc1\n\t"
    "s_add_u32 s43, s66, 16384\n\t"
    "buffer_load_dwordx4 a[192:195], v254, s[28:31], s43 offen offset:0 sc1\n\t"
    "buffer_load_dwordx4 a[196:199], v254, s[28:31], s43 offen offset:1024 sc1\n\t"
    "buffer_load_dwordx4 a[200:203], v254, s[28:31], s43 offen offset:2048 sc1\n\t"
    "buffer_load_dwordx4 a[204:207], v254, s[28:31], s43 offen offset:3072 sc1\n\t"
    "s_add_u32 s43, s66, 20480\n\t"
    "buffer_load_dwordx4 a[208:211], v254, s[28:31], s43 offen offset:0 sc1\n\t"
    "buffer_load_dwordx4 a[212:215], v254, s[28:31], s43 offen offset:1024 sc1\n\t"
    "buffer_load_dwordx4 a[216:219], v254, s[28:31], s43 offen offset:2048 sc1\n\t"
    "buffer_load_dwordx4 a[220:223], v254, s[28:31], s43 offen offset:3072 sc1\n\t"
    "s_add_u32 s43, s66, 24576\n\t"
    "buffer_load_dwordx4 a[224:227], v254, s[28:31], s43 offen offset:0 sc1\n\t"
    "buffer_load_dwordx4 a[228:231], v254, s[28:31], s43 offen offset:1024 sc1\n\t"
    "buffer_load_dwordx4 a[232:235], v254, s[28:31], s43 offen offset:2048 sc1\n\t"
    "buffer_load_dwordx4 a[236:239], v254, s[28:31], s43 offen offset:3072 sc1\n\t"
    "s_add_u32 s43, s66, 28672\n\t"
    "buffer_load_dwordx4 a[240:243], v254, s[28:31], s43 offen offset:0 sc1\n\t"
    "buffer_load_dwordx4 a[244:247], v254, s[28:31], s43 offen offset:1024 sc1\n\t"
    "buffer_load_dwordx4 a[248:251], v254, s[28:31], s43 offen offset:2048 sc1\n\t"
    "buffer_load_dwordx4 a[252:255], v254, s[28:31], s43 offen offset:3072 sc1\n\t"
    "s_add_u32 s43, s66, 32768\n\t"
    "buffer_load_dwordx4 v[128:131], v254, s[28:31], s43 offen offset:0 sc1\n\t"
    "buffer_load_dwordx4 v[132:135], v254, s[28:31], s43 offen offset:1024 sc1\n\t"
    "buffer_load_dwordx4 v[136:139], v254, s[28:31], s43 offen offset:2048 sc1\n\t"
    "buffer_load_dwordx4 v[140:143], v254, s[28:31], s43 offen offset:3072 sc1\n\t"
    "s_add_u32 s43, s66, 36864\n\t"
    "buffer_load_dwordx4 v[144:147], v254, s[28:31], s43 offen offset:0 sc1\n\t"
    "buffer_load_dwordx4 v[148:151], v254, s[28:31], s43 offen offset:1024 sc1\n\t"
    "buffer_load_dwordx4 v[152:155], v254, s[28:31], s43 offen offset:2048 sc1\n\t"
    "buffer_load_dwordx4 v[156:159], v254, s[28:31], s43 offen offset:3072 sc1\n\t"
    "s_add_u32 s43, s66, 40960\n\t"
    "buffer_load_dwordx4 v[160:163], v254, s[28:31], s43 offen offset:0 sc1\n\t"
    "buffer_load_dwordx4 v[164:167], v254, s[28:31], s43 offen offset:1024 sc1\n\t"
    "buffer_load_dwordx4 v[168:171], v254, s[28:31], s43 offen offset:2048 sc1\n\t"
    "buffer_load_dwordx4 v[172:175], v254, s[28:31], s43 offen offset:3072 sc1\n\t"
    "s_add_u32 s43, s66, 45056\n\t"
    "buffer_load_dwordx4 v[176:179], v254, s[28:31], s43 offen offset:0 sc1\n\t"
    "buffer_load_dwordx4 v[180:183], v254, s[28:31], s43 offen offset:1024 sc1\n\t"
    "buffer_load_dwordx4 v[184:187], v254, s[28:31], s43 offen offset:2048 sc1\n\t"
    "buffer_load_dwordx4 v[188:191], v254, s[28:31], s43 offen offset:3072 sc1\n\t"
    "s_add_u32 s43, s66, 49152\n\t"
    "buffer_load_dwordx4 v[192:195], v254, s[28:31], s43 offen offset:0 sc1\n\t"
    "buffer_load_dwordx4 v[196:199], v254, s[28:31], s43 offen offset:1024 sc1\n\t"
    "buffer_load_dwordx4 v[200:203], v254, s[28:31], s43 offen offset:2048 sc1\n\t"
    "buffer_load_dwordx4 v[204:207], v254, s[28:31], s43 offen offset:3072 sc1\n\t"
    "s_add_u32 s43, s66, 53248\n\t"
    "buffer_load_dwordx4 v[208:211], v254, s[28:31], s43 offen offset:0 sc1\n\t"
    "buffer_load_dwordx4 v[212:215], v254, s[28:31], s43 offen offset:1024 sc1\n\t"
    "buffer_load_dwordx4 v[216:219], v254, s[28:31], s43 offen offset:2048 sc1\n\t"
    "buffer_load_dwordx4 v[220:223], v254, s[28:31], s43 offen offset:3072 sc1\n\t"
    "s_add_u32 s43, s66, 57344\n\t"
    "buffer_load_dwordx4 v[224:227], v254, s[28:31], s43 offen offset:0 sc1\n\t"
    "buffer_load_dwordx4 v[228:231], v254, s[28:31], s43 offen offset:1024 sc1\n\t"
    "s_sleep 100\n\t"
    "L_k4%=:\n\t"
    "s_add_u32 s43, s77, 0\n\t"
    "s_and_b32 s43, s43, 3\n\t"
    "v_lshl_add_u32 v240, s43, 11, v254\n\t"
    "s_add_u32 s43, s77, 1\n\t"
    "s_and_b32 s43, s43, 3\n\t"
    "v_lshl_add_u32 v241, s43, 11, v254\n\t"
    "s_add_u32 s43, s77, 2\n\t"
    "s_and_b32 s43, s43, 3\n\t"
    "v_lshl_add_u32 v242, s43, 11, v254\n\t"
    "s_add_u32 s43, s77, 3\n\t"
    "s_and_b32 s43, s43, 3\n\t"
    "v_lshl_add_u32 v243, s43, 11, v254\n\t"
    "v_add_u32 v124, s37, v254\n\t"
    "v_add_u32 v124, 98304, v124\n\t"
    "v_lshlrev_b32 v125, 1, v243\n\t"
    "v_sub_u32 v125, v125, v254\n\t"
    "v_add_u32 v125, 98304, v125\n\t"
    "v_lshlrev_b32 v126, 1, v242\n\t"
    "v_sub_u32 v126, v126, v254\n\t"
    "v_add_u32 v126, 98304, v126\n\t"
    "v_lshlrev_b32 v127, 1, v241\n\t"
    "v_sub_u32 v127, v127, v254\n\t"
    "v_add_u32 v127, 98304, v127\n\t"
    "v_and_b32 v122, 15, v255\n\t"
    "v_mul_u32_u24 v120, 1280, v122\n\t"
    "v_and_b32 v121, 48, v255\n\t"
    "v_and_b32 v122, 0x300, v254\n\t"
    "v_and_b32 v123, 1, v255\n\t"
    "v_lshl_add_u32 v122, v123, 7, v122\n\t"
    "v_and_b32 v123, 14, v255\n\t"
    "v_lshl_add_u32 v122, v123, 3, v122\n\t"
    "v_bfe_u32 v123, v255, 4, 1\n\t"
    "v_lshl_add_u32 v120, v123, 5, v120\n\t"
    "v_lshrrev_b32 v123, 5, v255\n\t"
    "v_lshl_add_u32 v120, v123, 4, v120\n\t"
    "s_cmp_eq_u32 s77, 3\n\t"
    "s_cselect_b32 s99, 6, 7\n\t"
    "s_mov_b32 m0, s37\n\t"
    "s_add_u32 s43, s72, 77312\n\t"
    "v_add_u32 v121, s37, v121\n\t"
    "s_waitcnt vmcnt(62)\n\t"
    "buffer_load_dwordx4 v254, s[28:31], s43 offen sc1 lds\n\t"
    "s_waitcnt vmcnt(62)\n\t"
    "buffer_load_dword v76, off, s[28:31], s68 sc1\n\t"
    "s_add_u32 s43, s66, 57344\n\t"
    "s_waitcnt vmcnt(62)\n\t"
    "buffer_load_dwordx4 v[232:235], v254, s[28:31], s43 offen offset:2048 sc1\n\t"
    "s_waitcnt vmcnt(62)\n\t"
    "buffer_load_dwordx4 v[236:239], v254, s[28:31], s43 offen offset:3072 sc1\n\t"
    "s_add_u32 s43, s11, 61440\n\t"
    "s_waitcnt vmcnt(62)\n\t"
    "buffer_load_dwordx4 a[88:91], v254, s[28:31], s43 offen offset:0 sc1\n\t"
    "s_waitcnt vmcnt(62)\n\t"
    "buffer_load_dwordx4 a[92:95], v254, s[28:31], s43 offen offset:1024 sc1\n\t"
    "s_waitcnt vmcnt(62)\n\t"
    "buffer_load_dwordx4 a[96:99], v254, s[28:31], s43 offen offset:2048 sc1\n\t"
    "s_waitcnt vmcnt(62)\n\t"
    "buffer_load_dwordx4 a[100:103], v254, s[28:31], s43 offen offset:3072 sc1\n\t"
    "s_add_u32 s43, s11, 65536\n\t"
    "s_waitcnt vmcnt(62)\n\t"
    "buffer_load_dwordx4 a[104:107], v254, s[28:31], s43 offen offset:0 sc1\n\t"
    "s_waitcnt vmcnt(62)\n\t"
    "buffer_load_dwordx4 a[108:111], v254, s[28:31], s43 offen offset:1024 sc1\n\t"
    "s_waitcnt vmcnt(62)\n\t"
    "buffer_load_dwordx4 a[112:115], v254, s[28:31], s43 offen offset:2048 sc1\n\t"
    "s_waitcnt vmcnt(62)\n\t"
    "buffer_load_dwordx4 a[116:119], v254, s[28:31], s43 offen offset:3072 sc1\n\t"
    "s_add_u32 s43, s11, 69632\n\t"
    "s_waitcnt vmcnt(62)\n\t"
    "buffer_load_dwordx4 a[120:123], v254, s[28:31], s43 offen offset:0 sc1\n\t"
    "s_waitcnt vmcnt(62)\n\t"
    "buffer_load_dwordx4 a[124:127], v254, s[28:31], s43 offen offset:1024 sc1\n\t"
    "s_waitcnt vmcnt(12)\n\t"
    "v_readfirstlane_b32 s43, v76\n\t"
    "s_nop 0\n\t"
    "s_cmp_ge_u32 s43, s99\n\t"
    "s_cbranch_scc1 L_polld3%=\n\t"
    "L_pollb3%=:\n\t"
    "s_mov_b32 s38, 0x20000\n\t"
    "L_poll3%=:\n\t"
    "buffer_load_dword v76, off, s[28:31], s68 sc1\n\t"
    "s_waitcnt vmcnt(0)\n\t"
    "v_readfirstlane_b32 s43, v76\n\t"
    "s_nop 0\n\t"
    "s_cmp_ge_u32 s43, s99\n\t"
    "s_cbranch_scc1 L_polld3%=\n\t"
    "s_sleep 2\n\t"
    "s_sub_u32 s38, s38, 1\n\t"
    "s_cbranch_scc0 L_poll3%=\n\t"
    "L_polld3%=:\n\t"
    "s_add_u32 s43, s65, 0\n\t"
    "buffer_load_dwordx4 a[0:3], v240, s[28:31], s43 offen offset:0 sc1\n\t"
    "buffer_load_dwordx4 a[4:7], v240, s[28:31], s43 offen offset:1024 sc1\n\t"
    "buffer_load_dwordx4 a[8:11], v241, s[28:31], s43 offen offset:0 sc1\n\t"
    "buffer_load_dwordx4 a[12:15], v241, s[28:31], s43 offen offset:1024 sc1\n\t"
    "buffer_load_dwordx4 a[16:19], v242, s[28:31], s43 offen offset:0 sc1\n\t"
    "buffer_load_dwordx4 a[20:23], v242, s[28:31], s43 offen offset:1024 sc1\n\t"
    "buffer_load_dwordx4 a[24:27], v243, s[28:31], s43 offen offset:0 sc1\n\t"
    "buffer_load_dwordx4 a[28:31], v243, s[28:31], s43 offen offset:1024 sc1\n\t"
    "s_add_u32 s43, s65, 8192\n\t"
    "buffer_load_dwordx4 a[32:35], v240, s[28:31], s43 offen offset:0 sc1\n\t"
    "buffer_load_dwordx4 a[36:39], v240, s[28:31], s43 offen offset:1024 sc1\n\t"
    "buffer_load_dwordx4 a[40:43], v241, s[28:31], s43 offen offset:0 sc1\n\t"
    "buffer_load_dwordx4 a[44:47], v241, s[28:31], s43 offen offset:1024 sc1\n\t"
    "L_k4m%=:\n\t"
    "s_waitcnt vmcnt(10)\n\t"
    "v_mfma_f32_16x16x32_bf16 v[0:3], a[128:131], a[0:3], 0\n\t"
    "v_mfma_f32_16x16x32_bf16 v[0:3], a[148:151], a[4:7], v[0:3]\n\t"
    "s_add_u32 s43, s65, 8192\n\t"
    "buffer_load_dwordx4 a[48:51], v242, s[28:31], s43 offen offset:0 sc1\n\t"
    "v_mfma_f32_16x16x32_bf16 v[4:7], a[132:135], a[0:3], 0\n\t"
    "v_mfma_f32_16x16x32_bf16 v[4:7], a[152:155], a[4:7], v[4:7]\n\t"
    "v_mfma_f32_16x16x32_bf16 v[8:11], a[136:139], a[0:3], 0\n\t"
    "v_mfma_f32_16x16x32_bf16 v[8:11], a[156:159], a[4:7], v[8:11]\n\t"
    "buffer_load_dwordx4 a[52:55], v242, s[28:31], s43 offen offset:1024 sc1\n\t"
    "v_mfma_f32_16x16x32_bf16 v[12:15], a[140:143], a[0:3], 0\n\t"
    "v_mfma_f32_16x16x32_bf16 v[12:15], a[160:163], a[4:7], v[12:15]\n\t"
    "v_mfma_f32_16x16x32_bf16 v[16:19], a[144:147], a[0:3], 0\n\t"
    "v_mfma_f32_16x16x32_bf16 v[16:19], a[164:167], a[4:7], v[16:19]\n\t"
    "s_waitcnt vmcnt(10)\n\t"
    "v_mfma_f32_16x16x32_bf16 v[20:23], a[128:131], a[8:11], 0\n\t"
    "buffer_load_dwordx4 a[56:59], v243, s[28:31], s43 offen offset:0 sc1\n\t"
    "v_mfma_f32_16x16x32_bf16 v[20:23], a[148:151], a[12:15], v[20:23]\n\t"
    "v_mfma_f32_16x16x32_bf16 v[24:27], a[132:135], a[8:11], 0\n\t"
    "v_mfma_f32_16x16x32_bf16 v[24:27], a[152:155], a[12:15], v[24:27]\n\t"
    "v_mfma_f32_16x16x32_bf16 v[28:31], a[136:139], a[8:11], 0\n\t"
    "buffer_load_dwordx4 a[60:63], v243, s[28:31], s43 offen offset:1024 sc1\n\t"
    "v_mfma_f32_16x16x32_bf16 v[28:31], a[156:159], a[12:15], v[28:31]\n\t"
    "v_mfma_f32_16x16x32_bf16 v[32:35], a[140:143], a[8:11], 0\n\t"
    "v_mfma_f32_16x16x32_bf16 v[32:35], a[160:163], a[12:15], v[32:35]\n\t"
    "v_mfma_f32_16x16x32_bf16 v[36:39], a[144:147], a[8:11], 0\n\t"
    "v_mfma_f32_16x16x32_bf16 v[36:39], a[164:167], a[12:15], v[36:39]\n\t"
    "s_waitcnt vmcnt(10)\n\t"
    "s_add_u32 s43, s65, 16384\n\t"
    "buffer_load_dwordx4 a[64:67], v240, s[28:31], s43 offen offset:0 sc1\n\t"
    "v_mfma_f32_16x16x32_bf16 v[40:43], a[128:131], a[16:19], 0\n\t"
    "v_mfma_f32_16x16x32_bf16 v[40:43], a[148:151], a[20:23], v[40:43]\n\t"
    "v_mfma_f32_16x16x32_bf16 v[44:47], a[132:135], a[16:19], 0\n\t"
    "v_mfma_f32_16x16x32_bf16 v[44:47], a[152:155], a[20:23], v[44:47]\n\t"
    "buffer_load_dwordx4 a[68:71], v240, s[28:31], s43 offen offset:1024 sc1\n\t"
    "v_mfma_f32_16x16x32_bf16 v[48:51], a[136:139], a[16:19], 0\n\t"
    "v_mfma_f32_16x16x32_bf16 v[48:51], a[156:159], a[20:23], v[48:51]\n\t"
    "v_mfma_f32_16x16x32_bf16 v[52:55], a[140:143], a[16:19], 0\n\t"
    "v_mfma_f32_16x16x32_bf16 v[52:55], a[160:163], a[20:23], v[52:55]\n\t"
    "v_mfma_f32_16x16x32_bf16 v[56:59], a[144:147], a[16:19], 0\n\t"
    "buffer_load_dwordx4 a[72:75], v241, s[28:31], s43 offen offset:0 sc1\n\t"
    "v_mfma_f32_16x16x32_bf16 v[56:59], a[164:167], a[20:23], v[56:59]\n\t"
    "s_waitcnt vmcnt(11)\n\t"
    "v_mfma_f32_16x16x32_bf16 v[60:63], a[128:131], a[24:27], 0\n\t"
    "v_mfma_f32_16x16x32_bf16 v[60:63], a[148:151], a[28:31], v[60:63]\n\t"
    "v_mfma_f32_16x16x32_bf16 v[64:67], a[132:135], a[24:27], 0\n\t"
    "buffer_load_dwordx4 a[76:79], v241, s[28:31], s43 offen offset:1024 sc1\n\t"
    "v_mfma_f32_16x16x32_bf16 v[64:67], a[152:155], a[28:31], v[64:67]\n\t"
    "v_mfma_f32_16x16x32_bf16 v[68:71], a[136:139], a[24:27], 0\n\t"
    "v_mfma_f32_16x16x32_bf16 v[68:71], a[156:159], a[28:31], v[68:71]\n\t"
    "v_mfma_f32_16x16x32_bf16 v[72:75], a[140:143], a[24:27], 0\n\t"
    "v_mfma_f32_16x16x32_bf16 v[72:75], a[160:163], a[28:31], v[72:75]\n\t"
    "buffer_load_dwordx4 a[80:83], v242, s[28:31], s43 offen offset:0 sc1\n\t"
    "v_mfma_f32_16x16x32_bf16 v[76:79], a[144:147], a[24:27], 0\n\t"
    "v_mfma_f32_16x16x32_bf16 v[76:79], a[164:167], a[28:31], v[76:79]\n\t"
    "s_waitcnt vmcnt(11)\n\t"
    "v_mfma_f32_16x16x32_bf16 v[0:3], a[168:171], a[32:35], v[0:3]\n\t"
    "v_mfma_f32_16x16x32_bf16 v[0:3], a[188:191], a[36:39], v[0:3]\n\t"
    "buffer_load_dwordx4 a[84:87], v242, s[28:31], s43 offen offset:1024 sc1\n\t"
    "v_mfma_f32_16x16x32_bf16 v[4:7], a[172:175], a[32:35], v[4:7]\n\t"
    "v_mfma_f32_16x16x32_bf16 v[4:7], a[192:195], a[36:39], v[4:7]\n\t"
    "v_mfma_f32_16x16x32_bf16 v[8:11], a[176:179], a[32:35], v[8:11]\n\t"
    "v_mfma_f32_16x16x32_bf16 v[8:11], a[196:199], a[36:39], v[8:11]\n\t"
    "v_mfma_f32_16x16x32_bf16 v[12:15], a[180:183], a[32:35], v[12:15]\n\t"
    "buffer_load_dwordx4 v[80:83], v243, s[28:31], s43 offen offset:0 sc1\n\t"
    "v_mfma_f32_16x16x32_bf16 v[12:15], a[200:203], a[36:39], v[12:15]\n\t"
    "v_mfma_f32_16x16x32_bf16 v[16:19], a[184:187], a[32:35], v[16:19]\n\t"
    "v_mfma_f32_16x16x32_bf16 v[16:19], a[204:207], a[36:39], v[16:19]\n\t"
    "s_waitcnt vmcnt(11)\n\t"
    "v_mfma_f32_16x16x32_bf16 v[20:23], a[168:171], a[40:43], v[20:23]\n\t"
    "buffer_load_dwordx4 v[84:87], v243, s[28:31], s43 offen offset:1024 sc1\n\t"
    "v_mfma_f32_16x16x32_bf16 v[20:23], a[188:191], a[44:47], v[20:23]\n\t"
    "v_mfma_f32_16x16x32_bf16 v[24:27], a[172:175], a[40:43], v[24:27]\n\t"
    "v_mfma_f32_16x16x32_bf16 v[24:27], a[192:195], a[44:47], v[24:27]\n\t"
    "v_mfma_f32_16x16x32_bf16 v[28:31], a[176:179], a[40:43], v[28:31]\n\t"
    "v_mfma_f32_16x16x32_bf16 v[28:31], a[196:199], a[44:47], v[28:31]\n\t"
    "s_add_u32 s43, s65, 24576\n\t"
    "buffer_load_dwordx4 v[88:91], v240, s[28:31], s43 offen offset:0 sc1\n\t"
    "v_mfma_f32_16x16x32_bf16 v[32:35], a[180:183], a[40:43], v[32:35]\n\t"
    "v_mfma_f32_16x16x32_bf16 v[32:35], a[200:203], a[44:47], v[32:35]\n\t"
    "v_mfma_f32_16x16x32_bf16 v[36:39], a[184:187], a[40:43], v[36:39]\n\t"
    "v_mfma_f32_16x16x32_bf16 v[36:39], a[204:207], a[44:47], v[36:39]\n\t"
    "s_waitcnt vmcnt(11)\n\t"
    "buffer_load_dwordx4 v[92:95], v240, s[28:31], s43 offen offset:1024 sc1\n\t"
    "v_mfma_f32_16x16x32_bf16 v[40:43], a[168:171], a[48:51], v[40:43]\n\t"
    "v_mfma_f32_16x16x32_bf16 v[40:43], a[188:191], a[52:55], v[40:43]\n\t"
    "v_mfma_f32_16x16x32_bf16 v[44:47], a[172:175], a[48:51], v[44:47]\n\t"
    "v_mfma_f32_16x16x32_bf16 v[44:47], a[192:195], a[52:55], v[44:47]\n\t"
    "v_mfma_f32_16x16x32_bf16 v[48:51], a[176:179], a[48:51], v[48:51]\n\t"
    "buffer_load_dwordx4 v[96:99], v241, s[28:31], s43 offen offset:0 sc1\n\t"
    "v_mfma_f32_16x16x32_bf16 v[48:51], a[196:199], a[52:55], v[48:51]\n\t"
    "v_mfma_f32_16x16x32_bf16 v[52:55], a[180:183], a[48:51], v[52:55]\n\t"
    "v_mfma_f32_16x16x32_bf16 v[52:55], a[200:203], a[52:55], v[52:55]\n\t"
    "v_mfma_f32_16x16x32_bf16 v[56:59], a[184:187], a[48:51], v[56:59]\n\t"
    "buffer_load_dwordx4 v[100:103], v241, s[28:31], s43 offen offset:1024 sc1\n\t"
    "v_mfma_f32_16x16x32_bf16 v[56:59], a[204:207], a[52:55], v[56:59]\n\t"
    "s_waitcnt vmcnt(12)\n\t"
    "v_mfma_f32_16x16x32_bf16 v[60:63], a[168:171], a[56:59], v[60:63]\n\t"
    "v_mfma_f32_16x16x32_bf16 v[60:63], a[188:191], a[60:63], v[60:63]\n\t"
    "v_mfma_f32_16x16x32_bf16 v[64:67], a[172:175], a[56:59], v[64:67]\n\t"
    "v_mfma_f32_16x16x32_bf16 v[64:67], a[192:195], a[60:63], v[64:67]\n\t"
    "buffer_load_dwordx4 v[104:107], v242, s[28:31], s43 offen offset:0 sc1\n\t"
    "v_mfma_f32_16x16x32_bf16 v[68:71], a[176:179], a[56:59], v[68:71]\n\t"
    "v_mfma_f32_16x16x32_bf16 v[68:71], a[196:199], a[60:63], v[68:71]\n\t"
    "v_mfma_f32_16x16x32_bf16 v[72:75], a[180:183], a[56:59], v[72:75]\n\t"
    "v_mfma_f32_16x16x32_bf16 v[72:75], a[200:203], a[60:63], v[72:75]\n\t"
    "buffer_load_dwordx4 v[108:111], v242, s[28:31], s43 offen offset:1024 sc1\n\t"
    "v_mfma_f32_16x16x32_bf16 v[76:79], a[184:187], a[56:59], v[76:79]\n\t"
    "v_mfma_f32_16x16x32_bf16 v[76:79], a[204:207], a[60:63], v[76:79]\n\t"
    "s_waitcnt vmcnt(12)\n\t"
    "v_mfma_f32_16x16x32_bf16 v[0:3], a[208:211], a[64:67], v[0:3]\n\t"
    "v_mfma_f32_16x16x32_bf16 v[0:3], a[228:231], a[68:71], v[0:3]\n\t"
    "v_mfma_f32_16x16x32_bf16 v[4:7], a[212:215], a[64:67], v[4:7]\n\t"
    "buffer_load_dwordx4 v[112:115], v243, s[28:31], s43 offen offset:0 sc1\n\t"
    "v_mfma_f32_16x16x32_bf16 v[4:7], a[232:235], a[68:71], v[4:7]\n\t"
    "v_mfma_f32_16x16x32_bf16 v[8:11], a[216:219], a[64:67], v[8:11]\n\t"
    "v_mfma_f32_16x16x32_bf16 v[8:11], a[236:239], a[68:71], v[8:11]\n\t"
    "v_mfma_f32_16x16x32_bf16 v[12:15], a[220:223], a[64:67], v[12:15]\n\t"
    "buffer_load_dwordx4 v[116:119], v243, s[28:31], s43 offen offset:1024 sc1\n\t"
    "v_mfma_f32_16x16x32_bf16 v[12:15], a[240:243], a[68:71], v[12:15]\n\t"
    "v_mfma_f32_16x16x32_bf16 v[16:19], a[224:227], a[64:67], v[16:19]\n\t"
    "v_mfma_f32_16x16x32_bf16 v[16:19], a[244:247], a[68:71], v[16:19]\n\t"
    "s_waitcnt vmcnt(12)\n\t"
    "v_mfma_f32_16x16x32_bf16 v[20:23], a[208:211], a[72:75], v[20:23]\n\t"
    "v_mfma_f32_16x16x32_bf16 v[20:23], a[228:231], a[76:79], v[20:23]\n\t"
    "s_add_u32 s43, s65, 32768\n\t"
    "buffer_load_dwordx4 a[0:3], v240, s[28:31], s43 offen offset:0 sc1\n\t"
    "v_mfma_f32_16x16x32_bf16 v[24:27], a[212:215], a[72:75], v[24:27]\n\t"
    "v_mfma_f32_16x16x32_bf16 v[24:27], a[232:235], a[76:79], v[24:27]\n\t"
    "v_mfma_f32_16x16x32_bf16 v[28:31], a[216:219], a[72:75], v[28:31]\n\t"
    "v_mfma_f32_16x16x32_bf16 v[28:31], a[236:239], a[76:79], v[28:31]\n\t"
    "buffer_load_dwordx4 a[4:7], v240, s[28:31], s43 offen offset:1024 sc1\n\t"
    "v_mfma_f32_16x16x32_bf16 v[32:35], a[220:223], a[72:75], v[32:35]\n\t"
    "v_mfma_f32_16x16x32_bf16 v[32:35], a[240:243], a[76:79], v[32:35]\n\t"
    "v_mfma_f32_16x16x32_bf16 v[36:39], a[224:227], a[72:75], v[36:39]\n\t"
    "v_mfma_f32_16x16x32_bf16 v[36:39], a[244:247], a[76:79], v[36:39]\n\t"
    "s_waitcnt vmcnt(12)\n\t"
    "v_mfma_f32_16x16x32_bf16 v[40:43], a[208:211], a[80:83], v[40:43]\n\t"
    "buffer_load_dwordx4 a[8:11], v241, s[28:31], s43 offen offset:0 sc1\n\t"
    "v_mfma_f32_16x16x32_bf16 v[40:43], a[228:231], a[84:87], v[40:43]\n\t"
    "v_mfma_f32_16x16x32_bf16 v[44:47], a[212:215], a[80:83], v[44:47]\n\t"
    "v_mfma_f32_16x16x32_bf16 v[44:47], a[232:235], a[84:87], v[44:47]\n\t"
    "v_mfma_f32_16x16x32_bf16 v[48:51], a[216:219], a[80:83], v[48:51]\n\t"
    "buffer_load_dwordx4 a[12:15], v241, s[28:31], s43 offen offset:1024 sc1\n\t"
    "v_mfma_f32_16x16x32_bf16 v[48:51], a[236:239], a[84:87], v[48:51]\n\t"
    "v_mfma_f32_16x16x32_bf16 v[52:55], a[220:223], a[80:83], v[52:55]\n\t"
    "v_mfma_f32_16x16x32_bf16 v[52:55], a[240:243], a[84:87], v[52:55]\n\t"
    "v_mfma_f32_16x16x32_bf16 v[56:59], a[224:227], a[80:83], v[56:59]\n\t"
    "v_mfma_f32_16x16x32_bf16 v[56:59], a[244:247], a[84:87], v[56:59]\n\t"
    "s_waitcnt vmcnt(12)\n\t"
    "buffer_load_dwordx4 a[16:19], v242, s[28:31], s43 offen offset:0 sc1\n\t"
    "v_mfma_f32_16x16x32_bf16 v[60:63], a[208:211], v[80:83], v[60:63]\n\t"
    "v_mfma_f32_16x16x32_bf16 v[60:63], a[228:231], v[84:87], v[60:63]\n\t"
    "v_mfma_f32_16x16x32_bf16 v[64:67], a[212:215], v[80:83], v[64:67]\n\t"
    "v_mfma_f32_16x16x32_bf16 v[64:67], a[232:235], v[84:87], v[64:67]\n\t"
    "buffer_load_dwordx4 a[20:23], v242, s[28:31], s43 offen offset:1024 sc1\n\t"
    "v_mfma_f32_16x16x32_bf16 v[68:71], a[216:219], v[80:83], v[68:71]\n\t"
    "v_mfma_f32_16x16x32_bf16 v[68:71], a[236:239], v[84:87], v[68:71]\n\t"
    "v_mfma_f32_16x16x32_bf16 v[72:75], a[220:223], v[80:83], v[72:75]\n\t"
    "v_mfma_f32_16x16x32_bf16 v[72:75], a[240:243], v[84:87], v[72:75]\n\t"
    "v_mfma_f32_16x16x32_bf16 v[76:79], a[224:227], v[80:83], v[76:79]\n\t"
    "buffer_load_dwordx4 a[24:27], v243, s[28:31], s43 offen offset:0 sc1\n\t"
    "v_mfma_f32_16x16x32_bf16 v[76:79], a[244:247], v[84:87], v[76:79]\n\t"
    "s_waitcnt vmcnt(13)\n\t"
    "v_mfma_f32_16x16x32_bf16 v[0:3], a[248:251], v[88:91], v[0:3]\n\t"
    "v_mfma_f32_16x16x32_bf16 v[0:3], v[140:143], v[92:95], v[0:3]\n\t"
    "v_mfma_f32_16x16x32_bf16 v[4:7], a[252:255], v[88:91], v[4:7]\n\t"
    "buffer_load_dwordx4 a[28:31], v243, s[28:31], s43 offen offset:1024 sc1\n\t"
    "v_mfma_f32_16x16x32_bf16 v[4:7], v[144:147], v[92:95], v[4:7]\n\t"
    "v_mfma_f32_16x16x32_bf16 v[8:11], v[128:131], v[88:91], v[8:11]\n\t"
    "v_mfma_f32_16x16x32_bf16 v[8:11], v[148:151], v[92:95], v[8:11]\n\t"
    "v_mfma_f32_16x16x32_bf16 v[12:15], v[132:135], v[88:91], v[12:15]\n\t"
    "v_mfma_f32_16x16x32_bf16 v[12:15], v[152:155], v[92:95], v[12:15]\n\t"
    "s_add_u32 s43, s65, 40960\n\t"
    "buffer_load_dwordx4 a[40:43], v241, s[28:31], s43 offen offset:0 sc1\n\t"
    "v_mfma_f32_16x16x32_bf16 v[16:19], v[136:139], v[88:91], v[16:19]\n\t"
    "v_mfma_f32_16x16x32_bf16 v[16:19], v[156:159], v[92:95], v[16:19]\n\t"
    "s_waitcnt vmcnt(13)\n\t"
    "v_mfma_f32_16x16x32_bf16 v[20:23], a[248:251], v[96:99], v[20:23]\n\t"
    "v_mfma_f32_16x16x32_bf16 v[20:23], v[140:143], v[100:103], v[20:23]\n\t"
    "buffer_load_dwordx4 a[44:47], v241, s[28:31], s43 offen offset:1024 sc1\n\t"
    "v_mfma_f32_16x16x32_bf16 v[24:27], a[252:255], v[96:99], v[24:27]\n\t"
    "v_mfma_f32_16x16x32_bf16 v[24:27], v[144:147], v[100:103], v[24:27]\n\t"
    "v_mfma_f32_16x16x32_bf16 v[28:31], v[128:131], v[96:99], v[28:31]\n\t"
    "v_mfma_f32_16x16x32_bf16 v[28:31], v[148:151], v[100:103], v[28:31]\n\t"
    "v_mfma_f32_16x16x32_bf16 v[32:35], v[132:135], v[96:99], v[32:35]\n\t"
    "s_add_u32 s43, s12, 49152\n\t"
    "buffer_load_dwordx4 a[72:75], v241, s[28:31], s43 offen offset:0 sc1\n\t"
    "v_mfma_f32_16x16x32_bf16 v[32:35], v[152:155], v[100:103], v[32:35]\n\t"
    "v_mfma_f32_16x16x32_bf16 v[36:39], v[136:139], v[96:99], v[36:39]\n\t"
    "v_mfma_f32_16x16x32_bf16 v[36:39], v[156:159], v[100:103], v[36:39]\n\t"
    "s_waitcnt vmcnt(13)\n\t"
    "v_mfma_f32_16x16x32_bf16 v[40:43], a[248:251], v[104:107], v[40:43]\n\t"
    "buffer_load_dwordx4 a[76:79], v241, s[28:31], s43 offen offset:1024 sc1\n\t"
    "v_mfma_f32_16x16x32_bf16 v[40:43], v[140:143], v[108:111], v[40:43]\n\t"
    "v_mfma_f32_16x16x32_bf16 v[44:47], a[252:255], v[104:107], v[44:47]\n\t"
    "v_mfma_f32_16x16x32_bf16 v[44:47], v[144:147], v[108:111], v[44:47]\n\t"
    "v_mfma_f32_16x16x32_bf16 v[48:51], v[128:131], v[104:107], v[48:51]\n\t"
    "v_mfma_f32_16x16x32_bf16 v[48:51], v[148:151], v[108:111], v[48:51]\n\t"
    "s_add_u32 s43, s65, 40960\n\t"
    "buffer_load_dwordx4 a[48:51], v242, s[28:31], s43 offen offset:0 sc1\n\t"
    "v_mfma_f32_16x16x32_bf16 v[52:55], v[132:135], v[104:107], v[52:55]\n\t"
    "v_mfma_f32_16x16x32_bf16 v[52:55], v[152:155], v[108:111], v[52:55]\n\t"
    "v_mfma_f32_16x16x32_bf16 v[56:59], v[136:139], v[104:107], v[56:59]\n\t"
    "v_mfma_f32_16x16x32_bf16 v[56:59], v[156:159], v[108:111], v[56:59]\n\t"
    "s_waitcnt vmcnt(13)\n\t"
    "buffer_load_dwordx4 a[52:55], v242, s[28:31], s43 offen offset:1024 sc1\n\t"
    "v_mfma_f32_16x16x32_bf16 v[60:63], a[248:251], v[112:115], v[60:63]\n\t"
    "v_mfma_f32_16x16x32_bf16 v[60:63], v[140:143], v[116:119], v[60:63]\n\t"
    "v_mfma_f32_16x16x32_bf16 v[64:67], a[252:255], v[112:115], v[64:67]\n\t"
    "v_mfma_f32_16x16x32_bf16 v[64:67], v[144:147], v[116:119], v[64:67]\n\t"
    "v_mfma_f32_16x16x32_bf16 v[68:71], v[128:131], v[112:115], v[68:71]\n\t"
    "s_add_u32 s43, s12, 49152\n\t"
    "buffer_load_dwordx4 a[80:83], v242, s[28:31], s43 offen offset:0 sc1\n\t"
    "v_mfma_f32_16x16x32_bf16 v[68:71], v[148:151], v[116:119], v[68:71]\n\t"
    "v_mfma_f32_16x16x32_bf16 v[72:75], v[132:135], v[112:115], v[72:75]\n\t"
    "v_mfma_f32_16x16x32_bf16 v[72:75], v[152:155], v[116:119], v[72:75]\n\t"
    "v_mfma_f32_16x16x32_bf16 v[76:79], v[136:139], v[112:115], v[76:79]\n\t"
    "buffer_load_dwordx4 a[84:87], v242, s[28:31], s43 offen offset:1024 sc1\n\t"
    "v_mfma_f32_16x16x32_bf16 v[76:79], v[156:159], v[116:119], v[76:79]\n\t"
    "s_waitcnt vmcnt(14)\n\t"
    "v_mfma_f32_16x16x32_bf16 v[0:3], v[160:163], a[0:3], v[0:3]\n\t"
    "v_mfma_f32_16x16x32_bf16 v[0:3], v[180:183], a[4:7], v[0:3]\n\t"
    "v_mfma_f32_16x16x32_bf16 v[4:7], v[164:167], a[0:3], v[4:7]\n\t"
    "v_mfma_f32_16x16x32_bf16 v[4:7], v[184:187], a[4:7], v[4:7]\n\t"
    "s_add_u32 s43, s15, 0\n\t"
    "buffer_load_dwordx4 v[108:111], v122, s[28:31], s43 offen sc1\n\t"
    "v_mfma_f32_16x16x32_bf16 v[8:11], v[168:171], a[0:3], v[8:11]\n\t"
    "v_mfma_f32_16x16x32_bf16 v[8:11], v[188:191], a[4:7], v[8:11]\n\t"
    "v_mfma_f32_16x16x32_bf16 v[12:15], v[172:175], a[0:3], v[12:15]\n\t"
    "v_mfma_f32_16x16x32_bf16 v[12:15], v[192:195], a[4:7], v[12:15]\n\t"
    "s_add_u32 s43, s65, 40960\n\t"
    "buffer_load_dwordx4 a[56:59], v243, s[28:31], s43 offen offset:0 sc1\n\t"
    "v_mfma_f32_16x16x32_bf16 v[16:19], v[176:179], a[0:3], v[16:19]\n\t"
    "v_mfma_f32_16x16x32_bf16 v[16:19], v[196:199], a[4:7], v[16:19]\n\t"
    "s_waitcnt vmcnt(14)\n\t"
    "v_mfma_f32_16x16x32_bf16 v[20:23], v[160:163], a[8:11], v[20:23]\n\t"
    "ds_read_b128 v[88:91], v121 offset:0\n\t"
    "v_mfma_f32_16x16x32_bf16 v[20:23], v[180:183], a[12:15], v[20:23]\n\t"
    "v_mfma_f32_16x16x32_bf16 v[24:27], v[164:167], a[8:11], v[24:27]\n\t"
    "buffer_load_dwordx4 a[60:63], v243, s[28:31], s43 offen offset:1024 sc1\n\t"
    "v_mfma_f32_16x16x32_bf16 v[24:27], v[184:187], a[12:15], v[24:27]\n\t"
    "v_mfma_f32_16x16x32_bf16 v[28:31], v[168:171], a[8:11], v[28:31]\n\t"
    "v_mfma_f32_16x16x32_bf16 v[28:31], v[188:191], a[12:15], v[28:31]\n\t"
    "ds_read_b128 v[92:95], v121 offset:64\n\t"
    "v_mfma_f32_16x16x32_bf16 v[32:35], v[172:175], a[8:11], v[32:35]\n\t"
    "s_add_u32 s43, s12, 49152\n\t"
    "buffer_load_dwordx4 v[80:83], v243, s[28:31], s43 offen offset:0 sc1\n\t"
    "v_mfma_f32_16x16x32_bf16 v[32:35], v[192:195], a[12:15], v[32:35]\n\t"
    "v_mfma_f32_16x16x32_bf16 v[36:39], v[176:179], a[8:11], v[36:39]\n\t"
    "v_mfma_f32_16x16x32_bf16 v[36:39], v[196:199], a[12:15], v[36:39]\n\t"
    "s_waitcnt vmcnt(14)\n\t"
    "v_mfma_f32_16x16x32_bf16 v[40:43], v[160:163], a[16:19], v[40:43]\n\t"
    "v_mfma_f32_16x16x32_bf16 v[40:43], v[180:183], a[20:23], v[40:43]\n\t"
    "buffer_load_dwordx4 v[84:87], v243, s[28:31], s43 offen offset:1024 sc1\n\t"
    "v_mfma_f32_16x16x32_bf16 v[44:47], v[164:167], a[16:19], v[44:47]\n\t"
    "ds_read_b128 v[96:99], v121 offset:128\n\t"
    "v_mfma_f32_16x16x32_bf16 v[44:47], v[184:187], a[20:23], v[44:47]\n\t"
    "v_mfma_f32_16x16x32_bf16 v[48:51], v[168:171], a[16:19], v[48:51]\n\t"
    "v_mfma_f32_16x16x32_bf16 v[48:51], v[188:191], a[20:23], v[48:51]\n\t"
    "s_add_u32 s43, s65, 40960\n\t"
    "buffer_load_dwordx4 a[32:35], v240, s[28:31], s43 offen offset:0 sc1\n\t"
    "v_mfma_f32_16x16x32_bf16 v[52:55], v[172:175], a[16:19], v[52:55]\n\t"
    "v_mfma_f32_16x16x32_bf16 v[52:55], v[192:195], a[20:23], v[52:55]\n\t"
    "ds_read_b128 v[100:103], v121 offset:192\n\t"
    "v_mfma_f32_16x16x32_bf16 v[56:59], v[176:179], a[16:19], v[56:59]\n\t"
    "v_mfma_f32_16x16x32_bf16 v[56:59], v[196:199], a[20:23], v[56:59]\n\t"
    "s_waitcnt vmcnt(14)\n\t"
    "v_mfma_f32_16x16x32_bf16 v[60:63], v[160:163], a[24:27], v[60:63]\n\t"
    "buffer_load_dwordx4 a[36:39], v240, s[28:31], s43 offen offset:1024 sc1\n\t"
    "v_mfma_f32_16x16x32_bf16 v[60:63], v[180:183], a[28:31], v[60:63]\n\t"
    "v_mfma_f32_16x16x32_bf16 v[64:67], v[164:167], a[24:27], v[64:67]\n\t"
    "v_mfma_f32_16x16x32_bf16 v[64:67], v[184:187], a[28:31], v[64:67]\n\t"
    "ds_read_b128 v[104:107], v121 offset:256\n\t"
    "v_mfma_f32_16x16x32_bf16 v[68:71], v[168:171], a[24:27], v[68:71]\n\t"
    "s_add_u32 s43, s12, 49152\n\t"
    "buffer_load_dwordx4 a[64:67], v240, s[28:31], s43 offen offset:0 sc1\n\t"
    "v_mfma_f32_16x16x32_bf16 v[68:71], v[188:191], a[28:31], v[68:71]\n\t"
    "v_mfma_f32_16x16x32_bf16 v[72:75], v[172:175], a[24:27], v[72:75]\n\t"
    "v_mfma_f32_16x16x32_bf16 v[72:75], v[192:195], a[28:31], v[72:75]\n\t"
    "v_mfma_f32_16x16x32_bf16 v[76:79], v[176:179], a[24:27], v[76:79]\n\t"
    "v_mfma_f32_16x16x32_bf16 v[76:79], v[196:199], a[28:31], v[76:79]\n\t"
    "s_waitcnt vmcnt(12)\n\t"
    "buffer_load_dwordx4 a[68:71], v240, s[28:31], s43 offen offset:1024 sc1\n\t"
    "v_mfma_f32_16x16x32_bf16 v[20:23], v[200:203], a[40:43], v[20:23]\n\t"
    "v_mfma_f32_16x16x32_bf16 v[20:23], v[220:223], a[44:47], v[20:23]\n\t"
    "v_mfma_f32_16x16x32_bf16 v[20:23], a[88:91], a[72:75], v[20:23]\n\t"
    "v_lshlrev_b32 v128, 16, v244\n\t"
    "v_and_b32 v129, 0xffff0000, v244\n\t"
    "v_mfma_f32_16x16x32_bf16 v[20:23], a[108:111], a[76:79], v[20:23]\n\t"
    "v_lshlrev_b32 v130, 16, v245\n\t"
    "v_and_b32 v131, 0xffff0000, v245\n\t"
    "v_mfma_f32_16x16x32_bf16 v[24:27], v[204:207], a[40:43], v[24:27]\n\t"
    "v_lshlrev_b32 v132, 16, v246\n\t"
    "v_and_b32 v133, 0xffff0000, v246\n\t"
    "v_mfma_f32_16x16x32_bf16 v[24:27], v[224:227], a[44:47], v[24:27]\n\t"
    "v_lshlrev_b32 v134, 16, v247\n\t"
    "v_and_b32 v135, 0xffff0000, v247\n\t"
    "v_mfma_f32_16x16x32_bf16 v[24:27], a[92:95], a[72:75], v[24:27]\n\t"
    "v_lshlrev_b32 v136, 16, v248\n\t"
    "v_and_b32 v137, 0xffff0000, v248\n\t"
    "v_mfma_f32_16x16x32_bf16 v[24:27], a[112:115], a[76:79], v[24:27]\n\t"
    "v_lshlrev_b32 v138, 16, v249\n\t"
    "v_and_b32 v139, 0xffff0000, v249\n\t"
    "v_mfma_f32_16x16x32_bf16 v[28:31], v[208:211], a[40:43], v[28:31]\n\t"
    "v_lshlrev_b32 v140, 16, v250\n\t"
    "v_and_b32 v141, 0xffff0000, v250\n\t"
    "v_mfma_f32_16x16x32_bf16 v[28:31], v[228:231], a[44:47], v[28:31]\n\t"
    "v_lshlrev_b32 v142, 16, v251\n\t"
    "v_and_b32 v143, 0xffff0000, v251\n\t"
    "v_mfma_f32_16x16x32_bf16 v[28:31], a[96:99], a[72:75], v[28:31]\n\t"
    "v_lshlrev_b32 v144, 16, v252\n\t"
    "v_and_b32 v145, 0xffff0000, v252\n\t"
    "v_mfma_f32_16x16x32_bf16 v[28:31], a[116:119], a[76:79], v[28:31]\n\t"
    "v_lshlrev_b32 v146, 16, v253\n\t"
    "v_and_b32 v147, 0xffff0000, v253\n\t"
    "v_mfma_f32_16x16x32_bf16 v[32:35], v[212:215], a[40:43], v[32:35]\n\t"
    "ds_write_b128 v124, v[20:23] offset:0\n\t"
    "v_mfma_f32_16x16x32_bf16 v[32:35], v[232:235], a[44:47], v[32:35]\n\t"
    "v_mfma_f32_16x16x32_bf16 v[32:35], a[100:103], a[72:75], v[32:35]\n\t"
    "v_mfma_f32_16x16x32_bf16 v[32:35], a[120:123], a[76:79], v[32:35]\n\t"
    "v_mfma_f32_16x16x32_bf16 v[36:39], v[216:219], a[40:43], v[36:39]\n\t"
    "ds_write_b128 v124, v[24:27] offset:1024\n\t"
    "v_mfma_f32_16x16x32_bf16 v[36:39], v[236:239], a[44:47], v[36:39]\n\t"
    "v_mfma_f32_16x16x32_bf16 v[36:39], a[104:107], a[72:75], v[36:39]\n\t"
    "v_mfma_f32_16x16x32_bf16 v[36:39], a[124:127], a[76:79], v[36:39]\n\t"
    "s_waitcnt vmcnt(9)\n\t"
    "v_mfma_f32_16x16x32_bf16 v[40:43], v[200:203], a[48:51], v[40:43]\n\t"
    "ds_write_b128 v124, v[28:31] offset:2048\n\t"
    "v_mfma_f32_16x16x32_bf16 v[40:43], v[220:223], a[52:55], v[40:43]\n\t"
    "v_mfma_f32_16x16x32_bf16 v[40:43], a[88:91], a[80:83], v[40:43]\n\t"
    "v_mfma_f32_16x16x32_bf16 v[40:43], a[108:111], a[84:87], v[40:43]\n\t"
    "v_mfma_f32_16x16x32_bf16 v[44:47], v[204:207], a[48:51], v[44:47]\n\t"
    "ds_write_b128 v124, v[32:35] offset:3072\n\t"
    "v_mfma_f32_16x16x32_bf16 v[44:47], v[224:227], a[52:55], v[44:47]\n\t"
    "v_mfma_f32_16x16x32_bf16 v[44:47], a[92:95], a[80:83], v[44:47]\n\t"
    "v_mfma_f32_16x16x32_bf16 v[44:47], a[112:115], a[84:87], v[44:47]\n\t"
    "v_mfma_f32_16x16x32_bf16 v[48:51], v[208:211], a[48:51], v[48:51]\n\t"
    "ds_write_b128 v124, v[36:39] offset:16384\n\t"
    "v_mfma_f32_16x16x32_bf16 v[48:51], v[228:231], a[52:55], v[48:51]\n\t"
    "v_mfma_f32_16x16x32_bf16 v[48:51], a[96:99], a[80:83], v[48:51]\n\t"
    "v_mfma_f32_16x16x32_bf16 v[48:51], a[116:119], a[84:87], v[48:51]\n\t"
    "v_mfma_f32_16x16x32_bf16 v[52:55], v[212:215], a[48:51], v[52:55]\n\t"
    "ds_write_b128 v124, v[40:43] offset:17408\n\t"
    "v_mfma_f32_16x16x32_bf16 v[52:55], v[232:235], a[52:55], v[52:55]\n\t"
    "v_mfma_f32_16x16x32_bf16 v[52:55], a[100:103], a[80:83], v[52:55]\n\t"
    "v_mfma_f32_16x16x32_bf16 v[52:55], a[120:123], a[84:87], v[52:55]\n\t"
    "v_mfma_f32_16x16x32_bf16 v[56:59], v[216:219], a[48:51], v[56:59]\n\t"
    "ds_write_b128 v124, v[44:47] offset:18432\n\t"
    "v_mfma_f32_16x16x32_bf16 v[56:59], v[236:239], a[52:55], v[56:59]\n\t"
    "v_mfma_f32_16x16x32_bf16 v[56:59], a[104:107], a[80:83], v[56:59]\n\t"
    "v_mfma_f32_16x16x32_bf16 v[56:59], a[124:127], a[84:87], v[56:59]\n\t"
    "s_waitcnt vmcnt(4)\n\t"
    "v_mfma_f32_16x16x32_bf16 v[60:63], v[200:203], a[56:59], v[60:63]\n\t"
    "ds_write_b128 v124, v[48:51] offset:19456\n\t"
    "v_mfma_f32_16x16x32_bf16 v[60:63], v[220:223], a[60:63], v[60:63]\n\t"
    "s_mov_b32 vcc_lo, 0xaaaaaaaa\n\t"
    "s_mov_b32 vcc_hi, 0xaaaaaaaa\n\t"
    "v_mfma_f32_16x16x32_bf16 v[60:63], a[88:91], v[80:83], v[60:63]\n\t"
    "v_add_f32_dpp v108, v108, v108 quad_perm:[1,0,3,2] row_mask:0xf bank_mask:0xf\n\t"
    "v_add_f32_dpp v109, v109, v109 quad_perm:[1,0,3,2] row_mask:0xf bank_mask:0xf\n\t"
    "v_mfma_f32_16x16x32_bf16 v[60:63], a[108:111], v[84:87], v[60:63]\n\t"
    "v_add_f32_dpp v110, v110, v110 quad_perm:[1,0,3,2] row_mask:0xf bank_mask:0xf\n\t"
    "v_add_f32_dpp v111, v111, v111 quad_perm:[1,0,3,2] row_mask:0xf bank_mask:0xf\n\t"
    "v_mfma_f32_16x16x32_bf16 v[64:67], v[204:207], a[56:59], v[64:67]\n\t"
    "ds_write_b128 v124, v[52:55] offset:32768\n\t"
    "v_cndmask_b32 v108, v108, v110, vcc\n\t"
    "v_mfma_f32_16x16x32_bf16 v[64:67], v[224:227], a[60:63], v[64:67]\n\t"
    "v_cndmask_b32 v109, v109, v111, vcc\n\t"
    "v_mov_b32 v110, v108\n\t"
    "v_mfma_f32_16x16x32_bf16 v[64:67], a[92:95], v[80:83], v[64:67]\n\t"
    "v_mov_b32 v111, v109\n\t"
    "s_nop 0\n\t"
    "v_mfma_f32_16x16x32_bf16 v[64:67], a[112:115], v[84:87], v[64:67]\n\t"
    "v_permlane16_swap_b32 v108, v110\n\t"
    "v_permlane16_swap_b32 v109, v111\n\t"
    "v_mfma_f32_16x16x32_bf16 v[68:71], v[208:211], a[56:59], v[68:71]\n\t"
    "ds_write_b128 v124, v[56:59] offset:33792\n\t"
    "v_add_f32 v108, v108, v110\n\t"
    "v_mfma_f32_16x16x32_bf16 v[68:71], v[228:231], a[60:63], v[68:71]\n\t"
    "v_add_f32 v109, v109, v111\n\t"
    "v_mov_b32 v110, v108\n\t"
    "v_mfma_f32_16x16x32_bf16 v[68:71], a[96:99], v[80:83], v[68:71]\n\t"
    "v_mov_b32 v111, v109\n\t"
    "s_nop 0\n\t"
    "v_mfma_f32_16x16x32_bf16 v[68:71], a[116:119], v[84:87], v[68:71]\n\t"
    "v_permlane32_swap_b32 v108, v110\n\t"
    "v_permlane32_swap_b32 v109, v111\n\t"
    "v_mfma_f32_16x16x32_bf16 v[72:75], v[212:215], a[56:59], v[72:75]\n\t"
    "ds_write_b128 v124, v[60:63] offset:34816\n\t"
    "v_add_f32 v108, v108, v110\n\t"
    "v_mfma_f32_16x16x32_bf16 v[72:75], v[232:235], a[60:63], v[72:75]\n\t"
    "v_add_f32 v109, v109, v111\n\t"
    "v_mul_f32 v108, 0xbacccccd, v108\n\t"
    "v_mfma_f32_16x16x32_bf16 v[72:75], a[100:103], v[80:83], v[72:75]\n\t"
    "v_mul_f32 v110, v108, v108\n\t"
    "v_mul_f32 v109, 0x3acccccd, v109\n\t"
    "v_mfma_f32_16x16x32_bf16 v[72:75], a[120:123], v[84:87], v[72:75]\n\t"
    "v_sub_f32 v109, v109, v110\n\t"
    "v_max_f32 v109, 0, v109\n\t"
    "v_mfma_f32_16x16x32_bf16 v[76:79], v[216:219], a[56:59], v[76:79]\n\t"
    "ds_write_b128 v124, v[64:67] offset:35840\n\t"
    "v_add_f32 v109, 0x3727c5ac, v109\n\t"
    "v_mfma_f32_16x16x32_bf16 v[76:79], v[236:239], a[60:63], v[76:79]\n\t"
    "v_rcp_f32 v109, v109\n\t"
    "v_mfma_f32_16x16x32_bf16 v[76:79], a[104:107], v[80:83], v[76:79]\n\t"
    "s_nop 0\n\t"
    "v_mfma_f32_16x16x32_bf16 v[76:79], a[124:127], v[84:87], v[76:79]\n\t"
    "s_waitcnt vmcnt(0)\n\t"
    "v_mfma_f32_16x16x32_bf16 v[0:3], v[200:203], a[32:35], v[0:3]\n\t"
    "ds_write_b128 v124, v[68:71] offset:49152\n\t"
    "v_mfma_f32_16x16x32_bf16 v[0:3], v[220:223], a[36:39], v[0:3]\n\t"
    "v_mfma_f32_16x16x32_bf16 v[0:3], a[88:91], a[64:67], v[0:3]\n\t"
    "v_mfma_f32_16x16x32_bf16 v[0:3], a[108:111], a[68:71], v[0:3]\n\t"
    "v_mfma_f32_16x16x32_bf16 v[4:7], v[204:207], a[32:35], v[4:7]\n\t"
    "ds_write_b128 v124, v[72:75] offset:50176\n\t"
    "v_mfma_f32_16x16x32_bf16 v[4:7], v[224:227], a[36:39], v[4:7]\n\t"
    "v_mfma_f32_16x16x32_bf16 v[4:7], a[92:95], a[64:67], v[4:7]\n\t"
    "s_waitcnt lgkmcnt(15)\n\t"
    "v_mul_f32 v88, v88, v109\n\t"
    "v_mul_f32 v89, v89, v109\n\t"
    "v_mfma_f32_16x16x32_bf16 v[4:7], a[112:115], a[68:71], v[4:7]\n\t"
    "v_mul_f32 v90, v90, v109\n\t"
    "v_mul_f32 v91, v91, v109\n\t"
    "v_mfma_f32_16x16x32_bf16 v[8:11], v[208:211], a[32:35], v[8:11]\n\t"
    "ds_write_b128 v124, v[76:79] offset:51200\n\t"
    "v_mul_f32 v92, v92, v109\n\t"
    "v_mfma_f32_16x16x32_bf16 v[8:11], v[228:231], a[36:39], v[8:11]\n\t"
    "v_mul_f32 v93, v93, v109\n\t"
    "v_mul_f32 v94, v94, v109\n\t"
    "v_mfma_f32_16x16x32_bf16 v[8:11], a[96:99], a[64:67], v[8:11]\n\t"
    "v_mul_f32 v95, v95, v109\n\t"
    "v_mul_f32 v96, v96, v109\n\t"
    "v_mfma_f32_16x16x32_bf16 v[8:11], a[116:119], a[68:71], v[8:11]\n\t"
    "v_mul_f32 v97, v97, v109\n\t"
    "v_mul_f32 v98, v98, v109\n\t"
    "v_mfma_f32_16x16x32_bf16 v[12:15], v[212:215], a[32:35], v[12:15]\n\t"
    "v_mul_f32 v99, v99, v109\n\t"
    "v_mul_f32 v100, v100, v109\n\t"
    "v_mfma_f32_16x16x32_bf16 v[12:15], v[232:235], a[36:39], v[12:15]\n\t"
    "v_mul_f32 v101, v101, v109\n\t"
    "v_mul_f32 v102, v102, v109\n\t"
    "v_mfma_f32_16x16x32_bf16 v[12:15], a[100:103], a[64:67], v[12:15]\n\t"
    "s_waitcnt lgkmcnt(15)\n\t"
    "v_mul_f32 v103, v103, v109\n\t"
    "v_mul_f32 v104, v104, v109\n\t"
    "v_mfma_f32_16x16x32_bf16 v[12:15], a[120:123], a[68:71], v[12:15]\n\t"
    "v_mul_f32 v105, v105, v109\n\t"
    "v_mul_f32 v106, v106, v109\n\t"
    "v_mfma_f32_16x16x32_bf16 v[16:19], v[216:219], a[32:35], v[16:19]\n\t"
    "v_mul_f32 v107, v107, v109\n\t"
    "v_mfma_f32_16x16x32_bf16 v[16:19], v[236:239], a[36:39], v[16:19]\n\t"
    "v_mfma_f32_16x16x32_bf16 v[16:19], a[104:107], a[64:67], v[16:19]\n\t"
    "v_mfma_f32_16x16x32_bf16 v[16:19], a[124:127], a[68:71], v[16:19]\n\t"
    "L_k4e%=:\n\t"
    "s_waitcnt lgkmcnt(0)\n\t"
    "s_barrier\n\t"
    "ds_read_b128 v[20:23], v125 offset:0\n\t"
    "ds_read_b128 v[40:43], v126 offset:17408\n\t"
    "ds_read_b128 v[60:63], v127 offset:34816\n\t"
    "ds_read_b128 v[24:27], v125 offset:1024\n\t"
    "ds_read_b128 v[44:47], v126 offset:18432\n\t"
    "ds_read_b128 v[64:67], v127 offset:35840\n\t"
    "ds_read_b128 v[28:31], v125 offset:2048\n\t"
    "ds_read_b128 v[48:51], v126 offset:19456\n\t"
    "ds_read_b128 v[68:71], v127 offset:49152\n\t"
    "ds_read_b128 v[32:35], v125 offset:3072\n\t"
    "ds_read_b128 v[52:55], v126 offset:32768\n\t"
    "ds_read_b128 v[72:75], v127 offset:50176\n\t"
    "ds_read_b128 v[36:39], v125 offset:16384\n\t"
    "ds_read_b128 v[56:59], v126 offset:33792\n\t"
    "ds_read_b128 v[76:79], v127 offset:51200\n\t"
    "s_waitcnt lgkmcnt(14)\n\t"
    "v_pk_add_f32 v[0:1], v[0:1], v[20:21]\n\t"
    "s_waitcnt lgkmcnt(12)\n\t"
    "v_pk_add_f32 v[40:41], v[40:41], v[60:61]\n\t"
    "v_pk_add_f32 v[2:3], v[2:3], v[22:23]\n\t"
    "v_pk_add_f32 v[42:43], v[42:43], v[62:63]\n\t"
    "v_pk_add_f32 v[0:1], v[0:1], v[40:41]\n\t"
    "v_pk_add_f32 v[2:3], v[2:3], v[42:43]\n\t"
    "s_waitcnt lgkmcnt(11)\n\t"
    "v_pk_add_f32 v[4:5], v[4:5], v[24:25]\n\t"
    "s_waitcnt lgkmcnt(9)\n\t"
    "v_pk_add_f32 v[44:45], v[44:45], v[64:65]\n\t"
    "v_pk_add_f32 v[6:7], v[6:7], v[26:27]\n\t"
    "v_pk_add_f32 v[46:47], v[46:47], v[66:67]\n\t"
    "v_pk_add_f32 v[4:5], v[4:5], v[44:45]\n\t"
    "v_pk_add_f32 v[6:7], v[6:7], v[46:47]\n\t"
    "s_waitcnt lgkmcnt(8)\n\t"
    "v_pk_add_f32 v[8:9], v[8:9], v[28:29]\n\t"
    "s_waitcnt lgkmcnt(6)\n\t"
    "v_pk_add_f32 v[48:49], v[48:49], v[68:69]\n\t"
    "v_pk_add_f32 v[10:11], v[10:11], v[30:31]\n\t"
    "v_pk_add_f32 v[50:51], v[50:51], v[70:71]\n\t"
    "v_pk_add_f32 v[8:9], v[8:9], v[48:49]\n\t"
    "v_pk_add_f32 v[10:11], v[10:11], v[50:51]\n\t"
    "s_waitcnt lgkmcnt(5)\n\t"
    "v_pk_add_f32 v[12:13], v[12:13], v[32:33]\n\t"
    "s_waitcnt lgkmcnt(3)\n\t"
    "v_pk_add_f32 v[52:53], v[52:53], v[72:73]\n\t"
    "v_pk_add_f32 v[14:15], v[14:15], v[34:35]\n\t"
    "v_pk_add_f32 v[54:55], v[54:55], v[74:75]\n\t"
    "v_pk_add_f32 v[12:13], v[12:13], v[52:53]\n\t"
    "v_pk_add_f32 v[14:15], v[14:15], v[54:55]\n\t"
    "s_waitcnt lgkmcnt(2)\n\t"
    "v_pk_add_f32 v[16:17], v[16:17], v[36:37]\n\t"
    "s_waitcnt lgkmcnt(0)\n\t"
    "v_pk_add_f32 v[56:57], v[56:57], v[76:77]\n\t"
    "v_pk_add_f32 v[18:19], v[18:19], v[38:39]\n\t"
    "v_pk_add_f32 v[58:59], v[58:59], v[78:79]\n\t"
    "v_pk_add_f32 v[16:17], v[16:17], v[56:57]\n\t"
    "v_pk_add_f32 v[18:19], v[18:19], v[58:59]\n\t"
    "v_pk_fma_f32 v[0:1], v[0:1], v[88:89], v[128:129]\n\t"
    "v_pk_fma_f32 v[2:3], v[2:3], v[90:91], v[130:131]\n\t"
    "v_pk_fma_f32 v[4:5], v[4:5], v[92:93], v[132:133]\n\t"
    "v_pk_fma_f32 v[6:7], v[6:7], v[94:95], v[134:135]\n\t"
    "v_pk_fma_f32 v[8:9], v[8:9], v[96:97], v[136:137]\n\t"
    "v_pk_fma_f32 v[10:11], v[10:11], v[98:99], v[138:139]\n\t"
    "v_pk_fma_f32 v[12:13], v[12:13], v[100:101], v[140:141]\n\t"
    "v_pk_fma_f32 v[14:15], v[14:15], v[102:103], v[142:143]\n\t"
    "v_pk_fma_f32 v[16:17], v[16:17], v[104:105], v[144:145]\n\t"
    "v_pk_fma_f32 v[18:19], v[18:19], v[106:107], v[146:147]\n\t"
    "v_cvt_pk_bf16_f32 v20, v0, v1\n\t"
    "v_cvt_pk_bf16_f32 v21, v2, v3\n\t"
    "v_cvt_pk_bf16_f32 v22, v4, v5\n\t"
    "v_cvt_pk_bf16_f32 v23, v6, v7\n\t"
    "v_cvt_pk_bf16_f32 v24, v8, v9\n\t"
    "v_cvt_pk_bf16_f32 v25, v10, v11\n\t"
    "v_cvt_pk_bf16_f32 v26, v12, v13\n\t"
    "v_cvt_pk_bf16_f32 v27, v14, v15\n\t"
    "v_cvt_pk_bf16_f32 v28, v16, v17\n\t"
    "v_cvt_pk_bf16_f32 v29, v18, v19\n\t"
    "v_mov_b32 v30, v28\n\t"
    "v_mov_b32 v31, v29\n\t"
    "s_nop 1\n\t"
    "v_permlane16_swap_b32 v20, v22\n\t"
    "v_permlane16_swap_b32 v21, v23\n\t"
    "v_permlane16_swap_b32 v24, v26\n\t"
    "v_permlane16_swap_b32 v25, v27\n\t"
    "v_permlane16_swap_b32 v28, v30\n\t"
    "v_permlane16_swap_b32 v29, v31\n\t"
    "buffer_store_dwordx4 v[20:23], v120, s[56:59], s71 offen offset:0\n\t"
    "buffer_store_dwordx4 v[24:27], v120, s[56:59], s71 offen offset:64\n\t"
    "s_mov_b32 exec_lo, 0xffff\n\t"
    "s_mov_b32 exec_hi, 0xffff\n\t"
    "buffer_store_dwordx4 v[28:31], v120, s[56:59], s71 offen offset:128\n\t"
    "s_mov_b64 exec, -1\n\t"
    "s_endpgm\n\t"
    "L_bottom%=:\n\t"
    "L_pxw%=:\n\t"
    "s_mov_b32 s38, 0x20000\n\t"
    "L_pxl%=:\n\t"
    "buffer_load_dword v224, off, s[28:31], s1 sc1\n\t"
    "s_sub_u32 s38, s38, 1\n\t"
    "s_waitcnt vmcnt(0)\n\t"
    "s_cbranch_scc1 L_pxok%=\n\t"
    "v_readfirstlane_b32 s99, v224\n\t"
    "s_nop 0\n\t"
    "s_cmp_lg_u32 s99, s96\n\t"
    "s_cbranch_scc1 L_pxl%=\n\t"
    "s_branch L_pxok%=\n\t"
    "L_fb%=:\n\t"
    "s_mov_b32 s41, 1\n\t"
    "s_mov_b32 s43, 0\n\t"
    "v_mov_b32 v248, 0xff800000\n\t"
    "v_mov_b32 v249, 0xff800000\n\t"
    "v_mov_b32 v250, 0xff800000\n\t"
    "v_mov_b32 v251, 0xff800000\n\t"
    "v_and_b32 v242, 15, v255\n\t"
    "v_lshlrev_b32 v242, 8, v242\n\t"
    "v_and_b32 v243, 48, v255\n\t"
    "v_add_u32 v242, v242, v243\n\t"
    "s_waitcnt vmcnt(0)\n\t"
    "s_nop 7\n\t"
    "L_fbloop%=:\n\t"
    "buffer_load_dwordx4 a[64:67], v242, s[32:35], s43 offen offset:0\n\t"
    "buffer_load_dwordx4 a[68:71], v242, s[32:35], s43 offen offset:64\n\t"
    "buffer_load_dwordx4 a[72:75], v242, s[32:35], s43 offen offset:128\n\t"
    "buffer_load_dwordx4 a[76:79], v242, s[32:35], s43 offen offset:192\n\t"
    "s_waitcnt vmcnt(0)\n\t"
    "v_mfma_f32_16x16x32_bf16 v[0:3], a[64:67], a[0:3], 0\n\t"
    "v_mfma_f32_16x16x32_bf16 v[0:3], a[68:71], a[4:7], v[0:3]\n\t"
    "v_mfma_f32_16x16x32_bf16 v[0:3], a[72:75], a[8:11], v[0:3]\n\t"
    "v_mfma_f32_16x16x32_bf16 v[0:3], a[76:79], a[12:15], v[0:3]\n\t"
    "v_mfma_f32_16x16x32_bf16 v[4:7], a[64:67], a[16:19], 0\n\t"
    "v_mfma_f32_16x16x32_bf16 v[4:7], a[68:71], a[20:23], v[4:7]\n\t"
    "v_mfma_f32_16x16x32_bf16 v[4:7], a[72:75], a[24:27], v[4:7]\n\t"
    "v_mfma_f32_16x16x32_bf16 v[4:7], a[76:79], a[28:31], v[4:7]\n\t"
    "v_mfma_f32_16x16x32_bf16 v[8:11], a[64:67], a[32:35], 0\n\t"
    "v_mfma_f32_16x16x32_bf16 v[8:11], a[68:71], a[36:39], v[8:11]\n\t"
    "v_mfma_f32_16x16x32_bf16 v[8:11], a[72:75], a[40:43], v[8:11]\n\t"
    "v_mfma_f32_16x16x32_bf16 v[8:11], a[76:79], a[44:47], v[8:11]\n\t"
    "v_mfma_f32_16x16x32_bf16 v[12:15], a[64:67], v[176:179], 0\n\t"
    "v_mfma_f32_16x16x32_bf16 v[12:15], a[68:71], v[180:183], v[12:15]\n\t"
    "v_mfma_f32_16x16x32_bf16 v[12:15], a[72:75], v[184:187], v[12:15]\n\t"
    "v_mfma_f32_16x16x32_bf16 v[12:15], a[76:79], v[188:191], v[12:15]\n\t"
    "s_nop 7\n\t"
    "v_max3_f32 v248, v0, v1, v248\n\t"
    "v_max3_f32 v248, v2, v3, v248\n\t"
    "v_max3_f32 v249, v4, v5, v249\n\t"
    "v_max3_f32 v249, v6, v7, v249\n\t"
    "v_max3_f32 v250, v8, v9, v250\n\t"
    "v_max3_f32 v250, v10, v11, v250\n\t"
    "v_max3_f32 v251, v12, v13, v251\n\t"
    "v_max3_f32 v251, v14, v15, v251\n\t"
    "s_add_u32 s43, s43, 4096\n\t"
    "s_cmp_lt_u32 s43, 0x80000\n\t"
    "s_cbranch_scc1 L_fbloop%=\n\t"
    "L_fbdone%=:\n\t"
    "s_barrier\n\t"
    "s_add_u32 m0, s37, 32768\n\t"
    "s_add_u32 s36, s37, 0\n\t"
    "buffer_load_dwordx4 v236, s[32:35], s36 offen offset:0  lds\n\t"
    "buffer_load_dwordx4 v237, s[32:35], s36 offen offset:1024  lds\n\t"
    "buffer_load_dwordx4 v238, s[32:35], s36 offen offset:2048  lds\n\t"
    "buffer_load_dwordx4 v239, s[32:35], s36 offen offset:3072  lds\n\t"
    "s_add_u32 m0, s37, 49152\n\t"
    "s_add_u32 s36, s37, 16384\n\t"
    "buffer_load_dwordx4 v236, s[32:35], s36 offen offset:0  lds\n\t"
    "buffer_load_dwordx4 v237, s[32:35], s36 offen offset:1024  lds\n\t"
    "buffer_load_dwordx4 v238, s[32:35], s36 offen offset:2048  lds\n\t"
    "buffer_load_dwordx4 v239, s[32:35], s36 offen offset:3072  lds\n\t"
    "s_add_u32 m0, s37, 0\n\t"
    "s_add_u32 s36, s37, 32768\n\t"
    "buffer_load_dwordx4 v236, s[32:35], s36 offen offset:0  lds\n\t"
    "buffer_load_dwordx4 v237, s[32:35], s36 offen offset:1024  lds\n\t"
    "buffer_load_dwordx4 v238, s[32:35], s36 offen offset:2048  lds\n\t"
    "buffer_load_dwordx4 v239, s[32:35], s36 offen offset:3072  lds\n\t"
    "s_add_u32 m0, s37, 16384\n\t"
    "s_add_u32 s36, s37, 49152\n\t"
    "buffer_load_dwordx4 v236, s[32:35], s36 offen offset:0  lds\n\t"
    "buffer_load_dwordx4 v237, s[32:35], s36 offen offset:1024  lds\n\t"
    "buffer_load_dwordx4 v238, s[32:35], s36 offen offset:2048  lds\n\t"
    "buffer_load_dwordx4 v239, s[32:35], s36 offen offset:3072  lds\n\t"
    "s_add_u32 m0, s37, 65536\n\t"
    "s_add_u32 s36, s37, 0\n\t"
    "buffer_load_dwordx4 v240, s[24:27], s36 offen offset:0  lds\n\t"
    "buffer_load_dwordx4 v241, s[24:27], s36 offen offset:1024  lds\n\t"
    "buffer_load_dwordx4 v240, s[24:27], s36 offen offset:2048  lds\n\t"
    "buffer_load_dwordx4 v241, s[24:27], s36 offen offset:3072  lds\n\t"
    "s_add_u32 m0, s37, 81920\n\t"
    "s_add_u32 s36, s37, 16384\n\t"
    "buffer_load_dwordx4 v240, s[24:27], s36 offen offset:0  lds\n\t"
    "buffer_load_dwordx4 v241, s[24:27], s36 offen offset:1024  lds\n\t"
    "buffer_load_dwordx4 v240, s[24:27], s36 offen offset:2048  lds\n\t"
    "buffer_load_dwordx4 v241, s[24:27], s36 offen offset:3072  lds\n\t"
    "s_add_u32 m0, s37, 98304\n\t"
    "s_add_u32 s36, s37, 32768\n\t"
    "buffer_load_dwordx4 v240, s[24:27], s36 offen offset:0  lds\n\t"
    "buffer_load_dwordx4 v241, s[24:27], s36 offen offset:1024  lds\n\t"
    "buffer_load_dwordx4 v240, s[24:27], s36 offen offset:2048  lds\n\t"
    "buffer_load_dwordx4 v241, s[24:27], s36 offen offset:3072  lds\n\t"
    "v_mov_b32 v0, v248\n\t"
    "v_mov_b32 v4, v249\n\t"
    "v_mov_b32 v8, v250\n\t"
    "v_mov_b32 v12, v251\n\t"
    "v_permlane16_swap_b32 v248, v0\n\t"
    "v_permlane16_swap_b32 v249, v4\n\t"
    "v_permlane16_swap_b32 v250, v8\n\t"
    "v_permlane16_swap_b32 v251, v12\n\t"
    "v_max_f32 v248, v248, v0\n\t"
    "v_max_f32 v249, v249, v4\n\t"
    "v_max_f32 v250, v250, v8\n\t"
    "v_max_f32 v251, v251, v12\n\t"
    "v_mov_b32 v0, v248\n\t"
    "v_mov_b32 v4, v249\n\t"
    "v_mov_b32 v8, v250\n\t"
    "v_mov_b32 v12, v251\n\t"
    "v_permlane32_swap_b32 v248, v0\n\t"
    "v_permlane32_swap_b32 v249, v4\n\t"
    "v_permlane32_swap_b32 v250, v8\n\t"
    "v_permlane32_swap_b32 v251, v12\n\t"
    "v_max_f32 v248, v248, v0\n\t"
    "v_max_f32 v249, v249, v4\n\t"
    "v_max_f32 v250, v250, v8\n\t"
    "v_max_f32 v251, v251, v12\n\t"
    "v_sub_f32 v192, 0, v248\n\t"
    "v_sub_f32 v193, 0, v248\n\t"
    "v_sub_f32 v194, 0, v248\n\t"
    "v_sub_f32 v195, 0, v248\n\t"
    "v_sub_f32 v196, 0, v249\n\t"
    "v_sub_f32 v197, 0, v249\n\t"
    "v_sub_f32 v198, 0, v249\n\t"
    "v_sub_f32 v199, 0, v249\n\t"
    "v_sub_f32 v200, 0, v250\n\t"
    "v_sub_f32 v201, 0, v250\n\t"
    "v_sub_f32 v202, 0, v250\n\t"
    "v_sub_f32 v203, 0, v250\n\t"
    "v_sub_f32 v204, 0, v251\n\t"
    "v_sub_f32 v205, 0, v251\n\t"
    "v_sub_f32 v206, 0, v251\n\t"
    "v_sub_f32 v207, 0, v251\n\t"
    "v_mov_b32 v0, 0\n\t"
    "v_mov_b32 v1, 0\n\t"
    "v_mov_b32 v2, 0\n\t"
    "v_mov_b32 v3, 0\n\t"
    "v_mov_b32 v4, 0\n\t"
    "v_mov_b32 v5, 0\n\t"
    "v_mov_b32 v6, 0\n\t"
    "v_mov_b32 v7, 0\n\t"
    "v_mov_b32 v8, 0\n\t"
    "v_mov_b32 v9, 0\n\t"
    "v_mov_b32 v10, 0\n\t"
    "v_mov_b32 v11, 0\n\t"
    "v_mov_b32 v12, 0\n\t"
    "v_mov_b32 v13, 0\n\t"
    "v_mov_b32 v14, 0\n\t"
    "v_mov_b32 v15, 0\n\t"
    "v_mov_b32 v16, 0\n\t"
    "v_mov_b32 v17, 0\n\t"
    "v_mov_b32 v18, 0\n\t"
    "v_mov_b32 v19, 0\n\t"
    "v_mov_b32 v20, 0\n\t"
    "v_mov_b32 v21, 0\n\t"
    "v_mov_b32 v22, 0\n\t"
    "v_mov_b32 v23, 0\n\t"
    "v_mov_b32 v24, 0\n\t"
    "v_mov_b32 v25, 0\n\t"
    "v_mov_b32 v26, 0\n\t"
    "v_mov_b32 v27, 0\n\t"
    "v_mov_b32 v28, 0\n\t"
    "v_mov_b32 v29, 0\n\t"
    "v_mov_b32 v30, 0\n\t"
    "v_mov_b32 v31, 0\n\t"
    "v_mov_b32 v32, 0\n\t"
    "v_mov_b32 v33, 0\n\t"
    "v_mov_b32 v34, 0\n\t"
    "v_mov_b32 v35, 0\n\t"
    "v_mov_b32 v36, 0\n\t"
    "v_mov_b32 v37, 0\n\t"
    "v_mov_b32 v38, 0\n\t"
    "v_mov_b32 v39, 0\n\t"
    "v_mov_b32 v40, 0\n\t"
    "v_mov_b32 v41, 0\n\t"
    "v_mov_b32 v42, 0\n\t"
    "v_mov_b32 v43, 0\n\t"
    "v_mov_b32 v44, 0\n\t"
    "v_mov_b32 v45, 0\n\t"
    "v_mov_b32 v46, 0\n\t"
    "v_mov_b32 v47, 0\n\t"
    "v_mov_b32 v48, 0\n\t"
    "v_mov_b32 v49, 0\n\t"
    "v_mov_b32 v50, 0\n\t"
    "v_mov_b32 v51, 0\n\t"
    "v_mov_b32 v52, 0\n\t"
    "v_mov_b32 v53, 0\n\t"
    "v_mov_b32 v54, 0\n\t"
    "v_mov_b32 v55, 0\n\t"
    "v_mov_b32 v56, 0\n\t"
    "v_mov_b32 v57, 0\n\t"
    "v_mov_b32 v58, 0\n\t"
    "v_mov_b32 v59, 0\n\t"
    "v_mov_b32 v60, 0\n\t"
    "v_mov_b32 v61, 0\n\t"
    "v_mov_b32 v62, 0\n\t"
    "v_mov_b32 v63, 0\n\t"
    "v_mov_b32 v64, 0\n\t"
    "v_mov_b32 v65, 0\n\t"
    "v_mov_b32 v66, 0\n\t"
    "v_mov_b32 v67, 0\n\t"
    "v_mov_b32 v68, 0\n\t"
    "v_mov_b32 v69, 0\n\t"
    "v_mov_b32 v70, 0\n\t"
    "v_mov_b32 v71, 0\n\t"
    "v_mov_b32 v72, 0\n\t"
    "v_mov_b32 v73, 0\n\t"
    "v_mov_b32 v74, 0\n\t"
    "v_mov_b32 v75, 0\n\t"
    "v_mov_b32 v76, 0\n\t"
    "v_mov_b32 v77, 0\n\t"
    "v_mov_b32 v78, 0\n\t"
    "v_mov_b32 v79, 0\n\t"
    "v_mov_b32 v80, 0\n\t"
    "v_mov_b32 v81, 0\n\t"
    "v_mov_b32 v82, 0\n\t"
    "v_mov_b32 v83, 0\n\t"
    "v_mov_b32 v84, 0\n\t"
    "v_mov_b32 v85, 0\n\t"
    "v_mov_b32 v86, 0\n\t"
    "v_mov_b32 v87, 0\n\t"
    "v_mov_b32 v88, 0\n\t"
    "v_mov_b32 v89, 0\n\t"
    "v_mov_b32 v90, 0\n\t"
    "v_mov_b32 v91, 0\n\t"
    "v_mov_b32 v92, 0\n\t"
    "v_mov_b32 v93, 0\n\t"
    "v_mov_b32 v94, 0\n\t"
    "v_mov_b32 v95, 0\n\t"
    "v_mov_b32 v96, 0\n\t"
    "v_mov_b32 v97, 0\n\t"
    "v_mov_b32 v98, 0\n\t"
    "v_mov_b32 v99, 0\n\t"
    "v_mov_b32 v100, 0\n\t"
    "v_mov_b32 v101, 0\n\t"
    "v_mov_b32 v102, 0\n\t"
    "v_mov_b32 v103, 0\n\t"
    "v_mov_b32 v104, 0\n\t"
    "v_mov_b32 v105, 0\n\t"
    "v_mov_b32 v106, 0\n\t"
    "v_mov_b32 v107, 0\n\t"
    "v_mov_b32 v108, 0\n\t"
    "v_mov_b32 v109, 0\n\t"
    "v_mov_b32 v110, 0\n\t"
    "v_mov_b32 v111, 0\n\t"
    "v_mov_b32 v112, 0\n\t"
    "v_mov_b32 v113, 0\n\t"
    "v_mov_b32 v114, 0\n\t"
    "v_mov_b32 v115, 0\n\t"
    "v_mov_b32 v116, 0\n\t"
    "v_mov_b32 v117, 0\n\t"
    "v_mov_b32 v118, 0\n\t"
    "v_mov_b32 v119, 0\n\t"
    "v_mov_b32 v120, 0\n\t"
    "v_mov_b32 v121, 0\n\t"
    "v_mov_b32 v122, 0\n\t"
    "v_mov_b32 v123, 0\n\t"
    "v_mov_b32 v124, 0\n\t"
    "v_mov_b32 v125, 0\n\t"
    "v_mov_b32 v126, 0\n\t"
    "v_mov_b32 v127, 0\n\t"
    "v_mov_b32 v144, 0\n\t"
    "v_mov_b32 v145, 0\n\t"
    "v_mov_b32 v146, 0\n\t"
    "v_mov_b32 v147, 0\n\t"
    "v_mov_b32 v148, 0\n\t"
    "v_mov_b32 v149, 0\n\t"
    "v_mov_b32 v150, 0\n\t"
    "v_mov_b32 v151, 0\n\t"
    "v_mov_b32 v152, 0\n\t"
    "v_mov_b32 v153, 0\n\t"
    "v_mov_b32 v154, 0\n\t"
    "v_mov_b32 v155, 0\n\t"
    "v_mov_b32 v156, 0\n\t"
    "v_mov_b32 v157, 0\n\t"
    "v_mov_b32 v158, 0\n\t"
    "v_mov_b32 v159, 0\n\t"
    "v_mov_b32 v160, 0\n\t"
    "v_mov_b32 v161, 0\n\t"
    "v_mov_b32 v162, 0\n\t"
    "v_mov_b32 v163, 0\n\t"
    "v_mov_b32 v164, 0\n\t"
    "v_mov_b32 v165, 0\n\t"
    "v_mov_b32 v166, 0\n\t"
    "v_mov_b32 v167, 0\n\t"
    "v_mov_b32 v168, 0\n\t"
    "v_mov_b32 v169, 0\n\t"
    "v_mov_b32 v170, 0\n\t"
    "v_mov_b32 v171, 0\n\t"
    "v_mov_b32 v172, 0\n\t"
    "v_mov_b32 v173, 0\n\t"
    "v_mov_b32 v174, 0\n\t"
    "v_mov_b32 v175, 0\n\t"
    "v_mov_b32 v208, 0\n\t"
    "v_mov_b32 v209, 0\n\t"
    "v_mov_b32 v210, 0\n\t"
    "v_mov_b32 v211, 0\n\t"
    "v_mov_b32 v212, 0\n\t"
    "v_mov_b32 v213, 0\n\t"
    "v_mov_b32 v214, 0\n\t"
    "v_mov_b32 v215, 0\n\t"
    "v_mov_b32 v216, 0\n\t"
    "v_mov_b32 v217, 0\n\t"
    "v_mov_b32 v218, 0\n\t"
    "v_mov_b32 v219, 0\n\t"
    "v_mov_b32 v220, 0\n\t"
    "v_mov_b32 v221, 0\n\t"
    "v_mov_b32 v222, 0\n\t"
    "v_mov_b32 v223, 0\n\t"
    "v_mov_b32 v136, 0xff800000\n\t"
    "v_mov_b32 v137, 0xff800000\n\t"
    "v_mov_b32 v138, 0xff800000\n\t"
    "v_mov_b32 v139, 0xff800000\n\t"
    "v_mov_b32 v140, 0xff800000\n\t"
    "v_mov_b32 v141, 0xff800000\n\t"
    "v_mov_b32 v142, 0xff800000\n\t"
    "v_mov_b32 v143, 0xff800000\n\t"
    "v_accvgpr_write_b32 a128, 0\n\t"
    "v_accvgpr_write_b32 a129, 0\n\t"
    "v_accvgpr_write_b32 a130, 0\n\t"
    "v_accvgpr_write_b32 a131, 0\n\t"
    "v_accvgpr_write_b32 a132, 0\n\t"
    "v_accvgpr_write_b32 a133, 0\n\t"
    "v_accvgpr_write_b32 a134, 0\n\t"
    "v_accvgpr_write_b32 a135, 0\n\t"
    "v_accvgpr_write_b32 a136, 0\n\t"
    "v_accvgpr_write_b32 a137, 0\n\t"
    "v_accvgpr_write_b32 a138, 0\n\t"
    "v_accvgpr_write_b32 a139, 0\n\t"
    "v_accvgpr_write_b32 a140, 0\n\t"
    "v_accvgpr_write_b32 a141, 0\n\t"
    "v_accvgpr_write_b32 a142, 0\n\t"
    "v_accvgpr_write_b32 a143, 0\n\t"
    "v_accvgpr_write_b32 a144, 0\n\t"
    "v_accvgpr_write_b32 a145, 0\n\t"
    "v_accvgpr_write_b32 a146, 0\n\t"
    "v_accvgpr_write_b32 a147, 0\n\t"
    "v_accvgpr_write_b32 a148, 0\n\t"
    "v_accvgpr_write_b32 a149, 0\n\t"
    "v_accvgpr_write_b32 a150, 0\n\t"
    "v_accvgpr_write_b32 a151, 0\n\t"
    "v_accvgpr_write_b32 a152, 0\n\t"
    "v_accvgpr_write_b32 a153, 0\n\t"
    "v_accvgpr_write_b32 a154, 0\n\t"
    "v_accvgpr_write_b32 a155, 0\n\t"
    "v_accvgpr_write_b32 a156, 0\n\t"
    "v_accvgpr_write_b32 a157, 0\n\t"
    "v_accvgpr_write_b32 a158, 0\n\t"
    "v_accvgpr_write_b32 a159, 0\n\t"
    "v_accvgpr_write_b32 a160, 0\n\t"
    "v_accvgpr_write_b32 a161, 0\n\t"
    "v_accvgpr_write_b32 a162, 0\n\t"
    "v_accvgpr_write_b32 a163, 0\n\t"
    "v_accvgpr_write_b32 a164, 0\n\t"
    "v_accvgpr_write_b32 a165, 0\n\t"
    "v_accvgpr_write_b32 a166, 0\n\t"
    "v_accvgpr_write_b32 a167, 0\n\t"
    "v_accvgpr_write_b32 a168, 0\n\t"
    "v_accvgpr_write_b32 a169, 0\n\t"
    "v_accvgpr_write_b32 a170, 0\n\t"
    "v_accvgpr_write_b32 a171, 0\n\t"
    "v_accvgpr_write_b32 a172, 0\n\t"
    "v_accvgpr_write_b32 a173, 0\n\t"
    "v_accvgpr_write_b32 a174, 0\n\t"
    "v_accvgpr_write_b32 a175, 0\n\t"
    "v_accvgpr_write_b32 a176, 0\n\t"
    "v_accvgpr_write_b32 a177, 0\n\t"
    "v_accvgpr_write_b32 a178, 0\n\t"
    "v_accvgpr_write_b32 a179, 0\n\t"
    "v_accvgpr_write_b32 a180, 0\n\t"
    "v_accvgpr_write_b32 a181, 0\n\t"
    "v_accvgpr_write_b32 a182, 0\n\t"
    "v_accvgpr_write_b32 a183, 0\n\t"
    "v_accvgpr_write_b32 a184, 0\n\t"
    "v_accvgpr_write_b32 a185, 0\n\t"
    "v_accvgpr_write_b32 a186, 0\n\t"
    "v_accvgpr_write_b32 a187, 0\n\t"
    "v_accvgpr_write_b32 a188, 0\n\t"
    "v_accvgpr_write_b32 a189, 0\n\t"
    "v_accvgpr_write_b32 a190, 0\n\t"
    "v_accvgpr_write_b32 a191, 0\n\t"
    "s_waitcnt vmcnt(0)\n\t"
    "s_barrier\n\t"
    "ds_read_b128 a[64:67], v224 offset:32768\n\t"
    "ds_read_b128 a[68:71], v225 offset:32768\n\t"
    "ds_read_b128 a[72:75], v226 offset:32768\n\t"
    "ds_read_b128 a[76:79], v227 offset:32768\n\t"
    "ds_read_b128 a[80:83], v224 offset:36864\n\t"
    "ds_read_b128 a[84:87], v225 offset:36864\n\t"
    "ds_read_b128 a[88:91], v226 offset:36864\n\t"
    "ds_read_b128 a[92:95], v227 offset:36864\n\t"
    "s_waitcnt lgkmcnt(0)\n\t"
    "s_call_b64 s[98:99], L_entry%=\n\t"
    "L_fbdrv1%=:\n\t"
    "s_mov_b32 s97, 7\n\t"
    "L_fbdrv2%=:\n\t"
    "s_call_b64 s[98:99], L_loop%=\n\t"
    "L_fbdrv3%=:\n\t"
    "s_sub_u32 s97, s97, 1\n\t"
    "s_cmp_lg_u32 s97, 0\n\t"
    "s_cbranch_scc1 L_fbdrv2%=\n\t"
    "s_branch L_drain%=\n\t"
    "L_fbend%=:\n\t"
    :
    : "{s[0:1]}"(__builtin_amdgcn_kernarg_segment_ptr()), "{s2}"(blockIdx.x), "{v0}"(threadIdx.x), "{s3}"((unsigned)(size_t)lds)
    : "memory", "vcc", "scc", "v1", "v255", "a0", "a255", "s4", "s5", "s6", "s7", "s8", "s9", R10("s", 1), R10("s", 2), "s30", "s31", "s33", "s34", "s35", "s36", "s37", "s38", "s39", R10("s", 4), R10("s", 5), R10("s", 6),
      R10("s", 7), R10("s", 8), R10("s", 9));
}

extern "C" void kernel_launch(void* const* d_in, const int* in_sizes, int n_in, void* d_out, int out_size, void* d_ws, size_t ws_size, hipStream_t stream) {
  k_main<<<256, 256, 0, stream>>>(d_in[0], d_in[10], d_in[9], (char*)d_ws + (192u << 20), d_in[3], d_in[1], d_in[2], d_out, d_in[4], d_in[6], d_in[7], d_in[8]);
}
```
